```python
import torch

def _softmax(x, axis=-1):
    return torch.softmax(x, dim=axis)

def _gelu(x):
    return torch.nn.functional.gelu(x, approximate='tanh')

def _silu(x):
    return torch.nn.functional.silu(x)

def _sigmoid(x):
    return torch.sigmoid(x)

def _layernorm(x, axis=-1, eps=1e-05):
    m = x.mean(dim=axis, keepdim=True)
    v = x.var(dim=axis, keepdim=True, unbiased=False)
    return (x - m) / torch.sqrt(v + eps)

def _scatter_add(tgt, idx, src, axis=0):
    return tgt.clone().index_add_(axis, idx, src)

def _segment_sum(src, seg, n):
    out = torch.zeros((n,) + src.shape[1:], dtype=src.dtype)
    return out.index_add_(0, seg, src)

def _pad(x, pw):
    flat = tuple((v for pair in reversed(pw) for v in pair))
    return torch.nn.functional.pad(x, flat)
kernel_name = 'standalone__factorization_afm__recsys'

def setup_inputs():

    _w = lambda a: (lambda t: t.to(torch.bfloat16) if t.is_floating_point() else t)(torch.as_tensor(a))
    B, F, D, V = (1024, 26, 128, min(1000000, 100000))
    A = D
    ii, jj = tuple(torch.triu_indices(F, F, 1).unbind(0))
    return {'idx': _w((lambda lo, hi, *s: torch.randint(lo, hi, s))(0, V, B, F).to(torch.int64)), 'table': _w((lambda *s: torch.randn(*s, dtype=torch.bfloat16) * 0.8)(V, D)), 'ii': _w(ii.to(torch.int64)), 'jj': _w(jj.to(torch.int64)), 'W': _w((lambda *s: torch.randn(*s, dtype=torch.bfloat16) * 0.4)(D, A) * (1.0 / D ** 0.5)), 'b': _w((lambda *s: torch.randn(*s, dtype=torch.bfloat16) * 0.4)(A)), 'h': _w((lambda *s: torch.randn(*s, dtype=torch.bfloat16) * 0.4)(A)), 'p': _w((lambda *s: torch.randn(*s, dtype=torch.bfloat16) * 0.8)(D))}

def reference(idx, table, ii, jj, W, b, h, p):
    emb = table[idx]
    ei = emb[:, ii, :].clone()
    ej = emb[:, jj, :].clone()
    pair = ei * ej
    hid = pair @ W + b
    hid = 0.5 * (hid + torch.abs(hid))
    e = hid @ h
    alpha = _softmax(e, axis=-1)
    pooled = torch.einsum('bp,bpd->bd', alpha, pair)
    return pooled @ p
```

```cpp
#include <hip/hip_runtime.h>
#include <hip/hip_bf16.h>
#include <stdint.h>

#define AFM_HAS_STAMPS 0

typedef float  f32x2  __attribute__((ext_vector_type(2)));
typedef __bf16 bf16x2 __attribute__((ext_vector_type(2)));

__device__ __forceinline__ float bf2f(unsigned short u) { return __builtin_bit_cast(float, (unsigned)u << 16); }
__device__ __forceinline__ unsigned short f2bf(float x) {
  f32x2 v = {x, 0.f}; bf16x2 r = __builtin_convertvector(v, bf16x2);
  return (unsigned short)(__builtin_bit_cast(unsigned, r) & 0xffffu);
}
__device__ __forceinline__ float rbf(float x) { return bf2f(f2bf(x)); }

static __device__ const __attribute__((aligned(128), used)) unsigned afm_tab[4096] asm("afm_tab") = {
  40704, 41568, 38400, 41856, 41568, 40992, 38416, 42144, 41280, 40992, 38432, 42720, 39552, 40416, 38448, 41856, 39840, 38688, 38464, 42144, 39840, 38976, 38480, 42432,
  38688, 39552, 38496, 42720, 38976, 40128, 38512, 40704, 38400, 39264, 38528, 42144, 39840, 38400, 38544, 42432, 38688, 40128, 38560, 41856, 39264, 40128, 38576, 42144,
  40704, 40992, 38592, 42432, 40416, 38976, 38608, 42720, 41280, 41568, 38624, 40704, 41280, 40704, 38640, 40992, 40720, 41584, 40704, 41872, 41584, 41008, 40720, 42160,
  41296, 41008, 40736, 42736, 39568, 40432, 40752, 41872, 39856, 38704, 40768, 42160, 39856, 38992, 40784, 42448, 38704, 39568, 40800, 42736, 38992, 40144, 40816, 40720,
  38416, 39280, 40832, 42160, 39856, 38416, 40848, 42448, 38704, 40144, 40864, 41872, 39280, 40144, 40880, 42160, 40720, 41008, 40896, 42448, 40432, 38992, 40912, 42736,
  41296, 41584, 40928, 40720, 41296, 40720, 40944, 41008, 40736, 41600, 43008, 41888, 41600, 41024, 43024, 42176, 41312, 41024, 43040, 42752, 39584, 40448, 43056, 41888,
  39872, 38720, 43072, 42176, 39872, 39008, 43088, 42464, 38720, 39584, 43104, 42752, 39008, 40160, 43120, 40736, 38432, 39296, 43136, 42176, 39872, 38432, 43152, 42464,
  38720, 40160, 43168, 41888, 39296, 40160, 43184, 42176, 40736, 41024, 43200, 42464, 40448, 39008, 43216, 42752, 41312, 41600, 43232, 40736, 41312, 40736, 43248, 41024,
  40752, 41616, 37536, 41904, 41616, 41040, 37552, 42192, 41328, 41040, 37568, 42768, 39600, 40464, 37584, 41904, 39888, 38736, 37600, 42192, 39888, 39024, 37616, 42480,
  38736, 39600, 37632, 42768, 39024, 40176, 37648, 40752, 38448, 39312, 37664, 42192, 39888, 38448, 37680, 42480, 38736, 40176, 37696, 41904, 39312, 40176, 37712, 42192,
  40752, 41040, 37728, 42480, 40464, 39024, 37744, 42768, 41328, 41616, 37760, 40752, 41328, 40752, 37776, 41040, 44736, 43296, 46464, 82016, 44448, 43008, 46752, 82304,
  44736, 43872, 47040, 98496, 43008, 44160, 47328, 98784, 42144, 41280, 47616, 99072, 43296, 44160, 47616, 99360, 41856, 42720, 47616, 99648, 43872, 43008, 47616, 49920,
  45024, 44160, 63808, 49920, 43584, 44736, 64096, 49920, 41856, 40992, 64384, 49920, 42432, 41280, 64672, 49920, 44448, 43296, 64960, 49920, 43584, 44448, 81152, 49920,
  43584, 45024, 81440, 49920, 41568, 42432, 81728, 49920, 44752, 43312, 46480, 82032, 44464, 43024, 46768, 82320, 44752, 43888, 47056, 98512, 43024, 44176, 47344, 98800,
  42160, 41296, 47632, 99088, 43312, 44176, 47632, 99376, 41872, 42736, 47632, 99664, 43888, 43024, 47632, 49936, 45040, 44176, 63824, 49936, 43600, 44752, 64112, 49936,
  41872, 41008, 64400, 49936, 42448, 41296, 64688, 49936, 44464, 43312, 64976, 49936, 43600, 44464, 81168, 49936, 43600, 45040, 81456, 49936, 41584, 42448, 81744, 49936,
  44768, 43328, 46496, 82048, 44480, 43040, 46784, 82336, 44768, 43904, 47072, 98528, 43040, 44192, 47360, 98816, 42176, 41312, 47648, 99104, 43328, 44192, 47648, 99392,
  41888, 42752, 47648, 99680, 43904, 43040, 47648, 49952, 45056, 44192, 63840, 49952, 43616, 44768, 64128, 49952, 41888, 41024, 64416, 49952, 42464, 41312, 64704, 49952,
  44480, 43328, 64992, 49952, 43616, 44480, 81184, 49952, 43616, 45056, 81472, 49952, 41600, 42464, 81760, 49952, 44784, 43344, 46512, 82064, 44496, 43056, 46800, 82352,
  44784, 43920, 47088, 98544, 43056, 44208, 47376, 98832, 42192, 41328, 47664, 99120, 43344, 44208, 47664, 99408, 41904, 42768, 47664, 99696, 43920, 43056, 47664, 49968,
  45072, 44208, 63856, 49968, 43632, 44784, 64144, 49968, 41904, 41040, 64432, 49968, 42480, 41328, 64720, 49968, 44496, 43344, 65008, 49968, 43632, 44496, 81200, 49968,
  43632, 45072, 81488, 49968, 41616, 42480, 81776, 49968, 51712, 54272, 52992, 51152, 51716, 54352, 52996, 51236, 51720, 54432, 53000, 51244, 51724, 54512, 53004, 51404,
  51728, 54592, 53008, 51488, 51732, 54672, 53012, 51572, 51736, 54752, 53016, 51580, 51740, 54832, 53020, 51100, 51792, 54276, 53072, 51232, 51796, 54356, 53076, 51164,
  51800, 54436, 53080, 51400, 51804, 54516, 53084, 51484, 51808, 54596, 53088, 51568, 51812, 54676, 53092, 51500, 51816, 54756, 53096, 51096, 51820, 54836, 53100, 51180,
  51872, 54280, 53152, 52364, 51876, 54360, 53156, 52444, 51880, 54440, 53160, 52440, 51884, 54520, 53164, 51420, 51888, 54600, 53168, 51172, 51892, 54680, 53172, 51252,
  51896, 54760, 53176, 51396, 51900, 54840, 53180, 51560, 51952, 54284, 53232, 51084, 51956, 54364, 53236, 51092, 51960, 54444, 53240, 51556, 51964, 54524, 53244, 51564,
  51968, 54604, 53248, 52432, 51972, 54684, 53252, 51260, 51976, 54764, 53256, 52524, 51980, 54844, 53260, 52360, 52032, 54288, 53312, 52672, 52036, 54368, 53316, 52756,
  52040, 54448, 53320, 52540, 52044, 54528, 53324, 52684, 52048, 54608, 53328, 52768, 52052, 54688, 53332, 52852, 52056, 54768, 53336, 52860, 52060, 54848, 53340, 52380,
  52112, 54292, 53392, 52752, 52116, 54372, 53396, 52836, 52120, 54452, 53400, 52680, 52124, 54532, 53404, 52764, 52128, 54612, 53408, 52848, 52132, 54692, 53412, 52780,
  52136, 54772, 53416, 52376, 52140, 54852, 53420, 52460, 52192, 54296, 53472, 53712, 52196, 54376, 53476, 53796, 52200, 54456, 53480, 53804, 52204, 54536, 53484, 53964,
  52208, 54616, 53488, 54048, 52212, 54696, 53492, 54132, 52216, 54776, 53496, 54140, 52220, 54856, 53500, 53660, 52272, 54300, 53552, 53792, 52276, 54380, 53556, 53724,
  52280, 54460, 53560, 53960, 52284, 54540, 53564, 54044, 52288, 54620, 53568, 54128, 52292, 54700, 53572, 54060, 52296, 54780, 53576, 53656, 52300, 54860, 53580, 53740,
  46464, 82016, 53736, 38400, 46752, 82304, 53652, 38720, 47040, 98496, 54124, 33408, 47328, 98784, 53952, 33408, 47616, 99072, 52532, 33408, 47616, 99360, 53956, 33408,
  47616, 99648, 52700, 33408, 47616, 47904, 53644, 33408, 63808, 47904, 53980, 33408, 64096, 47904, 54120, 33408, 64384, 47904, 52448, 33408, 64672, 47904, 52536, 33408,
  64960, 47904, 53732, 33408, 81152, 47904, 54040, 33408, 81440, 47904, 53820, 33408, 81728, 47904, 52844, 33408, 46480, 82032, 52592, 38416, 46768, 82320, 52596, 38736,
  47056, 98512, 52600, 33424, 47344, 98800, 52604, 33424, 47632, 99088, 52608, 33424, 47632, 99376, 52612, 33424, 47632, 99664, 52616, 33424, 47632, 47920, 52620, 33424,
  63824, 47920, 52912, 33424, 64112, 47920, 52916, 33424, 64400, 47920, 52920, 33424, 64688, 47920, 52924, 33424, 64976, 47920, 52928, 33424, 81168, 47920, 52932, 33424,
  81456, 47920, 52936, 33424, 81744, 47920, 52940, 33424, 46496, 82048, 51312, 38432, 46784, 82336, 51316, 38752, 47072, 98528, 51320, 33440, 47360, 98816, 51324, 33440,
  47648, 99104, 51328, 33440, 47648, 99392, 51332, 33440, 47648, 99680, 51336, 33440, 47648, 47936, 51340, 33440, 63840, 47936, 51632, 33440, 64128, 47936, 51636, 33440,
  64416, 47936, 51640, 33440, 64704, 47936, 51644, 33440, 64992, 47936, 51648, 33440, 81184, 47936, 51652, 33440, 81472, 47936, 51656, 33440, 81760, 47936, 51660, 33440,
  46512, 82064, 53872, 38448, 46800, 82352, 53876, 38768, 47088, 98544, 53880, 33456, 47376, 98832, 53884, 33456, 47664, 99120, 53888, 33456, 47664, 99408, 53892, 33456,
  47664, 99696, 53896, 33456, 47664, 47952, 53900, 33456, 63856, 47952, 54192, 33456, 64144, 47952, 54196, 33456, 64432, 47952, 54200, 33456, 64720, 47952, 54204, 33456,
  65008, 47952, 54208, 33456, 81200, 47952, 54212, 33456, 81488, 47952, 54216, 33456, 81776, 47952, 54220, 33456, 58048, 58912, 55744, 59200, 58912, 58336, 55760, 59488,
  58624, 58336, 55776, 60064, 56896, 57760, 55792, 59200, 57184, 56032, 55808, 59488, 57184, 56320, 55824, 59776, 56032, 56896, 55840, 60064, 56320, 57472, 55856, 58048,
  55744, 56608, 55872, 59488, 57184, 55744, 55888, 59776, 56032, 57472, 55904, 59200, 56608, 57472, 55920, 59488, 58048, 58336, 55936, 59776, 57760, 56320, 55952, 60064,
  58624, 58912, 55968, 58048, 58624, 58048, 55984, 58336, 58064, 58928, 58048, 59216, 58928, 58352, 58064, 59504, 58640, 58352, 58080, 60080, 56912, 57776, 58096, 59216,
  57200, 56048, 58112, 59504, 57200, 56336, 58128, 59792, 56048, 56912, 58144, 60080, 56336, 57488, 58160, 58064, 55760, 56624, 58176, 59504, 57200, 55760, 58192, 59792,
  56048, 57488, 58208, 59216, 56624, 57488, 58224, 59504, 58064, 58352, 58240, 59792, 57776, 56336, 58256, 60080, 58640, 58928, 58272, 58064, 58640, 58064, 58288, 58352,
  58080, 58944, 60352, 59232, 58944, 58368, 60368, 59520, 58656, 58368, 60384, 60096, 56928, 57792, 60400, 59232, 57216, 56064, 60416, 59520, 57216, 56352, 60432, 59808,
  56064, 56928, 60448, 60096, 56352, 57504, 60464, 58080, 55776, 56640, 60480, 59520, 57216, 55776, 60496, 59808, 56064, 57504, 60512, 59232, 56640, 57504, 60528, 59520,
  58080, 58368, 60544, 59808, 57792, 56352, 60560, 60096, 58656, 58944, 60576, 58080, 58656, 58080, 60592, 58368, 58096, 58960, 54880, 59248, 58960, 58384, 54896, 59536,
  58672, 58384, 54912, 60112, 56944, 57808, 54928, 59248, 57232, 56080, 54944, 59536, 57232, 56368, 54960, 59824, 56080, 56944, 54976, 60112, 56368, 57520, 54992, 58096,
  55792, 56656, 55008, 59536, 57232, 55792, 55024, 59824, 56080, 57520, 55040, 59248, 56656, 57520, 55056, 59536, 58096, 58384, 55072, 59824, 57808, 56368, 55088, 60112,
  58672, 58960, 55104, 58096, 58672, 58096, 55120, 58384, 62080, 60640, 63808, 99360, 61792, 60352, 64096, 99648, 62080, 61216, 64384, 46464, 60352, 61504, 64672, 46752,
  59488, 58624, 64960, 47040, 60640, 61504, 64960, 47328, 59200, 60064, 64960, 47616, 61216, 60352, 64960, 67264, 62368, 61504, 81152, 67264, 60928, 62080, 81440, 67264,
  59200, 58336, 81728, 67264, 59776, 58624, 82016, 67264, 61792, 60640, 82304, 67264, 60928, 61792, 98496, 67264, 60928, 62368, 98784, 67264, 58912, 59776, 99072, 67264,
  62096, 60656, 63824, 99376, 61808, 60368, 64112, 99664, 62096, 61232, 64400, 46480, 60368, 61520, 64688, 46768, 59504, 58640, 64976, 47056, 60656, 61520, 64976, 47344,
  59216, 60080, 64976, 47632, 61232, 60368, 64976, 67280, 62384, 61520, 81168, 67280, 60944, 62096, 81456, 67280, 59216, 58352, 81744, 67280, 59792, 58640, 82032, 67280,
  61808, 60656, 82320, 67280, 60944, 61808, 98512, 67280, 60944, 62384, 98800, 67280, 58928, 59792, 99088, 67280, 62112, 60672, 63840, 99392, 61824, 60384, 64128, 99680,
  62112, 61248, 64416, 46496, 60384, 61536, 64704, 46784, 59520, 58656, 64992, 47072, 60672, 61536, 64992, 47360, 59232, 60096, 64992, 47648, 61248, 60384, 64992, 67296,
  62400, 61536, 81184, 67296, 60960, 62112, 81472, 67296, 59232, 58368, 81760, 67296, 59808, 58656, 82048, 67296, 61824, 60672, 82336, 67296, 60960, 61824, 98528, 67296,
  60960, 62400, 98816, 67296, 58944, 59808, 99104, 67296, 62128, 60688, 63856, 99408, 61840, 60400, 64144, 99696, 62128, 61264, 64432, 46512, 60400, 61552, 64720, 46800,
  59536, 58672, 65008, 47088, 60688, 61552, 65008, 47376, 59248, 60112, 65008, 47664, 61264, 60400, 65008, 67312, 62416, 61552, 81200, 67312, 60976, 62128, 81488, 67312,
  59248, 58384, 81776, 67312, 59824, 58672, 82064, 67312, 61840, 60688, 82352, 67312, 60976, 61840, 98544, 67312, 60976, 62416, 98832, 67312, 58960, 59824, 99120, 67312,
  69056, 71616, 70336, 68496, 69060, 71696, 70340, 68580, 69064, 71776, 70344, 68588, 69068, 71856, 70348, 68748, 69072, 71936, 70352, 68832, 69076, 72016, 70356, 68916,
  69080, 72096, 70360, 68924, 69084, 72176, 70364, 68444, 69136, 71620, 70416, 68576, 69140, 71700, 70420, 68508, 69144, 71780, 70424, 68744, 69148, 71860, 70428, 68828,
  69152, 71940, 70432, 68912, 69156, 72020, 70436, 68844, 69160, 72100, 70440, 68440, 69164, 72180, 70444, 68524, 69216, 71624, 70496, 69708, 69220, 71704, 70500, 69788,
  69224, 71784, 70504, 69784, 69228, 71864, 70508, 68764, 69232, 71944, 70512, 68516, 69236, 72024, 70516, 68596, 69240, 72104, 70520, 68740, 69244, 72184, 70524, 68904,
  69296, 71628, 70576, 68428, 69300, 71708, 70580, 68436, 69304, 71788, 70584, 68900, 69308, 71868, 70588, 68908, 69312, 71948, 70592, 69776, 69316, 72028, 70596, 68604,
  69320, 72108, 70600, 69868, 69324, 72188, 70604, 69704, 69376, 71632, 70656, 70016, 69380, 71712, 70660, 70100, 69384, 71792, 70664, 69884, 69388, 71872, 70668, 70028,
  69392, 71952, 70672, 70112, 69396, 72032, 70676, 70196, 69400, 72112, 70680, 70204, 69404, 72192, 70684, 69724, 69456, 71636, 70736, 70096, 69460, 71716, 70740, 70180,
  69464, 71796, 70744, 70024, 69468, 71876, 70748, 70108, 69472, 71956, 70752, 70192, 69476, 72036, 70756, 70124, 69480, 72116, 70760, 69720, 69484, 72196, 70764, 69804,
  69536, 71640, 70816, 71056, 69540, 71720, 70820, 71140, 69544, 71800, 70824, 71148, 69548, 71880, 70828, 71308, 69552, 71960, 70832, 71392, 69556, 72040, 70836, 71476,
  69560, 72120, 70840, 71484, 69564, 72200, 70844, 71004, 69616, 71644, 70896, 71136, 69620, 71724, 70900, 71068, 69624, 71804, 70904, 71304, 69628, 71884, 70908, 71388,
  69632, 71964, 70912, 71472, 69636, 72044, 70916, 71404, 69640, 72124, 70920, 71000, 69644, 72204, 70924, 71084, 63812, 99364, 71080, 55744, 64100, 99652, 70996, 56064,
  64388, 46468, 71468, 50752, 64676, 46756, 71296, 50752, 64964, 47044, 69876, 50752, 64964, 47332, 71300, 50752, 64964, 47620, 70044, 50752, 64964, 65248, 70988, 50752,
  81156, 65248, 71324, 50752, 81444, 65248, 71464, 50752, 81732, 65248, 69792, 50752, 82020, 65248, 69880, 50752, 82308, 65248, 71076, 50752, 98500, 65248, 71384, 50752,
  98788, 65248, 71164, 50752, 99076, 65248, 70188, 50752, 63828, 99380, 69936, 55760, 64116, 99668, 69940, 56080, 64404, 46484, 69944, 50768, 64692, 46772, 69948, 50768,
  64980, 47060, 69952, 50768, 64980, 47348, 69956, 50768, 64980, 47636, 69960, 50768, 64980, 65264, 69964, 50768, 81172, 65264, 70256, 50768, 81460, 65264, 70260, 50768,
  81748, 65264, 70264, 50768, 82036, 65264, 70268, 50768, 82324, 65264, 70272, 50768, 98516, 65264, 70276, 50768, 98804, 65264, 70280, 50768, 99092, 65264, 70284, 50768,
  63844, 99396, 68656, 55776, 64132, 99684, 68660, 56096, 64420, 46500, 68664, 50784, 64708, 46788, 68668, 50784, 64996, 47076, 68672, 50784, 64996, 47364, 68676, 50784,
  64996, 47652, 68680, 50784, 64996, 65280, 68684, 50784, 81188, 65280, 68976, 50784, 81476, 65280, 68980, 50784, 81764, 65280, 68984, 50784, 82052, 65280, 68988, 50784,
  82340, 65280, 68992, 50784, 98532, 65280, 68996, 50784, 98820, 65280, 69000, 50784, 99108, 65280, 69004, 50784, 63860, 99412, 71216, 55792, 64148, 99700, 71220, 56112,
  64436, 46516, 71224, 50800, 64724, 46804, 71228, 50800, 65012, 47092, 71232, 50800, 65012, 47380, 71236, 50800, 65012, 47668, 71240, 50800, 65012, 65296, 71244, 50800,
  81204, 65296, 71536, 50800, 81492, 65296, 71540, 50800, 81780, 65296, 71544, 50800, 82068, 65296, 71548, 50800, 82356, 65296, 71552, 50800, 98548, 65296, 71556, 50800,
  98836, 65296, 71560, 50800, 99124, 65296, 71564, 50800, 75392, 76256, 73088, 76544, 76256, 75680, 73104, 76832, 75968, 75680, 73120, 77408, 74240, 75104, 73136, 76544,
  74528, 73376, 73152, 76832, 74528, 73664, 73168, 77120, 73376, 74240, 73184, 77408, 73664, 74816, 73200, 75392, 73088, 73952, 73216, 76832, 74528, 73088, 73232, 77120,
  73376, 74816, 73248, 76544, 73952, 74816, 73264, 76832, 75392, 75680, 73280, 77120, 75104, 73664, 73296, 77408, 75968, 76256, 73312, 75392, 75968, 75392, 73328, 75680,
  75408, 76272, 75392, 76560, 76272, 75696, 75408, 76848, 75984, 75696, 75424, 77424, 74256, 75120, 75440, 76560, 74544, 73392, 75456, 76848, 74544, 73680, 75472, 77136,
  73392, 74256, 75488, 77424, 73680, 74832, 75504, 75408, 73104, 73968, 75520, 76848, 74544, 73104, 75536, 77136, 73392, 74832, 75552, 76560, 73968, 74832, 75568, 76848,
  75408, 75696, 75584, 77136, 75120, 73680, 75600, 77424, 75984, 76272, 75616, 75408, 75984, 75408, 75632, 75696, 75424, 76288, 77696, 76576, 76288, 75712, 77712, 76864,
  76000, 75712, 77728, 77440, 74272, 75136, 77744, 76576, 74560, 73408, 77760, 76864, 74560, 73696, 77776, 77152, 73408, 74272, 77792, 77440, 73696, 74848, 77808, 75424,
  73120, 73984, 77824, 76864, 74560, 73120, 77840, 77152, 73408, 74848, 77856, 76576, 73984, 74848, 77872, 76864, 75424, 75712, 77888, 77152, 75136, 73696, 77904, 77440,
  76000, 76288, 77920, 75424, 76000, 75424, 77936, 75712, 75440, 76304, 72224, 76592, 76304, 75728, 72240, 76880, 76016, 75728, 72256, 77456, 74288, 75152, 72272, 76592,
  74576, 73424, 72288, 76880, 74576, 73712, 72304, 77168, 73424, 74288, 72320, 77456, 73712, 74864, 72336, 75440, 73136, 74000, 72352, 76880, 74576, 73136, 72368, 77168,
  73424, 74864, 72384, 76592, 74000, 74864, 72400, 76880, 75440, 75728, 72416, 77168, 75152, 73712, 72432, 77456, 76016, 76304, 72448, 75440, 76016, 75440, 72464, 75728,
  79424, 77984, 81152, 47328, 79136, 77696, 81440, 47616, 79424, 78560, 81728, 63808, 77696, 78848, 82016, 64096, 76832, 75968, 82304, 64384, 77984, 78848, 82304, 64672,
  76544, 77408, 82304, 64960, 78560, 77696, 82304, 84608, 79712, 78848, 98496, 84608, 78272, 79424, 98784, 84608, 76544, 75680, 99072, 84608, 77120, 75968, 99360, 84608,
  79136, 77984, 99648, 84608, 78272, 79136, 46464, 84608, 78272, 79712, 46752, 84608, 76256, 77120, 47040, 84608, 79440, 78000, 81168, 47344, 79152, 77712, 81456, 47632,
  79440, 78576, 81744, 63824, 77712, 78864, 82032, 64112, 76848, 75984, 82320, 64400, 78000, 78864, 82320, 64688, 76560, 77424, 82320, 64976, 78576, 77712, 82320, 84624,
  79728, 78864, 98512, 84624, 78288, 79440, 98800, 84624, 76560, 75696, 99088, 84624, 77136, 75984, 99376, 84624, 79152, 78000, 99664, 84624, 78288, 79152, 46480, 84624,
  78288, 79728, 46768, 84624, 76272, 77136, 47056, 84624, 79456, 78016, 81184, 47360, 79168, 77728, 81472, 47648, 79456, 78592, 81760, 63840, 77728, 78880, 82048, 64128,
  76864, 76000, 82336, 64416, 78016, 78880, 82336, 64704, 76576, 77440, 82336, 64992, 78592, 77728, 82336, 84640, 79744, 78880, 98528, 84640, 78304, 79456, 98816, 84640,
  76576, 75712, 99104, 84640, 77152, 76000, 99392, 84640, 79168, 78016, 99680, 84640, 78304, 79168, 46496, 84640, 78304, 79744, 46784, 84640, 76288, 77152, 47072, 84640,
  79472, 78032, 81200, 47376, 79184, 77744, 81488, 47664, 79472, 78608, 81776, 63856, 77744, 78896, 82064, 64144, 76880, 76016, 82352, 64432, 78032, 78896, 82352, 64720,
  76592, 77456, 82352, 65008, 78608, 77744, 82352, 84656, 79760, 78896, 98544, 84656, 78320, 79472, 98832, 84656, 76592, 75728, 99120, 84656, 77168, 76016, 99408, 84656,
  79184, 78032, 99696, 84656, 78320, 79184, 46512, 84656, 78320, 79760, 46800, 84656, 76304, 77168, 47088, 84656, 86400, 88960, 87680, 85840, 86404, 89040, 87684, 85924,
  86408, 89120, 87688, 85932, 86412, 89200, 87692, 86092, 86416, 89280, 87696, 86176, 86420, 89360, 87700, 86260, 86424, 89440, 87704, 86268, 86428, 89520, 87708, 85788,
  86480, 88964, 87760, 85920, 86484, 89044, 87764, 85852, 86488, 89124, 87768, 86088, 86492, 89204, 87772, 86172, 86496, 89284, 87776, 86256, 86500, 89364, 87780, 86188,
  86504, 89444, 87784, 85784, 86508, 89524, 87788, 85868, 86560, 88968, 87840, 87052, 86564, 89048, 87844, 87132, 86568, 89128, 87848, 87128, 86572, 89208, 87852, 86108,
  86576, 89288, 87856, 85860, 86580, 89368, 87860, 85940, 86584, 89448, 87864, 86084, 86588, 89528, 87868, 86248, 86640, 88972, 87920, 85772, 86644, 89052, 87924, 85780,
  86648, 89132, 87928, 86244, 86652, 89212, 87932, 86252, 86656, 89292, 87936, 87120, 86660, 89372, 87940, 85948, 86664, 89452, 87944, 87212, 86668, 89532, 87948, 87048,
  86720, 88976, 88000, 87360, 86724, 89056, 88004, 87444, 86728, 89136, 88008, 87228, 86732, 89216, 88012, 87372, 86736, 89296, 88016, 87456, 86740, 89376, 88020, 87540,
  86744, 89456, 88024, 87548, 86748, 89536, 88028, 87068, 86800, 88980, 88080, 87440, 86804, 89060, 88084, 87524, 86808, 89140, 88088, 87368, 86812, 89220, 88092, 87452,
  86816, 89300, 88096, 87536, 86820, 89380, 88100, 87468, 86824, 89460, 88104, 87064, 86828, 89540, 88108, 87148, 86880, 88984, 88160, 88400, 86884, 89064, 88164, 88484,
  86888, 89144, 88168, 88492, 86892, 89224, 88172, 88652, 86896, 89304, 88176, 88736, 86900, 89384, 88180, 88820, 86904, 89464, 88184, 88828, 86908, 89544, 88188, 88348,
  86960, 88988, 88240, 88480, 86964, 89068, 88244, 88412, 86968, 89148, 88248, 88648, 86972, 89228, 88252, 88732, 86976, 89308, 88256, 88816, 86980, 89388, 88260, 88748,
  86984, 89468, 88264, 88344, 86988, 89548, 88268, 88428, 81160, 47336, 88424, 73088, 81448, 47624, 88340, 73408, 81736, 63816, 88812, 68096, 82024, 64104, 88640, 68096,
  82312, 64392, 87220, 68096, 82312, 64680, 88644, 68096, 82312, 64968, 87388, 68096, 82312, 82592, 88332, 68096, 98504, 82592, 88668, 68096, 98792, 82592, 88808, 68096,
  99080, 82592, 87136, 68096, 99368, 82592, 87224, 68096, 99656, 82592, 88420, 68096, 46472, 82592, 88728, 68096, 46760, 82592, 88508, 68096, 47048, 82592, 87532, 68096,
  81176, 47352, 87280, 73104, 81464, 47640, 87284, 73424, 81752, 63832, 87288, 68112, 82040, 64120, 87292, 68112, 82328, 64408, 87296, 68112, 82328, 64696, 87300, 68112,
  82328, 64984, 87304, 68112, 82328, 82608, 87308, 68112, 98520, 82608, 87600, 68112, 98808, 82608, 87604, 68112, 99096, 82608, 87608, 68112, 99384, 82608, 87612, 68112,
  99672, 82608, 87616, 68112, 46488, 82608, 87620, 68112, 46776, 82608, 87624, 68112, 47064, 82608, 87628, 68112, 81192, 47368, 86000, 73120, 81480, 47656, 86004, 73440,
  81768, 63848, 86008, 68128, 82056, 64136, 86012, 68128, 82344, 64424, 86016, 68128, 82344, 64712, 86020, 68128, 82344, 65000, 86024, 68128, 82344, 82624, 86028, 68128,
  98536, 82624, 86320, 68128, 98824, 82624, 86324, 68128, 99112, 82624, 86328, 68128, 99400, 82624, 86332, 68128, 99688, 82624, 86336, 68128, 46504, 82624, 86340, 68128,
  46792, 82624, 86344, 68128, 47080, 82624, 86348, 68128, 81208, 47384, 88560, 73136, 81496, 47672, 88564, 73456, 81784, 63864, 88568, 68144, 82072, 64152, 88572, 68144,
  82360, 64440, 88576, 68144, 82360, 64728, 88580, 68144, 82360, 65016, 88584, 68144, 82360, 82640, 88588, 68144, 98552, 82640, 88880, 68144, 98840, 82640, 88884, 68144,
  99128, 82640, 88888, 68144, 99416, 82640, 88892, 68144, 99704, 82640, 88896, 68144, 46520, 82640, 88900, 68144, 46808, 82640, 88904, 68144, 47096, 82640, 88908, 68144,
  92736, 93600, 90432, 93888, 93600, 93024, 90448, 94176, 93312, 93024, 90464, 94752, 91584, 92448, 90480, 93888, 91872, 90720, 90496, 94176, 91872, 91008, 90512, 94464,
  90720, 91584, 90528, 94752, 91008, 92160, 90544, 92736, 90432, 91296, 90560, 94176, 91872, 90432, 90576, 94464, 90720, 92160, 90592, 93888, 91296, 92160, 90608, 94176,
  92736, 93024, 90624, 94464, 92448, 91008, 90640, 94752, 93312, 93600, 90656, 92736, 93312, 92736, 90672, 93024, 92752, 93616, 92736, 93904, 93616, 93040, 92752, 94192,
  93328, 93040, 92768, 94768, 91600, 92464, 92784, 93904, 91888, 90736, 92800, 94192, 91888, 91024, 92816, 94480, 90736, 91600, 92832, 94768, 91024, 92176, 92848, 92752,
  90448, 91312, 92864, 94192, 91888, 90448, 92880, 94480, 90736, 92176, 92896, 93904, 91312, 92176, 92912, 94192, 92752, 93040, 92928, 94480, 92464, 91024, 92944, 94768,
  93328, 93616, 92960, 92752, 93328, 92752, 92976, 93040, 92768, 93632, 95040, 93920, 93632, 93056, 95056, 94208, 93344, 93056, 95072, 94784, 91616, 92480, 95088, 93920,
  91904, 90752, 95104, 94208, 91904, 91040, 95120, 94496, 90752, 91616, 95136, 94784, 91040, 92192, 95152, 92768, 90464, 91328, 95168, 94208, 91904, 90464, 95184, 94496,
  90752, 92192, 95200, 93920, 91328, 92192, 95216, 94208, 92768, 93056, 95232, 94496, 92480, 91040, 95248, 94784, 93344, 93632, 95264, 92768, 93344, 92768, 95280, 93056,
  92784, 93648, 89568, 93936, 93648, 93072, 89584, 94224, 93360, 93072, 89600, 94800, 91632, 92496, 89616, 93936, 91920, 90768, 89632, 94224, 91920, 91056, 89648, 94512,
  90768, 91632, 89664, 94800, 91056, 92208, 89680, 92784, 90480, 91344, 89696, 94224, 91920, 90480, 89712, 94512, 90768, 92208, 89728, 93936, 91344, 92208, 89744, 94224,
  92784, 93072, 89760, 94512, 92496, 91056, 89776, 94800, 93360, 93648, 89792, 92784, 93360, 92784, 89808, 93072, 96768, 95328, 98496, 64672, 96480, 95040, 98784, 64960,
  96768, 95904, 99072, 81152, 95040, 96192, 99360, 81440, 94176, 93312, 99648, 81728, 95328, 96192, 99648, 82016, 93888, 94752, 99648, 82304, 95904, 95040, 99648, 101952,
  97056, 96192, 46464, 101952, 95616, 96768, 46752, 101952, 93888, 93024, 47040, 101952, 94464, 93312, 47328, 101952, 96480, 95328, 47616, 101952, 95616, 96480, 63808, 101952,
  95616, 97056, 64096, 101952, 93600, 94464, 64384, 101952, 96784, 95344, 98512, 64688, 96496, 95056, 98800, 64976, 96784, 95920, 99088, 81168, 95056, 96208, 99376, 81456,
  94192, 93328, 99664, 81744, 95344, 96208, 99664, 82032, 93904, 94768, 99664, 82320, 95920, 95056, 99664, 101968, 97072, 96208, 46480, 101968, 95632, 96784, 46768, 101968,
  93904, 93040, 47056, 101968, 94480, 93328, 47344, 101968, 96496, 95344, 47632, 101968, 95632, 96496, 63824, 101968, 95632, 97072, 64112, 101968, 93616, 94480, 64400, 101968,
  96800, 95360, 98528, 64704, 96512, 95072, 98816, 64992, 96800, 95936, 99104, 81184, 95072, 96224, 99392, 81472, 94208, 93344, 99680, 81760, 95360, 96224, 99680, 82048,
  93920, 94784, 99680, 82336, 95936, 95072, 99680, 101984, 97088, 96224, 46496, 101984, 95648, 96800, 46784, 101984, 93920, 93056, 47072, 101984, 94496, 93344, 47360, 101984,
  96512, 95360, 47648, 101984, 95648, 96512, 63840, 101984, 95648, 97088, 64128, 101984, 93632, 94496, 64416, 101984, 96816, 95376, 98544, 64720, 96528, 95088, 98832, 65008,
  96816, 95952, 99120, 81200, 95088, 96240, 99408, 81488, 94224, 93360, 99696, 81776, 95376, 96240, 99696, 82064, 93936, 94800, 99696, 82352, 95952, 95088, 99696, 102000,
  97104, 96240, 46512, 102000, 95664, 96816, 46800, 102000, 93936, 93072, 47088, 102000, 94512, 93360, 47376, 102000, 96528, 95376, 47664, 102000, 95664, 96528, 63856, 102000,
  95664, 97104, 64144, 102000, 93648, 94512, 64432, 102000, 103744, 106304, 105024, 103184, 103748, 106384, 105028, 103268, 103752, 106464, 105032, 103276, 103756, 106544, 105036, 103436,
  103760, 106624, 105040, 103520, 103764, 106704, 105044, 103604, 103768, 106784, 105048, 103612, 103772, 106864, 105052, 103132, 103824, 106308, 105104, 103264, 103828, 106388, 105108, 103196,
  103832, 106468, 105112, 103432, 103836, 106548, 105116, 103516, 103840, 106628, 105120, 103600, 103844, 106708, 105124, 103532, 103848, 106788, 105128, 103128, 103852, 106868, 105132, 103212,
  103904, 106312, 105184, 104396, 103908, 106392, 105188, 104476, 103912, 106472, 105192, 104472, 103916, 106552, 105196, 103452, 103920, 106632, 105200, 103204, 103924, 106712, 105204, 103284,
  103928, 106792, 105208, 103428, 103932, 106872, 105212, 103592, 103984, 106316, 105264, 103116, 103988, 106396, 105268, 103124, 103992, 106476, 105272, 103588, 103996, 106556, 105276, 103596,
  104000, 106636, 105280, 104464, 104004, 106716, 105284, 103292, 104008, 106796, 105288, 104556, 104012, 106876, 105292, 104392, 104064, 106320, 105344, 104704, 104068, 106400, 105348, 104788,
  104072, 106480, 105352, 104572, 104076, 106560, 105356, 104716, 104080, 106640, 105360, 104800, 104084, 106720, 105364, 104884, 104088, 106800, 105368, 104892, 104092, 106880, 105372, 104412,
  104144, 106324, 105424, 104784, 104148, 106404, 105428, 104868, 104152, 106484, 105432, 104712, 104156, 106564, 105436, 104796, 104160, 106644, 105440, 104880, 104164, 106724, 105444, 104812,
  104168, 106804, 105448, 104408, 104172, 106884, 105452, 104492, 104224, 106328, 105504, 105744, 104228, 106408, 105508, 105828, 104232, 106488, 105512, 105836, 104236, 106568, 105516, 105996,
  104240, 106648, 105520, 106080, 104244, 106728, 105524, 106164, 104248, 106808, 105528, 106172, 104252, 106888, 105532, 105692, 104304, 106332, 105584, 105824, 104308, 106412, 105588, 105756,
  104312, 106492, 105592, 105992, 104316, 106572, 105596, 106076, 104320, 106652, 105600, 106160, 104324, 106732, 105604, 106092, 104328, 106812, 105608, 105688, 104332, 106892, 105612, 105772,
  98508, 64684, 105768, 90432, 98796, 64972, 105684, 90752, 99084, 81164, 106156, 85440, 99372, 81452, 105984, 85440, 99660, 81740, 104564, 85440, 99660, 82028, 105988, 85440,
  99660, 82316, 104732, 85440, 99660, 99936, 105676, 85440, 46476, 99936, 106012, 85440, 46764, 99936, 106152, 85440, 47052, 99936, 104480, 85440, 47340, 99936, 104568, 85440,
  47628, 99936, 105764, 85440, 63820, 99936, 106072, 85440, 64108, 99936, 105852, 85440, 64396, 99936, 104876, 85440, 98524, 64700, 104624, 90448, 98812, 64988, 104628, 90768,
  99100, 81180, 104632, 85456, 99388, 81468, 104636, 85456, 99676, 81756, 104640, 85456, 99676, 82044, 104644, 85456, 99676, 82332, 104648, 85456, 99676, 99952, 104652, 85456,
  46492, 99952, 104944, 85456, 46780, 99952, 104948, 85456, 47068, 99952, 104952, 85456, 47356, 99952, 104956, 85456, 47644, 99952, 104960, 85456, 63836, 99952, 104964, 85456,
  64124, 99952, 104968, 85456, 64412, 99952, 104972, 85456, 98540, 64716, 103344, 90464, 98828, 65004, 103348, 90784, 99116, 81196, 103352, 85472, 99404, 81484, 103356, 85472,
  99692, 81772, 103360, 85472, 99692, 82060, 103364, 85472, 99692, 82348, 103368, 85472, 99692, 99968, 103372, 85472, 46508, 99968, 103664, 85472, 46796, 99968, 103668, 85472,
  47084, 99968, 103672, 85472, 47372, 99968, 103676, 85472, 47660, 99968, 103680, 85472, 63852, 99968, 103684, 85472, 64140, 99968, 103688, 85472, 64428, 99968, 103692, 85472,
  98556, 64732, 105904, 90480, 98844, 65020, 105908, 90800, 99132, 81212, 105912, 85488, 99420, 81500, 105916, 85488, 99708, 81788, 105920, 85488, 99708, 82076, 105924, 85488,
  99708, 82364, 105928, 85488, 99708, 99984, 105932, 85488, 46524, 99984, 106224, 85488, 46812, 99984, 106228, 85488, 47100, 99984, 106232, 85488, 47388, 99984, 106236, 85488,
  47676, 99984, 106240, 85488, 63868, 99984, 106244, 85488, 64156, 99984, 106248, 85488, 64444, 99984, 106252, 85488};

__global__ __launch_bounds__(256, 1) __attribute__((aligned(4096)))
void afm_fast_kernel(const int* __restrict__ idx, const unsigned short* __restrict__ table,
                     const int* __restrict__ ii, const int* __restrict__ jj,
                     const unsigned short* __restrict__ W, const unsigned short* __restrict__ bvec,
                     const unsigned short* __restrict__ hvec, const unsigned short* __restrict__ pvec,
                     unsigned short* __restrict__ out, int B, int unused
                     ) {
  __shared__ __attribute__((aligned(16))) char smem[107776];
  const unsigned lbase = (unsigned)(uintptr_t)(__attribute__((address_space(3))) char*)smem;
  unsigned flag;
  asm volatile(
      "s_load_dwordx16 s[8:23], %1, 0x0\n"
      "s_load_dwordx4 s[24:27], %1, 0x40\n"
      "s_getpc_b64 s[56:57]\n"
      "s_add_u32 s96, s56, afm_tab@rel32@lo+4\n"
      "s_addc_u32 s97, s57, afm_tab@rel32@hi+12\n"
      "v_lshrrev_b32 v254, 6, %3\n"
      "v_and_b32 v255, 63, %3\n"
      "s_bfe_u32 s44, %2, 0x20003\n"
      "v_mad_u32_u24 v255, v254, 10, v255\n"
      "s_mov_b64 exec, 0x3ff\n"
      "v_lshl_add_u32 v255, v255, 2, s44\n"
      "v_lshlrev_b32 v254, 7, v255\n"
      "v_min_u32 v253, 16256, v254\n"
      "global_load_dword v254, v254, s[56:57] offset:1536\n"
      "global_load_dword v253, v253, s[96:97]\n"
      "s_mov_b64 exec, -1\n"
      "s_mov_b32 s41, 0\n"
      "s_mov_b32 s42, 0\n"
      "s_mov_b32 s58, 0\n"
      "s_mov_b32 s35, 0\n"
      "0:\n"
      "v_lshrrev_b32 v2, 6, %3\n"
      "v_bfe_u32 v4, %3, 4, 2\n"
      "v_and_b32 v3, 15, %3\n"
      "v_readfirstlane_b32 s53, v2\n"
      "v_mul_u32_u24 v10, 28, v4\n"
      "v_mad_u32_u24 v11, v4, 24, 4\n"
      "v_min_u32 v10, v10, v11\n"
      "s_and_b32 s50, %2, 7\n"
      "s_bfe_u32 s51, %2, 0x50003\n"
      "s_andn2_b32 s34, %2, 255\n"
      "s_lshl_b32 s50, s50, 5\n"
      "s_add_u32 s34, s34, s51\n"
      "s_add_u32 s34, s34, s50\n"
      "s_lshl2_add_u32 s34, s34, s53\n"
      "s_mul_i32 s50, s34, 104\n"
      "s_mov_b32 s33, s34\n"
      "v_add_u32 v10, s50, v10\n"
      "v_lshlrev_b32 v13, 4, v3\n"
      "v_lshlrev_b32 v225, 3, %3\n"
      "v_min_u32 v225, 1292, v225\n"
      "v_lshl_or_b32 v80, v4, 9, v13\n"
      "v_lshl_add_u32 v80, v2, 11, v80\n"
      "v_add_u32 v81, 8192, v80\n"
      "v_add_u32 v82, 16384, v80\n"
      "v_add_u32 v83, 24576, v80\n"
      "s_lshl2_add_u32 s44, s53, 37888\n"
      "v_mov_b32 v2, s44\n"
      "v_mov_b32 v5, 0\n"
      "v_mov_b32 v1, 0x7f000000\n"
      "s_cmp_lg_u32 s35, 0\n"
      "s_cbranch_scc1 2f\n"
      "ds_write2_b32 v2, v1, v5 offset0:0 offset1:4\n"
      "ds_write_b32 v2, v5 offset:32\n"
      "2:\n"
      "s_mul_i32 s38, s53, 17344\n"
      "s_add_u32 s38, s38, 38400\n"
      "s_add_u32 s54, s38, 12672\n"
      "s_mul_i32 s61, s53, 17088\n"
      "s_add_u32 s61, s61, 45312\n"
      "s_mov_b32 s43, 0xffff0000\n"
      "s_movk_i32 s36, 0x400\n"
      "s_mov_b32 s37, 0x64006400\n"
      "s_mov_b32 s56, 0\n"
      "s_mov_b32 s40, 0\n"
      "s_mov_b32 s60, s41\n"
      "s_mov_b32 s55, 0\n"
      "v_cmp_eq_u32 vcc, 1, v4\n"
      "v_mov_b32 v222, 0x3280\n"
      "v_mov_b32 v145, 0\n"
      "v_cndmask_b32 v144, 0, v222, vcc\n"
      "s_mov_b32 s46, 0\n"
      "s_mov_b32 s47, 0xc7ac4c39\n"
      "v_lshlrev_b32 v223, 4, v4\n"
      "v_lshrrev_b64 v[146:147], v223, s[46:47]\n"
      "s_lshl_b32 s48, s53, 4\n"
      "v_mov_b32 v147, 0\n"
      "v_and_b32 v146, 0xffff, v146\n"
      "v_mov_b32 v215, 0x4738aa00\n"
      "s_xor_b32 s46, s48, 0\n"
      "v_lshlrev_b64 v[128:129], s46, v[144:145]\n"
      "v_lshlrev_b64 v[136:137], s46, v[146:147]\n"
      "s_xor_b32 s46, s48, 16\n"
      "v_lshlrev_b64 v[130:131], s46, v[144:145]\n"
      "v_lshlrev_b64 v[138:139], s46, v[146:147]\n"
      "s_xor_b32 s46, s48, 32\n"
      "v_lshlrev_b64 v[132:133], s46, v[144:145]\n"
      "v_lshlrev_b64 v[140:141], s46, v[146:147]\n"
      "s_xor_b32 s46, s48, 48\n"
      "v_lshlrev_b64 v[134:135], s46, v[144:145]\n"
      "v_lshlrev_b64 v[142:143], s46, v[146:147]\n"
      "v_mov_b32 v222, 0x3a80\n"
      "v_and_b32 v227, 3, %3\n"
      "v_bfe_u32 v223, %3, 2, 2\n"
      "v_cmp_eq_u32 vcc, v227, v4\n"
      "v_mov_b64 v[84:85], 0\n"
      "v_mov_b64 v[88:89], 0\n"
      "v_cndmask_b32 v84, 0, v222, vcc\n"
      "v_lshlrev_b32 v222, 4, v223\n"
      "v_lshlrev_b64 v[86:87], v222, v[84:85]\n"
      "s_mul_i32 s46, s53, 2304\n"
      "v_mul_u32_u24 v222, 2304, v4\n"
      "v_mov_b32 v84, 0\n"
      "s_mov_b32 m0, s46\n"
      "s_movk_i32 s47, 272\n"
      "v_lshl_add_u32 v222, v223, 6, v222\n"
      "v_mad_u32_u24 v14, v227, s47, v222\n"
      "v_mul_u32_u24 v216, 272, v4\n"
      "v_add_u32 v213, s38, v13\n"
      "v_lshlrev_b32 v222, 3, v4\n"
      "s_mov_b32 s47, 0x09090005\n"
      "v_bfe_u32 v222, s47, v222, 8\n"
      "v_lshl_add_u32 v91, v222, 6, v213\n"
      "v_lshlrev_b32 v224, 4, v4\n"
      "v_add_u32 v95, s38, v224\n"
      "v_lshl_add_u32 v223, v223, 4, v216\n"
      "v_bfe_u32 v222, %3, 1, 1\n"
      "v_mul_u32_u24 v222, 9344, v222\n"
      "v_add3_u32 v223, v223, v222, s46\n"
      "v_and_b32 v222, 1, %3\n"
      "v_lshl_add_u32 v216, v222, 3, v223\n"
      "s_lshl_b32 s46, s53, 5\n"
      "s_add_u32 s46, s46, 37376\n"
      "v_and_b32 v222, 8, %3\n"
      "v_and_b32 v223, 7, %3\n"
      "v_lshl_add_u32 v222, v222, 4, s46\n"
      "v_lshl_add_u32 v217, v223, 2, v222\n"
      "v_lshlrev_b32 v227, 2, v227\n"
      "v_lshlrev_b32 v5, 5, v3\n"
      "v_mul_u32_u24 v222, 288, v4\n"
      "v_add_u32 v1, v222, v213\n"
      "s_movk_i32 s46, 288\n"
      "v_mul_u32_u24 v222, 80, v3\n"
      "v_add3_u32 v212, v222, v224, s54\n"
      "v_and_b32 v176, 7, %3\n"
      "v_bfe_u32 v177, %3, 3, 1\n"
      "v_mad_u32_u24 v201, v176, s46, v95\n"
      "v_mad_u32_u24 v203, v177, s46, v95\n"
      "v_add3_u32 v178, v176, v177, 1\n"
      "v_and_b32 v178, 7, v178\n"
      "v_mad_u32_u24 v202, v178, s46, v95\n"
      "v_and_b32 v222, 11, %3\n"
      "v_cmp_eq_u32 vcc, 3, v222\n"
      "v_lshrrev_b32 v222, 2, v3\n"
      "v_add_u32 v222, 24, v222\n"
      "v_mad_u32_u24 v222, v222, s46, v95\n"
      "v_mad_u32_u24 v223, v3, s46, v95\n"
      "v_add_u32 v224, 2304, v223\n"
      "v_cndmask_b32 v214, v223, v222, vcc\n"
      "v_cndmask_b32 v220, v224, v222, vcc\n"
      "v_xor_b32 v223, 1, v177\n"
      "v_lshl_add_u32 v223, v223, 4, v176\n"
      "v_mad_u32_u24 v223, v223, s46, v95\n"
      "v_cndmask_b32 v221, v223, v222, vcc\n"
      "s_movk_i32 s48, 320\n"
      "v_mad_u32_u24 v200, v177, s48, v95\n"
      "v_cvt_f32_u32 v223, v225\n"
      "v_fmaak_f32 v223, -2.0, v223, 0x45229000\n"
      "v_sqrt_f32 v223, v223\n"
      "v_lshrrev_b32 v224, 2, v225\n"
      "v_fmaak_f32 v223, -0.5, v223, 0x41cc28f6\n"
      "v_cvt_u32_f32 v208, v223\n"
      "v_add_u32 v224, 1, v224\n"
      "v_sub_u32 v223, 49, v208\n"
      "v_mul_u32_u24 v223, v208, v223\n"
      "v_lshrrev_b32 v223, 1, v223\n"
      "v_sub_u32 v210, v224, v223\n"
      "v_cmp_eq_u32 vcc, 25, v210\n"
      "v_add_u32 v223, 1, v208\n"
      "s_mov_b32 s59, 0\n"
      "s_mov_b32 s57, 0\n"
      "v_cndmask_b32 v209, v208, v223, vcc\n"
      "v_cndmask_b32 v211, v210, v223, vcc\n"
      "v_add_u32 v211, 1, v211\n"
      "s_mul_i32 s46, s53, 3072\n"
      "v_lshl_add_u32 v218, %3, 4, s46\n"
      "s_xor_b32 s46, s53, 1\n"
      "v_lshl_add_u32 v4, s46, 4, v14\n"
      "s_xor_b32 s46, s53, 2\n"
      "v_lshl_add_u32 v222, s46, 4, v14\n"
      "s_xor_b32 s46, s53, 3\n"
      "v_lshl_add_u32 v223, s46, 4, v14\n"
      "s_xor_b32 s46, s53, 0\n"
      "v_lshl_add_u32 v14, s46, 4, v14\n"
      "s_waitcnt lgkmcnt(0)\n"
      "s_cmp_lt_u32 s33, s26\n"
      "s_cbranch_scc0 36f\n"
      "37:\n"
      "global_load_dwordx4 v[6:9], v10, s[8:9] nt\n"
      "global_load_dwordx3 v[10:12], v10, s[8:9] offset:16 nt\n"
      "v_mov_b32 v92, s20\n"
      "v_mov_b32 v93, s21\n"
      "s_mov_b64 exec, 0xffff\n"
      "v_mov_b32 v92, s22\n"
      "v_mov_b32 v93, s23\n"
      "s_lshl_b64 exec, exec, 16\n"
      "v_mov_b32 v92, s18\n"
      "v_mov_b32 v93, s19\n"
      "s_mov_b64 exec, -1\n"
      "v_add_co_u32 v92, vcc, v92, v13\n"
      "v_addc_co_u32 v93, vcc, 0, v93, vcc\n"
      "global_load_dwordx4 v[192:195], v[92:93], off\n"
      "s_cmp_lg_u32 s35, 0\n"
      "s_cbranch_scc1 21f\n"
      "s_barrier\n"
      "21:\n"
      "global_load_dwordx4 v[96:99], v80, s[16:17] offset:0\n"
      "global_load_dwordx4 v[100:103], v80, s[16:17] offset:256\n"
      "global_load_dwordx4 v[104:107], v81, s[16:17] offset:0\n"
      "global_load_dwordx4 v[108:111], v81, s[16:17] offset:256\n"
      "global_load_dwordx4 v[112:115], v82, s[16:17] offset:0\n"
      "global_load_dwordx4 v[116:119], v82, s[16:17] offset:256\n"
      "s_mov_b64 exec, 0\n"
      "s_cmp_eq_u32 s53, 0\n"
      "s_cbranch_scc1 40f\n"
      "s_cmp_eq_u32 s53, 1\n"
      "s_cbranch_scc1 41f\n"
      "s_cmp_eq_u32 s53, 2\n"
      "s_cbranch_scc1 42f\n"
      "s_branch 43f\n"
      "44:\n"
      "s_mov_b64 exec, -1\n"
      "v_lshlrev_b32_e64 v3, 1, s33\n"
      "v_mov_b64 v[176:177], -1\n"
      "v_mov_b64 v[178:179], -1\n"
      "ds_write_b128 v1, v[176:179] offset:8064\n"
      "ds_write_b128 v1, v[176:179] offset:9216\n"
      "ds_write_b128 v1, v[176:179] offset:10368\n"
      "v_mov_b64 v[180:181], 0\n"
      "v_mov_b64 v[182:183], 0\n"
      "ds_write_b128 v1, v[180:183] offset:11520\n"
      "s_waitcnt vmcnt(8)\n"
      "v_lshl_add_u32 v7, v7, 8, v13\n"
      "global_load_dwordx4 v[228:231], v7, s[10:11] nt\n"
      "v_lshl_add_u32 v8, v8, 8, v13\n"
      "global_load_dwordx4 v[232:235], v8, s[10:11] nt\n"
      "s_waitcnt vmcnt(9)\n"
      "v_mov_b32_dpp v6, v12 quad_perm:[0,1,2,3] row_mask:0xc bank_mask:0xf\n"
      "v_lshl_add_u32 v9, v9, 8, v13\n"
      "v_lshl_add_u32 v10, v10, 8, v13\n"
      "v_lshl_add_u32 v11, v11, 8, v13\n"
      "v_lshl_add_u32 v12, v12, 8, v13\n"
      "v_lshl_add_u32 v6, v6, 8, v13\n"
      "v_lshl_add_u32 v8, %3, 2, s61\n"
      "s_waitcnt vmcnt(8)\n"
      "v_mfma_f32_16x16x16_bf16 v[48:51], v[192:193], v[136:137], 0\n"
      "v_max3_f32 v224, |v192|, |v193|, |v195|\n"
      "v_lshlrev_b32 v176, 16, v192\n"
      "v_mfma_f32_16x16x16_bf16 v[52:55], v[192:193], v[138:139], 0\n"
      "v_and_b32 v177, s43, v192\n"
      "v_subrev_u32 v224, 0x3d000000, v224\n"
      "v_mfma_f32_16x16x16_bf16 v[56:59], v[192:193], v[140:141], 0\n"
      "v_lshlrev_b32 v178, 16, v193\n"
      "v_and_b32 v179, s43, v193\n"
      "v_mfma_f32_16x16x16_bf16 v[60:63], v[192:193], v[142:143], 0\n"
      "v_cmp_gt_u32 vcc, 0x5800000, v224\n"
      "v_lshlrev_b32 v180, 16, v194\n"
      "v_mfma_f32_16x16x16_bf16 v[64:67], v[194:195], v[136:137], 0\n"
      "v_and_b32 v181, s43, v194\n"
      "s_and_b32 s44, vcc_lo, 0xffff\n"
      "v_mfma_f32_16x16x16_bf16 v[68:71], v[194:195], v[138:139], 0\n"
      "v_lshlrev_b32 v182, 16, v195\n"
      "s_cselect_b32 s44, vcc_hi, 0\n"
      "v_mfma_f32_16x16x16_bf16 v[72:75], v[194:195], v[140:141], 0\n"
      "v_and_b32 v183, s43, v195\n"
      "s_cmp_eq_u32 s44, 0\n"
      "s_cbranch_scc1 22f\n"
      "23:\n"
      "v_mfma_f32_16x16x16_bf16 v[76:79], v[194:195], v[142:143], 0\n"
      "v_cvt_pk_f16_f32 v184, v176, v177\n"
      "v_mfma_f32_16x16x16_bf16 v[16:19], v[192:193], v[128:129], 0\n"
      "v_cvt_pk_f16_f32 v185, v178, v179\n"
      "v_mfma_f32_16x16x16_bf16 v[20:23], v[192:193], v[130:131], 0\n"
      "v_cvt_pk_f16_f32 v186, v180, v181\n"
      "v_mfma_f32_16x16x16_bf16 v[24:27], v[192:193], v[132:133], 0\n"
      "v_cvt_pk_f16_f32 v187, v182, v183\n"
      "v_mfma_f32_16x16x16_bf16 v[28:31], v[192:193], v[134:135], 0\n"
      "v_pk_mul_f16 v184, v184, s37\n"
      "v_mfma_f32_16x16x16_bf16 v[32:35], v[194:195], v[128:129], 0\n"
      "v_pk_mul_f16 v185, v185, s37\n"
      "v_mfma_f32_16x16x16_bf16 v[36:39], v[194:195], v[130:131], 0\n"
      "v_pk_mul_f16 v186, v186, s37\n"
      "v_mfma_f32_16x16x16_bf16 v[40:43], v[194:195], v[132:133], 0\n"
      "v_pk_mul_f16 v187, v187, s37\n"
      "v_mfma_f32_16x16x16_bf16 v[44:47], v[194:195], v[134:135], 0\n"
      "ds_write_b128 v91, v[184:187] offset:16512\n"
      "ds_read_b128 a[184:187], v95 offset:17088\n"
      "ds_read_b128 a[188:191], v95 offset:17152\n"
      "ds_read_b128 a[192:195], v95 offset:17216\n"
      "ds_read_b128 a[196:199], v95 offset:17280\n"
      "global_load_dwordx4 v[120:123], v83, s[16:17] offset:0\n"
      "global_load_dwordx4 v[124:127], v83, s[16:17] offset:256\n"
      "s_cmp_lg_u32 s35, 0\n"
      "s_cbranch_scc1 24f\n"
      "s_barrier\n"
      "s_mov_b32 s35, 1\n"
      "24:\n"
      "global_load_dwordx4 v[236:239], v9, s[10:11] nt\n"
      "s_cmp_lg_u32 s58, 0\n"
      "s_cbranch_scc1 70f\n"
      "71:\n"
      "s_waitcnt vmcnt(10)\n"
      "v_mfma_f32_16x16x32_bf16 v[128:131], v[96:99], v[86:89], 0\n"
      "s_waitcnt vmcnt(9)\n"
      "v_mfma_f32_16x16x32_bf16 v[132:135], v[100:103], v[86:89], 0\n"
      "v_mfma_f32_16x16x32_bf16 v[136:139], v[96:99], v[84:87], 0\n"
      "v_mfma_f32_16x16x32_bf16 v[140:143], v[100:103], v[84:87], 0\n"
      "v_max3_f32 v224, |v97|, |v98|, |v103|\n"
      "s_waitcnt vmcnt(8)\n"
      "v_mfma_f32_16x16x32_bf16 v[144:147], v[104:107], v[86:89], 0\n"
      "s_nop 2\n"
      "v_cvt_pk_f16_f32 v164, v128, v132\n"
      "v_cvt_pk_f16_f32 v165, v129, v133\n"
      "v_cvt_pk_f16_f32 v166, v130, v134\n"
      "v_cvt_pk_f16_f32 v167, v131, v135\n"
      "s_waitcnt vmcnt(7)\n"
      "v_mfma_f32_16x16x32_bf16 v[148:151], v[108:111], v[86:89], 0\n"
      "ds_write_addtid_b32 v164 offset:0\n"
      "ds_write_addtid_b32 v165 offset:272\n"
      "ds_write_addtid_b32 v166 offset:544\n"
      "ds_write_addtid_b32 v167 offset:816\n"
      "v_mfma_f32_16x16x32_bf16 v[152:155], v[104:107], v[84:87], 0\n"
      "v_cvt_pk_f16_f32 v168, v136, v140\n"
      "v_cvt_pk_f16_f32 v169, v137, v141\n"
      "v_cvt_pk_f16_f32 v170, v138, v142\n"
      "v_cvt_pk_f16_f32 v171, v139, v143\n"
      "v_mfma_f32_16x16x32_bf16 v[156:159], v[108:111], v[84:87], 0\n"
      "ds_write_addtid_b32 v168 offset:1088\n"
      "ds_write_addtid_b32 v169 offset:1360\n"
      "ds_write_addtid_b32 v170 offset:1632\n"
      "ds_write_addtid_b32 v171 offset:1904\n"
      "s_waitcnt vmcnt(6)\n"
      "v_mfma_f32_16x16x32_bf16 v[128:131], v[112:115], v[86:89], 0\n"
      "v_cvt_pk_f16_f32 v164, v144, v148\n"
      "v_cvt_pk_f16_f32 v165, v145, v149\n"
      "v_cvt_pk_f16_f32 v166, v146, v150\n"
      "v_cvt_pk_f16_f32 v167, v147, v151\n"
      "s_waitcnt vmcnt(5)\n"
      "v_mfma_f32_16x16x32_bf16 v[132:135], v[116:119], v[86:89], 0\n"
      "ds_write_addtid_b32 v164 offset:9344\n"
      "ds_write_addtid_b32 v165 offset:9616\n"
      "ds_write_addtid_b32 v166 offset:9888\n"
      "ds_write_addtid_b32 v167 offset:10160\n"
      "v_mfma_f32_16x16x32_bf16 v[136:139], v[112:115], v[84:87], 0\n"
      "v_cvt_pk_f16_f32 v168, v152, v156\n"
      "v_cvt_pk_f16_f32 v169, v153, v157\n"
      "v_cvt_pk_f16_f32 v170, v154, v158\n"
      "v_cvt_pk_f16_f32 v171, v155, v159\n"
      "v_mfma_f32_16x16x32_bf16 v[140:143], v[116:119], v[84:87], 0\n"
      "ds_write_addtid_b32 v168 offset:10432\n"
      "ds_write_addtid_b32 v169 offset:10704\n"
      "ds_write_addtid_b32 v170 offset:10976\n"
      "ds_write_addtid_b32 v171 offset:11248\n"
      "v_max3_f32 v224, |v112|, |v117|, v224\n"
      "v_cmp_le_f32 vcc, 0x3d000000, v224\n"
      "v_cvt_pk_f16_f32 v164, v128, v132\n"
      "v_cvt_pk_f16_f32 v165, v129, v133\n"
      "v_cvt_pk_f16_f32 v166, v130, v134\n"
      "v_cvt_pk_f16_f32 v167, v131, v135\n"
      "ds_write_addtid_b32 v164 offset:18688\n"
      "ds_write_addtid_b32 v165 offset:18960\n"
      "ds_write_addtid_b32 v166 offset:19232\n"
      "ds_write_addtid_b32 v167 offset:19504\n"
      "v_cvt_pk_f16_f32 v168, v136, v140\n"
      "v_cvt_pk_f16_f32 v169, v137, v141\n"
      "v_cvt_pk_f16_f32 v170, v138, v142\n"
      "v_cvt_pk_f16_f32 v171, v139, v143\n"
      "ds_write_addtid_b32 v168 offset:19776\n"
      "ds_write_addtid_b32 v169 offset:20048\n"
      "ds_write_addtid_b32 v170 offset:20320\n"
      "ds_write_addtid_b32 v171 offset:20592\n"
      "s_bcnt1_i32_b64 s52, vcc\n"
      "s_cmp_ge_u32 s52, 32\n"
      "s_cselect_b32 s52, 13, 0x7f000005\n"
      "ds_write_b32 v2, v2 offset:16\n"
      "global_load_dwordx4 v[204:207], v218, s[96:97] offset:0\n"
      "ds_read_b64_tr_b16 a[152:153], v216 offset:0\n"
      "ds_read_b64_tr_b16 a[154:155], v216 offset:1088\n"
      "ds_read_b64_tr_b16 a[156:157], v216 offset:64\n"
      "ds_read_b64_tr_b16 a[158:159], v216 offset:1152\n"
      "ds_read_b64_tr_b16 a[160:161], v216 offset:128\n"
      "ds_read_b64_tr_b16 a[162:163], v216 offset:1216\n"
      "ds_read_b64_tr_b16 a[164:165], v216 offset:192\n"
      "ds_read_b64_tr_b16 a[166:167], v216 offset:1280\n"
      "s_waitcnt lgkmcnt(6)\n"
      "v_mfma_f32_16x16x32_f16 a[200:203], a[184:187], a[152:155], 0\n"
      "s_waitcnt lgkmcnt(4)\n"
      "v_mfma_f32_16x16x32_f16 a[200:203], a[188:191], a[156:159], a[200:203]\n"
      "v_mov_b32 v7, 0xf149f2ca\n"
      "ds_read_b32 v224, v227 offset:37904\n"
      "s_waitcnt lgkmcnt(3)\n"
      "v_mfma_f32_16x16x32_f16 a[200:203], a[192:195], a[160:163], a[200:203]\n"
      "s_waitcnt lgkmcnt(1)\n"
      "v_mfma_f32_16x16x32_f16 a[200:203], a[196:199], a[164:167], a[200:203]\n"
      "s_waitcnt lgkmcnt(0)\n"
      "v_cmp_eq_u32 vcc, 0, v224\n"
      "s_nop 0\n"
      "s_cmp_lg_u32 vcc_lo, 0\n"
      "s_cbranch_scc0 14f\n"
      "11:\n"
      "ds_read_b32 v224, v227 offset:37904\n"
      "s_waitcnt lgkmcnt(0)\n"
      "v_cmp_eq_u32 vcc, 0, v224\n"
      "s_nop 0\n"
      "s_cmp_lg_u32 vcc_lo, 0\n"
      "s_cbranch_scc1 11b\n"
      "14:\n"
      "ds_read_b128 a[0:3], v14 offset:0\n"
      "ds_read_b128 a[16:19], v4 offset:0\n"
      "ds_read_b128 a[32:35], v222 offset:0\n"
      "ds_read_b128 a[48:51], v223 offset:0\n"
      "ds_read_b128 a[64:67], v14 offset:1088\n"
      "ds_read_b128 a[80:83], v4 offset:1088\n"
      "ds_read_b128 a[96:99], v222 offset:1088\n"
      "ds_read_b128 a[112:115], v223 offset:1088\n"
      "ds_read_b128 a[4:7], v14 offset:9344\n"
      "ds_read_b128 a[20:23], v4 offset:9344\n"
      "ds_read_b128 a[36:39], v222 offset:9344\n"
      "ds_read_b128 a[52:55], v223 offset:9344\n"
      "ds_read_b128 a[68:71], v14 offset:10432\n"
      "ds_read_b128 a[84:87], v4 offset:10432\n"
      "ds_read_b128 a[100:103], v222 offset:10432\n"
      "ds_read_b128 a[116:119], v223 offset:10432\n"
      "ds_read_b128 a[8:11], v14 offset:18688\n"
      "ds_read_b128 a[24:27], v4 offset:18688\n"
      "ds_read_b128 a[40:43], v222 offset:18688\n"
      "ds_read_b128 a[56:59], v223 offset:18688\n"
      "ds_read_b128 a[72:75], v14 offset:19776\n"
      "ds_read_b128 a[88:91], v4 offset:19776\n"
      "ds_read_b128 a[104:107], v222 offset:19776\n"
      "ds_read_b128 a[120:123], v223 offset:19776\n"
      "global_load_dwordx4 v[240:243], v10, s[10:11] nt\n"
      "3:\n"
      "s_waitcnt vmcnt(6)\n"
      "v_sub_u16_sdwa v176, v228, s36 clamp dst_sel:WORD_1 dst_unused:UNUSED_PAD src0_sel:WORD_0 src1_sel:DWORD\n"
      "v_sub_u16_sdwa v177, v228, s36 clamp dst_sel:WORD_1 dst_unused:UNUSED_PAD src0_sel:WORD_1 src1_sel:DWORD\n"
      "v_sub_u16_sdwa v178, v229, s36 clamp dst_sel:WORD_1 dst_unused:UNUSED_PAD src0_sel:WORD_0 src1_sel:DWORD\n"
      "v_sub_u16_sdwa v179, v229, s36 clamp dst_sel:WORD_1 dst_unused:UNUSED_PAD src0_sel:WORD_1 src1_sel:DWORD\n"
      "v_sub_u16_sdwa v180, v230, s36 clamp dst_sel:WORD_1 dst_unused:UNUSED_PAD src0_sel:WORD_0 src1_sel:DWORD\n"
      "v_sub_u16_sdwa v181, v230, s36 clamp dst_sel:WORD_1 dst_unused:UNUSED_PAD src0_sel:WORD_1 src1_sel:DWORD\n"
      "v_sub_u16_sdwa v182, v231, s36 clamp dst_sel:WORD_1 dst_unused:UNUSED_PAD src0_sel:WORD_0 src1_sel:DWORD\n"
      "v_sub_u16_sdwa v183, v231, s36 clamp dst_sel:WORD_1 dst_unused:UNUSED_PAD src0_sel:WORD_1 src1_sel:DWORD\n"
      "v_cvt_pk_f16_f32 v184, v176, v177\n"
      "v_cvt_pk_f16_f32 v185, v178, v179\n"
      "v_cvt_pk_f16_f32 v186, v180, v181\n"
      "v_cvt_pk_f16_f32 v187, v182, v183\n"
      "ds_write_b128 v1, v[184:187] offset:0\n"
      "s_waitcnt vmcnt(5)\n"
      "v_sub_u16_sdwa v144, v232, s36 clamp dst_sel:WORD_1 dst_unused:UNUSED_PAD src0_sel:WORD_0 src1_sel:DWORD\n"
      "v_sub_u16_sdwa v145, v232, s36 clamp dst_sel:WORD_1 dst_unused:UNUSED_PAD src0_sel:WORD_1 src1_sel:DWORD\n"
      "v_sub_u16_sdwa v146, v233, s36 clamp dst_sel:WORD_1 dst_unused:UNUSED_PAD src0_sel:WORD_0 src1_sel:DWORD\n"
      "v_sub_u16_sdwa v147, v233, s36 clamp dst_sel:WORD_1 dst_unused:UNUSED_PAD src0_sel:WORD_1 src1_sel:DWORD\n"
      "v_sub_u16_sdwa v148, v234, s36 clamp dst_sel:WORD_1 dst_unused:UNUSED_PAD src0_sel:WORD_0 src1_sel:DWORD\n"
      "v_sub_u16_sdwa v149, v234, s36 clamp dst_sel:WORD_1 dst_unused:UNUSED_PAD src0_sel:WORD_1 src1_sel:DWORD\n"
      "v_sub_u16_sdwa v150, v235, s36 clamp dst_sel:WORD_1 dst_unused:UNUSED_PAD src0_sel:WORD_0 src1_sel:DWORD\n"
      "v_sub_u16_sdwa v151, v235, s36 clamp dst_sel:WORD_1 dst_unused:UNUSED_PAD src0_sel:WORD_1 src1_sel:DWORD\n"
      "v_cvt_pk_f16_f32 v152, v144, v145\n"
      "v_cvt_pk_f16_f32 v153, v146, v147\n"
      "v_cvt_pk_f16_f32 v154, v148, v149\n"
      "v_cvt_pk_f16_f32 v155, v150, v151\n"
      "ds_write_b128 v1, v[152:155] offset:1152\n"
      "ds_read_b128 v[128:131], v201 offset:0\n"
      "ds_read_b128 v[160:163], v202 offset:0\n"
      "ds_read_b128 v[132:135], v201 offset:64\n"
      "ds_read_b128 v[164:167], v202 offset:64\n"
      "s_or_b32 s44, s55, s58\n"
      "s_cbranch_scc1 74f\n"
      "75:\n"
      "s_waitcnt vmcnt(4)\n"
      "v_mfma_f32_16x16x32_bf16 v[100:103], v[120:123], v[86:89], 0\n"
      "ds_read_b128 v[136:139], v201 offset:128\n"
      "s_waitcnt vmcnt(3)\n"
      "v_mfma_f32_16x16x32_bf16 v[104:107], v[124:127], v[86:89], 0\n"
      "ds_read_b128 v[168:171], v202 offset:128\n"
      "v_mfma_f32_16x16x32_bf16 v[108:111], v[120:123], v[84:87], 0\n"
      "ds_read_b128 v[140:143], v201 offset:192\n"
      "v_mfma_f32_16x16x32_bf16 v[112:115], v[124:127], v[84:87], 0\n"
      "ds_read_b128 v[172:175], v202 offset:192\n"
      "s_waitcnt lgkmcnt(6)\n"
      "v_pk_mul_f16 v96, v128, v160\n"
      "v_pk_mul_f16 v97, v129, v161\n"
      "v_pk_mul_f16 v98, v130, v162\n"
      "v_pk_mul_f16 v99, v131, v163\n"
      "v_cvt_pk_f16_f32 v116, v100, v104\n"
      "v_cvt_pk_f16_f32 v117, v101, v105\n"
      "v_cvt_pk_f16_f32 v118, v102, v106\n"
      "v_cvt_pk_f16_f32 v119, v103, v107\n"
      "ds_write_addtid_b32 v116 offset:28032\n"
      "ds_write_addtid_b32 v117 offset:28304\n"
      "ds_write_addtid_b32 v118 offset:28576\n"
      "ds_write_addtid_b32 v119 offset:28848\n"
      "v_cvt_pk_f16_f32 v184, v108, v112\n"
      "v_cvt_pk_f16_f32 v185, v109, v113\n"
      "v_cvt_pk_f16_f32 v186, v110, v114\n"
      "v_cvt_pk_f16_f32 v187, v111, v115\n"
      "ds_write_addtid_b32 v184 offset:29120\n"
      "ds_write_addtid_b32 v185 offset:29392\n"
      "ds_write_addtid_b32 v186 offset:29664\n"
      "ds_write_addtid_b32 v187 offset:29936\n"
      "ds_write_b32 v2, v2 offset:32\n"
      "31:\n"
      "v_mfma_f32_16x16x32_f16 v[80:83], a[0:3], v[96:99], v[16:19]\n"
      "s_waitcnt lgkmcnt(13)\n"
      "v_pk_mul_f16 v100, v132, v164\n"
      "v_pk_mul_f16 v101, v133, v165\n"
      "v_mfma_f32_16x16x32_f16 v[84:87], a[16:19], v[96:99], v[20:23]\n"
      "v_pk_mul_f16 v102, v134, v166\n"
      "v_pk_mul_f16 v103, v135, v167\n"
      "v_mfma_f32_16x16x32_f16 v[88:91], a[32:35], v[96:99], v[24:27]\n"
      "s_waitcnt lgkmcnt(11)\n"
      "v_pk_mul_f16 v104, v136, v168\n"
      "v_pk_mul_f16 v105, v137, v169\n"
      "v_mfma_f32_16x16x32_f16 v[92:95], a[48:51], v[96:99], v[28:31]\n"
      "s_waitcnt vmcnt(2)\n"
      "v_sub_u16_sdwa v176, v236, s36 clamp dst_sel:WORD_1 dst_unused:UNUSED_PAD src0_sel:WORD_0 src1_sel:DWORD\n"
      "v_sub_u16_sdwa v177, v236, s36 clamp dst_sel:WORD_1 dst_unused:UNUSED_PAD src0_sel:WORD_1 src1_sel:DWORD\n"
      "v_sub_u16_sdwa v178, v237, s36 clamp dst_sel:WORD_1 dst_unused:UNUSED_PAD src0_sel:WORD_0 src1_sel:DWORD\n"
      "v_sub_u16_sdwa v179, v237, s36 clamp dst_sel:WORD_1 dst_unused:UNUSED_PAD src0_sel:WORD_1 src1_sel:DWORD\n"
      "v_mfma_f32_16x16x32_f16 v[244:247], a[64:67], v[96:99], v[32:35]\n"
      "v_sub_u16_sdwa v180, v238, s36 clamp dst_sel:WORD_1 dst_unused:UNUSED_PAD src0_sel:WORD_0 src1_sel:DWORD\n"
      "v_sub_u16_sdwa v181, v238, s36 clamp dst_sel:WORD_1 dst_unused:UNUSED_PAD src0_sel:WORD_1 src1_sel:DWORD\n"
      "v_mfma_f32_16x16x32_f16 v[248:251], a[80:83], v[96:99], v[36:39]\n"
      "v_sub_u16_sdwa v182, v239, s36 clamp dst_sel:WORD_1 dst_unused:UNUSED_PAD src0_sel:WORD_0 src1_sel:DWORD\n"
      "v_sub_u16_sdwa v183, v239, s36 clamp dst_sel:WORD_1 dst_unused:UNUSED_PAD src0_sel:WORD_1 src1_sel:DWORD\n"
      "v_mfma_f32_16x16x32_f16 v[252:255], a[96:99], v[96:99], v[40:43]\n"
      "v_cvt_pk_f16_f32 v184, v176, v177\n"
      "v_cvt_pk_f16_f32 v185, v178, v179\n"
      "v_mfma_f32_16x16x32_f16 v[192:195], a[112:115], v[96:99], v[44:47]\n"
      "v_cvt_pk_f16_f32 v186, v180, v181\n"
      "v_cvt_pk_f16_f32 v187, v182, v183\n"
      "v_mfma_f32_16x16x32_f16 v[80:83], a[4:7], v[100:103], v[80:83]\n"
      "ds_write_b128 v1, v[184:187] offset:2304\n"
      "v_mfma_f32_16x16x32_f16 v[84:87], a[20:23], v[100:103], v[84:87]\n"
      "ds_read_b32 v224, v227 offset:37920\n"
      "v_pk_mul_f16 v106, v138, v170\n"
      "v_mfma_f32_16x16x32_f16 v[88:91], a[36:39], v[100:103], v[88:91]\n"
      "s_waitcnt lgkmcnt(11)\n"
      "v_pk_mul_f16 v107, v139, v171\n"
      "v_pk_mul_f16 v108, v140, v172\n"
      "v_mfma_f32_16x16x32_f16 v[92:95], a[52:55], v[100:103], v[92:95]\n"
      "v_pk_mul_f16 v109, v141, v173\n"
      "v_pk_mul_f16 v110, v142, v174\n"
      "v_mfma_f32_16x16x32_f16 v[244:247], a[68:71], v[100:103], v[244:247]\n"
      "ds_read_b128 v[144:147], v203 offset:2304\n"
      "v_pk_mul_f16 v111, v143, v175\n"
      "v_mfma_f32_16x16x32_f16 v[248:251], a[84:87], v[100:103], v[248:251]\n"
      "ds_read_b128 v[148:151], v203 offset:2368\n"
      "global_load_dwordx2 v[196:197], v225, s[12:13]\n"
      "v_mfma_f32_16x16x32_f16 v[252:255], a[100:103], v[100:103], v[252:255]\n"
      "ds_read_b128 v[152:155], v203 offset:2432\n"
      "global_load_dwordx2 v[198:199], v225, s[14:15]\n"
      "v_mfma_f32_16x16x32_f16 v[192:195], a[116:119], v[100:103], v[192:195]\n"
      "s_waitcnt lgkmcnt(3)\n"
      "ds_read_b128 v[156:159], v203 offset:2496\n"
      "v_cmp_eq_u32 vcc, 0, v224\n"
      "v_mfma_f32_16x16x32_f16 v[80:83], a[8:11], v[104:107], v[80:83]\n"
      "global_load_ushort a140, v3, s[24:25]\n"
      "ds_read_b128 v[160:163], v203 offset:2880\n"
      "v_mfma_f32_16x16x32_f16 v[84:87], a[24:27], v[104:107], v[84:87]\n"
      "s_cbranch_vccz 13f\n"
      "12:\n"
      "ds_read_b32 v224, v227 offset:37920\n"
      "s_waitcnt lgkmcnt(0)\n"
      "v_cmp_eq_u32 vcc, 0, v224\n"
      "s_nop 4\n"
      "s_cbranch_vccnz 12b\n"
      "13:\n"
      "ds_read_b128 a[12:15], v14 offset:28032\n"
      "v_mfma_f32_16x16x32_f16 v[88:91], a[40:43], v[104:107], v[88:91]\n"
      "ds_read_b128 a[28:31], v4 offset:28032\n"
      "v_max3_f32 v226, |v228|, |v229|, |v230|\n"
      "v_mfma_f32_16x16x32_f16 v[92:95], a[56:59], v[104:107], v[92:95]\n"
      "ds_read_b128 a[44:47], v222 offset:28032\n"
      "s_mov_b32 s44, 0x3f400000\n"
      "v_max_f32 v226, |v231|, v226\n"
      "v_mfma_f32_16x16x32_f16 v[244:247], a[72:75], v[104:107], v[244:247]\n"
      "s_waitcnt lgkmcnt(7)\n"
      "ds_read_b128 a[60:63], v223 offset:28032\n"
      "v_pk_mul_f16 v112, v128, v144\n"
      "v_mfma_f32_16x16x32_f16 v[248:251], a[88:91], v[104:107], v[248:251]\n"
      "ds_read_b128 a[76:79], v14 offset:29120\n"
      "v_pk_mul_f16 v113, v129, v145\n"
      "v_mfma_f32_16x16x32_f16 v[252:255], a[104:107], v[104:107], v[252:255]\n"
      "s_waitcnt lgkmcnt(8)\n"
      "ds_read_b128 a[92:95], v4 offset:29120\n"
      "v_pk_mul_f16 v116, v132, v148\n"
      "v_mfma_f32_16x16x32_f16 v[192:195], a[120:123], v[104:107], v[192:195]\n"
      "s_waitcnt lgkmcnt(4)\n"
      "ds_read_b128 a[108:111], v222 offset:29120\n"
      "v_pk_mul_f16 v114, v130, v146\n"
      "v_mfma_f32_16x16x32_f16 v[80:83], a[12:15], v[108:111], v[80:83]\n"
      "ds_read_b128 a[124:127], v223 offset:29120\n"
      "v_pk_mul_f16 v117, v133, v149\n"
      "v_mfma_f32_16x16x32_f16 v[84:87], a[28:31], v[108:111], v[84:87]\n"
      "s_waitcnt lgkmcnt(4)\n"
      "v_pk_mul_f16 v120, v136, v152\n"
      "v_pk_mul_f16 v115, v131, v147\n"
      "v_mfma_f32_16x16x32_f16 v[88:91], a[44:47], v[108:111], v[88:91]\n"
      "v_pk_mul_f16 v118, v134, v150\n"
      "v_pk_mul_f16 v121, v137, v153\n"
      "v_mfma_f32_16x16x32_f16 v[92:95], a[60:63], v[108:111], v[92:95]\n"
      "s_waitcnt lgkmcnt(2)\n"
      "v_mul_f32 v188, |v80|, v48\n"
      "v_fma_f32 v188, |v81|, v49, v188\n"
      "v_mfma_f32_16x16x32_f16 v[244:247], a[76:79], v[108:111], v[244:247]\n"
      "v_pk_mul_f16 v124, v140, v156\n"
      "v_fma_f32 v188, |v82|, v50, v188\n"
      "v_mfma_f32_16x16x32_f16 v[248:251], a[92:95], v[108:111], v[248:251]\n"
      "s_waitcnt lgkmcnt(0)\n"
      "v_fma_f32 v188, |v83|, v51, v188\n"
      "v_pk_mul_f16 v119, v135, v151\n"
      "v_mfma_f32_16x16x32_f16 v[252:255], a[108:111], v[108:111], v[252:255]\n"
      "v_pk_mul_f16 v122, v138, v154\n"
      "v_pk_mul_f16 v125, v141, v157\n"
      "v_mfma_f32_16x16x32_f16 v[192:195], a[124:127], v[108:111], v[192:195]\n"
      "v_pk_mul_f16 v123, v139, v155\n"
      "v_pk_mul_f16 v126, v142, v158\n"
      "v_mfma_f32_16x16x32_f16 v[80:83], a[0:3], v[112:115], v[16:19]\n"
      "v_pk_mul_f16 v127, v143, v159\n"
      "v_fma_f32 v188, |v84|, v52, v188\n"
      "v_mfma_f32_16x16x32_f16 v[80:83], a[4:7], v[116:119], v[80:83]\n"
      "v_fma_f32 v188, |v85|, v53, v188\n"
      "v_fma_f32 v188, |v86|, v54, v188\n"
      "v_mfma_f32_16x16x32_f16 v[80:83], a[8:11], v[120:123], v[80:83]\n"
      "40:\n"
      "s_cbranch_execz 44b\n"
      "v_mfma_f32_16x16x32_f16 v[80:83], a[12:15], v[124:127], v[80:83]\n"
      "v_fma_f32 v188, |v87|, v55, v188\n"
      "v_fma_f32 v188, |v88|, v56, v188\n"
      "v_mfma_f32_16x16x32_f16 v[84:87], a[28:31], v[124:127], v[20:23]\n"
      "v_fma_f32 v188, |v89|, v57, v188\n"
      "v_fma_f32 v188, |v90|, v58, v188\n"
      "v_mfma_f32_16x16x32_f16 v[84:87], a[24:27], v[120:123], v[84:87]\n"
      "v_fma_f32 v188, |v91|, v59, v188\n"
      "v_fma_f32 v188, |v92|, v60, v188\n"
      "v_mfma_f32_16x16x32_f16 v[84:87], a[20:23], v[116:119], v[84:87]\n"
      "ds_read_b128 v[164:167], v203 offset:2944\n"
      "v_fma_f32 v188, |v93|, v61, v188\n"
      "v_mfma_f32_16x16x32_f16 v[84:87], a[16:19], v[112:115], v[84:87]\n"
      "v_fma_f32 v188, |v94|, v62, v188\n"
      "v_fma_f32 v188, |v95|, v63, v188\n"
      "v_mfma_f32_16x16x32_f16 v[88:91], a[32:35], v[112:115], v[24:27]\n"
      "v_pk_mul_f16 v96, v128, v160\n"
      "v_pk_mul_f16 v97, v129, v161\n"
      "v_mfma_f32_16x16x32_f16 v[88:91], a[36:39], v[116:119], v[88:91]\n"
      "v_fma_f32 v188, |v244|, v64, v188\n"
      "v_pk_mul_f16 v98, v130, v162\n"
      "v_mfma_f32_16x16x32_f16 v[88:91], a[40:43], v[120:123], v[88:91]\n"
      "v_mul_f32 v189, |v80|, v48\n"
      "v_fma_f32 v189, |v81|, v49, v189\n"
      "v_mfma_f32_16x16x32_f16 v[88:91], a[44:47], v[124:127], v[88:91]\n"
      "v_cmp_le_f32 vcc, 0x43000000, v226\n"
      "v_pk_mul_f16 v99, v131, v163\n"
      "v_mfma_f32_16x16x32_f16 v[92:95], a[60:63], v[124:127], v[28:31]\n"
      "v_fma_f32 v188, |v245|, v65, v188\n"
      "v_fma_f32 v189, |v82|, v50, v189\n"
      "v_mfma_f32_16x16x32_f16 v[92:95], a[56:59], v[120:123], v[92:95]\n"
      "v_fma_f32 v189, |v83|, v51, v189\n"
      "v_cmp_le_f32 s[28:29], s44, v226\n"
      "v_mfma_f32_16x16x32_f16 v[92:95], a[52:55], v[116:119], v[92:95]\n"
      "ds_read_b128 v[168:171], v203 offset:3008\n"
      "s_or_b32 vcc_lo, vcc_lo, s60\n"
      "v_fma_f32 v188, |v246|, v66, v188\n"
      "v_mfma_f32_16x16x32_f16 v[92:95], a[48:51], v[112:115], v[92:95]\n"
      "s_bcnt1_i32_b64 s44, s[28:29]\n"
      "v_fma_f32 v189, |v84|, v52, v189\n"
      "v_fma_f32 v189, |v85|, v53, v189\n"
      "v_mfma_f32_16x16x32_f16 v[80:83], a[64:67], v[112:115], v[32:35]\n"
      "s_cmp_lt_u32 s44, 32\n"
      "s_cselect_b64 vcc, -1, vcc\n"
      "v_fma_f32 v188, |v247|, v67, v188\n"
      "v_mfma_f32_16x16x32_f16 v[80:83], a[68:71], v[116:119], v[80:83]\n"
      "s_waitcnt lgkmcnt(1)\n"
      "v_pk_mul_f16 v100, v132, v164\n"
      "v_fma_f32 v189, |v86|, v54, v189\n"
      "v_mfma_f32_16x16x32_f16 v[80:83], a[72:75], v[120:123], v[80:83]\n"
      "v_fma_f32 v189, |v87|, v55, v189\n"
      "v_pk_mul_f16 v101, v133, v165\n"
      "v_mfma_f32_16x16x32_f16 v[80:83], a[76:79], v[124:127], v[80:83]\n"
      "v_pk_mul_f16 v102, v134, v166\n"
      "v_fma_f32 v188, |v248|, v68, v188\n"
      "v_mfma_f32_16x16x32_f16 v[84:87], a[92:95], v[124:127], v[36:39]\n"
      "v_pk_mul_f16 v103, v135, v167\n"
      "v_fma_f32 v189, |v88|, v56, v189\n"
      "v_mfma_f32_16x16x32_f16 v[84:87], a[88:91], v[120:123], v[84:87]\n"
      "ds_read_b128 v[172:175], v203 offset:3072\n"
      "v_fma_f32 v189, |v89|, v57, v189\n"
      "v_mfma_f32_16x16x32_f16 v[84:87], a[84:87], v[116:119], v[84:87]\n"
      "v_fma_f32 v188, |v249|, v69, v188\n"
      "v_fma_f32 v189, |v90|, v58, v189\n"
      "v_mfma_f32_16x16x32_f16 v[84:87], a[80:83], v[112:115], v[84:87]\n"
      "s_waitcnt lgkmcnt(1)\n"
      "v_fma_f32 v189, |v91|, v59, v189\n"
      "v_pk_mul_f16 v104, v136, v168\n"
      "v_mfma_f32_16x16x32_f16 v[88:91], a[96:99], v[112:115], v[40:43]\n"
      "v_fma_f32 v188, |v250|, v70, v188\n"
      "v_pk_mul_f16 v105, v137, v169\n"
      "v_mfma_f32_16x16x32_f16 v[88:91], a[100:103], v[116:119], v[88:91]\n"
      "v_fma_f32 v189, |v92|, v60, v189\n"
      "v_fma_f32 v189, |v93|, v61, v189\n"
      "v_mfma_f32_16x16x32_f16 v[88:91], a[104:107], v[120:123], v[88:91]\n"
      "v_pk_mul_f16 v106, v138, v170\n"
      "v_fma_f32 v188, |v251|, v71, v188\n"
      "v_mfma_f32_16x16x32_f16 v[88:91], a[108:111], v[124:127], v[88:91]\n"
      "v_fma_f32 v189, |v94|, v62, v189\n"
      "v_fma_f32 v189, |v95|, v63, v189\n"
      "v_pk_mul_f16 v107, v139, v171\n"
      "v_mfma_f32_16x16x32_f16 v[92:95], a[124:127], v[124:127], v[44:47]\n"
      "v_fma_f32 v188, |v252|, v72, v188\n"
      "v_fma_f32 v189, |v80|, v64, v189\n"
      "v_mfma_f32_16x16x32_f16 v[92:95], a[120:123], v[120:123], v[92:95]\n"
      "s_waitcnt lgkmcnt(0)\n"
      "v_fma_f32 v189, |v81|, v65, v189\n"
      "v_pk_mul_f16 v108, v140, v172\n"
      "v_mfma_f32_16x16x32_f16 v[92:95], a[116:119], v[116:119], v[92:95]\n"
      "v_fma_f32 v188, |v253|, v73, v188\n"
      "v_fma_f32 v189, |v82|, v66, v189\n"
      "v_mfma_f32_16x16x32_f16 v[92:95], a[112:115], v[112:115], v[92:95]\n"
      "v_fma_f32 v189, |v83|, v67, v189\n"
      "v_pk_mul_f16 v109, v141, v173\n"
      "v_mfma_f32_16x16x32_f16 v[80:83], a[0:3], v[96:99], v[16:19]\n"
      "v_pk_mul_f16 v110, v142, v174\n"
      "v_fma_f32 v188, |v254|, v74, v188\n"
      "v_mfma_f32_16x16x32_f16 v[80:83], a[4:7], v[100:103], v[80:83]\n"
      "v_pk_mul_f16 v111, v143, v175\n"
      "v_fma_f32 v189, |v84|, v68, v189\n"
      "v_mfma_f32_16x16x32_f16 v[80:83], a[8:11], v[104:107], v[80:83]\n"
      "v_fma_f32 v189, |v85|, v69, v189\n"
      "v_fma_f32 v188, |v255|, v75, v188\n"
      "v_mfma_f32_16x16x32_f16 v[80:83], a[12:15], v[108:111], v[80:83]\n"
      "v_fma_f32 v189, |v86|, v70, v189\n"
      "v_fma_f32 v189, |v87|, v71, v189\n"
      "v_mfma_f32_16x16x32_f16 v[84:87], a[28:31], v[108:111], v[20:23]\n"
      "s_waitcnt vmcnt(4)\n"
      "ds_read_b128 v[112:115], v204 offset:0\n"
      "v_fma_f32 v188, |v192|, v76, v188\n"
      "v_mfma_f32_16x16x32_f16 v[84:87], a[24:27], v[104:107], v[84:87]\n"
      "ds_read_b128 v[160:163], v205 offset:0\n"
      "v_fma_f32 v189, |v88|, v72, v189\n"
      "v_mfma_f32_16x16x32_f16 v[84:87], a[20:23], v[100:103], v[84:87]\n"
      "v_fma_f32 v189, |v89|, v73, v189\n"
      "v_fma_f32 v188, |v193|, v77, v188\n"
      "v_mfma_f32_16x16x32_f16 v[84:87], a[16:19], v[96:99], v[84:87]\n"
      "v_fma_f32 v189, |v90|, v74, v189\n"
      "v_fma_f32 v189, |v91|, v75, v189\n"
      "v_mfma_f32_16x16x32_f16 v[88:91], a[32:35], v[96:99], v[24:27]\n"
      "v_fma_f32 v188, |v194|, v78, v188\n"
      "v_fma_f32 v189, |v92|, v76, v189\n"
      "v_mfma_f32_16x16x32_f16 v[88:91], a[36:39], v[100:103], v[88:91]\n"
      "v_fma_f32 v189, |v93|, v77, v189\n"
      "v_fma_f32 v188, |v195|, v79, v188\n"
      "v_mfma_f32_16x16x32_f16 v[88:91], a[40:43], v[104:107], v[88:91]\n"
      "v_fma_f32 v189, |v94|, v78, v189\n"
      "v_fma_f32 v189, |v95|, v79, v189\n"
      "v_mfma_f32_16x16x32_f16 v[88:91], a[44:47], v[108:111], v[88:91]\n"
      "ds_read_b128 v[116:119], v204 offset:64\n"
      "ds_read_b128 v[164:167], v205 offset:64\n"
      "v_mfma_f32_16x16x32_f16 v[92:95], a[60:63], v[108:111], v[28:31]\n"
      "v_mul_f32 v190, |v80|, v48\n"
      "v_fma_f32 v190, |v81|, v49, v190\n"
      "v_mfma_f32_16x16x32_f16 v[92:95], a[56:59], v[104:107], v[92:95]\n"
      "s_waitcnt lgkmcnt(2)\n"
      "v_pk_mul_f16 v112, v112, v160\n"
      "v_pk_mul_f16 v113, v113, v161\n"
      "v_mfma_f32_16x16x32_f16 v[92:95], a[52:55], v[100:103], v[92:95]\n"
      "v_fma_f32 v190, |v82|, v50, v190\n"
      "v_fma_f32 v190, |v83|, v51, v190\n"
      "v_mfma_f32_16x16x32_f16 v[92:95], a[48:51], v[96:99], v[92:95]\n"
      "v_pk_mul_f16 v114, v114, v162\n"
      "v_pk_mul_f16 v115, v115, v163\n"
      "v_mfma_f32_16x16x32_f16 v[80:83], a[64:67], v[96:99], v[32:35]\n"
      "ds_read_b128 v[120:123], v204 offset:128\n"
      "ds_read_b128 v[168:171], v205 offset:128\n"
      "v_mfma_f32_16x16x32_f16 v[80:83], a[68:71], v[100:103], v[80:83]\n"
      "s_cbranch_vccnz 33f\n"
      "global_load_dwordx4 v[244:247], v11, s[10:11] nt\n"
      "global_load_dwordx4 v[248:251], v12, s[10:11] nt\n"
      "global_load_dwordx4 v[252:255], v6, s[10:11] nt\n"
      "32:\n"
      "v_mfma_f32_16x16x32_f16 v[80:83], a[72:75], v[104:107], v[80:83]\n"
      "v_fma_f32 v190, |v84|, v52, v190\n"
      "v_fma_f32 v190, |v85|, v53, v190\n"
      "v_mfma_f32_16x16x32_f16 v[80:83], a[76:79], v[108:111], v[80:83]\n"
      "s_waitcnt lgkmcnt(2)\n"
      "v_fma_f32 v190, |v86|, v54, v190\n"
      "v_fma_f32 v190, |v87|, v55, v190\n"
      "v_pk_mul_f16 v116, v116, v164\n"
      "v_mfma_f32_16x16x32_f16 v[84:87], a[92:95], v[108:111], v[36:39]\n"
      "v_pk_mul_f16 v117, v117, v165\n"
      "v_pk_mul_f16 v118, v118, v166\n"
      "v_mfma_f32_16x16x32_f16 v[84:87], a[88:91], v[104:107], v[84:87]\n"
      "v_fma_f32 v190, |v88|, v56, v190\n"
      "v_fma_f32 v190, |v89|, v57, v190\n"
      "v_mfma_f32_16x16x32_f16 v[84:87], a[84:87], v[100:103], v[84:87]\n"
      "ds_read_b128 v[124:127], v204 offset:192\n"
      "ds_read_b128 v[172:175], v205 offset:192\n"
      "v_pk_mul_f16 v119, v119, v167\n"
      "v_mfma_f32_16x16x32_f16 v[84:87], a[80:83], v[96:99], v[84:87]\n"
      "v_fma_f32 v190, |v90|, v58, v190\n"
      "v_fma_f32 v190, |v91|, v59, v190\n"
      "v_mfma_f32_16x16x32_f16 v[88:91], a[96:99], v[96:99], v[40:43]\n"
      "s_waitcnt vmcnt(6) lgkmcnt(2)\n"
      "v_sub_u16_sdwa v176, v240, s36 clamp dst_sel:WORD_1 dst_unused:UNUSED_PAD src0_sel:WORD_0 src1_sel:DWORD\n"
      "v_pk_mul_f16 v120, v120, v168\n"
      "v_mfma_f32_16x16x32_f16 v[88:91], a[100:103], v[100:103], v[88:91]\n"
      "v_sub_u16_sdwa v177, v240, s36 clamp dst_sel:WORD_1 dst_unused:UNUSED_PAD src0_sel:WORD_1 src1_sel:DWORD\n"
      "v_pk_mul_f16 v121, v121, v169\n"
      "v_mfma_f32_16x16x32_f16 v[88:91], a[104:107], v[104:107], v[88:91]\n"
      "v_fma_f32 v190, |v92|, v60, v190\n"
      "v_fma_f32 v190, |v93|, v61, v190\n"
      "v_sub_u16_sdwa v178, v241, s36 clamp dst_sel:WORD_1 dst_unused:UNUSED_PAD src0_sel:WORD_0 src1_sel:DWORD\n"
      "v_pk_mul_f16 v122, v122, v170\n"
      "v_mfma_f32_16x16x32_f16 v[88:91], a[108:111], v[108:111], v[88:91]\n"
      "v_fma_f32 v190, |v94|, v62, v190\n"
      "v_fma_f32 v190, |v95|, v63, v190\n"
      "v_sub_u16_sdwa v179, v241, s36 clamp dst_sel:WORD_1 dst_unused:UNUSED_PAD src0_sel:WORD_1 src1_sel:DWORD\n"
      "v_pk_mul_f16 v123, v123, v171\n"
      "v_mfma_f32_16x16x32_f16 v[92:95], a[124:127], v[108:111], v[44:47]\n"
      "v_sub_u16_sdwa v180, v242, s36 clamp dst_sel:WORD_1 dst_unused:UNUSED_PAD src0_sel:WORD_0 src1_sel:DWORD\n"
      "v_sub_u16_sdwa v181, v242, s36 clamp dst_sel:WORD_1 dst_unused:UNUSED_PAD src0_sel:WORD_1 src1_sel:DWORD\n"
      "v_mfma_f32_16x16x32_f16 v[92:95], a[120:123], v[104:107], v[92:95]\n"
      "v_fma_f32 v190, |v80|, v64, v190\n"
      "v_fma_f32 v190, |v81|, v65, v190\n"
      "v_mfma_f32_16x16x32_f16 v[92:95], a[116:119], v[100:103], v[92:95]\n"
      "s_waitcnt lgkmcnt(0)\n"
      "v_sub_u16_sdwa v182, v243, s36 clamp dst_sel:WORD_1 dst_unused:UNUSED_PAD src0_sel:WORD_0 src1_sel:DWORD\n"
      "v_pk_mul_f16 v124, v124, v172\n"
      "v_mfma_f32_16x16x32_f16 v[92:95], a[112:115], v[96:99], v[92:95]\n"
      "v_fma_f32 v190, |v82|, v66, v190\n"
      "v_fma_f32 v190, |v83|, v67, v190\n"
      "v_sub_u16_sdwa v183, v243, s36 clamp dst_sel:WORD_1 dst_unused:UNUSED_PAD src0_sel:WORD_1 src1_sel:DWORD\n"
      "v_pk_mul_f16 v125, v125, v173\n"
      "v_mfma_f32_16x16x32_f16 v[80:83], a[0:3], v[112:115], v[16:19]\n"
      "v_cvt_pk_f16_f32 v184, v176, v177\n"
      "v_pk_mul_f16 v126, v126, v174\n"
      "v_mfma_f32_16x16x32_f16 v[80:83], a[4:7], v[116:119], v[80:83]\n"
      "v_cvt_pk_f16_f32 v185, v178, v179\n"
      "v_pk_mul_f16 v127, v127, v175\n"
      "v_mfma_f32_16x16x32_f16 v[80:83], a[8:11], v[120:123], v[80:83]\n"
      "v_fma_f32 v190, |v84|, v68, v190\n"
      "v_fma_f32 v190, |v85|, v69, v190\n"
      "v_cvt_pk_f16_f32 v186, v180, v181\n"
      "v_mfma_f32_16x16x32_f16 v[80:83], a[12:15], v[124:127], v[80:83]\n"
      "v_fma_f32 v190, |v86|, v70, v190\n"
      "v_fma_f32 v190, |v87|, v71, v190\n"
      "v_cvt_pk_f16_f32 v187, v182, v183\n"
      "v_mfma_f32_16x16x32_f16 v[84:87], a[28:31], v[124:127], v[20:23]\n"
      "ds_write_b128 v1, v[184:187] offset:3456\n"
      "v_mfma_f32_16x16x32_f16 v[84:87], a[24:27], v[120:123], v[84:87]\n"
      "ds_read_b128 v[160:163], v203 offset:3456\n"
      "v_fma_f32 v190, |v88|, v72, v190\n"
      "v_mfma_f32_16x16x32_f16 v[84:87], a[20:23], v[116:119], v[84:87]\n"
      "v_fma_f32 v190, |v89|, v73, v190\n"
      "v_fma_f32 v190, |v90|, v74, v190\n"
      "v_mfma_f32_16x16x32_f16 v[84:87], a[16:19], v[112:115], v[84:87]\n"
      "v_fma_f32 v190, |v91|, v75, v190\n"
      "v_fma_f32 v190, |v92|, v76, v190\n"
      "v_mfma_f32_16x16x32_f16 v[88:91], a[32:35], v[112:115], v[24:27]\n"
      "v_fma_f32 v190, |v93|, v77, v190\n"
      "v_fma_f32 v190, |v94|, v78, v190\n"
      "v_mfma_f32_16x16x32_f16 v[88:91], a[36:39], v[116:119], v[88:91]\n"
      "ds_read_b128 v[164:167], v203 offset:3520\n"
      "v_fma_f32 v190, |v95|, v79, v190\n"
      "v_mfma_f32_16x16x32_f16 v[88:91], a[40:43], v[120:123], v[88:91]\n"
      "v_mul_f32 v194, |v80|, v48\n"
      "v_fma_f32 v194, |v81|, v49, v194\n"
      "v_mfma_f32_16x16x32_f16 v[88:91], a[44:47], v[124:127], v[88:91]\n"
      "v_fma_f32 v194, |v82|, v50, v194\n"
      "v_fma_f32 v194, |v83|, v51, v194\n"
      "v_mfma_f32_16x16x32_f16 v[92:95], a[60:63], v[124:127], v[28:31]\n"
      "ds_read_b128 v[168:171], v203 offset:3584\n"
      "v_fma_f32 v194, |v84|, v52, v194\n"
      "v_mfma_f32_16x16x32_f16 v[92:95], a[56:59], v[120:123], v[92:95]\n"
      "s_waitcnt lgkmcnt(2)\n"
      "v_pk_mul_f16 v96, v128, v160\n"
      "v_pk_mul_f16 v97, v129, v161\n"
      "v_mfma_f32_16x16x32_f16 v[92:95], a[52:55], v[116:119], v[92:95]\n"
      "v_pk_mul_f16 v98, v130, v162\n"
      "v_pk_mul_f16 v99, v131, v163\n"
      "v_mfma_f32_16x16x32_f16 v[92:95], a[48:51], v[112:115], v[92:95]\n"
      "v_fma_f32 v194, |v85|, v53, v194\n"
      "v_fma_f32 v194, |v86|, v54, v194\n"
      "v_mfma_f32_16x16x32_f16 v[80:83], a[64:67], v[112:115], v[32:35]\n"
      "global_load_dwordx4 v[10:13], v218, s[96:97] offset:1024\n"
      "v_fma_f32 v194, |v87|, v55, v194\n"
      "v_mfma_f32_16x16x32_f16 v[80:83], a[68:71], v[116:119], v[80:83]\n"
      "s_waitcnt lgkmcnt(1)\n"
      "v_pk_mul_f16 v100, v132, v164\n"
      "v_pk_mul_f16 v101, v133, v165\n"
      "v_mfma_f32_16x16x32_f16 v[80:83], a[72:75], v[120:123], v[80:83]\n"
      "v_pk_mul_f16 v102, v134, v166\n"
      "v_pk_mul_f16 v103, v135, v167\n"
      "v_mfma_f32_16x16x32_f16 v[80:83], a[76:79], v[124:127], v[80:83]\n"
      "v_fma_f32 v194, |v88|, v56, v194\n"
      "v_fma_f32 v194, |v89|, v57, v194\n"
      "v_mfma_f32_16x16x32_f16 v[84:87], a[92:95], v[124:127], v[36:39]\n"
      "ds_read_b128 v[172:175], v203 offset:3648\n"
      "v_fma_f32 v194, |v90|, v58, v194\n"
      "v_mfma_f32_16x16x32_f16 v[84:87], a[88:91], v[120:123], v[84:87]\n"
      "s_waitcnt lgkmcnt(1)\n"
      "v_fma_f32 v194, |v91|, v59, v194\n"
      "v_pk_mul_f16 v104, v136, v168\n"
      "v_mfma_f32_16x16x32_f16 v[84:87], a[84:87], v[116:119], v[84:87]\n"
      "v_pk_mul_f16 v105, v137, v169\n"
      "v_pk_mul_f16 v106, v138, v170\n"
      "v_mfma_f32_16x16x32_f16 v[84:87], a[80:83], v[112:115], v[84:87]\n"
      "v_fma_f32 v194, |v92|, v60, v194\n"
      "v_fma_f32 v194, |v93|, v61, v194\n"
      "v_mfma_f32_16x16x32_f16 v[88:91], a[96:99], v[112:115], v[40:43]\n"
      "v_pk_mul_f16 v107, v139, v171\n"
      "v_fma_f32 v194, |v94|, v62, v194\n"
      "v_mfma_f32_16x16x32_f16 v[88:91], a[100:103], v[116:119], v[88:91]\n"
      "v_fma_f32 v194, |v95|, v63, v194\n"
      "v_fma_f32 v194, |v80|, v64, v194\n"
      "v_mfma_f32_16x16x32_f16 v[88:91], a[104:107], v[120:123], v[88:91]\n"
      "ds_read_b128 v[160:163], v203 offset:4032\n"
      "v_fma_f32 v194, |v81|, v65, v194\n"
      "v_mfma_f32_16x16x32_f16 v[88:91], a[108:111], v[124:127], v[88:91]\n"
      "v_fma_f32 v194, |v82|, v66, v194\n"
      "v_fma_f32 v194, |v83|, v67, v194\n"
      "v_mfma_f32_16x16x32_f16 v[92:95], a[124:127], v[124:127], v[44:47]\n"
      "s_waitcnt lgkmcnt(1)\n"
      "v_pk_mul_f16 v108, v140, v172\n"
      "v_pk_mul_f16 v109, v141, v173\n"
      "v_mfma_f32_16x16x32_f16 v[92:95], a[120:123], v[120:123], v[92:95]\n"
      "v_pk_mul_f16 v110, v142, v174\n"
      "v_pk_mul_f16 v111, v143, v175\n"
      "v_mfma_f32_16x16x32_f16 v[92:95], a[116:119], v[116:119], v[92:95]\n"
      "v_fma_f32 v194, |v84|, v68, v194\n"
      "v_fma_f32 v194, |v85|, v69, v194\n"
      "v_mfma_f32_16x16x32_f16 v[92:95], a[112:115], v[112:115], v[92:95]\n"
      "v_fma_f32 v194, |v86|, v70, v194\n"
      "v_fma_f32 v194, |v87|, v71, v194\n"
      "v_mfma_f32_16x16x32_f16 v[80:83], a[0:3], v[96:99], v[16:19]\n"
      "v_fma_f32 v194, |v88|, v72, v194\n"
      "v_fma_f32 v194, |v89|, v73, v194\n"
      "v_mfma_f32_16x16x32_f16 v[80:83], a[4:7], v[100:103], v[80:83]\n"
      "ds_read_b128 v[164:167], v203 offset:4096\n"
      "v_fma_f32 v194, |v90|, v74, v194\n"
      "v_mfma_f32_16x16x32_f16 v[80:83], a[8:11], v[104:107], v[80:83]\n"
      "s_waitcnt lgkmcnt(1)\n"
      "v_pk_mul_f16 v112, v128, v160\n"
      "v_fma_f32 v194, |v91|, v75, v194\n"
      "v_mfma_f32_16x16x32_f16 v[80:83], a[12:15], v[108:111], v[80:83]\n"
      "ds_read_b64_tr_b16 a[168:169], v216 offset:18688\n"
      "v_pk_mul_f16 v113, v129, v161\n"
      "v_mfma_f32_16x16x32_f16 v[84:87], a[28:31], v[108:111], v[20:23]\n"
      "ds_read_b64_tr_b16 a[170:171], v216 offset:19776\n"
      "v_pk_mul_f16 v114, v130, v162\n"
      "v_mfma_f32_16x16x32_f16 v[84:87], a[24:27], v[104:107], v[84:87]\n"
      "ds_read_b64_tr_b16 a[172:173], v216 offset:18752\n"
      "v_pk_mul_f16 v115, v131, v163\n"
      "v_mfma_f32_16x16x32_f16 v[84:87], a[20:23], v[100:103], v[84:87]\n"
      "v_fma_f32 v194, |v92|, v76, v194\n"
      "v_fma_f32 v194, |v93|, v77, v194\n"
      "v_mfma_f32_16x16x32_f16 v[84:87], a[16:19], v[96:99], v[84:87]\n"
      "ds_read_b64_tr_b16 a[174:175], v216 offset:19840\n"
      "v_fma_f32 v194, |v94|, v78, v194\n"
      "v_mfma_f32_16x16x32_f16 v[88:91], a[32:35], v[96:99], v[24:27]\n"
      "ds_read_b64_tr_b16 a[176:177], v216 offset:18816\n"
      "v_fma_f32 v194, |v95|, v79, v194\n"
      "v_mfma_f32_16x16x32_f16 v[88:91], a[36:39], v[100:103], v[88:91]\n"
      "ds_read_b64_tr_b16 a[178:179], v216 offset:19904\n"
      "v_mul_f32 v191, |v80|, v48\n"
      "v_mfma_f32_16x16x32_f16 v[88:91], a[40:43], v[104:107], v[88:91]\n"
      "ds_read_b64_tr_b16 a[180:181], v216 offset:18880\n"
      "v_fma_f32 v191, |v81|, v49, v191\n"
      "v_mfma_f32_16x16x32_f16 v[88:91], a[44:47], v[108:111], v[88:91]\n"
      "s_waitcnt lgkmcnt(7)\n"
      "ds_read_b64_tr_b16 a[182:183], v216 offset:19968\n"
      "v_pk_mul_f16 v116, v132, v164\n"
      "v_mfma_f32_16x16x32_f16 v[92:95], a[60:63], v[108:111], v[28:31]\n"
      "v_fma_f32 v191, |v82|, v50, v191\n"
      "v_fma_f32 v191, |v83|, v51, v191\n"
      "v_mfma_f32_16x16x32_f16 v[92:95], a[56:59], v[104:107], v[92:95]\n"
      "v_pk_mul_f16 v117, v133, v165\n"
      "v_pk_mul_f16 v118, v134, v166\n"
      "v_mfma_f32_16x16x32_f16 v[92:95], a[52:55], v[100:103], v[92:95]\n"
      "v_pk_mul_f16 v119, v135, v167\n"
      "v_fma_f32 v191, |v84|, v52, v191\n"
      "v_mfma_f32_16x16x32_f16 v[92:95], a[48:51], v[96:99], v[92:95]\n"
      "ds_read_b128 v[168:171], v203 offset:4160\n"
      "v_fma_f32 v191, |v85|, v53, v191\n"
      "v_mfma_f32_16x16x32_f16 v[80:83], a[64:67], v[96:99], v[32:35]\n"
      "v_fma_f32 v191, |v86|, v54, v191\n"
      "v_fma_f32 v191, |v87|, v55, v191\n"
      "v_mfma_f32_16x16x32_f16 v[80:83], a[68:71], v[100:103], v[80:83]\n"
      "v_fma_f32 v191, |v88|, v56, v191\n"
      "v_fma_f32 v191, |v89|, v57, v191\n"
      "v_mfma_f32_16x16x32_f16 v[80:83], a[72:75], v[104:107], v[80:83]\n"
      "ds_read_b128 v[172:175], v203 offset:4224\n"
      "v_fma_f32 v191, |v90|, v58, v191\n"
      "v_mfma_f32_16x16x32_f16 v[80:83], a[76:79], v[108:111], v[80:83]\n"
      "s_waitcnt vmcnt(6)\n"
      "v_fma_f32 v191, |v91|, v59, v191\n"
      "v_cmp_eq_u64 s[46:47], v[196:197], v[208:209]\n"
      "v_mfma_f32_16x16x32_f16 v[84:87], a[92:95], v[108:111], v[36:39]\n"
      "s_waitcnt vmcnt(5)\n"
      "v_cmp_eq_u64 s[48:49], v[198:199], v[210:211]\n"
      "v_fma_f32 v191, |v92|, v60, v191\n"
      "v_mfma_f32_16x16x32_f16 v[84:87], a[88:91], v[104:107], v[84:87]\n"
      "s_waitcnt lgkmcnt(8)\n"
      "v_fma_f32 v191, |v93|, v61, v191\n"
      "v_fma_f32 v191, |v94|, v62, v191\n"
      "v_mfma_f32_16x16x32_f16 a[204:207], a[184:187], a[168:171], 0\n"
      "s_waitcnt lgkmcnt(6)\n"
      "ds_read_b128 v[144:147], v201 offset:2304\n"
      "s_and_b64 s[46:47], s[46:47], s[48:49]\n"
      "v_fma_f32 v191, |v95|, v63, v191\n"
      "v_mfma_f32_16x16x32_f16 a[204:207], a[188:191], a[172:175], a[204:207]\n"
      "s_waitcnt lgkmcnt(2)\n"
      "v_pk_mul_f16 v120, v136, v168\n"
      "v_pk_mul_f16 v121, v137, v169\n"
      "v_mfma_f32_16x16x32_f16 a[204:207], a[192:195], a[176:179], a[204:207]\n"
      "s_cmp_eq_u64 s[46:47], -1\n"
      "v_pk_mul_f16 v122, v138, v170\n"
      "v_pk_mul_f16 v123, v139, v171\n"
      "v_mfma_f32_16x16x32_f16 a[204:207], a[196:199], a[180:183], a[204:207]\n"
      "ds_read_b128 v[148:151], v201 offset:2368\n"
      "s_cselect_b32 s52, s52, 0x7f000001\n"
      "v_fma_f32 v191, |v80|, v64, v191\n"
      "v_mfma_f32_16x16x32_f16 v[84:87], a[84:87], v[100:103], v[84:87]\n"
      "v_mov_b32 v223, s52\n"
      "v_fma_f32 v191, |v81|, v65, v191\n"
      "v_mfma_f32_16x16x32_f16 v[84:87], a[80:83], v[96:99], v[84:87]\n"
      "s_waitcnt lgkmcnt(2)\n"
      "ds_read_b128 v[152:155], v201 offset:2432\n"
      "v_pk_mul_f16 v124, v140, v172\n"
      "v_mfma_f32_16x16x32_f16 v[88:91], a[96:99], v[96:99], v[40:43]\n"
      "ds_write2_b32 v217, a200, a204 offset0:0 offset1:64\n"
      "v_mfma_f32_16x16x32_f16 v[88:91], a[100:103], v[100:103], v[88:91]\n"
      "ds_write_b32 v2, v223\n"
      "v_mfma_f32_16x16x32_f16 v[88:91], a[104:107], v[104:107], v[88:91]\n"
      "ds_read_b128 v[160:163], v207 offset:0\n"
      "v_fma_f32 v191, |v82|, v66, v191\n"
      "v_mfma_f32_16x16x32_f16 v[88:91], a[108:111], v[108:111], v[88:91]\n"
      "v_fma_f32 v191, |v83|, v67, v191\n"
      "v_pk_mul_f16 v125, v141, v173\n"
      "v_mfma_f32_16x16x32_f16 v[92:95], a[124:127], v[108:111], v[44:47]\n"
      "ds_read_b128 v[156:159], v201 offset:2496\n"
      "v_pk_mul_f16 v126, v142, v174\n"
      "v_mfma_f32_16x16x32_f16 v[92:95], a[120:123], v[104:107], v[92:95]\n"
      "v_pk_mul_f16 v127, v143, v175\n"
      "v_fma_f32 v191, |v84|, v68, v191\n"
      "v_mfma_f32_16x16x32_f16 v[92:95], a[116:119], v[100:103], v[92:95]\n"
      "v_fma_f32 v191, |v85|, v69, v191\n"
      "v_fma_f32 v191, |v86|, v70, v191\n"
      "v_mfma_f32_16x16x32_f16 v[92:95], a[112:115], v[96:99], v[92:95]\n"
      "v_fma_f32 v191, |v87|, v71, v191\n"
      "v_fma_f32 v191, |v88|, v72, v191\n"
      "v_mfma_f32_16x16x32_f16 v[80:83], a[0:3], v[112:115], v[16:19]\n"
      "ds_read_b128 v[164:167], v207 offset:64\n"
      "v_fma_f32 v191, |v89|, v73, v191\n"
      "v_mfma_f32_16x16x32_f16 v[80:83], a[4:7], v[116:119], v[80:83]\n"
      "v_fma_f32 v191, |v90|, v74, v191\n"
      "v_fma_f32 v191, |v91|, v75, v191\n"
      "v_mfma_f32_16x16x32_f16 v[80:83], a[8:11], v[120:123], v[80:83]\n"
      "s_waitcnt lgkmcnt(2)\n"
      "v_pk_mul_f16 v96, v144, v160\n"
      "v_pk_mul_f16 v97, v145, v161\n"
      "v_mfma_f32_16x16x32_f16 v[80:83], a[12:15], v[124:127], v[80:83]\n"
      "v_pk_mul_f16 v98, v146, v162\n"
      "v_pk_mul_f16 v99, v147, v163\n"
      "v_mfma_f32_16x16x32_f16 v[84:87], a[28:31], v[124:127], v[20:23]\n"
      "v_fma_f32 v191, |v92|, v76, v191\n"
      "v_fma_f32 v191, |v93|, v77, v191\n"
      "v_mfma_f32_16x16x32_f16 v[84:87], a[24:27], v[120:123], v[84:87]\n"
      "v_fma_f32 v191, |v94|, v78, v191\n"
      "v_fma_f32 v191, |v95|, v79, v191\n"
      "v_mfma_f32_16x16x32_f16 v[84:87], a[20:23], v[116:119], v[84:87]\n"
      "v_mul_f32 v192, |v80|, v48\n"
      "v_fma_f32 v192, |v81|, v49, v192\n"
      "v_mfma_f32_16x16x32_f16 v[84:87], a[16:19], v[112:115], v[84:87]\n"
      "v_permlane32_swap_b32 v189, v191\n"
      "v_mfma_f32_16x16x32_f16 v[88:91], a[32:35], v[112:115], v[24:27]\n"
      "s_waitcnt lgkmcnt(0)\n"
      "ds_read_b128 v[168:171], v207 offset:128\n"
      "v_pk_mul_f16 v100, v148, v164\n"
      "v_mfma_f32_16x16x32_f16 v[88:91], a[36:39], v[116:119], v[88:91]\n"
      "v_fma_f32 v192, |v82|, v50, v192\n"
      "v_fma_f32 v192, |v83|, v51, v192\n"
      "v_mfma_f32_16x16x32_f16 v[88:91], a[40:43], v[120:123], v[88:91]\n"
      "v_pk_mul_f16 v101, v149, v165\n"
      "v_pk_mul_f16 v102, v150, v166\n"
      "v_mfma_f32_16x16x32_f16 v[88:91], a[44:47], v[124:127], v[88:91]\n"
      "v_pk_mul_f16 v103, v151, v167\n"
      "v_fma_f32 v192, |v84|, v52, v192\n"
      "v_mfma_f32_16x16x32_f16 v[92:95], a[60:63], v[124:127], v[28:31]\n"
      "v_fma_f32 v192, |v85|, v53, v192\n"
      "v_fma_f32 v192, |v86|, v54, v192\n"
      "v_mfma_f32_16x16x32_f16 v[92:95], a[56:59], v[120:123], v[92:95]\n"
      "s_waitcnt vmcnt(3)\n"
      "v_fma_f32 v192, |v87|, v55, v192\n"
      "v_sub_u16_sdwa v176, v244, s36 clamp dst_sel:WORD_1 dst_unused:UNUSED_PAD src0_sel:WORD_0 src1_sel:DWORD\n"
      "v_mfma_f32_16x16x32_f16 v[92:95], a[52:55], v[116:119], v[92:95]\n"
      "global_load_dwordx4 v[208:211], v218, s[96:97] offset:2048\n"
      "global_load_dwordx4 v[216:219], v218, s[96:97] offset:3072\n"
      "v_mfma_f32_16x16x32_f16 v[92:95], a[48:51], v[112:115], v[92:95]\n"
      "v_sub_u16_sdwa v177, v244, s36 clamp dst_sel:WORD_1 dst_unused:UNUSED_PAD src0_sel:WORD_1 src1_sel:DWORD\n"
      "v_sub_u16_sdwa v178, v245, s36 clamp dst_sel:WORD_1 dst_unused:UNUSED_PAD src0_sel:WORD_0 src1_sel:DWORD\n"
      "v_mfma_f32_16x16x32_f16 v[80:83], a[64:67], v[112:115], v[32:35]\n"
      "v_fma_f32 v192, |v88|, v56, v192\n"
      "v_fma_f32 v192, |v89|, v57, v192\n"
      "v_mfma_f32_16x16x32_f16 v[80:83], a[68:71], v[116:119], v[80:83]\n"
      "s_waitcnt lgkmcnt(0)\n"
      "v_sub_u16_sdwa v179, v245, s36 clamp dst_sel:WORD_1 dst_unused:UNUSED_PAD src0_sel:WORD_1 src1_sel:DWORD\n"
      "v_pk_mul_f16 v104, v152, v168\n"
      "v_mfma_f32_16x16x32_f16 v[80:83], a[72:75], v[120:123], v[80:83]\n"
      "ds_read_b128 v[172:175], v207 offset:192\n"
      "v_fma_f32 v192, |v90|, v58, v192\n"
      "v_mfma_f32_16x16x32_f16 v[80:83], a[76:79], v[124:127], v[80:83]\n"
      "v_fma_f32 v192, |v91|, v59, v192\n"
      "v_sub_u16_sdwa v180, v246, s36 clamp dst_sel:WORD_1 dst_unused:UNUSED_PAD src0_sel:WORD_0 src1_sel:DWORD\n"
      "v_mfma_f32_16x16x32_f16 v[84:87], a[92:95], v[124:127], v[36:39]\n"
      "v_pk_mul_f16 v105, v153, v169\n"
      "v_sub_u16_sdwa v181, v246, s36 clamp dst_sel:WORD_1 dst_unused:UNUSED_PAD src0_sel:WORD_1 src1_sel:DWORD\n"
      "v_mfma_f32_16x16x32_f16 v[84:87], a[88:91], v[120:123], v[84:87]\n"
      "v_pk_mul_f16 v106, v154, v170\n"
      "v_sub_u16_sdwa v182, v247, s36 clamp dst_sel:WORD_1 dst_unused:UNUSED_PAD src0_sel:WORD_0 src1_sel:DWORD\n"
      "v_mfma_f32_16x16x32_f16 v[84:87], a[84:87], v[116:119], v[84:87]\n"
      "v_pk_mul_f16 v107, v155, v171\n"
      "v_fma_f32 v192, |v92|, v60, v192\n"
      "v_mfma_f32_16x16x32_f16 v[84:87], a[80:83], v[112:115], v[84:87]\n"
      "v_fma_f32 v192, |v93|, v61, v192\n"
      "v_sub_u16_sdwa v183, v247, s36 clamp dst_sel:WORD_1 dst_unused:UNUSED_PAD src0_sel:WORD_1 src1_sel:DWORD\n"
      "v_mfma_f32_16x16x32_f16 v[88:91], a[96:99], v[112:115], v[40:43]\n"
      "v_fma_f32 v192, |v94|, v62, v192\n"
      "v_fma_f32 v192, |v95|, v63, v192\n"
      "v_mfma_f32_16x16x32_f16 v[88:91], a[100:103], v[116:119], v[88:91]\n"
      "v_cvt_pk_f16_f32 v184, v176, v177\n"
      "v_cvt_pk_f16_f32 v185, v178, v179\n"
      "v_mfma_f32_16x16x32_f16 v[88:91], a[104:107], v[120:123], v[88:91]\n"
      "v_cvt_pk_f16_f32 v186, v180, v181\n"
      "v_fma_f32 v192, |v80|, v64, v192\n"
      "v_mfma_f32_16x16x32_f16 v[88:91], a[108:111], v[124:127], v[88:91]\n"
      "v_fma_f32 v192, |v81|, v65, v192\n"
      "v_cvt_pk_f16_f32 v187, v182, v183\n"
      "v_mfma_f32_16x16x32_f16 v[92:95], a[124:127], v[124:127], v[44:47]\n"
      "s_waitcnt lgkmcnt(0)\n"
      "v_pk_mul_f16 v108, v156, v172\n"
      "v_fma_f32 v192, |v82|, v66, v192\n"
      "v_mfma_f32_16x16x32_f16 v[92:95], a[120:123], v[120:123], v[92:95]\n"
      "ds_write_b128 v1, v[184:187] offset:4608\n"
      "v_mfma_f32_16x16x32_f16 v[92:95], a[116:119], v[116:119], v[92:95]\n"
      "ds_read_b128 v[160:163], v203 offset:4608\n"
      "v_fma_f32 v192, |v83|, v67, v192\n"
      "v_mfma_f32_16x16x32_f16 v[92:95], a[112:115], v[112:115], v[92:95]\n"
      "v_pk_mul_f16 v109, v157, v173\n"
      "v_pk_mul_f16 v110, v158, v174\n"
      "v_mfma_f32_16x16x32_f16 v[80:83], a[0:3], v[96:99], v[16:19]\n"
      "v_pk_mul_f16 v111, v159, v175\n"
      "v_fma_f32 v192, |v84|, v68, v192\n"
      "v_mfma_f32_16x16x32_f16 v[80:83], a[4:7], v[100:103], v[80:83]\n"
      "v_fma_f32 v192, |v85|, v69, v192\n"
      "v_fma_f32 v192, |v86|, v70, v192\n"
      "v_mfma_f32_16x16x32_f16 v[80:83], a[8:11], v[104:107], v[80:83]\n"
      "41:\n"
      "s_cbranch_execz 44b\n"
      "v_mfma_f32_16x16x32_f16 v[80:83], a[12:15], v[108:111], v[80:83]\n"
      "v_fma_f32 v192, |v87|, v71, v192\n"
      "v_fma_f32 v192, |v88|, v72, v192\n"
      "v_mfma_f32_16x16x32_f16 v[84:87], a[28:31], v[108:111], v[20:23]\n"
      "ds_read_b128 v[164:167], v203 offset:4672\n"
      "v_fma_f32 v192, |v89|, v73, v192\n"
      "v_mfma_f32_16x16x32_f16 v[84:87], a[24:27], v[104:107], v[84:87]\n"
      "v_fma_f32 v192, |v90|, v74, v192\n"
      "v_fma_f32 v192, |v91|, v75, v192\n"
      "v_mfma_f32_16x16x32_f16 v[84:87], a[20:23], v[100:103], v[84:87]\n"
      "s_waitcnt lgkmcnt(1)\n"
      "v_pk_mul_f16 v112, v128, v160\n"
      "v_pk_mul_f16 v113, v129, v161\n"
      "v_mfma_f32_16x16x32_f16 v[84:87], a[16:19], v[96:99], v[84:87]\n"
      "v_pk_mul_f16 v114, v130, v162\n"
      "v_pk_mul_f16 v115, v131, v163\n"
      "v_mfma_f32_16x16x32_f16 v[88:91], a[32:35], v[96:99], v[24:27]\n"
      "v_fma_f32 v192, |v92|, v76, v192\n"
      "v_fma_f32 v192, |v93|, v77, v192\n"
      "v_mfma_f32_16x16x32_f16 v[88:91], a[36:39], v[100:103], v[88:91]\n"
      "v_fma_f32 v192, |v94|, v78, v192\n"
      "v_fma_f32 v192, |v95|, v79, v192\n"
      "v_mfma_f32_16x16x32_f16 v[88:91], a[40:43], v[104:107], v[88:91]\n"
      "v_mul_f32 v193, |v80|, v48\n"
      "v_fma_f32 v193, |v81|, v49, v193\n"
      "v_mfma_f32_16x16x32_f16 v[88:91], a[44:47], v[108:111], v[88:91]\n"
      "ds_read_b128 v[168:171], v203 offset:4736\n"
      "v_fma_f32 v193, |v82|, v50, v193\n"
      "v_mfma_f32_16x16x32_f16 v[92:95], a[60:63], v[108:111], v[28:31]\n"
      "s_waitcnt lgkmcnt(1)\n"
      "v_pk_mul_f16 v116, v132, v164\n"
      "v_fma_f32 v193, |v83|, v51, v193\n"
      "v_mfma_f32_16x16x32_f16 v[92:95], a[56:59], v[104:107], v[92:95]\n"
      "v_pk_mul_f16 v117, v133, v165\n"
      "v_pk_mul_f16 v118, v134, v166\n"
      "v_mfma_f32_16x16x32_f16 v[92:95], a[52:55], v[100:103], v[92:95]\n"
      "v_pk_mul_f16 v119, v135, v167\n"
      "v_fma_f32 v193, |v84|, v52, v193\n"
      "v_mfma_f32_16x16x32_f16 v[92:95], a[48:51], v[96:99], v[92:95]\n"
      "v_fma_f32 v193, |v85|, v53, v193\n"
      "v_fma_f32 v193, |v86|, v54, v193\n"
      "v_mfma_f32_16x16x32_f16 v[80:83], a[64:67], v[96:99], v[32:35]\n"
      "v_fma_f32 v193, |v87|, v55, v193\n"
      "v_fma_f32 v193, |v88|, v56, v193\n"
      "v_mfma_f32_16x16x32_f16 v[80:83], a[68:71], v[100:103], v[80:83]\n"
      "ds_read_b128 v[172:175], v203 offset:4800\n"
      "v_fma_f32 v193, |v89|, v57, v193\n"
      "v_mfma_f32_16x16x32_f16 v[80:83], a[72:75], v[104:107], v[80:83]\n"
      "v_fma_f32 v193, |v90|, v58, v193\n"
      "v_fma_f32 v193, |v91|, v59, v193\n"
      "v_mfma_f32_16x16x32_f16 v[80:83], a[76:79], v[108:111], v[80:83]\n"
      "s_waitcnt lgkmcnt(1)\n"
      "v_pk_mul_f16 v120, v136, v168\n"
      "v_pk_mul_f16 v121, v137, v169\n"
      "v_mfma_f32_16x16x32_f16 v[84:87], a[92:95], v[108:111], v[36:39]\n"
      "v_pk_mul_f16 v122, v138, v170\n"
      "v_pk_mul_f16 v123, v139, v171\n"
      "v_mfma_f32_16x16x32_f16 v[84:87], a[88:91], v[104:107], v[84:87]\n"
      "v_fma_f32 v193, |v92|, v60, v193\n"
      "v_fma_f32 v193, |v93|, v61, v193\n"
      "v_mfma_f32_16x16x32_f16 v[84:87], a[84:87], v[100:103], v[84:87]\n"
      "v_fma_f32 v193, |v94|, v62, v193\n"
      "v_fma_f32 v193, |v95|, v63, v193\n"
      "v_mfma_f32_16x16x32_f16 v[84:87], a[80:83], v[96:99], v[84:87]\n"
      "v_fma_f32 v193, |v80|, v64, v193\n"
      "v_fma_f32 v193, |v81|, v65, v193\n"
      "v_mfma_f32_16x16x32_f16 v[88:91], a[96:99], v[96:99], v[40:43]\n"
      "s_waitcnt vmcnt(4)\n"
      "v_sub_u16_sdwa v176, v248, s36 clamp dst_sel:WORD_1 dst_unused:UNUSED_PAD src0_sel:WORD_0 src1_sel:DWORD\n"
      "v_fma_f32 v193, |v82|, v66, v193\n"
      "v_mfma_f32_16x16x32_f16 v[88:91], a[100:103], v[100:103], v[88:91]\n"
      "s_waitcnt lgkmcnt(0)\n"
      "v_pk_mul_f16 v124, v140, v172\n"
      "v_fma_f32 v193, |v83|, v67, v193\n"
      "v_mfma_f32_16x16x32_f16 v[88:91], a[104:107], v[104:107], v[88:91]\n"
      "v_sub_u16_sdwa v177, v248, s36 clamp dst_sel:WORD_1 dst_unused:UNUSED_PAD src0_sel:WORD_1 src1_sel:DWORD\n"
      "v_pk_mul_f16 v125, v141, v173\n"
      "v_mfma_f32_16x16x32_f16 v[88:91], a[108:111], v[108:111], v[88:91]\n"
      "v_sub_u16_sdwa v178, v249, s36 clamp dst_sel:WORD_1 dst_unused:UNUSED_PAD src0_sel:WORD_0 src1_sel:DWORD\n"
      "v_pk_mul_f16 v126, v142, v174\n"
      "v_mfma_f32_16x16x32_f16 v[92:95], a[124:127], v[108:111], v[44:47]\n"
      "v_add_f32 v189, v189, v191\n"
      "v_sub_u16_sdwa v179, v249, s36 clamp dst_sel:WORD_1 dst_unused:UNUSED_PAD src0_sel:WORD_1 src1_sel:DWORD\n"
      "v_mfma_f32_16x16x32_f16 v[92:95], a[120:123], v[104:107], v[92:95]\n"
      "v_permlane32_swap_b32 v190, v192\n"
      "v_mfma_f32_16x16x32_f16 v[92:95], a[116:119], v[100:103], v[92:95]\n"
      "v_pk_mul_f16 v127, v143, v175\n"
      "v_fma_f32 v193, |v84|, v68, v193\n"
      "v_mfma_f32_16x16x32_f16 v[92:95], a[112:115], v[96:99], v[92:95]\n"
      "v_fma_f32 v193, |v85|, v69, v193\n"
      "v_sub_u16_sdwa v180, v250, s36 clamp dst_sel:WORD_1 dst_unused:UNUSED_PAD src0_sel:WORD_0 src1_sel:DWORD\n"
      "v_mfma_f32_16x16x32_f16 v[80:83], a[0:3], v[112:115], v[16:19]\n"
      "v_add_f32 v190, v190, v192\n"
      "v_fma_f32 v193, |v86|, v70, v193\n"
      "v_mfma_f32_16x16x32_f16 v[80:83], a[4:7], v[116:119], v[80:83]\n"
      "v_fma_f32 v193, |v87|, v71, v193\n"
      "v_sub_u16_sdwa v181, v250, s36 clamp dst_sel:WORD_1 dst_unused:UNUSED_PAD src0_sel:WORD_1 src1_sel:DWORD\n"
      "v_mfma_f32_16x16x32_f16 v[80:83], a[8:11], v[120:123], v[80:83]\n"
      "v_permlane16_swap_b32 v189, v190\n"
      "v_mfma_f32_16x16x32_f16 v[80:83], a[12:15], v[124:127], v[80:83]\n"
      "v_sub_u16_sdwa v182, v251, s36 clamp dst_sel:WORD_1 dst_unused:UNUSED_PAD src0_sel:WORD_0 src1_sel:DWORD\n"
      "v_sub_u16_sdwa v183, v251, s36 clamp dst_sel:WORD_1 dst_unused:UNUSED_PAD src0_sel:WORD_1 src1_sel:DWORD\n"
      "v_mfma_f32_16x16x32_f16 v[84:87], a[28:31], v[124:127], v[20:23]\n"
      "v_add_f32 v189, v189, v190\n"
      "v_fma_f32 v193, |v88|, v72, v193\n"
      "v_mfma_f32_16x16x32_f16 v[84:87], a[24:27], v[120:123], v[84:87]\n"
      "v_fma_f32 v193, |v89|, v73, v193\n"
      "v_cvt_pk_f16_f32 v184, v176, v177\n"
      "v_mfma_f32_16x16x32_f16 v[84:87], a[20:23], v[116:119], v[84:87]\n"
      "v_pk_mul_f16 v96, v144, v160\n"
      "v_fma_f32 v193, |v90|, v74, v193\n"
      "v_mfma_f32_16x16x32_f16 v[84:87], a[16:19], v[112:115], v[84:87]\n"
      "v_fma_f32 v193, |v91|, v75, v193\n"
      "v_cvt_pk_f16_f32 v185, v178, v179\n"
      "v_pk_mul_f16 v97, v145, v161\n"
      "v_mfma_f32_16x16x32_f16 v[88:91], a[32:35], v[112:115], v[24:27]\n"
      "v_cvt_pk_f16_f32 v186, v180, v181\n"
      "v_pk_mul_f16 v98, v146, v162\n"
      "v_mfma_f32_16x16x32_f16 v[88:91], a[36:39], v[116:119], v[88:91]\n"
      "v_cvt_pk_f16_f32 v187, v182, v183\n"
      "v_pk_mul_f16 v99, v147, v163\n"
      "v_mfma_f32_16x16x32_f16 v[88:91], a[40:43], v[120:123], v[88:91]\n"
      "ds_write_b128 v1, v[184:187] offset:5760\n"
      "v_fma_f32 v193, |v92|, v76, v193\n"
      "v_fma_f32 v193, |v93|, v77, v193\n"
      "v_mfma_f32_16x16x32_f16 v[88:91], a[44:47], v[124:127], v[88:91]\n"
      "v_fma_f32 v193, |v94|, v78, v193\n"
      "v_fma_f32 v193, |v95|, v79, v193\n"
      "v_mfma_f32_16x16x32_f16 v[92:95], a[60:63], v[124:127], v[28:31]\n"
      "v_mul_f32 v190, |v80|, v48\n"
      "v_fma_f32 v190, |v81|, v49, v190\n"
      "v_mfma_f32_16x16x32_f16 v[92:95], a[56:59], v[120:123], v[92:95]\n"
      "v_pk_mul_f16 v100, v148, v164\n"
      "v_fma_f32 v190, |v82|, v50, v190\n"
      "v_mfma_f32_16x16x32_f16 v[92:95], a[52:55], v[116:119], v[92:95]\n"
      "v_fma_f32 v190, |v83|, v51, v190\n"
      "v_pk_mul_f16 v101, v149, v165\n"
      "v_mfma_f32_16x16x32_f16 v[92:95], a[48:51], v[112:115], v[92:95]\n"
      "v_pk_mul_f16 v102, v150, v166\n"
      "v_pk_mul_f16 v103, v151, v167\n"
      "v_mfma_f32_16x16x32_f16 v[80:83], a[64:67], v[112:115], v[32:35]\n"
      "v_fma_f32 v190, |v84|, v52, v190\n"
      "v_fma_f32 v190, |v85|, v53, v190\n"
      "v_mfma_f32_16x16x32_f16 v[80:83], a[68:71], v[116:119], v[80:83]\n"
      "v_fma_f32 v190, |v86|, v54, v190\n"
      "v_fma_f32 v190, |v87|, v55, v190\n"
      "v_mfma_f32_16x16x32_f16 v[80:83], a[72:75], v[120:123], v[80:83]\n"
      "v_fma_f32 v190, |v88|, v56, v190\n"
      "v_fma_f32 v190, |v89|, v57, v190\n"
      "v_mfma_f32_16x16x32_f16 v[80:83], a[76:79], v[124:127], v[80:83]\n"
      "v_pk_mul_f16 v104, v152, v168\n"
      "v_fma_f32 v190, |v90|, v58, v190\n"
      "v_mfma_f32_16x16x32_f16 v[84:87], a[92:95], v[124:127], v[36:39]\n"
      "v_fma_f32 v190, |v91|, v59, v190\n"
      "v_pk_mul_f16 v105, v153, v169\n"
      "v_mfma_f32_16x16x32_f16 v[84:87], a[88:91], v[120:123], v[84:87]\n"
      "v_pk_mul_f16 v106, v154, v170\n"
      "v_pk_mul_f16 v107, v155, v171\n"
      "v_mfma_f32_16x16x32_f16 v[84:87], a[84:87], v[116:119], v[84:87]\n"
      "v_fma_f32 v190, |v92|, v60, v190\n"
      "v_fma_f32 v190, |v93|, v61, v190\n"
      "v_mfma_f32_16x16x32_f16 v[84:87], a[80:83], v[112:115], v[84:87]\n"
      "v_fma_f32 v190, |v94|, v62, v190\n"
      "v_fma_f32 v190, |v95|, v63, v190\n"
      "v_mfma_f32_16x16x32_f16 v[88:91], a[96:99], v[112:115], v[40:43]\n"
      "v_permlane32_swap_b32 v188, v193\n"
      "v_mfma_f32_16x16x32_f16 v[88:91], a[100:103], v[116:119], v[88:91]\n"
      "v_fma_f32 v190, |v80|, v64, v190\n"
      "v_fma_f32 v190, |v81|, v65, v190\n"
      "v_mfma_f32_16x16x32_f16 v[88:91], a[104:107], v[120:123], v[88:91]\n"
      "ds_read_b128 v[160:163], v203 offset:5184\n"
      "v_pk_mul_f16 v108, v156, v172\n"
      "v_mfma_f32_16x16x32_f16 v[88:91], a[108:111], v[124:127], v[88:91]\n"
      "v_fma_f32 v190, |v82|, v66, v190\n"
      "v_fma_f32 v190, |v83|, v67, v190\n"
      "v_mfma_f32_16x16x32_f16 v[92:95], a[124:127], v[124:127], v[44:47]\n"
      "v_pk_mul_f16 v109, v157, v173\n"
      "v_pk_mul_f16 v110, v158, v174\n"
      "v_mfma_f32_16x16x32_f16 v[92:95], a[120:123], v[120:123], v[92:95]\n"
      "v_pk_mul_f16 v111, v159, v175\n"
      "v_fma_f32 v190, |v84|, v68, v190\n"
      "v_mfma_f32_16x16x32_f16 v[92:95], a[116:119], v[116:119], v[92:95]\n"
      "s_waitcnt vmcnt(3)\n"
      "v_fma_f32 v190, |v85|, v69, v190\n"
      "v_sub_u16_sdwa v176, v252, s36 clamp dst_sel:WORD_1 dst_unused:UNUSED_PAD src0_sel:WORD_0 src1_sel:DWORD\n"
      "v_mfma_f32_16x16x32_f16 v[92:95], a[112:115], v[112:115], v[92:95]\n"
      "v_fma_f32 v190, |v86|, v70, v190\n"
      "v_fma_f32 v190, |v87|, v71, v190\n"
      "v_mfma_f32_16x16x32_f16 v[80:83], a[0:3], v[96:99], v[16:19]\n"
      "v_sub_u16_sdwa v177, v252, s36 clamp dst_sel:WORD_1 dst_unused:UNUSED_PAD src0_sel:WORD_1 src1_sel:DWORD\n"
      "v_sub_u16_sdwa v178, v253, s36 clamp dst_sel:WORD_1 dst_unused:UNUSED_PAD src0_sel:WORD_0 src1_sel:DWORD\n"
      "v_mfma_f32_16x16x32_f16 v[80:83], a[4:7], v[100:103], v[80:83]\n"
      "v_sub_u16_sdwa v179, v253, s36 clamp dst_sel:WORD_1 dst_unused:UNUSED_PAD src0_sel:WORD_1 src1_sel:DWORD\n"
      "v_fma_f32 v190, |v88|, v72, v190\n"
      "v_mfma_f32_16x16x32_f16 v[80:83], a[8:11], v[104:107], v[80:83]\n"
      "v_fma_f32 v190, |v89|, v73, v190\n"
      "v_sub_u16_sdwa v180, v254, s36 clamp dst_sel:WORD_1 dst_unused:UNUSED_PAD src0_sel:WORD_0 src1_sel:DWORD\n"
      "v_mfma_f32_16x16x32_f16 v[80:83], a[12:15], v[108:111], v[80:83]\n"
      "s_waitcnt lgkmcnt(0)\n"
      "ds_read_b128 v[164:167], v203 offset:5248\n"
      "v_pk_mul_f16 v112, v128, v160\n"
      "v_mfma_f32_16x16x32_f16 v[84:87], a[28:31], v[108:111], v[20:23]\n"
      "v_fma_f32 v190, |v90|, v74, v190\n"
      "v_fma_f32 v190, |v91|, v75, v190\n"
      "v_mfma_f32_16x16x32_f16 v[84:87], a[24:27], v[104:107], v[84:87]\n"
      "v_sub_u16_sdwa v181, v254, s36 clamp dst_sel:WORD_1 dst_unused:UNUSED_PAD src0_sel:WORD_1 src1_sel:DWORD\n"
      "v_pk_mul_f16 v113, v129, v161\n"
      "v_mfma_f32_16x16x32_f16 v[84:87], a[20:23], v[100:103], v[84:87]\n"
      "v_sub_u16_sdwa v182, v255, s36 clamp dst_sel:WORD_1 dst_unused:UNUSED_PAD src0_sel:WORD_0 src1_sel:DWORD\n"
      "v_pk_mul_f16 v114, v130, v162\n"
      "v_mfma_f32_16x16x32_f16 v[84:87], a[16:19], v[96:99], v[84:87]\n"
      "v_sub_u16_sdwa v183, v255, s36 clamp dst_sel:WORD_1 dst_unused:UNUSED_PAD src0_sel:WORD_1 src1_sel:DWORD\n"
      "v_pk_mul_f16 v115, v131, v163\n"
      "v_mfma_f32_16x16x32_f16 v[88:91], a[32:35], v[96:99], v[24:27]\n"
      "v_fma_f32 v190, |v92|, v76, v190\n"
      "v_fma_f32 v190, |v93|, v77, v190\n"
      "v_mfma_f32_16x16x32_f16 v[88:91], a[36:39], v[100:103], v[88:91]\n"
      "v_cvt_pk_f16_f32 v184, v176, v177\n"
      "v_fma_f32 v190, |v94|, v78, v190\n"
      "v_mfma_f32_16x16x32_f16 v[88:91], a[40:43], v[104:107], v[88:91]\n"
      "v_fma_f32 v190, |v95|, v79, v190\n"
      "v_cvt_pk_f16_f32 v185, v178, v179\n"
      "v_mfma_f32_16x16x32_f16 v[88:91], a[44:47], v[108:111], v[88:91]\n"
      "v_cvt_pk_f16_f32 v186, v180, v181\n"
      "v_cvt_pk_f16_f32 v187, v182, v183\n"
      "v_mfma_f32_16x16x32_f16 v[92:95], a[60:63], v[108:111], v[28:31]\n"
      "ds_write_b128 v1, v[184:187] offset:6912\n"
      "v_mfma_f32_16x16x32_f16 v[92:95], a[56:59], v[104:107], v[92:95]\n"
      "v_mul_f32 v191, |v80|, v48\n"
      "v_fma_f32 v191, |v81|, v49, v191\n"
      "v_mfma_f32_16x16x32_f16 v[92:95], a[52:55], v[100:103], v[92:95]\n"
      "s_waitcnt lgkmcnt(1)\n"
      "ds_read_b128 v[168:171], v203 offset:5312\n"
      "v_pk_mul_f16 v116, v132, v164\n"
      "v_mfma_f32_16x16x32_f16 v[92:95], a[48:51], v[96:99], v[92:95]\n"
      "v_fma_f32 v191, |v82|, v50, v191\n"
      "v_fma_f32 v191, |v83|, v51, v191\n"
      "v_pk_mul_f16 v117, v133, v165\n"
      "v_mfma_f32_16x16x32_f16 v[80:83], a[64:67], v[96:99], v[32:35]\n"
      "v_pk_mul_f16 v118, v134, v166\n"
      "v_pk_mul_f16 v119, v135, v167\n"
      "v_mfma_f32_16x16x32_f16 v[80:83], a[68:71], v[100:103], v[80:83]\n"
      "v_fma_f32 v191, |v84|, v52, v191\n"
      "v_fma_f32 v191, |v85|, v53, v191\n"
      "v_mfma_f32_16x16x32_f16 v[80:83], a[72:75], v[104:107], v[80:83]\n"
      "v_fma_f32 v191, |v86|, v54, v191\n"
      "v_fma_f32 v191, |v87|, v55, v191\n"
      "v_mfma_f32_16x16x32_f16 v[80:83], a[76:79], v[108:111], v[80:83]\n"
      "v_fma_f32 v191, |v88|, v56, v191\n"
      "v_fma_f32 v191, |v89|, v57, v191\n"
      "v_mfma_f32_16x16x32_f16 v[84:87], a[92:95], v[108:111], v[36:39]\n"
      "ds_read_b128 v[172:175], v203 offset:5376\n"
      "v_fma_f32 v191, |v90|, v58, v191\n"
      "v_mfma_f32_16x16x32_f16 v[84:87], a[88:91], v[104:107], v[84:87]\n"
      "ds_read_b128 v[176:179], v206 offset:864\n"
      "v_fma_f32 v191, |v91|, v59, v191\n"
      "v_mfma_f32_16x16x32_f16 v[84:87], a[84:87], v[100:103], v[84:87]\n"
      "s_waitcnt lgkmcnt(2)\n"
      "v_pk_mul_f16 v120, v136, v168\n"
      "v_pk_mul_f16 v121, v137, v169\n"
      "v_mfma_f32_16x16x32_f16 v[84:87], a[80:83], v[96:99], v[84:87]\n"
      "ds_read_b128 v[180:183], v206 offset:2016\n"
      "v_pk_mul_f16 v122, v138, v170\n"
      "v_mfma_f32_16x16x32_f16 v[88:91], a[96:99], v[96:99], v[40:43]\n"
      "ds_read2_b32 v[184:185], v8 offset0:0 offset1:72\n"
      "v_pk_mul_f16 v123, v139, v171\n"
      "v_mfma_f32_16x16x32_f16 v[88:91], a[100:103], v[100:103], v[88:91]\n"
      "v_fma_f32 v191, |v92|, v60, v191\n"
      "v_fma_f32 v191, |v93|, v61, v191\n"
      "v_mfma_f32_16x16x32_f16 v[88:91], a[104:107], v[104:107], v[88:91]\n"
      "v_fma_f32 v191, |v94|, v62, v191\n"
      "v_fma_f32 v191, |v95|, v63, v191\n"
      "v_mfma_f32_16x16x32_f16 v[88:91], a[108:111], v[108:111], v[88:91]\n"
      "v_fma_f32 v191, |v80|, v64, v191\n"
      "v_fma_f32 v191, |v81|, v65, v191\n"
      "v_mfma_f32_16x16x32_f16 v[92:95], a[124:127], v[108:111], v[44:47]\n"
      "s_waitcnt lgkmcnt(3)\n"
      "v_pk_mul_f16 v124, v140, v172\n"
      "v_fma_f32 v191, |v82|, v66, v191\n"
      "v_mfma_f32_16x16x32_f16 v[92:95], a[120:123], v[104:107], v[92:95]\n"
      "v_fma_f32 v191, |v83|, v67, v191\n"
      "v_pk_mul_f16 v125, v141, v173\n"
      "v_mfma_f32_16x16x32_f16 v[92:95], a[116:119], v[100:103], v[92:95]\n"
      "v_pk_mul_f16 v126, v142, v174\n"
      "v_add_f32 v188, v188, v193\n"
      "v_mfma_f32_16x16x32_f16 v[92:95], a[112:115], v[96:99], v[92:95]\n"
      "s_waitcnt lgkmcnt(1)\n"
      "v_pk_mul_f16 v176, v176, v180\n"
      "v_pk_mul_f16 v177, v177, v181\n"
      "v_mfma_f32_16x16x32_f16 v[80:83], a[0:3], v[112:115], v[16:19]\n"
      "v_pk_mul_f16 v127, v143, v175\n"
      "v_fma_f32 v191, |v84|, v68, v191\n"
      "v_mfma_f32_16x16x32_f16 v[80:83], a[4:7], v[116:119], v[80:83]\n"
      "v_fma_f32 v191, |v85|, v69, v191\n"
      "v_pk_mul_f16 v178, v178, v182\n"
      "v_mfma_f32_16x16x32_f16 v[80:83], a[8:11], v[120:123], v[80:83]\n"
      "v_fma_f32 v191, |v86|, v70, v191\n"
      "v_fma_f32 v191, |v87|, v71, v191\n"
      "v_mfma_f32_16x16x32_f16 v[80:83], a[12:15], v[124:127], v[80:83]\n"
      "s_waitcnt lgkmcnt(0)\n"
      "v_pk_mul_f16 v179, v179, v183\n"
      "v_pk_mul_f16 v184, v184, v185\n"
      "v_mfma_f32_16x16x32_f16 v[84:87], a[28:31], v[124:127], v[20:23]\n"
      "ds_write_b128 v1, v[176:179] offset:8064\n"
      "v_mfma_f32_16x16x32_f16 v[84:87], a[24:27], v[120:123], v[84:87]\n"
      "v_fma_f32 v191, |v88|, v72, v191\n"
      "v_fma_f32 v191, |v89|, v73, v191\n"
      "v_mfma_f32_16x16x32_f16 v[84:87], a[20:23], v[116:119], v[84:87]\n"
      "ds_write_b32 v8, v184 offset:2304\n"
      "v_pk_mul_f16 v96, v144, v160\n"
      "v_mfma_f32_16x16x32_f16 v[84:87], a[16:19], v[112:115], v[84:87]\n"
      "v_fma_f32 v191, |v90|, v74, v191\n"
      "v_fma_f32 v191, |v91|, v75, v191\n"
      "v_pk_mul_f16 v97, v145, v161\n"
      "v_mfma_f32_16x16x32_f16 v[88:91], a[32:35], v[112:115], v[24:27]\n"
      "v_pk_mul_f16 v98, v146, v162\n"
      "v_pk_mul_f16 v99, v147, v163\n"
      "v_mfma_f32_16x16x32_f16 v[88:91], a[36:39], v[116:119], v[88:91]\n"
      "v_fma_f32 v191, |v92|, v76, v191\n"
      "v_fma_f32 v191, |v93|, v77, v191\n"
      "v_mfma_f32_16x16x32_f16 v[88:91], a[40:43], v[120:123], v[88:91]\n"
      "v_fma_f32 v191, |v94|, v78, v191\n"
      "v_fma_f32 v191, |v95|, v79, v191\n"
      "v_mfma_f32_16x16x32_f16 v[88:91], a[44:47], v[124:127], v[88:91]\n"
      "v_mul_f32 v192, |v80|, v48\n"
      "v_fma_f32 v192, |v81|, v49, v192\n"
      "v_mfma_f32_16x16x32_f16 v[92:95], a[60:63], v[124:127], v[28:31]\n"
      "v_pk_mul_f16 v100, v148, v164\n"
      "v_fma_f32 v192, |v82|, v50, v192\n"
      "v_mfma_f32_16x16x32_f16 v[92:95], a[56:59], v[120:123], v[92:95]\n"
      "v_fma_f32 v192, |v83|, v51, v192\n"
      "v_pk_mul_f16 v101, v149, v165\n"
      "v_mfma_f32_16x16x32_f16 v[92:95], a[52:55], v[116:119], v[92:95]\n"
      "v_pk_mul_f16 v102, v150, v166\n"
      "v_pk_mul_f16 v103, v151, v167\n"
      "v_mfma_f32_16x16x32_f16 v[92:95], a[48:51], v[112:115], v[92:95]\n"
      "v_fma_f32 v192, |v84|, v52, v192\n"
      "v_fma_f32 v192, |v85|, v53, v192\n"
      "v_mfma_f32_16x16x32_f16 v[80:83], a[64:67], v[112:115], v[32:35]\n"
      "v_fma_f32 v192, |v86|, v54, v192\n"
      "v_fma_f32 v192, |v87|, v55, v192\n"
      "v_mfma_f32_16x16x32_f16 v[80:83], a[68:71], v[116:119], v[80:83]\n"
      "v_fma_f32 v192, |v88|, v56, v192\n"
      "v_fma_f32 v192, |v89|, v57, v192\n"
      "v_mfma_f32_16x16x32_f16 v[80:83], a[72:75], v[120:123], v[80:83]\n"
      "ds_read_b32 v224, v227 offset:37888\n"
      "v_pk_mul_f16 v104, v152, v168\n"
      "v_mfma_f32_16x16x32_f16 v[80:83], a[76:79], v[124:127], v[80:83]\n"
      "v_fma_f32 v192, |v90|, v58, v192\n"
      "v_fma_f32 v192, |v91|, v59, v192\n"
      "v_mfma_f32_16x16x32_f16 v[84:87], a[92:95], v[124:127], v[36:39]\n"
      "ds_read_b128 v[176:179], v5 offset:37376\n"
      "v_pk_mul_f16 v105, v153, v169\n"
      "v_mfma_f32_16x16x32_f16 v[84:87], a[88:91], v[120:123], v[84:87]\n"
      "ds_read_b128 v[180:183], v5 offset:37392\n"
      "s_mov_b32 s44, 0x3d800000\n"
      "v_pk_mul_f16 v106, v154, v170\n"
      "v_mfma_f32_16x16x32_f16 v[84:87], a[84:87], v[116:119], v[84:87]\n"
      "v_pk_mul_f16 v107, v155, v171\n"
      "v_fma_f32 v192, |v92|, v60, v192\n"
      "v_mfma_f32_16x16x32_f16 v[84:87], a[80:83], v[112:115], v[84:87]\n"
      "v_fma_f32 v192, |v93|, v61, v192\n"
      "v_fma_f32 v192, |v94|, v62, v192\n"
      "v_mfma_f32_16x16x32_f16 v[88:91], a[96:99], v[112:115], v[40:43]\n"
      "v_fma_f32 v192, |v95|, v63, v192\n"
      "v_fma_f32 v192, |v80|, v64, v192\n"
      "v_mfma_f32_16x16x32_f16 v[88:91], a[100:103], v[116:119], v[88:91]\n"
      "v_fma_f32 v192, |v81|, v65, v192\n"
      "v_pk_mul_f16 v108, v156, v172\n"
      "v_mfma_f32_16x16x32_f16 v[88:91], a[104:107], v[120:123], v[88:91]\n"
      "ds_read_b128 v[160:163], v203 offset:5760\n"
      "v_fma_f32 v192, |v82|, v66, v192\n"
      "v_mfma_f32_16x16x32_f16 v[88:91], a[108:111], v[124:127], v[88:91]\n"
      "v_fma_f32 v192, |v83|, v67, v192\n"
      "v_pk_mul_f16 v109, v157, v173\n"
      "v_mfma_f32_16x16x32_f16 v[92:95], a[124:127], v[124:127], v[44:47]\n"
      "v_pk_mul_f16 v110, v158, v174\n"
      "v_pk_mul_f16 v111, v159, v175\n"
      "v_mfma_f32_16x16x32_f16 v[92:95], a[120:123], v[120:123], v[92:95]\n"
      "s_waitcnt lgkmcnt(1)\n"
      "v_max3_f32 v225, |v176|, |v181|, |v224|\n"
      "v_cmp_le_f32 vcc, 0x43800000, v225\n"
      "v_mfma_f32_16x16x32_f16 v[92:95], a[116:119], v[116:119], v[92:95]\n"
      "v_fma_f32 v192, |v84|, v68, v192\n"
      "v_fma_f32 v192, |v85|, v69, v192\n"
      "v_mfma_f32_16x16x32_f16 v[92:95], a[112:115], v[112:115], v[92:95]\n"
      "v_cmp_le_f32 s[28:29], s44, v225\n"
      "v_fma_f32 v192, |v86|, v70, v192\n"
      "v_mfma_f32_16x16x32_f16 v[80:83], a[0:3], v[96:99], v[16:19]\n"
      "v_fma_f32 v192, |v87|, v71, v192\n"
      "v_fma_f32 v192, |v88|, v72, v192\n"
      "v_mfma_f32_16x16x32_f16 v[80:83], a[4:7], v[100:103], v[80:83]\n"
      "ds_read_b128 v[164:167], v203 offset:5824\n"
      "s_cmp_eq_u32 s28, 0\n"
      "v_fma_f32 v192, |v89|, v73, v192\n"
      "v_mfma_f32_16x16x32_f16 v[80:83], a[8:11], v[104:107], v[80:83]\n"
      "s_waitcnt lgkmcnt(1)\n"
      "v_pk_mul_f16 v112, v128, v160\n"
      "v_fma_f32 v192, |v90|, v74, v192\n"
      "v_mfma_f32_16x16x32_f16 v[80:83], a[12:15], v[108:111], v[80:83]\n"
      "s_cselect_b64 vcc, -1, vcc\n"
      "v_fma_f32 v192, |v91|, v75, v192\n"
      "v_pk_mul_f16 v113, v129, v161\n"
      "v_mfma_f32_16x16x32_f16 v[84:87], a[28:31], v[108:111], v[20:23]\n"
      "s_cbranch_vccnz 5f\n"
      "4:\n"
      "v_mfma_f32_16x16x32_f16 v[84:87], a[24:27], v[104:107], v[84:87]\n"
      "v_cvt_pk_f16_f32 v184, v176, v177\n"
      "v_cvt_pk_f16_f32 v185, v178, v179\n"
      "v_mfma_f32_16x16x32_f16 v[84:87], a[20:23], v[100:103], v[84:87]\n"
      "v_pk_mul_f16 v114, v130, v162\n"
      "v_cvt_pk_f16_f32 v186, v180, v181\n"
      "v_mfma_f32_16x16x32_f16 v[84:87], a[16:19], v[96:99], v[84:87]\n"
      "v_pk_mul_f16 v115, v131, v163\n"
      "v_cvt_pk_f16_f32 v187, v182, v183\n"
      "v_mfma_f32_16x16x32_f16 v[88:91], a[32:35], v[96:99], v[24:27]\n"
      "ds_write_b128 v213, v[184:187] offset:16512\n"
      "s_lshl_b32 s49, s40, 1\n"
      "v_mfma_f32_16x16x32_f16 v[88:91], a[36:39], v[100:103], v[88:91]\n"
      "s_add_u32 s51, s57, s49\n"
      "v_fma_f32 v192, |v92|, v76, v192\n"
      "v_fma_f32 v192, |v93|, v77, v192\n"
      "v_mfma_f32_16x16x32_f16 v[88:91], a[40:43], v[104:107], v[88:91]\n"
      "s_add_u32 s51, s51, s58\n"
      "v_fma_f32 v192, |v94|, v78, v192\n"
      "v_fma_f32 v192, |v95|, v79, v192\n"
      "v_mfma_f32_16x16x32_f16 v[88:91], a[44:47], v[108:111], v[88:91]\n"
      "s_add_u32 s51, s51, s56\n"
      "v_mul_f32 v193, |v80|, v48\n"
      "v_fma_f32 v193, |v81|, v49, v193\n"
      "v_mfma_f32_16x16x32_f16 v[92:95], a[60:63], v[108:111], v[28:31]\n"
      "s_waitcnt lgkmcnt(1)\n"
      "ds_read_b128 v[168:171], v203 offset:5888\n"
      "v_pk_mul_f16 v116, v132, v164\n"
      "v_mfma_f32_16x16x32_f16 v[92:95], a[56:59], v[104:107], v[92:95]\n"
      "s_sub_u32 s51, 0, s51\n"
      "v_fma_f32 v193, |v82|, v50, v193\n"
      "v_fma_f32 v193, |v83|, v51, v193\n"
      "v_mfma_f32_16x16x32_f16 v[92:95], a[52:55], v[100:103], v[92:95]\n"
      "s_add_u32 s49, s59, s49\n"
      "v_pk_mul_f16 v117, v133, v165\n"
      "v_pk_mul_f16 v118, v134, v166\n"
      "v_mfma_f32_16x16x32_f16 v[92:95], a[48:51], v[96:99], v[92:95]\n"
      "s_sub_u32 s39, 6, s49\n"
      "v_pk_mul_f16 v119, v135, v167\n"
      "v_fma_f32 v193, |v84|, v52, v193\n"
      "v_mfma_f32_16x16x32_f16 v[80:83], a[64:67], v[96:99], v[32:35]\n"
      "s_movk_i32 s45, 0x207\n"
      "v_fma_f32 v193, |v85|, v53, v193\n"
      "v_fma_f32 v193, |v86|, v54, v193\n"
      "v_mfma_f32_16x16x32_f16 v[80:83], a[68:71], v[100:103], v[80:83]\n"
      "s_mov_b32 s49, 0x71800000\n"
      "v_fma_f32 v193, |v87|, v55, v193\n"
      "v_fma_f32 v193, |v88|, v56, v193\n"
      "v_mfma_f32_16x16x32_f16 v[80:83], a[72:75], v[104:107], v[80:83]\n"
      "ds_read_b128 v[172:175], v203 offset:5952\n"
      "v_fma_f32 v193, |v89|, v57, v193\n"
      "v_mfma_f32_16x16x32_f16 v[80:83], a[76:79], v[108:111], v[80:83]\n"
      "v_fma_f32 v193, |v90|, v58, v193\n"
      "v_fma_f32 v193, |v91|, v59, v193\n"
      "v_mfma_f32_16x16x32_f16 v[84:87], a[92:95], v[108:111], v[36:39]\n"
      "s_waitcnt lgkmcnt(1)\n"
      "v_pk_mul_f16 v120, v136, v168\n"
      "v_pk_mul_f16 v121, v137, v169\n"
      "v_mfma_f32_16x16x32_f16 v[84:87], a[88:91], v[104:107], v[84:87]\n"
      "v_ldexp_f32 v215, v215, s51\n"
      "v_pk_mul_f16 v122, v138, v170\n"
      "v_mfma_f32_16x16x32_f16 v[84:87], a[84:87], v[100:103], v[84:87]\n"
      "v_pk_mul_f16 v123, v139, v171\n"
      "v_fma_f32 v193, |v92|, v60, v193\n"
      "v_mfma_f32_16x16x32_f16 v[84:87], a[80:83], v[96:99], v[84:87]\n"
      "v_fma_f32 v193, |v93|, v61, v193\n"
      "v_fma_f32 v193, |v94|, v62, v193\n"
      "v_mfma_f32_16x16x32_f16 v[88:91], a[96:99], v[96:99], v[40:43]\n"
      "ds_read_b128 v[240:243], v201 offset:4608\n"
      "v_fma_f32 v193, |v95|, v63, v193\n"
      "v_mfma_f32_16x16x32_f16 v[88:91], a[100:103], v[100:103], v[88:91]\n"
      "ds_read_b128 v[244:247], v201 offset:4672\n"
      "v_fma_f32 v193, |v80|, v64, v193\n"
      "v_mfma_f32_16x16x32_f16 v[88:91], a[104:107], v[104:107], v[88:91]\n"
      "ds_read_b128 v[248:251], v201 offset:4736\n"
      "v_fma_f32 v193, |v81|, v65, v193\n"
      "v_mfma_f32_16x16x32_f16 v[88:91], a[108:111], v[108:111], v[88:91]\n"
      "s_waitcnt lgkmcnt(3)\n"
      "v_pk_mul_f16 v124, v140, v172\n"
      "v_fma_f32 v193, |v82|, v66, v193\n"
      "v_mfma_f32_16x16x32_f16 v[92:95], a[124:127], v[108:111], v[44:47]\n"
      "ds_read_b128 v[252:255], v201 offset:4800\n"
      "v_fma_f32 v193, |v83|, v67, v193\n"
      "v_mfma_f32_16x16x32_f16 v[92:95], a[120:123], v[104:107], v[92:95]\n"
      "v_pk_mul_f16 v125, v141, v173\n"
      "v_pk_mul_f16 v126, v142, v174\n"
      "v_mfma_f32_16x16x32_f16 v[92:95], a[116:119], v[100:103], v[92:95]\n"
      "ds_read_b128 v[176:179], v200 offset:16512\n"
      "v_pk_mul_f16 v127, v143, v175\n"
      "v_mfma_f32_16x16x32_f16 v[92:95], a[112:115], v[96:99], v[92:95]\n"
      "v_fma_f32 v193, |v84|, v68, v193\n"
      "v_fma_f32 v193, |v85|, v69, v193\n"
      "v_mfma_f32_16x16x32_f16 v[80:83], a[0:3], v[112:115], v[16:19]\n"
      "v_fma_f32 v193, |v86|, v70, v193\n"
      "v_fma_f32 v193, |v87|, v71, v193\n"
      "v_mfma_f32_16x16x32_f16 v[80:83], a[4:7], v[116:119], v[80:83]\n"
      "v_fma_f32 v193, |v88|, v72, v193\n"
      "v_fma_f32 v193, |v89|, v73, v193\n"
      "v_mfma_f32_16x16x32_f16 v[80:83], a[8:11], v[120:123], v[80:83]\n"
      "ds_read_b128 a[152:155], v214 offset:0\n"
      "v_pk_mul_f16 v96, v144, v160\n"
      "v_mfma_f32_16x16x32_f16 v[80:83], a[12:15], v[124:127], v[80:83]\n"
      "v_fma_f32 v193, |v90|, v74, v193\n"
      "v_fma_f32 v193, |v91|, v75, v193\n"
      "v_mfma_f32_16x16x32_f16 v[84:87], a[28:31], v[124:127], v[20:23]\n"
      "v_pk_mul_f16 v97, v145, v161\n"
      "v_pk_mul_f16 v98, v146, v162\n"
      "v_mfma_f32_16x16x32_f16 v[84:87], a[24:27], v[120:123], v[84:87]\n"
      "v_pk_mul_f16 v99, v147, v163\n"
      "v_fma_f32 v193, |v92|, v76, v193\n"
      "v_mfma_f32_16x16x32_f16 v[84:87], a[20:23], v[116:119], v[84:87]\n"
      "v_fma_f32 v193, |v93|, v77, v193\n"
      "v_fma_f32 v193, |v94|, v78, v193\n"
      "v_mfma_f32_16x16x32_f16 v[84:87], a[16:19], v[112:115], v[84:87]\n"
      "s_waitcnt lgkmcnt(1)\n"
      "v_fma_f32 v193, |v95|, v79, v193\n"
      "v_pk_mul_f16 v228, v128, v176\n"
      "v_mfma_f32_16x16x32_f16 v[88:91], a[32:35], v[112:115], v[24:27]\n"
      "v_pk_mul_f16 v229, v129, v177\n"
      "v_pk_mul_f16 v230, v130, v178\n"
      "v_mfma_f32_16x16x32_f16 v[88:91], a[36:39], v[116:119], v[88:91]\n"
      "v_pk_mul_f16 v231, v131, v179\n"
      "v_mul_f32 v195, |v80|, v48\n"
      "v_mfma_f32_16x16x32_f16 v[88:91], a[40:43], v[120:123], v[88:91]\n"
      "42:\n"
      "s_cbranch_execz 44b\n"
      "v_mfma_f32_16x16x32_f16 v[88:91], a[44:47], v[124:127], v[88:91]\n"
      "v_fma_f32 v195, |v81|, v49, v195\n"
      "v_pk_mul_f16 v100, v148, v164\n"
      "v_mfma_f32_16x16x32_f16 v[92:95], a[60:63], v[124:127], v[28:31]\n"
      "ds_read_b128 a[168:171], v220 offset:0\n"
      "v_fma_f32 v195, |v82|, v50, v195\n"
      "v_mfma_f32_16x16x32_f16 v[92:95], a[56:59], v[120:123], v[92:95]\n"
      "v_fma_f32 v195, |v83|, v51, v195\n"
      "v_pk_mul_f16 v101, v149, v165\n"
      "v_mfma_f32_16x16x32_f16 v[92:95], a[52:55], v[116:119], v[92:95]\n"
      "v_pk_mul_f16 v102, v150, v166\n"
      "v_pk_mul_f16 v103, v151, v167\n"
      "v_mfma_f32_16x16x32_f16 v[92:95], a[48:51], v[112:115], v[92:95]\n"
      "s_waitcnt lgkmcnt(1)\n"
      "v_fma_f32 v195, |v84|, v52, v195\n"
      "v_fma_f32 v195, |v85|, v53, v195\n"
      "v_mfma_f32_16x16x32_f16 a[128:131], v[228:231], a[152:155], 0\n"
      "v_fma_f32 v195, |v86|, v54, v195\n"
      "v_fma_f32 v195, |v87|, v55, v195\n"
      "v_mfma_f32_16x16x32_f16 v[80:83], a[64:67], v[112:115], v[32:35]\n"
      "v_pk_mul_f16 v232, v144, v176\n"
      "v_pk_mul_f16 v233, v145, v177\n"
      "v_mfma_f32_16x16x32_f16 v[80:83], a[68:71], v[116:119], v[80:83]\n"
      "v_pk_mul_f16 v234, v146, v178\n"
      "v_pk_mul_f16 v235, v147, v179\n"
      "v_mfma_f32_16x16x32_f16 v[80:83], a[72:75], v[120:123], v[80:83]\n"
      "v_fma_f32 v195, |v88|, v56, v195\n"
      "v_fma_f32 v195, |v89|, v57, v195\n"
      "v_mfma_f32_16x16x32_f16 v[80:83], a[76:79], v[124:127], v[80:83]\n"
      "ds_read_b128 a[184:187], v221 offset:0\n"
      "v_pk_mul_f16 v104, v152, v168\n"
      "v_mfma_f32_16x16x32_f16 v[84:87], a[92:95], v[124:127], v[36:39]\n"
      "v_fma_f32 v195, |v90|, v58, v195\n"
      "v_fma_f32 v195, |v91|, v59, v195\n"
      "v_mfma_f32_16x16x32_f16 v[84:87], a[88:91], v[120:123], v[84:87]\n"
      "v_pk_mul_f16 v105, v153, v169\n"
      "v_pk_mul_f16 v106, v154, v170\n"
      "v_mfma_f32_16x16x32_f16 v[84:87], a[84:87], v[116:119], v[84:87]\n"
      "v_pk_mul_f16 v107, v155, v171\n"
      "v_fma_f32 v195, |v92|, v60, v195\n"
      "v_mfma_f32_16x16x32_f16 v[84:87], a[80:83], v[112:115], v[84:87]\n"
      "s_waitcnt lgkmcnt(1)\n"
      "v_fma_f32 v195, |v93|, v61, v195\n"
      "v_fma_f32 v195, |v94|, v62, v195\n"
      "v_mfma_f32_16x16x32_f16 a[132:135], v[232:235], a[168:171], 0\n"
      "v_fma_f32 v195, |v95|, v63, v195\n"
      "v_pk_mul_f16 v236, v240, v176\n"
      "v_mfma_f32_16x16x32_f16 v[88:91], a[96:99], v[112:115], v[40:43]\n"
      "v_pk_mul_f16 v237, v241, v177\n"
      "v_pk_mul_f16 v238, v242, v178\n"
      "v_mfma_f32_16x16x32_f16 v[88:91], a[100:103], v[116:119], v[88:91]\n"
      "v_pk_mul_f16 v239, v243, v179\n"
      "v_fma_f32 v195, |v80|, v64, v195\n"
      "v_mfma_f32_16x16x32_f16 v[88:91], a[104:107], v[120:123], v[88:91]\n"
      "v_fma_f32 v195, |v81|, v65, v195\n"
      "v_pk_mul_f16 v108, v156, v172\n"
      "v_mfma_f32_16x16x32_f16 v[88:91], a[108:111], v[124:127], v[88:91]\n"
      "ds_read_b128 v[160:163], v203 offset:6336\n"
      "v_fma_f32 v195, |v82|, v66, v195\n"
      "v_mfma_f32_16x16x32_f16 v[92:95], a[124:127], v[124:127], v[44:47]\n"
      "v_fma_f32 v195, |v83|, v67, v195\n"
      "v_pk_mul_f16 v109, v157, v173\n"
      "v_mfma_f32_16x16x32_f16 v[92:95], a[120:123], v[120:123], v[92:95]\n"
      "ds_read_b128 v[176:179], v200 offset:16576\n"
      "v_pk_mul_f16 v110, v158, v174\n"
      "v_mfma_f32_16x16x32_f16 v[92:95], a[116:119], v[116:119], v[92:95]\n"
      "v_pk_mul_f16 v111, v159, v175\n"
      "v_fma_f32 v195, |v84|, v68, v195\n"
      "v_mfma_f32_16x16x32_f16 v[92:95], a[112:115], v[112:115], v[92:95]\n"
      "s_waitcnt lgkmcnt(2)\n"
      "v_fma_f32 v195, |v85|, v69, v195\n"
      "v_fma_f32 v195, |v86|, v70, v195\n"
      "v_mfma_f32_16x16x32_f16 a[136:139], v[236:239], a[184:187], 0\n"
      "v_fma_f32 v195, |v87|, v71, v195\n"
      "v_fma_f32 v195, |v88|, v72, v195\n"
      "v_mfma_f32_16x16x32_f16 v[80:83], a[0:3], v[96:99], v[16:19]\n"
      "ds_read_b128 a[156:159], v214 offset:64\n"
      "v_fma_f32 v195, |v89|, v73, v195\n"
      "v_mfma_f32_16x16x32_f16 v[80:83], a[4:7], v[100:103], v[80:83]\n"
      "v_fma_f32 v195, |v90|, v74, v195\n"
      "v_fma_f32 v195, |v91|, v75, v195\n"
      "v_mfma_f32_16x16x32_f16 v[80:83], a[8:11], v[104:107], v[80:83]\n"
      "s_waitcnt lgkmcnt(2)\n"
      "v_pk_mul_f16 v112, v128, v160\n"
      "v_pk_mul_f16 v113, v129, v161\n"
      "v_mfma_f32_16x16x32_f16 v[80:83], a[12:15], v[108:111], v[80:83]\n"
      "ds_read_b128 v[164:167], v203 offset:6400\n"
      "v_pk_mul_f16 v114, v130, v162\n"
      "v_mfma_f32_16x16x32_f16 v[84:87], a[28:31], v[108:111], v[20:23]\n"
      "v_pk_mul_f16 v115, v131, v163\n"
      "v_fma_f32 v195, |v92|, v76, v195\n"
      "v_mfma_f32_16x16x32_f16 v[84:87], a[24:27], v[104:107], v[84:87]\n"
      "v_fma_f32 v195, |v93|, v77, v195\n"
      "v_fma_f32 v195, |v94|, v78, v195\n"
      "v_mfma_f32_16x16x32_f16 v[84:87], a[20:23], v[100:103], v[84:87]\n"
      "s_waitcnt lgkmcnt(2)\n"
      "v_fma_f32 v195, |v95|, v79, v195\n"
      "v_pk_mul_f16 v228, v132, v176\n"
      "v_mfma_f32_16x16x32_f16 v[84:87], a[16:19], v[96:99], v[84:87]\n"
      "v_pk_mul_f16 v229, v133, v177\n"
      "v_pk_mul_f16 v230, v134, v178\n"
      "v_mfma_f32_16x16x32_f16 v[88:91], a[32:35], v[96:99], v[24:27]\n"
      "v_permlane32_swap_b32 v190, v195\n"
      "v_mfma_f32_16x16x32_f16 v[88:91], a[36:39], v[100:103], v[88:91]\n"
      "v_pk_mul_f16 v231, v135, v179\n"
      "v_mul_f32 v196, |v80|, v48\n"
      "v_mfma_f32_16x16x32_f16 v[88:91], a[40:43], v[104:107], v[88:91]\n"
      "ds_read_b128 a[172:175], v220 offset:64\n"
      "v_fma_f32 v196, |v81|, v49, v196\n"
      "v_mfma_f32_16x16x32_f16 v[88:91], a[44:47], v[108:111], v[88:91]\n"
      "s_waitcnt lgkmcnt(1)\n"
      "v_pk_mul_f16 v116, v132, v164\n"
      "v_fma_f32 v196, |v82|, v50, v196\n"
      "v_mfma_f32_16x16x32_f16 v[92:95], a[60:63], v[108:111], v[28:31]\n"
      "v_fma_f32 v196, |v83|, v51, v196\n"
      "v_pk_mul_f16 v117, v133, v165\n"
      "v_mfma_f32_16x16x32_f16 v[92:95], a[56:59], v[104:107], v[92:95]\n"
      "ds_read_b128 v[168:171], v203 offset:6464\n"
      "v_pk_mul_f16 v118, v134, v166\n"
      "v_mfma_f32_16x16x32_f16 v[92:95], a[52:55], v[100:103], v[92:95]\n"
      "v_pk_mul_f16 v119, v135, v167\n"
      "v_fma_f32 v196, |v84|, v52, v196\n"
      "v_mfma_f32_16x16x32_f16 v[92:95], a[48:51], v[96:99], v[92:95]\n"
      "v_fma_f32 v196, |v85|, v53, v196\n"
      "v_fma_f32 v196, |v86|, v54, v196\n"
      "v_mfma_f32_16x16x32_f16 a[128:131], v[228:231], a[156:159], a[128:131]\n"
      "v_fma_f32 v196, |v87|, v55, v196\n"
      "v_pk_mul_f16 v232, v148, v176\n"
      "v_mfma_f32_16x16x32_f16 v[80:83], a[64:67], v[96:99], v[32:35]\n"
      "v_pk_mul_f16 v233, v149, v177\n"
      "v_pk_mul_f16 v234, v150, v178\n"
      "v_mfma_f32_16x16x32_f16 v[80:83], a[68:71], v[100:103], v[80:83]\n"
      "v_pk_mul_f16 v235, v151, v179\n"
      "v_fma_f32 v196, |v88|, v56, v196\n"
      "v_mfma_f32_16x16x32_f16 v[80:83], a[72:75], v[104:107], v[80:83]\n"
      "ds_read_b128 a[188:191], v221 offset:64\n"
      "v_fma_f32 v196, |v89|, v57, v196\n"
      "v_mfma_f32_16x16x32_f16 v[80:83], a[76:79], v[108:111], v[80:83]\n"
      "v_fma_f32 v196, |v90|, v58, v196\n"
      "v_fma_f32 v196, |v91|, v59, v196\n"
      "v_mfma_f32_16x16x32_f16 v[84:87], a[92:95], v[108:111], v[36:39]\n"
      "s_waitcnt lgkmcnt(1)\n"
      "v_pk_mul_f16 v120, v136, v168\n"
      "v_pk_mul_f16 v121, v137, v169\n"
      "v_mfma_f32_16x16x32_f16 v[84:87], a[88:91], v[104:107], v[84:87]\n"
      "s_waitcnt vmcnt(2)\n"
      "ds_read_b128 a[216:219], v12 offset:0\n"
      "v_pk_mul_f16 v122, v138, v170\n"
      "v_mfma_f32_16x16x32_f16 v[84:87], a[84:87], v[100:103], v[84:87]\n"
      "ds_read_b128 a[220:223], v12 offset:64\n"
      "v_pk_mul_f16 v123, v139, v171\n"
      "v_mfma_f32_16x16x32_f16 v[84:87], a[80:83], v[96:99], v[84:87]\n"
      "v_fma_f32 v196, |v92|, v60, v196\n"
      "v_fma_f32 v196, |v93|, v61, v196\n"
      "v_mfma_f32_16x16x32_f16 a[132:135], v[232:235], a[172:175], a[132:135]\n"
      "ds_read_b128 a[224:227], v12 offset:128\n"
      "v_fma_f32 v196, |v94|, v62, v196\n"
      "v_mfma_f32_16x16x32_f16 v[88:91], a[96:99], v[96:99], v[40:43]\n"
      "v_fma_f32 v196, |v95|, v63, v196\n"
      "v_pk_mul_f16 v236, v244, v176\n"
      "v_mfma_f32_16x16x32_f16 v[88:91], a[100:103], v[100:103], v[88:91]\n"
      "ds_read_b128 a[228:231], v12 offset:192\n"
      "v_pk_mul_f16 v237, v245, v177\n"
      "v_mfma_f32_16x16x32_f16 v[88:91], a[104:107], v[104:107], v[88:91]\n"
      "v_pk_mul_f16 v238, v246, v178\n"
      "v_pk_mul_f16 v239, v247, v179\n"
      "v_mfma_f32_16x16x32_f16 v[88:91], a[108:111], v[108:111], v[88:91]\n"
      "v_fma_f32 v196, |v80|, v64, v196\n"
      "v_fma_f32 v196, |v81|, v65, v196\n"
      "v_mfma_f32_16x16x32_f16 v[92:95], a[124:127], v[108:111], v[44:47]\n"
      "v_fma_f32 v196, |v82|, v66, v196\n"
      "v_fma_f32 v196, |v83|, v67, v196\n"
      "v_mfma_f32_16x16x32_f16 v[92:95], a[120:123], v[104:107], v[92:95]\n"
      "ds_read_b128 a[232:235], v13 offset:0\n"
      "v_fma_f32 v196, |v84|, v68, v196\n"
      "v_mfma_f32_16x16x32_f16 v[92:95], a[116:119], v[100:103], v[92:95]\n"
      "ds_read_b128 a[236:239], v13 offset:64\n"
      "v_fma_f32 v196, |v85|, v69, v196\n"
      "v_mfma_f32_16x16x32_f16 v[92:95], a[112:115], v[96:99], v[92:95]\n"
      "s_waitcnt lgkmcnt(6)\n"
      "ds_read_b128 a[240:243], v13 offset:128\n"
      "v_fma_f32 v196, |v86|, v70, v196\n"
      "v_mfma_f32_16x16x32_f16 a[136:139], v[236:239], a[188:191], a[136:139]\n"
      "s_waitcnt lgkmcnt(6)\n"
      "ds_read_b128 a[244:247], v13 offset:192\n"
      "v_fma_f32 v196, |v87|, v71, v196\n"
      "v_mfma_f32_16x16x32_f16 v[80:83], a[0:3], a[216:219], v[16:19]\n"
      "s_waitcnt lgkmcnt(6)\n"
      "v_fma_f32 v196, |v88|, v72, v196\n"
      "v_fma_f32 v196, |v89|, v73, v196\n"
      "v_mfma_f32_16x16x32_f16 v[80:83], a[4:7], a[220:223], v[80:83]\n"
      "s_waitcnt lgkmcnt(5)\n"
      "ds_read_b128 v[172:175], v203 offset:6528\n"
      "v_fma_f32 v196, |v90|, v74, v196\n"
      "v_mfma_f32_16x16x32_f16 v[80:83], a[8:11], a[224:227], v[80:83]\n"
      "s_waitcnt lgkmcnt(5)\n"
      "v_fma_f32 v196, |v91|, v75, v196\n"
      "v_fma_f32 v196, |v92|, v76, v196\n"
      "v_mfma_f32_16x16x32_f16 v[80:83], a[12:15], a[228:231], v[80:83]\n"
      "v_fma_f32 v196, |v93|, v77, v196\n"
      "v_fma_f32 v196, |v94|, v78, v196\n"
      "v_mfma_f32_16x16x32_f16 v[84:87], a[76:79], a[228:231], v[32:35]\n"
      "ds_read_b128 v[176:179], v200 offset:16640\n"
      "v_fma_f32 v196, |v95|, v79, v196\n"
      "v_mfma_f32_16x16x32_f16 v[84:87], a[72:75], a[224:227], v[84:87]\n"
      "ds_read_b128 a[160:163], v214 offset:128\n"
      "v_pk_mul_f16 v96, v144, v160\n"
      "v_mfma_f32_16x16x32_f16 v[84:87], a[68:71], a[220:223], v[84:87]\n"
      "v_permlane32_swap_b32 v191, v196\n"
      "v_mfma_f32_16x16x32_f16 v[84:87], a[64:67], a[216:219], v[84:87]\n"
      "s_waitcnt lgkmcnt(6)\n"
      "v_mul_f32 v199, |v80|, v48\n"
      "v_fma_f32 v199, |v81|, v49, v199\n"
      "v_mfma_f32_16x16x32_f16 v[88:91], a[0:3], a[232:235], v[16:19]\n"
      "s_waitcnt lgkmcnt(5)\n"
      "v_fma_f32 v199, |v82|, v50, v199\n"
      "v_fma_f32 v199, |v83|, v51, v199\n"
      "v_mfma_f32_16x16x32_f16 v[88:91], a[4:7], a[236:239], v[88:91]\n"
      "s_waitcnt lgkmcnt(2)\n"
      "v_pk_mul_f16 v124, v140, v172\n"
      "v_pk_mul_f16 v125, v141, v173\n"
      "v_mfma_f32_16x16x32_f16 v[88:91], a[8:11], a[240:243], v[88:91]\n"
      "v_pk_mul_f16 v126, v142, v174\n"
      "v_pk_mul_f16 v127, v143, v175\n"
      "v_mfma_f32_16x16x32_f16 v[88:91], a[12:15], a[244:247], v[88:91]\n"
      "v_fma_f32 v199, |v84|, v64, v199\n"
      "v_fma_f32 v199, |v85|, v65, v199\n"
      "v_mfma_f32_16x16x32_f16 v[92:95], a[76:79], a[244:247], v[32:35]\n"
      "v_fma_f32 v199, |v86|, v66, v199\n"
      "v_fma_f32 v199, |v87|, v67, v199\n"
      "v_mfma_f32_16x16x32_f16 v[92:95], a[72:75], a[240:243], v[92:95]\n"
      "s_waitcnt vmcnt(0)\n"
      "ds_write_b32 v216, v199 offset:1440\n"
      "v_mfma_f32_16x16x32_f16 v[92:95], a[68:71], a[236:239], v[92:95]\n"
      "v_mul_f32 v225, |v88|, v48\n"
      "v_fma_f32 v225, |v89|, v49, v225\n"
      "v_mfma_f32_16x16x32_f16 v[92:95], a[64:67], a[232:235], v[92:95]\n"
      "v_fma_f32 v225, |v90|, v50, v225\n"
      "v_fma_f32 v225, |v91|, v51, v225\n"
      "v_mfma_f32_16x16x32_f16 v[80:83], a[0:3], v[112:115], v[16:19]\n"
      "v_pk_mul_f16 v97, v145, v161\n"
      "v_pk_mul_f16 v98, v146, v162\n"
      "v_mfma_f32_16x16x32_f16 v[80:83], a[4:7], v[116:119], v[80:83]\n"
      "s_waitcnt lgkmcnt(2)\n"
      "v_pk_mul_f16 v99, v147, v163\n"
      "v_pk_mul_f16 v228, v136, v176\n"
      "v_mfma_f32_16x16x32_f16 v[80:83], a[8:11], v[120:123], v[80:83]\n"
      "v_fma_f32 v225, |v92|, v64, v225\n"
      "v_fma_f32 v225, |v93|, v65, v225\n"
      "v_mfma_f32_16x16x32_f16 v[80:83], a[12:15], v[124:127], v[80:83]\n"
      "v_fma_f32 v225, |v94|, v66, v225\n"
      "v_fma_f32 v225, |v95|, v67, v225\n"
      "v_mfma_f32_16x16x32_f16 v[84:87], a[28:31], v[124:127], v[20:23]\n"
      "ds_write_b32 v217, v225 offset:1440\n"
      "v_mfma_f32_16x16x32_f16 v[84:87], a[24:27], v[120:123], v[84:87]\n"
      "v_pk_mul_f16 v229, v137, v177\n"
      "v_pk_mul_f16 v230, v138, v178\n"
      "v_mfma_f32_16x16x32_f16 v[84:87], a[20:23], v[116:119], v[84:87]\n"
      "v_pk_mul_f16 v231, v139, v179\n"
      "v_mul_f32 v197, |v80|, v48\n"
      "v_mfma_f32_16x16x32_f16 v[84:87], a[16:19], v[112:115], v[84:87]\n"
      "v_fma_f32 v197, |v81|, v49, v197\n"
      "v_pk_mul_f16 v100, v148, v164\n"
      "v_mfma_f32_16x16x32_f16 v[88:91], a[32:35], v[112:115], v[24:27]\n"
      "ds_read_b128 a[176:179], v220 offset:128\n"
      "v_fma_f32 v197, |v82|, v50, v197\n"
      "v_mfma_f32_16x16x32_f16 v[88:91], a[36:39], v[116:119], v[88:91]\n"
      "v_fma_f32 v197, |v83|, v51, v197\n"
      "v_pk_mul_f16 v101, v149, v165\n"
      "v_mfma_f32_16x16x32_f16 v[88:91], a[40:43], v[120:123], v[88:91]\n"
      "v_pk_mul_f16 v102, v150, v166\n"
      "v_pk_mul_f16 v103, v151, v167\n"
      "v_mfma_f32_16x16x32_f16 v[88:91], a[44:47], v[124:127], v[88:91]\n"
      "v_fma_f32 v197, |v84|, v52, v197\n"
      "v_fma_f32 v197, |v85|, v53, v197\n"
      "v_mfma_f32_16x16x32_f16 v[92:95], a[60:63], v[124:127], v[28:31]\n"
      "v_fma_f32 v197, |v86|, v54, v197\n"
      "v_fma_f32 v197, |v87|, v55, v197\n"
      "v_mfma_f32_16x16x32_f16 v[92:95], a[56:59], v[120:123], v[92:95]\n"
      "v_pk_mul_f16 v232, v152, v176\n"
      "v_pk_mul_f16 v233, v153, v177\n"
      "v_mfma_f32_16x16x32_f16 v[92:95], a[52:55], v[116:119], v[92:95]\n"
      "v_pk_mul_f16 v234, v154, v178\n"
      "v_pk_mul_f16 v235, v155, v179\n"
      "v_mfma_f32_16x16x32_f16 v[92:95], a[48:51], v[112:115], v[92:95]\n"
      "s_waitcnt lgkmcnt(3)\n"
      "v_fma_f32 v197, |v88|, v56, v197\n"
      "v_fma_f32 v197, |v89|, v57, v197\n"
      "v_mfma_f32_16x16x32_f16 a[128:131], v[228:231], a[160:163], a[128:131]\n"
      "ds_read_b128 a[192:195], v221 offset:128\n"
      "v_pk_mul_f16 v104, v152, v168\n"
      "v_mfma_f32_16x16x32_f16 v[80:83], a[64:67], v[112:115], v[32:35]\n"
      "v_fma_f32 v197, |v90|, v58, v197\n"
      "v_fma_f32 v197, |v91|, v59, v197\n"
      "v_mfma_f32_16x16x32_f16 v[80:83], a[68:71], v[116:119], v[80:83]\n"
      "v_pk_mul_f16 v105, v153, v169\n"
      "v_pk_mul_f16 v106, v154, v170\n"
      "v_mfma_f32_16x16x32_f16 v[80:83], a[72:75], v[120:123], v[80:83]\n"
      "v_pk_mul_f16 v107, v155, v171\n"
      "v_fma_f32 v197, |v92|, v60, v197\n"
      "v_mfma_f32_16x16x32_f16 v[80:83], a[76:79], v[124:127], v[80:83]\n"
      "v_fma_f32 v197, |v93|, v61, v197\n"
      "v_fma_f32 v197, |v94|, v62, v197\n"
      "v_mfma_f32_16x16x32_f16 v[84:87], a[92:95], v[124:127], v[36:39]\n"
      "v_fma_f32 v197, |v95|, v63, v197\n"
      "v_pk_mul_f16 v236, v248, v176\n"
      "v_mfma_f32_16x16x32_f16 v[84:87], a[88:91], v[120:123], v[84:87]\n"
      "v_pk_mul_f16 v237, v249, v177\n"
      "v_pk_mul_f16 v238, v250, v178\n"
      "v_mfma_f32_16x16x32_f16 v[84:87], a[84:87], v[116:119], v[84:87]\n"
      "v_pk_mul_f16 v239, v251, v179\n"
      "v_fma_f32 v197, |v80|, v64, v197\n"
      "v_mfma_f32_16x16x32_f16 v[84:87], a[80:83], v[112:115], v[84:87]\n"
      "s_waitcnt lgkmcnt(1)\n"
      "v_fma_f32 v197, |v81|, v65, v197\n"
      "v_pk_mul_f16 v108, v156, v172\n"
      "v_mfma_f32_16x16x32_f16 a[132:135], v[232:235], a[176:179], a[132:135]\n"
      "ds_read_b128 v[160:163], v202 offset:4608\n"
      "v_fma_f32 v197, |v82|, v66, v197\n"
      "v_mfma_f32_16x16x32_f16 v[88:91], a[96:99], v[112:115], v[40:43]\n"
      "v_fma_f32 v197, |v83|, v67, v197\n"
      "v_pk_mul_f16 v109, v157, v173\n"
      "v_mfma_f32_16x16x32_f16 v[88:91], a[100:103], v[116:119], v[88:91]\n"
      "ds_read_b128 v[176:179], v200 offset:16704\n"
      "v_pk_mul_f16 v110, v158, v174\n"
      "v_mfma_f32_16x16x32_f16 v[88:91], a[104:107], v[120:123], v[88:91]\n"
      "v_add_f32 v190, v190, v195\n"
      "v_pk_mul_f16 v111, v159, v175\n"
      "v_mfma_f32_16x16x32_f16 v[88:91], a[108:111], v[124:127], v[88:91]\n"
      "v_fma_f32 v197, |v84|, v68, v197\n"
      "v_fma_f32 v197, |v85|, v69, v197\n"
      "v_mfma_f32_16x16x32_f16 v[92:95], a[124:127], v[124:127], v[44:47]\n"
      "v_fma_f32 v197, |v86|, v70, v197\n"
      "v_fma_f32 v197, |v87|, v71, v197\n"
      "v_mfma_f32_16x16x32_f16 v[92:95], a[120:123], v[120:123], v[92:95]\n"
      "ds_read_b128 a[164:167], v214 offset:192\n"
      "v_add_f32 v191, v191, v196\n"
      "v_mfma_f32_16x16x32_f16 v[92:95], a[116:119], v[116:119], v[92:95]\n"
      "v_fma_f32 v197, |v88|, v72, v197\n"
      "v_fma_f32 v197, |v89|, v73, v197\n"
      "v_mfma_f32_16x16x32_f16 v[92:95], a[112:115], v[112:115], v[92:95]\n"
      "s_waitcnt lgkmcnt(2)\n"
      "v_pk_mul_f16 v112, v240, v160\n"
      "v_fma_f32 v197, |v90|, v74, v197\n"
      "v_mfma_f32_16x16x32_f16 a[136:139], v[236:239], a[192:195], a[136:139]\n"
      "v_fma_f32 v197, |v91|, v75, v197\n"
      "v_pk_mul_f16 v113, v241, v161\n"
      "v_mfma_f32_16x16x32_f16 v[80:83], a[0:3], v[96:99], v[16:19]\n"
      "ds_read_b128 v[164:167], v202 offset:4672\n"
      "v_pk_mul_f16 v114, v242, v162\n"
      "v_mfma_f32_16x16x32_f16 v[80:83], a[4:7], v[100:103], v[80:83]\n"
      "v_pk_mul_f16 v115, v243, v163\n"
      "v_fma_f32 v197, |v92|, v76, v197\n"
      "v_mfma_f32_16x16x32_f16 v[80:83], a[8:11], v[104:107], v[80:83]\n"
      "v_fma_f32 v197, |v93|, v77, v197\n"
      "v_fma_f32 v197, |v94|, v78, v197\n"
      "v_mfma_f32_16x16x32_f16 v[80:83], a[12:15], v[108:111], v[80:83]\n"
      "s_waitcnt lgkmcnt(2)\n"
      "v_fma_f32 v197, |v95|, v79, v197\n"
      "v_pk_mul_f16 v228, v140, v176\n"
      "v_mfma_f32_16x16x32_f16 v[84:87], a[28:31], v[108:111], v[20:23]\n"
      "v_pk_mul_f16 v229, v141, v177\n"
      "v_pk_mul_f16 v230, v142, v178\n"
      "v_mfma_f32_16x16x32_f16 v[84:87], a[24:27], v[104:107], v[84:87]\n"
      "v_permlane32_swap_b32 v192, v197\n"
      "v_mfma_f32_16x16x32_f16 v[84:87], a[20:23], v[100:103], v[84:87]\n"
      "v_pk_mul_f16 v231, v143, v179\n"
      "v_mul_f32 v198, |v80|, v48\n"
      "v_mfma_f32_16x16x32_f16 v[84:87], a[16:19], v[96:99], v[84:87]\n"
      "ds_read_b128 a[180:183], v220 offset:192\n"
      "v_fma_f32 v198, |v81|, v49, v198\n"
      "v_mfma_f32_16x16x32_f16 v[88:91], a[32:35], v[96:99], v[24:27]\n"
      "s_waitcnt lgkmcnt(1)\n"
      "v_pk_mul_f16 v116, v244, v164\n"
      "v_fma_f32 v198, |v82|, v50, v198\n"
      "v_mfma_f32_16x16x32_f16 v[88:91], a[36:39], v[100:103], v[88:91]\n"
      "v_fma_f32 v198, |v83|, v51, v198\n"
      "v_pk_mul_f16 v117, v245, v165\n"
      "v_mfma_f32_16x16x32_f16 v[88:91], a[40:43], v[104:107], v[88:91]\n"
      "ds_read_b128 v[168:171], v202 offset:4736\n"
      "v_pk_mul_f16 v118, v246, v166\n"
      "v_mfma_f32_16x16x32_f16 v[88:91], a[44:47], v[108:111], v[88:91]\n"
      "v_pk_mul_f16 v119, v247, v167\n"
      "v_fma_f32 v198, |v84|, v52, v198\n"
      "v_mfma_f32_16x16x32_f16 v[92:95], a[60:63], v[108:111], v[28:31]\n"
      "v_fma_f32 v198, |v85|, v53, v198\n"
      "v_fma_f32 v198, |v86|, v54, v198\n"
      "v_mfma_f32_16x16x32_f16 v[92:95], a[56:59], v[104:107], v[92:95]\n"
      "v_fma_f32 v198, |v87|, v55, v198\n"
      "v_pk_mul_f16 v232, v156, v176\n"
      "v_mfma_f32_16x16x32_f16 v[92:95], a[52:55], v[100:103], v[92:95]\n"
      "v_pk_mul_f16 v233, v157, v177\n"
      "v_pk_mul_f16 v234, v158, v178\n"
      "v_mfma_f32_16x16x32_f16 v[92:95], a[48:51], v[96:99], v[92:95]\n"
      "v_pk_mul_f16 v235, v159, v179\n"
      "v_fma_f32 v198, |v88|, v56, v198\n"
      "v_mfma_f32_16x16x32_f16 a[128:131], v[228:231], a[164:167], a[128:131]\n"
      "ds_read_b128 a[196:199], v221 offset:192\n"
      "v_fma_f32 v198, |v89|, v57, v198\n"
      "v_mfma_f32_16x16x32_f16 v[80:83], a[64:67], v[96:99], v[32:35]\n"
      "v_fma_f32 v198, |v90|, v58, v198\n"
      "v_fma_f32 v198, |v91|, v59, v198\n"
      "v_mfma_f32_16x16x32_f16 v[80:83], a[68:71], v[100:103], v[80:83]\n"
      "s_waitcnt lgkmcnt(1)\n"
      "v_pk_mul_f16 v120, v248, v168\n"
      "v_pk_mul_f16 v121, v249, v169\n"
      "v_mfma_f32_16x16x32_f16 v[80:83], a[72:75], v[104:107], v[80:83]\n"
      "ds_write_b128 v212, a[128:131] offset:0\n"
      "v_mfma_f32_16x16x32_f16 v[80:83], a[76:79], v[108:111], v[80:83]\n"
      "v_pk_mul_f16 v122, v250, v170\n"
      "v_pk_mul_f16 v123, v251, v171\n"
      "v_mfma_f32_16x16x32_f16 v[84:87], a[92:95], v[108:111], v[36:39]\n"
      "v_fma_f32 v198, |v92|, v60, v198\n"
      "v_fma_f32 v198, |v93|, v61, v198\n"
      "v_mfma_f32_16x16x32_f16 v[84:87], a[88:91], v[104:107], v[84:87]\n"
      "v_fma_f32 v198, |v94|, v62, v198\n"
      "v_fma_f32 v198, |v95|, v63, v198\n"
      "v_mfma_f32_16x16x32_f16 v[84:87], a[84:87], v[100:103], v[84:87]\n"
      "ds_read_b128 v[172:175], v202 offset:4800\n"
      "v_pk_mul_f16 v236, v252, v176\n"
      "v_mfma_f32_16x16x32_f16 v[84:87], a[80:83], v[96:99], v[84:87]\n"
      "v_pk_mul_f16 v237, v253, v177\n"
      "v_pk_mul_f16 v238, v254, v178\n"
      "v_mfma_f32_16x16x32_f16 a[132:135], v[232:235], a[180:183], a[132:135]\n"
      "v_pk_mul_f16 v239, v255, v179\n"
      "v_fma_f32 v198, |v80|, v64, v198\n"
      "v_mfma_f32_16x16x32_f16 v[88:91], a[96:99], v[96:99], v[40:43]\n"
      "v_fma_f32 v198, |v81|, v65, v198\n"
      "v_fma_f32 v198, |v82|, v66, v198\n"
      "v_mfma_f32_16x16x32_f16 v[88:91], a[100:103], v[100:103], v[88:91]\n"
      "v_fma_f32 v198, |v83|, v67, v198\n"
      "v_fma_f32 v198, |v84|, v68, v198\n"
      "v_mfma_f32_16x16x32_f16 v[88:91], a[104:107], v[104:107], v[88:91]\n"
      "ds_write_b128 v212, a[132:135] offset:1280\n"
      "v_mfma_f32_16x16x32_f16 v[88:91], a[108:111], v[108:111], v[88:91]\n"
      "v_fma_f32 v198, |v85|, v69, v198\n"
      "v_fma_f32 v198, |v86|, v70, v198\n"
      "v_mfma_f32_16x16x32_f16 v[92:95], a[124:127], v[108:111], v[44:47]\n"
      "v_fma_f32 v198, |v87|, v71, v198\n"
      "v_add_f32 v192, v192, v197\n"
      "v_mfma_f32_16x16x32_f16 v[92:95], a[120:123], v[104:107], v[92:95]\n"
      "s_waitcnt lgkmcnt(1)\n"
      "v_pk_mul_f16 v124, v252, v172\n"
      "v_pk_mul_f16 v125, v253, v173\n"
      "v_mfma_f32_16x16x32_f16 v[92:95], a[116:119], v[100:103], v[92:95]\n"
      "v_pk_mul_f16 v126, v254, v174\n"
      "v_pk_mul_f16 v127, v255, v175\n"
      "v_mfma_f32_16x16x32_f16 v[92:95], a[112:115], v[96:99], v[92:95]\n"
      "v_permlane16_swap_b32 v190, v192\n"
      "v_mfma_f32_16x16x32_f16 a[136:139], v[236:239], a[196:199], a[136:139]\n"
      "ds_read_b128 v[96:99], v10 offset:0\n"
      "v_fma_f32 v198, |v88|, v72, v198\n"
      "v_mfma_f32_16x16x32_f16 v[80:83], a[0:3], v[112:115], v[16:19]\n"
      "ds_read_b128 v[160:163], v11 offset:0\n"
      "v_fma_f32 v198, |v89|, v73, v198\n"
      "v_mfma_f32_16x16x32_f16 v[80:83], a[4:7], v[116:119], v[80:83]\n"
      "v_fma_f32 v198, |v90|, v74, v198\n"
      "v_fma_f32 v198, |v91|, v75, v198\n"
      "v_mfma_f32_16x16x32_f16 v[80:83], a[8:11], v[120:123], v[80:83]\n"
      "ds_write_b128 v212, a[136:139] offset:2560\n"
      "v_mfma_f32_16x16x32_f16 v[80:83], a[12:15], v[124:127], v[80:83]\n"
      "v_fma_f32 v198, |v92|, v76, v198\n"
      "v_fma_f32 v198, |v93|, v77, v198\n"
      "v_mfma_f32_16x16x32_f16 v[84:87], a[28:31], v[124:127], v[20:23]\n"
      "v_fma_f32 v198, |v94|, v78, v198\n"
      "v_fma_f32 v198, |v95|, v79, v198\n"
      "v_mfma_f32_16x16x32_f16 v[84:87], a[24:27], v[120:123], v[84:87]\n"
      "v_add_f32 v190, v190, v192\n"
      "v_mfma_f32_16x16x32_f16 v[84:87], a[20:23], v[116:119], v[84:87]\n"
      "v_permlane32_swap_b32 v193, v198\n"
      "v_mfma_f32_16x16x32_f16 v[84:87], a[16:19], v[112:115], v[84:87]\n"
      "ds_read_b128 v[100:103], v10 offset:64\n"
      "v_mul_f32 v195, |v80|, v48\n"
      "v_mfma_f32_16x16x32_f16 v[88:91], a[32:35], v[112:115], v[24:27]\n"
      "s_waitcnt lgkmcnt(2)\n"
      "ds_read_b128 v[164:167], v11 offset:64\n"
      "v_pk_mul_f16 v96, v96, v160\n"
      "v_mfma_f32_16x16x32_f16 v[88:91], a[36:39], v[116:119], v[88:91]\n"
      "v_fma_f32 v195, |v81|, v49, v195\n"
      "v_pk_mul_f16 v97, v97, v161\n"
      "v_mfma_f32_16x16x32_f16 v[88:91], a[40:43], v[120:123], v[88:91]\n"
      "v_fma_f32 v195, |v82|, v50, v195\n"
      "v_fma_f32 v195, |v83|, v51, v195\n"
      "v_mfma_f32_16x16x32_f16 v[88:91], a[44:47], v[124:127], v[88:91]\n"
      "v_pk_mul_f16 v98, v98, v162\n"
      "v_pk_mul_f16 v99, v99, v163\n"
      "v_mfma_f32_16x16x32_f16 v[92:95], a[60:63], v[124:127], v[28:31]\n"
      "ds_read_b128 v[104:107], v10 offset:128\n"
      "v_fma_f32 v195, |v84|, v52, v195\n"
      "v_mfma_f32_16x16x32_f16 v[92:95], a[56:59], v[120:123], v[92:95]\n"
      "ds_read_b128 v[168:171], v11 offset:128\n"
      "v_fma_f32 v195, |v85|, v53, v195\n"
      "v_mfma_f32_16x16x32_f16 v[92:95], a[52:55], v[116:119], v[92:95]\n"
      "v_fma_f32 v195, |v86|, v54, v195\n"
      "v_fma_f32 v195, |v87|, v55, v195\n"
      "v_mfma_f32_16x16x32_f16 v[92:95], a[48:51], v[112:115], v[92:95]\n"
      "v_fma_f32 v195, |v88|, v56, v195\n"
      "v_fma_f32 v195, |v89|, v57, v195\n"
      "v_mfma_f32_16x16x32_f16 v[80:83], a[64:67], v[112:115], v[32:35]\n"
      "s_waitcnt lgkmcnt(2)\n"
      "v_pk_mul_f16 v100, v100, v164\n"
      "v_pk_mul_f16 v101, v101, v165\n"
      "v_mfma_f32_16x16x32_f16 v[80:83], a[68:71], v[116:119], v[80:83]\n"
      "v_pk_mul_f16 v102, v102, v166\n"
      "v_pk_mul_f16 v103, v103, v167\n"
      "v_mfma_f32_16x16x32_f16 v[80:83], a[72:75], v[120:123], v[80:83]\n"
      "ds_read_b128 v[108:111], v10 offset:192\n"
      "v_fma_f32 v195, |v90|, v58, v195\n"
      "v_mfma_f32_16x16x32_f16 v[80:83], a[76:79], v[124:127], v[80:83]\n"
      "ds_read_b128 v[172:175], v11 offset:192\n"
      "v_fma_f32 v195, |v91|, v59, v195\n"
      "v_mfma_f32_16x16x32_f16 v[84:87], a[92:95], v[124:127], v[36:39]\n"
      "v_fma_f32 v195, |v92|, v60, v195\n"
      "v_fma_f32 v195, |v93|, v61, v195\n"
      "v_mfma_f32_16x16x32_f16 v[84:87], a[88:91], v[120:123], v[84:87]\n"
      "s_waitcnt lgkmcnt(2)\n"
      "v_pk_mul_f16 v104, v104, v168\n"
      "v_pk_mul_f16 v105, v105, v169\n"
      "v_mfma_f32_16x16x32_f16 v[84:87], a[84:87], v[116:119], v[84:87]\n"
      "v_pk_mul_f16 v106, v106, v170\n"
      "v_fma_f32 v195, |v94|, v62, v195\n"
      "v_mfma_f32_16x16x32_f16 v[84:87], a[80:83], v[112:115], v[84:87]\n"
      "v_fma_f32 v195, |v95|, v63, v195\n"
      "v_pk_mul_f16 v107, v107, v171\n"
      "v_mfma_f32_16x16x32_f16 v[88:91], a[96:99], v[112:115], v[40:43]\n"
      "v_fma_f32 v195, |v80|, v64, v195\n"
      "v_fma_f32 v195, |v81|, v65, v195\n"
      "v_mfma_f32_16x16x32_f16 v[88:91], a[100:103], v[116:119], v[88:91]\n"
      "43:\n"
      "s_cbranch_execz 44b\n"
      "v_mfma_f32_16x16x32_f16 v[88:91], a[104:107], v[120:123], v[88:91]\n"
      "ds_read_b128 v[160:163], v203 offset:6912\n"
      "v_fma_f32 v195, |v82|, v66, v195\n"
      "v_mfma_f32_16x16x32_f16 v[88:91], a[108:111], v[124:127], v[88:91]\n"
      "s_waitcnt lgkmcnt(1)\n"
      "v_pk_mul_f16 v108, v108, v172\n"
      "v_fma_f32 v195, |v83|, v67, v195\n"
      "v_mfma_f32_16x16x32_f16 v[92:95], a[124:127], v[124:127], v[44:47]\n"
      "v_pk_mul_f16 v109, v109, v173\n"
      "v_pk_mul_f16 v110, v110, v174\n"
      "v_mfma_f32_16x16x32_f16 v[92:95], a[120:123], v[120:123], v[92:95]\n"
      "v_pk_mul_f16 v111, v111, v175\n"
      "v_fma_f32 v195, |v84|, v68, v195\n"
      "v_mfma_f32_16x16x32_f16 v[92:95], a[116:119], v[116:119], v[92:95]\n"
      "v_fma_f32 v195, |v85|, v69, v195\n"
      "v_fma_f32 v195, |v86|, v70, v195\n"
      "v_mfma_f32_16x16x32_f16 v[92:95], a[112:115], v[112:115], v[92:95]\n"
      "v_fma_f32 v195, |v87|, v71, v195\n"
      "v_fma_f32 v195, |v88|, v72, v195\n"
      "v_mfma_f32_16x16x32_f16 v[80:83], a[0:3], v[96:99], v[16:19]\n"
      "ds_read_b128 v[164:167], v203 offset:6976\n"
      "v_fma_f32 v195, |v89|, v73, v195\n"
      "v_mfma_f32_16x16x32_f16 v[80:83], a[4:7], v[100:103], v[80:83]\n"
      "v_fma_f32 v195, |v90|, v74, v195\n"
      "v_fma_f32 v195, |v91|, v75, v195\n"
      "v_mfma_f32_16x16x32_f16 v[80:83], a[8:11], v[104:107], v[80:83]\n"
      "s_waitcnt lgkmcnt(1)\n"
      "v_pk_mul_f16 v112, v128, v160\n"
      "v_pk_mul_f16 v113, v129, v161\n"
      "v_mfma_f32_16x16x32_f16 v[80:83], a[12:15], v[108:111], v[80:83]\n"
      "v_pk_mul_f16 v114, v130, v162\n"
      "v_pk_mul_f16 v115, v131, v163\n"
      "v_mfma_f32_16x16x32_f16 v[84:87], a[28:31], v[108:111], v[20:23]\n"
      "v_fma_f32 v195, |v92|, v76, v195\n"
      "v_fma_f32 v195, |v93|, v77, v195\n"
      "v_mfma_f32_16x16x32_f16 v[84:87], a[24:27], v[104:107], v[84:87]\n"
      "v_fma_f32 v195, |v94|, v78, v195\n"
      "v_fma_f32 v195, |v95|, v79, v195\n"
      "v_mfma_f32_16x16x32_f16 v[84:87], a[20:23], v[100:103], v[84:87]\n"
      "v_add_f32 v193, v193, v198\n"
      "v_mul_f32 v196, |v80|, v48\n"
      "v_mfma_f32_16x16x32_f16 v[84:87], a[16:19], v[96:99], v[84:87]\n"
      "v_permlane32_swap_b32 v194, v195\n"
      "v_mfma_f32_16x16x32_f16 v[88:91], a[32:35], v[96:99], v[24:27]\n"
      "v_permlane16_swap_b32 v191, v193\n"
      "v_mfma_f32_16x16x32_f16 v[88:91], a[36:39], v[100:103], v[88:91]\n"
      "s_waitcnt lgkmcnt(0)\n"
      "v_fma_f32 v196, |v81|, v49, v196\n"
      "v_pk_mul_f16 v116, v132, v164\n"
      "v_mfma_f32_16x16x32_f16 v[88:91], a[40:43], v[104:107], v[88:91]\n"
      "ds_read_b128 v[168:171], v203 offset:7040\n"
      "v_fma_f32 v196, |v82|, v50, v196\n"
      "v_mfma_f32_16x16x32_f16 v[88:91], a[44:47], v[108:111], v[88:91]\n"
      "v_fma_f32 v196, |v83|, v51, v196\n"
      "v_pk_mul_f16 v117, v133, v165\n"
      "v_mfma_f32_16x16x32_f16 v[92:95], a[60:63], v[108:111], v[28:31]\n"
      "v_pk_mul_f16 v118, v134, v166\n"
      "v_add_f32 v191, v191, v193\n"
      "v_mfma_f32_16x16x32_f16 v[92:95], a[56:59], v[104:107], v[92:95]\n"
      "v_pk_mul_f16 v119, v135, v167\n"
      "v_fma_f32 v196, |v84|, v52, v196\n"
      "v_mfma_f32_16x16x32_f16 v[92:95], a[52:55], v[100:103], v[92:95]\n"
      "v_fma_f32 v196, |v85|, v53, v196\n"
      "v_fma_f32 v196, |v86|, v54, v196\n"
      "v_mfma_f32_16x16x32_f16 v[92:95], a[48:51], v[96:99], v[92:95]\n"
      "v_fma_f32 v196, |v87|, v55, v196\n"
      "v_fma_f32 v196, |v88|, v56, v196\n"
      "v_mfma_f32_16x16x32_f16 v[80:83], a[64:67], v[96:99], v[32:35]\n"
      "ds_read_b128 v[172:175], v203 offset:7104\n"
      "v_fma_f32 v196, |v89|, v57, v196\n"
      "v_mfma_f32_16x16x32_f16 v[80:83], a[68:71], v[100:103], v[80:83]\n"
      "v_fma_f32 v196, |v90|, v58, v196\n"
      "v_fma_f32 v196, |v91|, v59, v196\n"
      "v_mfma_f32_16x16x32_f16 v[80:83], a[72:75], v[104:107], v[80:83]\n"
      "s_waitcnt lgkmcnt(1)\n"
      "v_pk_mul_f16 v120, v136, v168\n"
      "v_pk_mul_f16 v121, v137, v169\n"
      "v_mfma_f32_16x16x32_f16 v[80:83], a[76:79], v[108:111], v[80:83]\n"
      "v_pk_mul_f16 v122, v138, v170\n"
      "v_pk_mul_f16 v123, v139, v171\n"
      "v_mfma_f32_16x16x32_f16 v[84:87], a[92:95], v[108:111], v[36:39]\n"
      "v_fma_f32 v196, |v92|, v60, v196\n"
      "v_fma_f32 v196, |v93|, v61, v196\n"
      "v_mfma_f32_16x16x32_f16 v[84:87], a[88:91], v[104:107], v[84:87]\n"
      "v_fma_f32 v196, |v94|, v62, v196\n"
      "v_fma_f32 v196, |v95|, v63, v196\n"
      "v_mfma_f32_16x16x32_f16 v[84:87], a[84:87], v[100:103], v[84:87]\n"
      "ds_read2_b32 v[228:229], v208 offset0:0 offset1:8\n"
      "v_fma_f32 v196, |v80|, v64, v196\n"
      "v_mfma_f32_16x16x32_f16 v[84:87], a[80:83], v[96:99], v[84:87]\n"
      "ds_read2_b32 v[230:231], v209 offset0:0 offset1:8\n"
      "v_fma_f32 v196, |v81|, v65, v196\n"
      "v_mfma_f32_16x16x32_f16 v[88:91], a[96:99], v[96:99], v[40:43]\n"
      "s_waitcnt lgkmcnt(2)\n"
      "ds_read2_b32 v[232:233], v210 offset0:0 offset1:8\n"
      "v_pk_mul_f16 v124, v140, v172\n"
      "v_mfma_f32_16x16x32_f16 v[88:91], a[100:103], v[100:103], v[88:91]\n"
      "v_fma_f32 v196, |v82|, v66, v196\n"
      "v_fma_f32 v196, |v83|, v67, v196\n"
      "v_mfma_f32_16x16x32_f16 v[88:91], a[104:107], v[104:107], v[88:91]\n"
      "ds_read2_b32 v[234:235], v211 offset0:0 offset1:8\n"
      "v_pk_mul_f16 v125, v141, v173\n"
      "v_mfma_f32_16x16x32_f16 v[88:91], a[108:111], v[108:111], v[88:91]\n"
      "ds_read2_b32 v[236:237], v218 offset0:0 offset1:8\n"
      "v_pk_mul_f16 v126, v142, v174\n"
      "v_mfma_f32_16x16x32_f16 v[92:95], a[124:127], v[108:111], v[44:47]\n"
      "v_pk_mul_f16 v127, v143, v175\n"
      "v_fma_f32 v196, |v84|, v68, v196\n"
      "v_mfma_f32_16x16x32_f16 v[92:95], a[120:123], v[104:107], v[92:95]\n"
      "v_fma_f32 v196, |v85|, v69, v196\n"
      "v_fma_f32 v196, |v86|, v70, v196\n"
      "v_mfma_f32_16x16x32_f16 v[92:95], a[116:119], v[100:103], v[92:95]\n"
      "v_fma_f32 v196, |v87|, v71, v196\n"
      "v_fma_f32 v196, |v88|, v72, v196\n"
      "v_mfma_f32_16x16x32_f16 v[92:95], a[112:115], v[96:99], v[92:95]\n"
      "v_fma_f32 v196, |v89|, v73, v196\n"
      "v_pk_mul_f16 v96, v144, v160\n"
      "v_mfma_f32_16x16x32_f16 v[80:83], a[0:3], v[112:115], v[16:19]\n"
      "v_fma_f32 v196, |v90|, v74, v196\n"
      "v_fma_f32 v196, |v91|, v75, v196\n"
      "v_mfma_f32_16x16x32_f16 v[80:83], a[4:7], v[116:119], v[80:83]\n"
      "v_pk_mul_f16 v97, v145, v161\n"
      "v_pk_mul_f16 v98, v146, v162\n"
      "v_mfma_f32_16x16x32_f16 v[80:83], a[8:11], v[120:123], v[80:83]\n"
      "v_pk_mul_f16 v99, v147, v163\n"
      "v_fma_f32 v196, |v92|, v76, v196\n"
      "v_mfma_f32_16x16x32_f16 v[80:83], a[12:15], v[124:127], v[80:83]\n"
      "v_fma_f32 v196, |v93|, v77, v196\n"
      "v_fma_f32 v196, |v94|, v78, v196\n"
      "v_mfma_f32_16x16x32_f16 v[84:87], a[28:31], v[124:127], v[20:23]\n"
      "s_waitcnt lgkmcnt(4)\n"
      "v_fma_f32 v196, |v95|, v79, v196\n"
      "v_fmac_f32 v189, v215, v228\n"
      "v_mfma_f32_16x16x32_f16 v[84:87], a[24:27], v[120:123], v[84:87]\n"
      "s_waitcnt lgkmcnt(3)\n"
      "v_add_f32 v194, v194, v195\n"
      "v_fmac_f32 v190, v215, v230\n"
      "v_mfma_f32_16x16x32_f16 v[84:87], a[20:23], v[116:119], v[84:87]\n"
      "v_mul_f32 v192, |v80|, v48\n"
      "v_fma_f32 v192, |v81|, v49, v192\n"
      "v_mfma_f32_16x16x32_f16 v[84:87], a[16:19], v[112:115], v[84:87]\n"
      "v_permlane16_swap_b32 v188, v194\n"
      "v_mfma_f32_16x16x32_f16 v[88:91], a[32:35], v[112:115], v[24:27]\n"
      "s_waitcnt lgkmcnt(2)\n"
      "v_pk_mul_f16 v100, v148, v164\n"
      "v_fmac_f32 v191, v215, v232\n"
      "v_mfma_f32_16x16x32_f16 v[88:91], a[36:39], v[116:119], v[88:91]\n"
      "v_fma_f32 v192, |v82|, v50, v192\n"
      "v_fma_f32 v192, |v83|, v51, v192\n"
      "v_mfma_f32_16x16x32_f16 v[88:91], a[40:43], v[120:123], v[88:91]\n"
      "v_pk_mul_f16 v101, v149, v165\n"
      "v_max3_f32 v214, v189, v190, v191\n"
      "v_mfma_f32_16x16x32_f16 v[88:91], a[44:47], v[124:127], v[88:91]\n"
      "v_pk_mul_f16 v102, v150, v166\n"
      "v_add_f32 v188, v188, v194\n"
      "v_mfma_f32_16x16x32_f16 v[92:95], a[60:63], v[124:127], v[28:31]\n"
      "v_min3_f32 v226, v189, v190, v191\n"
      "v_pk_mul_f16 v103, v151, v167\n"
      "v_mfma_f32_16x16x32_f16 v[92:95], a[56:59], v[120:123], v[92:95]\n"
      "v_max_f32_dpp v222, v214, v214 row_ror:4 row_mask:0xf bank_mask:0xf\n"
      "v_fma_f32 v192, |v84|, v52, v192\n"
      "v_mfma_f32_16x16x32_f16 v[92:95], a[52:55], v[116:119], v[92:95]\n"
      "v_fma_f32 v192, |v85|, v53, v192\n"
      "v_fma_f32 v192, |v86|, v54, v192\n"
      "v_mfma_f32_16x16x32_f16 v[92:95], a[48:51], v[112:115], v[92:95]\n"
      "v_fma_f32 v192, |v87|, v55, v192\n"
      "v_max_f32_dpp v222, v214, v222 row_ror:8 row_mask:0xf bank_mask:0xf\n"
      "v_mfma_f32_16x16x32_f16 v[80:83], a[64:67], v[112:115], v[32:35]\n"
      "v_fma_f32 v192, |v88|, v56, v192\n"
      "v_fma_f32 v192, |v89|, v57, v192\n"
      "v_mfma_f32_16x16x32_f16 v[80:83], a[68:71], v[116:119], v[80:83]\n"
      "v_max_f32_dpp v222, v214, v222 row_ror:12 row_mask:0xf bank_mask:0xf\n"
      "v_pk_mul_f16 v104, v152, v168\n"
      "v_mfma_f32_16x16x32_f16 v[80:83], a[72:75], v[120:123], v[80:83]\n"
      "v_fma_f32 v192, |v90|, v58, v192\n"
      "v_fma_f32 v192, |v91|, v59, v192\n"
      "v_mfma_f32_16x16x32_f16 v[80:83], a[76:79], v[124:127], v[80:83]\n"
      "v_pk_mul_f16 v105, v153, v169\n"
      "v_max_f32_dpp v214, v222, v222 row_ror:1 row_mask:0xf bank_mask:0xf\n"
      "v_mfma_f32_16x16x32_f16 v[84:87], a[92:95], v[124:127], v[36:39]\n"
      "v_pk_mul_f16 v106, v154, v170\n"
      "v_pk_mul_f16 v107, v155, v171\n"
      "v_mfma_f32_16x16x32_f16 v[84:87], a[88:91], v[120:123], v[84:87]\n"
      "v_max_f32_dpp v214, v222, v214 row_ror:2 row_mask:0xf bank_mask:0xf\n"
      "v_fma_f32 v192, |v92|, v60, v192\n"
      "v_mfma_f32_16x16x32_f16 v[84:87], a[84:87], v[116:119], v[84:87]\n"
      "v_fma_f32 v192, |v93|, v61, v192\n"
      "v_fma_f32 v192, |v94|, v62, v192\n"
      "v_mfma_f32_16x16x32_f16 v[84:87], a[80:83], v[112:115], v[84:87]\n"
      "v_fma_f32 v192, |v95|, v63, v192\n"
      "v_max_f32_dpp v214, v222, v214 row_ror:3 row_mask:0xf bank_mask:0xf\n"
      "v_mfma_f32_16x16x32_f16 v[88:91], a[96:99], v[112:115], v[40:43]\n"
      "v_fma_f32 v192, |v80|, v64, v192\n"
      "v_fma_f32 v192, |v81|, v65, v192\n"
      "v_mfma_f32_16x16x32_f16 v[88:91], a[100:103], v[116:119], v[88:91]\n"
      "v_max_f32_dpp v214, v214, v214 row_bcast:15 row_mask:0xa bank_mask:0xf\n"
      "v_pk_mul_f16 v108, v156, v172\n"
      "v_mfma_f32_16x16x32_f16 v[88:91], a[104:107], v[120:123], v[88:91]\n"
      "v_fma_f32 v192, |v82|, v66, v192\n"
      "v_fma_f32 v192, |v83|, v67, v192\n"
      "v_mfma_f32_16x16x32_f16 v[88:91], a[108:111], v[124:127], v[88:91]\n"
      "v_pk_mul_f16 v109, v157, v173\n"
      "v_max_f32_dpp v214, v214, v214 row_bcast:31 row_mask:0xc bank_mask:0xf\n"
      "v_mfma_f32_16x16x32_f16 v[92:95], a[124:127], v[124:127], v[44:47]\n"
      "v_pk_mul_f16 v110, v158, v174\n"
      "v_readlane_b32 s62, v214, 63\n"
      "v_mfma_f32_16x16x32_f16 v[92:95], a[120:123], v[120:123], v[92:95]\n"
      "v_pk_mul_f16 v111, v159, v175\n"
      "v_subrev_f32 v189, s62, v189\n"
      "v_mfma_f32_16x16x32_f16 v[92:95], a[116:119], v[116:119], v[92:95]\n"
      "v_fma_f32 v192, |v84|, v68, v192\n"
      "v_fma_f32 v192, |v85|, v69, v192\n"
      "v_mfma_f32_16x16x32_f16 v[92:95], a[112:115], v[112:115], v[92:95]\n"
      "v_subrev_f32 v190, s62, v190\n"
      "v_fma_f32 v192, |v86|, v70, v192\n"
      "v_mfma_f32_16x16x32_f16 v[80:83], a[0:3], v[96:99], v[16:19]\n"
      "v_fma_f32 v192, |v87|, v71, v192\n"
      "v_subrev_f32 v191, s62, v191\n"
      "v_mfma_f32_16x16x32_f16 v[80:83], a[4:7], v[100:103], v[80:83]\n"
      "v_exp_f32 v189, v189\n"
      "v_mfma_f32_16x16x32_f16 v[80:83], a[8:11], v[104:107], v[80:83]\n"
      "v_exp_f32 v190, v190\n"
      "v_mfma_f32_16x16x32_f16 v[80:83], a[12:15], v[108:111], v[80:83]\n"
      "v_exp_f32 v191, v191\n"
      "v_mfma_f32_16x16x32_f16 v[84:87], a[28:31], v[108:111], v[20:23]\n"
      "v_fma_f32 v192, |v88|, v72, v192\n"
      "v_fma_f32 v192, |v89|, v73, v192\n"
      "v_mfma_f32_16x16x32_f16 v[84:87], a[24:27], v[104:107], v[84:87]\n"
      "v_pk_mul_f16 v112, v240, v160\n"
      "v_fma_f32 v192, |v90|, v74, v192\n"
      "v_mfma_f32_16x16x32_f16 v[84:87], a[20:23], v[100:103], v[84:87]\n"
      "v_fma_f32 v192, |v91|, v75, v192\n"
      "v_pk_mul_f16 v113, v241, v161\n"
      "v_mfma_f32_16x16x32_f16 v[84:87], a[16:19], v[96:99], v[84:87]\n"
      "v_mul_f32 v221, v189, v229\n"
      "v_pk_mul_f16 v114, v242, v162\n"
      "v_mfma_f32_16x16x32_f16 v[88:91], a[32:35], v[96:99], v[24:27]\n"
      "v_fma_f32 v220, v226, 0, v189\n"
      "v_pk_mul_f16 v115, v243, v163\n"
      "v_mfma_f32_16x16x32_f16 v[88:91], a[36:39], v[100:103], v[88:91]\n"
      "v_fmac_f32 v221, v190, v231\n"
      "v_fma_f32 v192, |v92|, v76, v192\n"
      "v_mfma_f32_16x16x32_f16 v[88:91], a[40:43], v[104:107], v[88:91]\n"
      "v_fma_f32 v192, |v93|, v77, v192\n"
      "v_add_f32 v220, v220, v190\n"
      "v_mfma_f32_16x16x32_f16 v[88:91], a[44:47], v[108:111], v[88:91]\n"
      "v_fma_f32 v192, |v94|, v78, v192\n"
      "v_fma_f32 v192, |v95|, v79, v192\n"
      "v_fmac_f32 v221, v191, v233\n"
      "v_mfma_f32_16x16x32_f16 v[92:95], a[60:63], v[108:111], v[28:31]\n"
      "v_add_f32 v220, v220, v191\n"
      "v_mul_f32 v193, |v80|, v48\n"
      "v_mfma_f32_16x16x32_f16 v[92:95], a[56:59], v[104:107], v[92:95]\n"
      "v_fma_f32 v193, |v81|, v49, v193\n"
      "v_pk_mul_f16 v116, v244, v164\n"
      "v_mfma_f32_16x16x32_f16 v[92:95], a[52:55], v[100:103], v[92:95]\n"
      "v_fma_f32 v193, |v82|, v50, v193\n"
      "v_fma_f32 v193, |v83|, v51, v193\n"
      "v_mfma_f32_16x16x32_f16 v[92:95], a[48:51], v[96:99], v[92:95]\n"
      "v_pk_mul_f16 v117, v245, v165\n"
      "v_pk_mul_f16 v118, v246, v166\n"
      "v_mfma_f32_16x16x32_f16 v[80:83], a[64:67], v[96:99], v[32:35]\n"
      "v_pk_mul_f16 v119, v247, v167\n"
      "v_fma_f32 v193, |v84|, v52, v193\n"
      "v_mfma_f32_16x16x32_f16 v[80:83], a[68:71], v[100:103], v[80:83]\n"
      "v_fma_f32 v193, |v85|, v53, v193\n"
      "v_fma_f32 v193, |v86|, v54, v193\n"
      "v_mfma_f32_16x16x32_f16 v[80:83], a[72:75], v[104:107], v[80:83]\n"
      "v_fma_f32 v193, |v87|, v55, v193\n"
      "v_fma_f32 v193, |v88|, v56, v193\n"
      "v_mfma_f32_16x16x32_f16 v[80:83], a[76:79], v[108:111], v[80:83]\n"
      "v_fma_f32 v193, |v89|, v57, v193\n"
      "v_pk_mul_f16 v120, v248, v168\n"
      "v_mfma_f32_16x16x32_f16 v[84:87], a[92:95], v[108:111], v[36:39]\n"
      "v_fma_f32 v193, |v90|, v58, v193\n"
      "v_fma_f32 v193, |v91|, v59, v193\n"
      "v_mfma_f32_16x16x32_f16 v[84:87], a[88:91], v[104:107], v[84:87]\n"
      "v_pk_mul_f16 v121, v249, v169\n"
      "v_pk_mul_f16 v122, v250, v170\n"
      "v_mfma_f32_16x16x32_f16 v[84:87], a[84:87], v[100:103], v[84:87]\n"
      "v_pk_mul_f16 v123, v251, v171\n"
      "v_fma_f32 v193, |v92|, v60, v193\n"
      "v_mfma_f32_16x16x32_f16 v[84:87], a[80:83], v[96:99], v[84:87]\n"
      "v_fma_f32 v193, |v93|, v61, v193\n"
      "v_fma_f32 v193, |v94|, v62, v193\n"
      "v_mfma_f32_16x16x32_f16 v[88:91], a[96:99], v[96:99], v[40:43]\n"
      "v_fma_f32 v193, |v95|, v63, v193\n"
      "v_fma_f32 v193, |v80|, v64, v193\n"
      "v_mfma_f32_16x16x32_f16 v[88:91], a[100:103], v[100:103], v[88:91]\n"
      "v_permlane32_swap_b32 v196, v192\n"
      "v_mfma_f32_16x16x32_f16 v[88:91], a[104:107], v[104:107], v[88:91]\n"
      "v_fma_f32 v193, |v81|, v65, v193\n"
      "v_pk_mul_f16 v124, v252, v172\n"
      "v_mfma_f32_16x16x32_f16 v[88:91], a[108:111], v[108:111], v[88:91]\n"
      "v_fma_f32 v193, |v82|, v66, v193\n"
      "v_fma_f32 v193, |v83|, v67, v193\n"
      "v_mfma_f32_16x16x32_f16 v[92:95], a[124:127], v[108:111], v[44:47]\n"
      "v_pk_mul_f16 v125, v253, v173\n"
      "v_pk_mul_f16 v126, v254, v174\n"
      "v_mfma_f32_16x16x32_f16 v[92:95], a[120:123], v[104:107], v[92:95]\n"
      "v_pk_mul_f16 v127, v255, v175\n"
      "v_fma_f32 v193, |v84|, v68, v193\n"
      "v_mfma_f32_16x16x32_f16 v[92:95], a[116:119], v[100:103], v[92:95]\n"
      "v_fma_f32 v193, |v85|, v69, v193\n"
      "v_fma_f32 v193, |v86|, v70, v193\n"
      "v_mfma_f32_16x16x32_f16 v[92:95], a[112:115], v[96:99], v[92:95]\n"
      "v_fma_f32 v193, |v87|, v71, v193\n"
      "v_fma_f32 v193, |v88|, v72, v193\n"
      "v_mfma_f32_16x16x32_f16 v[80:83], a[0:3], v[112:115], v[16:19]\n"
      "v_fma_f32 v193, |v89|, v73, v193\n"
      "v_fma_f32 v193, |v90|, v74, v193\n"
      "v_mfma_f32_16x16x32_f16 v[80:83], a[4:7], v[116:119], v[80:83]\n"
      "ds_read_b128 v[184:187], v12 offset:1440\n"
      "v_fma_f32 v193, |v91|, v75, v193\n"
      "v_mfma_f32_16x16x32_f16 v[80:83], a[8:11], v[120:123], v[80:83]\n"
      "v_fma_f32 v193, |v92|, v76, v193\n"
      "v_fma_f32 v193, |v93|, v77, v193\n"
      "v_mfma_f32_16x16x32_f16 v[80:83], a[12:15], v[124:127], v[80:83]\n"
      "v_fma_f32 v193, |v94|, v78, v193\n"
      "v_fma_f32 v193, |v95|, v79, v193\n"
      "v_mfma_f32_16x16x32_f16 v[84:87], a[28:31], v[124:127], v[20:23]\n"
      "s_waitcnt lgkmcnt(1)\n"
      "ds_read_b128 a[200:203], v219 offset:16512\n"
      "v_fma_f32 v236, v236, v215, -s62\n"
      "v_mfma_f32_16x16x32_f16 v[84:87], a[24:27], v[120:123], v[84:87]\n"
      "ds_read_b128 a[204:207], v219 offset:16576\n"
      "v_add_f32 v196, v196, v192\n"
      "v_mfma_f32_16x16x32_f16 v[84:87], a[20:23], v[116:119], v[84:87]\n"
      "v_mul_f32 v197, |v80|, v48\n"
      "v_fma_f32 v197, |v81|, v49, v197\n"
      "v_mfma_f32_16x16x32_f16 v[84:87], a[16:19], v[112:115], v[84:87]\n"
      "v_fma_f32 v197, |v82|, v50, v197\n"
      "v_fma_f32 v197, |v83|, v51, v197\n"
      "v_mfma_f32_16x16x32_f16 v[88:91], a[32:35], v[112:115], v[24:27]\n"
      "ds_read_b128 a[208:211], v219 offset:16640\n"
      "v_fmac_f32 v188, v215, v234\n"
      "v_mfma_f32_16x16x32_f16 v[88:91], a[36:39], v[116:119], v[88:91]\n"
      "s_waitcnt lgkmcnt(3)\n"
      "v_pk_add_f32 v[184:185], v[184:185], v[186:187]\n"
      "v_add_f32 v199, v184, v185\n"
      "v_mfma_f32_16x16x32_f16 v[88:91], a[40:43], v[120:123], v[88:91]\n"
      "v_fma_f32 v197, |v84|, v52, v197\n"
      "v_fma_f32 v197, |v85|, v53, v197\n"
      "v_mfma_f32_16x16x32_f16 v[88:91], a[44:47], v[124:127], v[88:91]\n"
      "v_fma_f32 v197, |v86|, v54, v197\n"
      "v_fma_f32 v197, |v87|, v55, v197\n"
      "v_mfma_f32_16x16x32_f16 v[92:95], a[60:63], v[124:127], v[28:31]\n"
      "ds_read_b128 a[212:215], v219 offset:16704\n"
      "v_cmp_u_f32 vcc, v199, v199\n"
      "v_mfma_f32_16x16x32_f16 v[92:95], a[56:59], v[120:123], v[92:95]\n"
      "v_subrev_f32 v188, s62, v188\n"
      "v_add_f32_dpp v196, v236, v196 quad_perm:[0,1,2,3] row_mask:0x5 bank_mask:0xf\n"
      "v_mfma_f32_16x16x32_f16 v[92:95], a[52:55], v[116:119], v[92:95]\n"
      "v_fma_f32 v197, |v88|, v56, v197\n"
      "v_fma_f32 v197, |v89|, v57, v197\n"
      "v_mfma_f32_16x16x32_f16 v[92:95], a[48:51], v[112:115], v[92:95]\n"
      "v_fma_f32 v197, |v90|, v58, v197\n"
      "v_fma_f32 v197, |v91|, v59, v197\n"
      "v_mfma_f32_16x16x32_f16 v[80:83], a[64:67], v[112:115], v[32:35]\n"
      "s_cbranch_vccnz 17f\n"
      "18:\n"
      "v_mfma_f32_16x16x32_f16 v[80:83], a[68:71], v[116:119], v[80:83]\n"
      "v_permlane32_swap_b32 v199, v184\n"
      "v_mfma_f32_16x16x32_f16 v[80:83], a[72:75], v[120:123], v[80:83]\n"
      "s_nop 0\n"
      "v_fma_f32 v197, |v92|, v60, v197\n"
      "v_fma_f32 v197, |v93|, v61, v197\n"
      "v_mfma_f32_16x16x32_f16 v[80:83], a[76:79], v[124:127], v[80:83]\n"
      "v_fma_f32 v197, |v94|, v62, v197\n"
      "v_fma_f32 v197, |v95|, v63, v197\n"
      "v_mfma_f32_16x16x32_f16 v[84:87], a[92:95], v[124:127], v[36:39]\n"
      "v_add_f32 v199, v199, v184\n"
      "v_add_f32_dpp v193, v236, v193 quad_perm:[0,1,2,3] row_mask:0x2 bank_mask:0xf\n"
      "v_mfma_f32_16x16x32_f16 v[84:87], a[88:91], v[120:123], v[84:87]\n"
      "v_exp_f32 v222, v188\n"
      "v_mfma_f32_16x16x32_f16 v[84:87], a[84:87], v[116:119], v[84:87]\n"
      "v_fma_f32 v197, |v80|, v64, v197\n"
      "v_fma_f32 v197, |v81|, v65, v197\n"
      "v_mfma_f32_16x16x32_f16 v[84:87], a[80:83], v[112:115], v[84:87]\n"
      "v_fma_f32 v197, |v82|, v66, v197\n"
      "v_fma_f32 v197, |v83|, v67, v197\n"
      "v_mfma_f32_16x16x32_f16 v[88:91], a[96:99], v[112:115], v[40:43]\n"
      "v_permlane16_swap_b32 v199, v185\n"
      "v_mfma_f32_16x16x32_f16 v[88:91], a[100:103], v[116:119], v[88:91]\n"
      "v_add_f32 v199, v199, v185\n"
      "v_add_f32_dpp v197, v236, v197 quad_perm:[0,1,2,3] row_mask:0x8 bank_mask:0xf\n"
      "v_mfma_f32_16x16x32_f16 v[88:91], a[104:107], v[120:123], v[88:91]\n"
      "s_mov_b64 vcc, 31\n"
      "v_fma_f32 v197, |v84|, v68, v197\n"
      "v_fma_f32 v197, |v85|, v69, v197\n"
      "v_mfma_f32_16x16x32_f16 v[88:91], a[108:111], v[124:127], v[88:91]\n"
      "v_fma_f32 v197, |v86|, v70, v197\n"
      "v_fma_f32 v197, |v87|, v71, v197\n"
      "v_mfma_f32_16x16x32_f16 v[92:95], a[124:127], v[124:127], v[44:47]\n"
      "v_subrev_f32 v199, s62, v199\n"
      "v_cndmask_b32 v199, v7, v199, vcc\n"
      "v_mfma_f32_16x16x32_f16 v[92:95], a[120:123], v[120:123], v[92:95]\n"
      "v_fma_f32 v214, v188, 0, v220\n"
      "v_fma_f32 v226, v222, v235, v221\n"
      "v_mfma_f32_16x16x32_f16 v[92:95], a[116:119], v[116:119], v[92:95]\n"
      "v_fma_f32 v197, |v88|, v72, v197\n"
      "v_fma_f32 v197, |v89|, v73, v197\n"
      "v_mfma_f32_16x16x32_f16 v[92:95], a[112:115], v[112:115], v[92:95]\n"
      "s_waitcnt lgkmcnt(3)\n"
      "v_fma_f32 v197, |v90|, v74, v197\n"
      "v_fma_f32 v197, |v91|, v75, v197\n"
      "v_mfma_f32_16x16x32_f16 v[180:183], a[200:203], a[216:219], 0\n"
      "s_waitcnt lgkmcnt(2)\n"
      "v_add_f32 v214, v214, v222\n"
      "v_fmac_f32 v214, 0, v199\n"
      "v_mfma_f32_16x16x32_f16 v[180:183], a[204:207], a[220:223], v[180:183]\n"
      "s_waitcnt lgkmcnt(1)\n"
      "v_fma_f32 v197, |v92|, v76, v197\n"
      "v_fma_f32 v197, |v93|, v77, v197\n"
      "v_mfma_f32_16x16x32_f16 v[180:183], a[208:211], a[224:227], v[180:183]\n"
      "s_waitcnt lgkmcnt(0)\n"
      "v_fma_f32 v197, |v94|, v78, v197\n"
      "v_fma_f32 v197, |v95|, v79, v197\n"
      "v_mfma_f32_16x16x32_f16 v[180:183], a[212:215], a[228:231], v[180:183]\n"
      "s_nop 0\n"
      "v_permlane32_swap_b32 v193, v197\n"
      "v_add_f32 v193, v193, v197\n"
      "s_nop 1\n"
      "v_permlane16_swap_b32 v196, v193\n"
      "v_add_f32 v196, v196, v193\n"
      "v_exp_f32 v222, v196\n"
      "v_fmac_f32_dpp v199, v180, v215 quad_perm:[0,1,2,3] row_mask:0x1 bank_mask:0x3\n"
      "v_fma_f32 v214, v196, 0, v214\n"
      "v_exp_f32 v223, v199\n"
      "v_fmac_f32 v226, v222, v237\n"
      "v_add_f32 v214, v214, v222\n"
      "v_fmac_f32_dpp v226, v181, v223 quad_perm:[0,1,2,3] row_mask:0x1 bank_mask:0x3\n"
      "v_add_f32 v214, v214, v223\n"
      "s_nop 1\n"
      "v_add_f32_dpp v214, v214, v214 row_ror:8 row_mask:0xf bank_mask:0xf\n"
      "v_add_f32_dpp v226, v226, v226 row_ror:8 row_mask:0xf bank_mask:0xf\n"
      "s_nop 0\n"
      "v_add_f32_dpp v214, v214, v214 row_ror:4 row_mask:0xf bank_mask:0xf\n"
      "v_add_f32_dpp v226, v226, v226 row_ror:4 row_mask:0xf bank_mask:0xf\n"
      "s_nop 0\n"
      "v_add_f32_dpp v214, v214, v214 row_ror:2 row_mask:0xf bank_mask:0xf\n"
      "v_add_f32_dpp v226, v226, v226 row_ror:2 row_mask:0xf bank_mask:0xf\n"
      "s_nop 0\n"
      "v_add_f32_dpp v214, v214, v214 row_ror:1 row_mask:0xf bank_mask:0xf\n"
      "v_add_f32_dpp v226, v226, v226 row_ror:1 row_mask:0xf bank_mask:0xf\n"
      "s_nop 0\n"
      "v_add_f32_dpp v214, v214, v214 row_bcast:15 row_mask:0xa bank_mask:0xf\n"
      "v_add_f32_dpp v226, v226, v226 row_bcast:15 row_mask:0xa bank_mask:0xf\n"
      "s_nop 0\n"
      "v_add_f32_dpp v214, v214, v214 row_bcast:31 row_mask:0xc bank_mask:0xf\n"
      "v_add_f32_dpp v226, v226, v226 row_bcast:31 row_mask:0xc bank_mask:0xf\n"
      "v_rcp_f32 v222, v214\n"
      "v_ldexp_f32 v226, v226, s39\n"
      "s_lshl_b64 exec, 1, 63\n"
      "v_mul_f32 v226, v226, v222\n"
      "v_cvt_pk_bf16_f32 v222, v226, v226\n"
      "global_store_short v3, v222, s[24:25]\n"
      "v_cmp_class_f32 vcc, v226, s45\n"
      "v_cmp_lt_f32 s[28:29], s49, v214\n"
      "s_nop 0\n"
      "s_or_b32 vcc_hi, vcc_hi, s29\n"
      "s_cmp_lt_i32 vcc_hi, 0\n"
      "s_cbranch_scc1 19f\n"
      "20:\n"
      "s_endpgm\n"
      "s_mov_b64 exec, -1\n"
      "s_branch 9f\n"
      "33:\n"
      "s_cmp_lg_u32 s55, 0\n"
      "s_cbranch_scc1 34f\n"
      "global_load_dwordx4 v[244:247], v11, s[10:11] nt\n"
      "global_load_dwordx4 v[248:251], v12, s[10:11] nt\n"
      "global_load_dwordx4 v[252:255], v6, s[10:11] nt\n"
      "s_branch 6f\n"
      "34:\n"
      "v_accvgpr_read_b32 v244, a216\n"
      "v_accvgpr_read_b32 v245, a217\n"
      "v_accvgpr_read_b32 v246, a218\n"
      "v_accvgpr_read_b32 v247, a219\n"
      "v_accvgpr_read_b32 v248, a220\n"
      "v_accvgpr_read_b32 v249, a221\n"
      "v_accvgpr_read_b32 v250, a222\n"
      "v_accvgpr_read_b32 v251, a223\n"
      "v_accvgpr_read_b32 v252, a224\n"
      "v_accvgpr_read_b32 v253, a225\n"
      "v_accvgpr_read_b32 v254, a226\n"
      "v_accvgpr_read_b32 v255, a227\n"
      "s_branch 32b\n"
      "5:\n"
      "s_waitcnt lgkmcnt(0)\n"
      "ds_read_b32 v224, v227 offset:37888\n"
      "s_waitcnt lgkmcnt(0)\n"
      "v_and_b32 v224, 15, v224\n"
      "v_cmp_eq_u32 vcc, 0, v224\n"
      "s_nop 4\n"
      "s_cmp_lg_u32 vcc_lo, 0\n"
      "s_cbranch_scc1 5b\n"
      "v_cmp_gt_u32 vcc, 4, v224\n"
      "s_nop 4\n"
      "s_cmp_lg_u32 vcc_lo, 0\n"
      "s_cbranch_scc1 8f\n"
      "v_and_b32 v225, 2, v224\n"
      "v_cmp_ne_u32 vcc, 0, v225\n"
      "s_nop 4\n"
      "s_cmp_eq_u32 vcc_lo, 0\n"
      "s_cbranch_scc1 53f\n"
      "s_waitcnt vmcnt(0)\n"
      "v_add_u32 v216, 2592, v8\n"
      "v_mov_b32 v217, v216\n"
      "53:\n"
      "v_cmp_le_u32 vcc, 8, v224\n"
      "s_nop 4\n"
      "s_or_b32 s44, vcc_lo, s58\n"
      "s_cbranch_scc1 52f\n"
      "v_and_b32 v224, 63, %3\n"
      "v_lshlrev_b32 v224, 9, v224\n"
      "global_load_dwordx4 v[176:179], v224, s[16:17]\n"
      "global_load_dwordx4 v[180:183], v224, s[16:17] offset:336\n"
      "s_waitcnt vmcnt(0)\n"
      "v_max3_f32 v224, |v176|, |v177|, |v178|\n"
      "v_max3_f32 v224, |v179|, |v180|, v224\n"
      "v_max3_f32 v224, |v181|, |v182|, v224\n"
      "v_max_f32 v224, |v183|, v224\n"
      "s_nop 1\n"
      "v_max_f32_dpp v224, v224, v224 row_ror:8 row_mask:0xf bank_mask:0xf\n"
      "s_nop 1\n"
      "v_max_f32_dpp v224, v224, v224 row_ror:4 row_mask:0xf bank_mask:0xf\n"
      "s_nop 1\n"
      "v_max_f32_dpp v224, v224, v224 row_ror:2 row_mask:0xf bank_mask:0xf\n"
      "s_nop 1\n"
      "v_max_f32_dpp v224, v224, v224 row_ror:1 row_mask:0xf bank_mask:0xf\n"
      "s_nop 0\n"
      "v_readlane_b32 s44, v224, 0\n"
      "v_readlane_b32 s45, v224, 16\n"
      "v_readlane_b32 s46, v224, 32\n"
      "v_readlane_b32 s47, v224, 48\n"
      "s_max_u32 s44, s44, s45\n"
      "s_max_u32 s46, s46, s47\n"
      "s_max_u32 s44, s44, s46\n"
      "s_cmp_ge_u32 s44, 0x43000000\n"
      "s_cbranch_scc1 52f\n"
      "s_lshr_b32 s44, s44, 16\n"
      "s_cmp_eq_u32 s44, 0\n"
      "s_cbranch_scc1 52f\n"
      "s_lshr_b32 s44, s44, 7\n"
      "s_sub_i32 s58, 134, s44\n"
      "s_cmp_gt_u32 s58, 50\n"
      "s_cbranch_scc1 8f\n"
      "s_mov_b32 s41, 0\n"
      "s_mov_b32 s35, 0\n"
      "s_waitcnt vmcnt(0) lgkmcnt(0)\n"
      "s_barrier\n"
      "s_branch 0b\n"
      "52:\n"
      "ds_read_b128 v[176:179], v5 offset:37376\n"
      "ds_read_b128 v[180:183], v5 offset:37392\n"
      "s_waitcnt lgkmcnt(0)\n"
      "v_max3_f32 v225, |v176|, |v177|, |v178|\n"
      "v_max3_f32 v225, |v179|, |v180|, v225\n"
      "v_max3_f32 v225, |v181|, |v182|, v225\n"
      "v_max_f32 v225, |v183|, v225\n"
      "s_nop 1\n"
      "v_max_f32_dpp v225, v225, v225 row_ror:8 row_mask:0xf bank_mask:0xf\n"
      "s_nop 1\n"
      "v_max_f32_dpp v225, v225, v225 row_ror:4 row_mask:0xf bank_mask:0xf\n"
      "s_nop 1\n"
      "v_max_f32_dpp v225, v225, v225 row_ror:2 row_mask:0xf bank_mask:0xf\n"
      "s_nop 1\n"
      "v_max_f32_dpp v225, v225, v225 row_ror:1 row_mask:0xf bank_mask:0xf\n"
      "v_frexp_exp_i32_f32 v224, v225\n"
      "v_sub_u32 v224, 0, v224\n"
      "v_ldexp_f32 v176, v176, v224\n"
      "v_ldexp_f32 v177, v177, v224\n"
      "v_ldexp_f32 v178, v178, v224\n"
      "v_ldexp_f32 v179, v179, v224\n"
      "v_ldexp_f32 v180, v180, v224\n"
      "v_ldexp_f32 v181, v181, v224\n"
      "v_ldexp_f32 v182, v182, v224\n"
      "v_ldexp_f32 v183, v183, v224\n"
      "v_readfirstlane_b32 s56, v224\n"
      "s_nop 1\n"
      "s_branch 4b\n"
      "36:\n"
      "s_sub_u32 s33, s26, 1\n"
      "s_sub_u32 s44, s34, s33\n"
      "s_mul_i32 s44, s44, 104\n"
      "v_subrev_u32 v10, s44, v10\n"
      "s_branch 37b\n"
      "22:\n"
      "v_max3_f32 v224, |v176|, |v177|, |v178|\n"
      "v_max3_f32 v224, |v179|, |v180|, v224\n"
      "v_max3_f32 v224, |v181|, |v182|, v224\n"
      "v_max_f32 v224, |v183|, v224\n"
      "s_nop 1\n"
      "v_max_f32_dpp v224, v224, v224 row_ror:8 row_mask:0xf bank_mask:0xf\n"
      "s_nop 1\n"
      "v_max_f32_dpp v224, v224, v224 row_ror:4 row_mask:0xf bank_mask:0xf\n"
      "s_nop 1\n"
      "v_max_f32_dpp v224, v224, v224 row_ror:2 row_mask:0xf bank_mask:0xf\n"
      "s_nop 1\n"
      "v_max_f32_dpp v224, v224, v224 row_ror:1 row_mask:0xf bank_mask:0xf\n"
      "v_frexp_exp_i32_f32 v90, v224\n"
      "v_sub_u32 v90, 0, v90\n"
      "v_ldexp_f32 v176, v176, v90\n"
      "v_ldexp_f32 v177, v177, v90\n"
      "v_ldexp_f32 v178, v178, v90\n"
      "v_ldexp_f32 v179, v179, v90\n"
      "v_ldexp_f32 v180, v180, v90\n"
      "v_ldexp_f32 v181, v181, v90\n"
      "v_ldexp_f32 v182, v182, v90\n"
      "v_ldexp_f32 v183, v183, v90\n"
      "v_readlane_b32 s59, v90, 0\n"
      "v_readlane_b32 s57, v90, 32\n"
      "s_branch 23b\n"
      "70:\n"
      "s_add_u32 s44, s58, 127\n"
      "s_lshl_b32 s44, s44, 23\n"
      "s_waitcnt vmcnt(0) lgkmcnt(0)\n"
      "s_nop 7\n"
      "v_lshlrev_b32 v176, 16, v96\n"
      "v_and_b32 v177, s43, v96\n"
      "v_mul_f32 v176, s44, v176\n"
      "v_mul_f32 v177, s44, v177\n"
      "v_cvt_pk_bf16_f32 v96, v176, v177\n"
      "v_lshlrev_b32 v176, 16, v97\n"
      "v_and_b32 v177, s43, v97\n"
      "v_mul_f32 v176, s44, v176\n"
      "v_mul_f32 v177, s44, v177\n"
      "v_cvt_pk_bf16_f32 v97, v176, v177\n"
      "v_lshlrev_b32 v176, 16, v98\n"
      "v_and_b32 v177, s43, v98\n"
      "v_mul_f32 v176, s44, v176\n"
      "v_mul_f32 v177, s44, v177\n"
      "v_cvt_pk_bf16_f32 v98, v176, v177\n"
      "v_lshlrev_b32 v176, 16, v99\n"
      "v_and_b32 v177, s43, v99\n"
      "v_mul_f32 v176, s44, v176\n"
      "v_mul_f32 v177, s44, v177\n"
      "v_cvt_pk_bf16_f32 v99, v176, v177\n"
      "v_lshlrev_b32 v176, 16, v100\n"
      "v_and_b32 v177, s43, v100\n"
      "v_mul_f32 v176, s44, v176\n"
      "v_mul_f32 v177, s44, v177\n"
      "v_cvt_pk_bf16_f32 v100, v176, v177\n"
      "v_lshlrev_b32 v176, 16, v101\n"
      "v_and_b32 v177, s43, v101\n"
      "v_mul_f32 v176, s44, v176\n"
      "v_mul_f32 v177, s44, v177\n"
      "v_cvt_pk_bf16_f32 v101, v176, v177\n"
      "v_lshlrev_b32 v176, 16, v102\n"
      "v_and_b32 v177, s43, v102\n"
      "v_mul_f32 v176, s44, v176\n"
      "v_mul_f32 v177, s44, v177\n"
      "v_cvt_pk_bf16_f32 v102, v176, v177\n"
      "v_lshlrev_b32 v176, 16, v103\n"
      "v_and_b32 v177, s43, v103\n"
      "v_mul_f32 v176, s44, v176\n"
      "v_mul_f32 v177, s44, v177\n"
      "v_cvt_pk_bf16_f32 v103, v176, v177\n"
      "v_lshlrev_b32 v176, 16, v104\n"
      "v_and_b32 v177, s43, v104\n"
      "v_mul_f32 v176, s44, v176\n"
      "v_mul_f32 v177, s44, v177\n"
      "v_cvt_pk_bf16_f32 v104, v176, v177\n"
      "v_lshlrev_b32 v176, 16, v105\n"
      "v_and_b32 v177, s43, v105\n"
      "v_mul_f32 v176, s44, v176\n"
      "v_mul_f32 v177, s44, v177\n"
      "v_cvt_pk_bf16_f32 v105, v176, v177\n"
      "v_lshlrev_b32 v176, 16, v106\n"
      "v_and_b32 v177, s43, v106\n"
      "v_mul_f32 v176, s44, v176\n"
      "v_mul_f32 v177, s44, v177\n"
      "v_cvt_pk_bf16_f32 v106, v176, v177\n"
      "v_lshlrev_b32 v176, 16, v107\n"
      "v_and_b32 v177, s43, v107\n"
      "v_mul_f32 v176, s44, v176\n"
      "v_mul_f32 v177, s44, v177\n"
      "v_cvt_pk_bf16_f32 v107, v176, v177\n"
      "v_lshlrev_b32 v176, 16, v108\n"
      "v_and_b32 v177, s43, v108\n"
      "v_mul_f32 v176, s44, v176\n"
      "v_mul_f32 v177, s44, v177\n"
      "v_cvt_pk_bf16_f32 v108, v176, v177\n"
      "v_lshlrev_b32 v176, 16, v109\n"
      "v_and_b32 v177, s43, v109\n"
      "v_mul_f32 v176, s44, v176\n"
      "v_mul_f32 v177, s44, v177\n"
      "v_cvt_pk_bf16_f32 v109, v176, v177\n"
      "v_lshlrev_b32 v176, 16, v110\n"
      "v_and_b32 v177, s43, v110\n"
      "v_mul_f32 v176, s44, v176\n"
      "v_mul_f32 v177, s44, v177\n"
      "v_cvt_pk_bf16_f32 v110, v176, v177\n"
      "v_lshlrev_b32 v176, 16, v111\n"
      "v_and_b32 v177, s43, v111\n"
      "v_mul_f32 v176, s44, v176\n"
      "v_mul_f32 v177, s44, v177\n"
      "v_cvt_pk_bf16_f32 v111, v176, v177\n"
      "v_lshlrev_b32 v176, 16, v112\n"
      "v_and_b32 v177, s43, v112\n"
      "v_mul_f32 v176, s44, v176\n"
      "v_mul_f32 v177, s44, v177\n"
      "v_cvt_pk_bf16_f32 v112, v176, v177\n"
      "v_lshlrev_b32 v176, 16, v113\n"
      "v_and_b32 v177, s43, v113\n"
      "v_mul_f32 v176, s44, v176\n"
      "v_mul_f32 v177, s44, v177\n"
      "v_cvt_pk_bf16_f32 v113, v176, v177\n"
      "v_lshlrev_b32 v176, 16, v114\n"
      "v_and_b32 v177, s43, v114\n"
      "v_mul_f32 v176, s44, v176\n"
      "v_mul_f32 v177, s44, v177\n"
      "v_cvt_pk_bf16_f32 v114, v176, v177\n"
      "v_lshlrev_b32 v176, 16, v115\n"
      "v_and_b32 v177, s43, v115\n"
      "v_mul_f32 v176, s44, v176\n"
      "v_mul_f32 v177, s44, v177\n"
      "v_cvt_pk_bf16_f32 v115, v176, v177\n"
      "v_lshlrev_b32 v176, 16, v116\n"
      "v_and_b32 v177, s43, v116\n"
      "v_mul_f32 v176, s44, v176\n"
      "v_mul_f32 v177, s44, v177\n"
      "v_cvt_pk_bf16_f32 v116, v176, v177\n"
      "v_lshlrev_b32 v176, 16, v117\n"
      "v_and_b32 v177, s43, v117\n"
      "v_mul_f32 v176, s44, v176\n"
      "v_mul_f32 v177, s44, v177\n"
      "v_cvt_pk_bf16_f32 v117, v176, v177\n"
      "v_lshlrev_b32 v176, 16, v118\n"
      "v_and_b32 v177, s43, v118\n"
      "v_mul_f32 v176, s44, v176\n"
      "v_mul_f32 v177, s44, v177\n"
      "v_cvt_pk_bf16_f32 v118, v176, v177\n"
      "v_lshlrev_b32 v176, 16, v119\n"
      "v_and_b32 v177, s43, v119\n"
      "v_mul_f32 v176, s44, v176\n"
      "v_mul_f32 v177, s44, v177\n"
      "v_cvt_pk_bf16_f32 v119, v176, v177\n"
      "s_sub_u32 s44, 0, s58\n"
      "v_ldexp_f32 v16, v16, s58\n"
      "v_ldexp_f32 v48, v48, s44\n"
      "v_ldexp_f32 v17, v17, s58\n"
      "v_ldexp_f32 v49, v49, s44\n"
      "v_ldexp_f32 v18, v18, s58\n"
      "v_ldexp_f32 v50, v50, s44\n"
      "v_ldexp_f32 v19, v19, s58\n"
      "v_ldexp_f32 v51, v51, s44\n"
      "v_ldexp_f32 v20, v20, s58\n"
      "v_ldexp_f32 v52, v52, s44\n"
      "v_ldexp_f32 v21, v21, s58\n"
      "v_ldexp_f32 v53, v53, s44\n"
      "v_ldexp_f32 v22, v22, s58\n"
      "v_ldexp_f32 v54, v54, s44\n"
      "v_ldexp_f32 v23, v23, s58\n"
      "v_ldexp_f32 v55, v55, s44\n"
      "v_ldexp_f32 v24, v24, s58\n"
      "v_ldexp_f32 v56, v56, s44\n"
      "v_ldexp_f32 v25, v25, s58\n"
      "v_ldexp_f32 v57, v57, s44\n"
      "v_ldexp_f32 v26, v26, s58\n"
      "v_ldexp_f32 v58, v58, s44\n"
      "v_ldexp_f32 v27, v27, s58\n"
      "v_ldexp_f32 v59, v59, s44\n"
      "v_ldexp_f32 v28, v28, s58\n"
      "v_ldexp_f32 v60, v60, s44\n"
      "v_ldexp_f32 v29, v29, s58\n"
      "v_ldexp_f32 v61, v61, s44\n"
      "v_ldexp_f32 v30, v30, s58\n"
      "v_ldexp_f32 v62, v62, s44\n"
      "v_ldexp_f32 v31, v31, s58\n"
      "v_ldexp_f32 v63, v63, s44\n"
      "v_ldexp_f32 v32, v32, s58\n"
      "v_ldexp_f32 v64, v64, s44\n"
      "v_ldexp_f32 v33, v33, s58\n"
      "v_ldexp_f32 v65, v65, s44\n"
      "v_ldexp_f32 v34, v34, s58\n"
      "v_ldexp_f32 v66, v66, s44\n"
      "v_ldexp_f32 v35, v35, s58\n"
      "v_ldexp_f32 v67, v67, s44\n"
      "v_ldexp_f32 v36, v36, s58\n"
      "v_ldexp_f32 v68, v68, s44\n"
      "v_ldexp_f32 v37, v37, s58\n"
      "v_ldexp_f32 v69, v69, s44\n"
      "v_ldexp_f32 v38, v38, s58\n"
      "v_ldexp_f32 v70, v70, s44\n"
      "v_ldexp_f32 v39, v39, s58\n"
      "v_ldexp_f32 v71, v71, s44\n"
      "v_ldexp_f32 v40, v40, s58\n"
      "v_ldexp_f32 v72, v72, s44\n"
      "v_ldexp_f32 v41, v41, s58\n"
      "v_ldexp_f32 v73, v73, s44\n"
      "v_ldexp_f32 v42, v42, s58\n"
      "v_ldexp_f32 v74, v74, s44\n"
      "v_ldexp_f32 v43, v43, s58\n"
      "v_ldexp_f32 v75, v75, s44\n"
      "v_ldexp_f32 v44, v44, s58\n"
      "v_ldexp_f32 v76, v76, s44\n"
      "v_ldexp_f32 v45, v45, s58\n"
      "v_ldexp_f32 v77, v77, s44\n"
      "v_ldexp_f32 v46, v46, s58\n"
      "v_ldexp_f32 v78, v78, s44\n"
      "v_ldexp_f32 v47, v47, s58\n"
      "v_ldexp_f32 v79, v79, s44\n"
      "s_nop 1\n"
      "s_branch 71b\n"
      "74:\n"
      "s_cmp_eq_u32 s55, 0\n"
      "s_cbranch_scc1 76f\n"
      "ds_read_b128 v[136:139], v201 offset:128\n"
      "ds_read_b128 v[168:171], v202 offset:128\n"
      "ds_read_b128 v[140:143], v201 offset:192\n"
      "ds_read_b128 v[172:175], v202 offset:192\n"
      "s_waitcnt lgkmcnt(0)\n"
      "v_pk_mul_f16 v96, v128, v160\n"
      "v_pk_mul_f16 v97, v129, v161\n"
      "v_pk_mul_f16 v98, v130, v162\n"
      "v_pk_mul_f16 v99, v131, v163\n"
      "s_branch 31b\n"
      "76:\n"
      "s_add_u32 s44, s58, 127\n"
      "s_lshl_b32 s44, s44, 23\n"
      "s_waitcnt vmcnt(0) lgkmcnt(0)\n"
      "s_nop 7\n"
      "v_lshlrev_b32 v176, 16, v120\n"
      "v_and_b32 v177, s43, v120\n"
      "v_mul_f32 v176, s44, v176\n"
      "v_mul_f32 v177, s44, v177\n"
      "v_cvt_pk_bf16_f32 v120, v176, v177\n"
      "v_lshlrev_b32 v176, 16, v121\n"
      "v_and_b32 v177, s43, v121\n"
      "v_mul_f32 v176, s44, v176\n"
      "v_mul_f32 v177, s44, v177\n"
      "v_cvt_pk_bf16_f32 v121, v176, v177\n"
      "v_lshlrev_b32 v176, 16, v122\n"
      "v_and_b32 v177, s43, v122\n"
      "v_mul_f32 v176, s44, v176\n"
      "v_mul_f32 v177, s44, v177\n"
      "v_cvt_pk_bf16_f32 v122, v176, v177\n"
      "v_lshlrev_b32 v176, 16, v123\n"
      "v_and_b32 v177, s43, v123\n"
      "v_mul_f32 v176, s44, v176\n"
      "v_mul_f32 v177, s44, v177\n"
      "v_cvt_pk_bf16_f32 v123, v176, v177\n"
      "v_lshlrev_b32 v176, 16, v124\n"
      "v_and_b32 v177, s43, v124\n"
      "v_mul_f32 v176, s44, v176\n"
      "v_mul_f32 v177, s44, v177\n"
      "v_cvt_pk_bf16_f32 v124, v176, v177\n"
      "v_lshlrev_b32 v176, 16, v125\n"
      "v_and_b32 v177, s43, v125\n"
      "v_mul_f32 v176, s44, v176\n"
      "v_mul_f32 v177, s44, v177\n"
      "v_cvt_pk_bf16_f32 v125, v176, v177\n"
      "v_lshlrev_b32 v176, 16, v126\n"
      "v_and_b32 v177, s43, v126\n"
      "v_mul_f32 v176, s44, v176\n"
      "v_mul_f32 v177, s44, v177\n"
      "v_cvt_pk_bf16_f32 v126, v176, v177\n"
      "v_lshlrev_b32 v176, 16, v127\n"
      "v_and_b32 v177, s43, v127\n"
      "v_mul_f32 v176, s44, v176\n"
      "v_mul_f32 v177, s44, v177\n"
      "v_cvt_pk_bf16_f32 v127, v176, v177\n"
      "s_nop 1\n"
      "s_branch 75b\n"
      "6:\n"
      "s_waitcnt vmcnt(0) lgkmcnt(0)\n"
      "s_nop 7\n"
      "s_mov_b32 s55, 1\n"
      "s_or_b32 s52, s52, 0x7f000002\n"
      "s_mov_b32 s60, 1\n"
      "v_max_f32_dpp v226, v226, v226 row_ror:8 row_mask:0xf bank_mask:0xf\n"
      "s_nop 1\n"
      "v_max_f32_dpp v226, v226, v226 row_ror:4 row_mask:0xf bank_mask:0xf\n"
      "s_nop 1\n"
      "v_max_f32_dpp v226, v226, v226 row_ror:2 row_mask:0xf bank_mask:0xf\n"
      "s_nop 1\n"
      "v_max_f32_dpp v226, v226, v226 row_ror:1 row_mask:0xf bank_mask:0xf\n"
      "s_nop 0\n"
      "v_readlane_b32 s44, v226, 0\n"
      "v_readlane_b32 s45, v226, 16\n"
      "v_readlane_b32 s46, v226, 32\n"
      "v_readlane_b32 s47, v226, 48\n"
      "s_max_u32 s44, s44, s45\n"
      "s_max_u32 s46, s46, s47\n"
      "s_max_u32 s44, s44, s46\n"
      "s_lshr_b32 s44, s44, 23\n"
      "s_sub_i32 s40, 135, s44\n"
      "s_cmp_lg_u32 s41, 0\n"
      "s_cbranch_scc1 61f\n"
      "s_cmp_ge_u32 s44, 2\n"
      "s_cbranch_scc1 62f\n"
      "61:\n"
      "s_mov_b32 s41, 1\n"
      "s_mov_b32 s45, 0x7fff7fff\n"
      "v_mov_b32 v226, 0\n"
      "v_and_b32 v176, s45, v228\n"
      "v_pk_max_u16 v226, v226, v176\n"
      "v_and_b32 v176, s45, v229\n"
      "v_pk_max_u16 v226, v226, v176\n"
      "v_and_b32 v176, s45, v230\n"
      "v_pk_max_u16 v226, v226, v176\n"
      "v_and_b32 v176, s45, v231\n"
      "v_pk_max_u16 v226, v226, v176\n"
      "v_and_b32 v176, s45, v232\n"
      "v_pk_max_u16 v226, v226, v176\n"
      "v_and_b32 v176, s45, v233\n"
      "v_pk_max_u16 v226, v226, v176\n"
      "v_and_b32 v176, s45, v234\n"
      "v_pk_max_u16 v226, v226, v176\n"
      "v_and_b32 v176, s45, v235\n"
      "v_pk_max_u16 v226, v226, v176\n"
      "v_and_b32 v176, s45, v236\n"
      "v_pk_max_u16 v226, v226, v176\n"
      "v_and_b32 v176, s45, v237\n"
      "v_pk_max_u16 v226, v226, v176\n"
      "v_and_b32 v176, s45, v238\n"
      "v_pk_max_u16 v226, v226, v176\n"
      "v_and_b32 v176, s45, v239\n"
      "v_pk_max_u16 v226, v226, v176\n"
      "v_and_b32 v176, s45, v240\n"
      "v_pk_max_u16 v226, v226, v176\n"
      "v_and_b32 v176, s45, v241\n"
      "v_pk_max_u16 v226, v226, v176\n"
      "v_and_b32 v176, s45, v242\n"
      "v_pk_max_u16 v226, v226, v176\n"
      "v_and_b32 v176, s45, v243\n"
      "v_pk_max_u16 v226, v226, v176\n"
      "v_and_b32 v176, s45, v244\n"
      "v_pk_max_u16 v226, v226, v176\n"
      "v_and_b32 v176, s45, v245\n"
      "v_pk_max_u16 v226, v226, v176\n"
      "v_and_b32 v176, s45, v246\n"
      "v_pk_max_u16 v226, v226, v176\n"
      "v_and_b32 v176, s45, v247\n"
      "v_pk_max_u16 v226, v226, v176\n"
      "v_and_b32 v176, s45, v248\n"
      "v_pk_max_u16 v226, v226, v176\n"
      "v_and_b32 v176, s45, v249\n"
      "v_pk_max_u16 v226, v226, v176\n"
      "v_and_b32 v176, s45, v250\n"
      "v_pk_max_u16 v226, v226, v176\n"
      "v_and_b32 v176, s45, v251\n"
      "v_pk_max_u16 v226, v226, v176\n"
      "v_and_b32 v176, s45, v252\n"
      "v_pk_max_u16 v226, v226, v176\n"
      "v_and_b32 v176, s45, v253\n"
      "v_pk_max_u16 v226, v226, v176\n"
      "v_and_b32 v176, s45, v254\n"
      "v_pk_max_u16 v226, v226, v176\n"
      "v_and_b32 v176, s45, v255\n"
      "v_pk_max_u16 v226, v226, v176\n"
      "v_lshlrev_b32 v176, 16, v226\n"
      "v_max_u32 v226, v226, v176\n"
      "s_nop 1\n"
      "v_max_u32_dpp v226, v226, v226 row_ror:8 row_mask:0xf bank_mask:0xf\n"
      "s_nop 1\n"
      "v_max_u32_dpp v226, v226, v226 row_ror:4 row_mask:0xf bank_mask:0xf\n"
      "s_nop 1\n"
      "v_max_u32_dpp v226, v226, v226 row_ror:2 row_mask:0xf bank_mask:0xf\n"
      "s_nop 1\n"
      "v_max_u32_dpp v226, v226, v226 row_ror:1 row_mask:0xf bank_mask:0xf\n"
      "s_nop 0\n"
      "v_readlane_b32 s44, v226, 0\n"
      "v_readlane_b32 s45, v226, 16\n"
      "v_readlane_b32 s46, v226, 32\n"
      "v_readlane_b32 s47, v226, 48\n"
      "s_max_u32 s44, s44, s45\n"
      "s_max_u32 s46, s46, s47\n"
      "s_max_u32 s44, s44, s46\n"
      "s_lshr_b32 s44, s44, 23\n"
      "s_sub_i32 s40, 136, s44\n"
      "s_cmp_eq_u32 s44, 0\n"
      "s_cselect_b32 s40, 0, s40\n"
      "62:\n"
      "s_add_i32 s45, s40, 32\n"
      "s_cmp_gt_u32 s45, 80\n"
      "s_cselect_b32 s40, 0, s40\n"
      "s_cselect_b32 s46, 0x7fc00000, 0\n"
      "s_cselect_b32 s41, 1, s41\n"
      "s_add_u32 s39, s40, 127\n"
      "s_lshl_b32 s39, s39, 23\n"
      "s_lshl_b32 s44, s40, 1\n"
      "s_sub_u32 s45, 127, s44\n"
      "s_add_u32 s44, s44, 127\n"
      "s_lshl_b32 s44, s44, 23\n"
      "s_lshl_b32 s45, s45, 23\n"
      "s_or_b32 s44, s44, s46\n"
      "v_mul_f32 v16, s44, v16\n"
      "v_mul_f32 v48, s45, v48\n"
      "v_mul_f32 v17, s44, v17\n"
      "v_mul_f32 v49, s45, v49\n"
      "v_mul_f32 v18, s44, v18\n"
      "v_mul_f32 v50, s45, v50\n"
      "v_mul_f32 v19, s44, v19\n"
      "v_mul_f32 v51, s45, v51\n"
      "v_mul_f32 v20, s44, v20\n"
      "v_mul_f32 v52, s45, v52\n"
      "v_mul_f32 v21, s44, v21\n"
      "v_mul_f32 v53, s45, v53\n"
      "v_mul_f32 v22, s44, v22\n"
      "v_mul_f32 v54, s45, v54\n"
      "v_mul_f32 v23, s44, v23\n"
      "v_mul_f32 v55, s45, v55\n"
      "v_mul_f32 v24, s44, v24\n"
      "v_mul_f32 v56, s45, v56\n"
      "v_mul_f32 v25, s44, v25\n"
      "v_mul_f32 v57, s45, v57\n"
      "v_mul_f32 v26, s44, v26\n"
      "v_mul_f32 v58, s45, v58\n"
      "v_mul_f32 v27, s44, v27\n"
      "v_mul_f32 v59, s45, v59\n"
      "v_mul_f32 v28, s44, v28\n"
      "v_mul_f32 v60, s45, v60\n"
      "v_mul_f32 v29, s44, v29\n"
      "v_mul_f32 v61, s45, v61\n"
      "v_mul_f32 v30, s44, v30\n"
      "v_mul_f32 v62, s45, v62\n"
      "v_mul_f32 v31, s44, v31\n"
      "v_mul_f32 v63, s45, v63\n"
      "v_mul_f32 v32, s44, v32\n"
      "v_mul_f32 v64, s45, v64\n"
      "v_mul_f32 v33, s44, v33\n"
      "v_mul_f32 v65, s45, v65\n"
      "v_mul_f32 v34, s44, v34\n"
      "v_mul_f32 v66, s45, v66\n"
      "v_mul_f32 v35, s44, v35\n"
      "v_mul_f32 v67, s45, v67\n"
      "v_mul_f32 v36, s44, v36\n"
      "v_mul_f32 v68, s45, v68\n"
      "v_mul_f32 v37, s44, v37\n"
      "v_mul_f32 v69, s45, v69\n"
      "v_mul_f32 v38, s44, v38\n"
      "v_mul_f32 v70, s45, v70\n"
      "v_mul_f32 v39, s44, v39\n"
      "v_mul_f32 v71, s45, v71\n"
      "v_mul_f32 v40, s44, v40\n"
      "v_mul_f32 v72, s45, v72\n"
      "v_mul_f32 v41, s44, v41\n"
      "v_mul_f32 v73, s45, v73\n"
      "v_mul_f32 v42, s44, v42\n"
      "v_mul_f32 v74, s45, v74\n"
      "v_mul_f32 v43, s44, v43\n"
      "v_mul_f32 v75, s45, v75\n"
      "v_mul_f32 v44, s44, v44\n"
      "v_mul_f32 v76, s45, v76\n"
      "v_mul_f32 v45, s44, v45\n"
      "v_mul_f32 v77, s45, v77\n"
      "v_mul_f32 v46, s44, v46\n"
      "v_mul_f32 v78, s45, v78\n"
      "v_mul_f32 v47, s44, v47\n"
      "v_mul_f32 v79, s45, v79\n"
      "v_mov_b32 v178, s39\n"
      "v_lshlrev_b32 v176, 16, v228\n"
      "v_and_b32 v177, s43, v228\n"
      "v_fmaak_f32 v176, v178, v176, 0x12800000\n"
      "v_fmaak_f32 v177, v178, v177, 0x12800000\n"
      "v_add_f32 v176, 0x92800000, v176\n"
      "v_add_f32 v177, 0x92800000, v177\n"
      "v_cvt_pk_bf16_f32 v228, v176, v177\n"
      "v_lshlrev_b32 v176, 16, v229\n"
      "v_and_b32 v177, s43, v229\n"
      "v_fmaak_f32 v176, v178, v176, 0x12800000\n"
      "v_fmaak_f32 v177, v178, v177, 0x12800000\n"
      "v_add_f32 v176, 0x92800000, v176\n"
      "v_add_f32 v177, 0x92800000, v177\n"
      "v_cvt_pk_bf16_f32 v229, v176, v177\n"
      "v_lshlrev_b32 v176, 16, v230\n"
      "v_and_b32 v177, s43, v230\n"
      "v_fmaak_f32 v176, v178, v176, 0x12800000\n"
      "v_fmaak_f32 v177, v178, v177, 0x12800000\n"
      "v_add_f32 v176, 0x92800000, v176\n"
      "v_add_f32 v177, 0x92800000, v177\n"
      "v_cvt_pk_bf16_f32 v230, v176, v177\n"
      "v_lshlrev_b32 v176, 16, v231\n"
      "v_and_b32 v177, s43, v231\n"
      "v_fmaak_f32 v176, v178, v176, 0x12800000\n"
      "v_fmaak_f32 v177, v178, v177, 0x12800000\n"
      "v_add_f32 v176, 0x92800000, v176\n"
      "v_add_f32 v177, 0x92800000, v177\n"
      "v_cvt_pk_bf16_f32 v231, v176, v177\n"
      "v_lshlrev_b32 v176, 16, v232\n"
      "v_and_b32 v177, s43, v232\n"
      "v_fmaak_f32 v176, v178, v176, 0x12800000\n"
      "v_fmaak_f32 v177, v178, v177, 0x12800000\n"
      "v_add_f32 v176, 0x92800000, v176\n"
      "v_add_f32 v177, 0x92800000, v177\n"
      "v_cvt_pk_bf16_f32 v232, v176, v177\n"
      "v_lshlrev_b32 v176, 16, v233\n"
      "v_and_b32 v177, s43, v233\n"
      "v_fmaak_f32 v176, v178, v176, 0x12800000\n"
      "v_fmaak_f32 v177, v178, v177, 0x12800000\n"
      "v_add_f32 v176, 0x92800000, v176\n"
      "v_add_f32 v177, 0x92800000, v177\n"
      "v_cvt_pk_bf16_f32 v233, v176, v177\n"
      "v_lshlrev_b32 v176, 16, v234\n"
      "v_and_b32 v177, s43, v234\n"
      "v_fmaak_f32 v176, v178, v176, 0x12800000\n"
      "v_fmaak_f32 v177, v178, v177, 0x12800000\n"
      "v_add_f32 v176, 0x92800000, v176\n"
      "v_add_f32 v177, 0x92800000, v177\n"
      "v_cvt_pk_bf16_f32 v234, v176, v177\n"
      "v_lshlrev_b32 v176, 16, v235\n"
      "v_and_b32 v177, s43, v235\n"
      "v_fmaak_f32 v176, v178, v176, 0x12800000\n"
      "v_fmaak_f32 v177, v178, v177, 0x12800000\n"
      "v_add_f32 v176, 0x92800000, v176\n"
      "v_add_f32 v177, 0x92800000, v177\n"
      "v_cvt_pk_bf16_f32 v235, v176, v177\n"
      "v_lshlrev_b32 v176, 16, v236\n"
      "v_and_b32 v177, s43, v236\n"
      "v_fmaak_f32 v176, v178, v176, 0x12800000\n"
      "v_fmaak_f32 v177, v178, v177, 0x12800000\n"
      "v_add_f32 v176, 0x92800000, v176\n"
      "v_add_f32 v177, 0x92800000, v177\n"
      "v_cvt_pk_bf16_f32 v236, v176, v177\n"
      "v_lshlrev_b32 v176, 16, v237\n"
      "v_and_b32 v177, s43, v237\n"
      "v_fmaak_f32 v176, v178, v176, 0x12800000\n"
      "v_fmaak_f32 v177, v178, v177, 0x12800000\n"
      "v_add_f32 v176, 0x92800000, v176\n"
      "v_add_f32 v177, 0x92800000, v177\n"
      "v_cvt_pk_bf16_f32 v237, v176, v177\n"
      "v_lshlrev_b32 v176, 16, v238\n"
      "v_and_b32 v177, s43, v238\n"
      "v_fmaak_f32 v176, v178, v176, 0x12800000\n"
      "v_fmaak_f32 v177, v178, v177, 0x12800000\n"
      "v_add_f32 v176, 0x92800000, v176\n"
      "v_add_f32 v177, 0x92800000, v177\n"
      "v_cvt_pk_bf16_f32 v238, v176, v177\n"
      "v_lshlrev_b32 v176, 16, v239\n"
      "v_and_b32 v177, s43, v239\n"
      "v_fmaak_f32 v176, v178, v176, 0x12800000\n"
      "v_fmaak_f32 v177, v178, v177, 0x12800000\n"
      "v_add_f32 v176, 0x92800000, v176\n"
      "v_add_f32 v177, 0x92800000, v177\n"
      "v_cvt_pk_bf16_f32 v239, v176, v177\n"
      "v_lshlrev_b32 v176, 16, v240\n"
      "v_and_b32 v177, s43, v240\n"
      "v_fmaak_f32 v176, v178, v176, 0x12800000\n"
      "v_fmaak_f32 v177, v178, v177, 0x12800000\n"
      "v_add_f32 v176, 0x92800000, v176\n"
      "v_add_f32 v177, 0x92800000, v177\n"
      "v_cvt_pk_bf16_f32 v240, v176, v177\n"
      "v_lshlrev_b32 v176, 16, v241\n"
      "v_and_b32 v177, s43, v241\n"
      "v_fmaak_f32 v176, v178, v176, 0x12800000\n"
      "v_fmaak_f32 v177, v178, v177, 0x12800000\n"
      "v_add_f32 v176, 0x92800000, v176\n"
      "v_add_f32 v177, 0x92800000, v177\n"
      "v_cvt_pk_bf16_f32 v241, v176, v177\n"
      "v_lshlrev_b32 v176, 16, v242\n"
      "v_and_b32 v177, s43, v242\n"
      "v_fmaak_f32 v176, v178, v176, 0x12800000\n"
      "v_fmaak_f32 v177, v178, v177, 0x12800000\n"
      "v_add_f32 v176, 0x92800000, v176\n"
      "v_add_f32 v177, 0x92800000, v177\n"
      "v_cvt_pk_bf16_f32 v242, v176, v177\n"
      "v_lshlrev_b32 v176, 16, v243\n"
      "v_and_b32 v177, s43, v243\n"
      "v_fmaak_f32 v176, v178, v176, 0x12800000\n"
      "v_fmaak_f32 v177, v178, v177, 0x12800000\n"
      "v_add_f32 v176, 0x92800000, v176\n"
      "v_add_f32 v177, 0x92800000, v177\n"
      "v_cvt_pk_bf16_f32 v243, v176, v177\n"
      "v_lshlrev_b32 v176, 16, v244\n"
      "v_and_b32 v177, s43, v244\n"
      "v_fmaak_f32 v176, v178, v176, 0x12800000\n"
      "v_fmaak_f32 v177, v178, v177, 0x12800000\n"
      "v_add_f32 v176, 0x92800000, v176\n"
      "v_add_f32 v177, 0x92800000, v177\n"
      "v_cvt_pk_bf16_f32 v244, v176, v177\n"
      "v_lshlrev_b32 v176, 16, v245\n"
      "v_and_b32 v177, s43, v245\n"
      "v_fmaak_f32 v176, v178, v176, 0x12800000\n"
      "v_fmaak_f32 v177, v178, v177, 0x12800000\n"
      "v_add_f32 v176, 0x92800000, v176\n"
      "v_add_f32 v177, 0x92800000, v177\n"
      "v_cvt_pk_bf16_f32 v245, v176, v177\n"
      "v_lshlrev_b32 v176, 16, v246\n"
      "v_and_b32 v177, s43, v246\n"
      "v_fmaak_f32 v176, v178, v176, 0x12800000\n"
      "v_fmaak_f32 v177, v178, v177, 0x12800000\n"
      "v_add_f32 v176, 0x92800000, v176\n"
      "v_add_f32 v177, 0x92800000, v177\n"
      "v_cvt_pk_bf16_f32 v246, v176, v177\n"
      "v_lshlrev_b32 v176, 16, v247\n"
      "v_and_b32 v177, s43, v247\n"
      "v_fmaak_f32 v176, v178, v176, 0x12800000\n"
      "v_fmaak_f32 v177, v178, v177, 0x12800000\n"
      "v_add_f32 v176, 0x92800000, v176\n"
      "v_add_f32 v177, 0x92800000, v177\n"
      "v_cvt_pk_bf16_f32 v247, v176, v177\n"
      "v_lshlrev_b32 v176, 16, v248\n"
      "v_and_b32 v177, s43, v248\n"
      "v_fmaak_f32 v176, v178, v176, 0x12800000\n"
      "v_fmaak_f32 v177, v178, v177, 0x12800000\n"
      "v_add_f32 v176, 0x92800000, v176\n"
      "v_add_f32 v177, 0x92800000, v177\n"
      "v_cvt_pk_bf16_f32 v248, v176, v177\n"
      "v_lshlrev_b32 v176, 16, v249\n"
      "v_and_b32 v177, s43, v249\n"
      "v_fmaak_f32 v176, v178, v176, 0x12800000\n"
      "v_fmaak_f32 v177, v178, v177, 0x12800000\n"
      "v_add_f32 v176, 0x92800000, v176\n"
      "v_add_f32 v177, 0x92800000, v177\n"
      "v_cvt_pk_bf16_f32 v249, v176, v177\n"
      "v_lshlrev_b32 v176, 16, v250\n"
      "v_and_b32 v177, s43, v250\n"
      "v_fmaak_f32 v176, v178, v176, 0x12800000\n"
      "v_fmaak_f32 v177, v178, v177, 0x12800000\n"
      "v_add_f32 v176, 0x92800000, v176\n"
      "v_add_f32 v177, 0x92800000, v177\n"
      "v_cvt_pk_bf16_f32 v250, v176, v177\n"
      "v_lshlrev_b32 v176, 16, v251\n"
      "v_and_b32 v177, s43, v251\n"
      "v_fmaak_f32 v176, v178, v176, 0x12800000\n"
      "v_fmaak_f32 v177, v178, v177, 0x12800000\n"
      "v_add_f32 v176, 0x92800000, v176\n"
      "v_add_f32 v177, 0x92800000, v177\n"
      "v_cvt_pk_bf16_f32 v251, v176, v177\n"
      "v_lshlrev_b32 v176, 16, v252\n"
      "v_and_b32 v177, s43, v252\n"
      "v_fmaak_f32 v176, v178, v176, 0x12800000\n"
      "v_fmaak_f32 v177, v178, v177, 0x12800000\n"
      "v_add_f32 v176, 0x92800000, v176\n"
      "v_add_f32 v177, 0x92800000, v177\n"
      "v_cvt_pk_bf16_f32 v252, v176, v177\n"
      "v_lshlrev_b32 v176, 16, v253\n"
      "v_and_b32 v177, s43, v253\n"
      "v_fmaak_f32 v176, v178, v176, 0x12800000\n"
      "v_fmaak_f32 v177, v178, v177, 0x12800000\n"
      "v_add_f32 v176, 0x92800000, v176\n"
      "v_add_f32 v177, 0x92800000, v177\n"
      "v_cvt_pk_bf16_f32 v253, v176, v177\n"
      "v_lshlrev_b32 v176, 16, v254\n"
      "v_and_b32 v177, s43, v254\n"
      "v_fmaak_f32 v176, v178, v176, 0x12800000\n"
      "v_fmaak_f32 v177, v178, v177, 0x12800000\n"
      "v_add_f32 v176, 0x92800000, v176\n"
      "v_add_f32 v177, 0x92800000, v177\n"
      "v_cvt_pk_bf16_f32 v254, v176, v177\n"
      "v_lshlrev_b32 v176, 16, v255\n"
      "v_and_b32 v177, s43, v255\n"
      "v_fmaak_f32 v176, v178, v176, 0x12800000\n"
      "v_fmaak_f32 v177, v178, v177, 0x12800000\n"
      "v_add_f32 v176, 0x92800000, v176\n"
      "v_add_f32 v177, 0x92800000, v177\n"
      "v_cvt_pk_bf16_f32 v255, v176, v177\n"
      "v_accvgpr_write_b32 a216, v244\n"
      "v_accvgpr_write_b32 a217, v245\n"
      "v_accvgpr_write_b32 a218, v246\n"
      "v_accvgpr_write_b32 a219, v247\n"
      "v_accvgpr_write_b32 a220, v248\n"
      "v_accvgpr_write_b32 a221, v249\n"
      "v_accvgpr_write_b32 a222, v250\n"
      "v_accvgpr_write_b32 a223, v251\n"
      "v_accvgpr_write_b32 a224, v252\n"
      "v_accvgpr_write_b32 a225, v253\n"
      "v_accvgpr_write_b32 a226, v254\n"
      "v_accvgpr_write_b32 a227, v255\n"
      "s_branch 3b\n"
      "7:\n"
      "s_mov_b64 exec, -1\n"
      "s_cmp_lg_u32 s41, 0\n"
      "s_cbranch_scc1 8f\n"
      "s_mov_b32 s41, 1\n"
      "s_waitcnt vmcnt(0)\n"
      "s_branch 0b\n"
      "17:\n"
      "v_mfma_f32_16x16x32_f16 v[180:183], a[0:3], a[216:219], v[16:19]\n"
      "v_mfma_f32_16x16x32_f16 v[180:183], a[4:7], a[220:223], v[180:183]\n"
      "v_mfma_f32_16x16x32_f16 v[180:183], a[8:11], a[224:227], v[180:183]\n"
      "v_mfma_f32_16x16x32_f16 v[180:183], a[12:15], a[228:231], v[180:183]\n"
      "s_nop 7\n"
      "v_mul_f32 v184, |v180|, v48\n"
      "v_fma_f32 v184, |v181|, v49, v184\n"
      "v_fma_f32 v184, |v182|, v50, v184\n"
      "v_fma_f32 v184, |v183|, v51, v184\n"
      "v_mfma_f32_16x16x32_f16 v[180:183], a[76:79], a[228:231], v[32:35]\n"
      "v_mfma_f32_16x16x32_f16 v[180:183], a[72:75], a[224:227], v[180:183]\n"
      "v_mfma_f32_16x16x32_f16 v[180:183], a[68:71], a[220:223], v[180:183]\n"
      "v_mfma_f32_16x16x32_f16 v[180:183], a[64:67], a[216:219], v[180:183]\n"
      "s_nop 7\n"
      "v_fma_f32 v184, |v180|, v64, v184\n"
      "v_fma_f32 v184, |v181|, v65, v184\n"
      "v_fma_f32 v184, |v182|, v66, v184\n"
      "v_fma_f32 v184, |v183|, v67, v184\n"
      "v_mfma_f32_16x16x32_f16 v[180:183], a[16:19], a[216:219], v[20:23]\n"
      "v_mfma_f32_16x16x32_f16 v[180:183], a[20:23], a[220:223], v[180:183]\n"
      "v_mfma_f32_16x16x32_f16 v[180:183], a[24:27], a[224:227], v[180:183]\n"
      "v_mfma_f32_16x16x32_f16 v[180:183], a[28:31], a[228:231], v[180:183]\n"
      "s_nop 7\n"
      "v_mul_f32 v185, |v180|, v52\n"
      "v_fma_f32 v185, |v181|, v53, v185\n"
      "v_fma_f32 v185, |v182|, v54, v185\n"
      "v_fma_f32 v185, |v183|, v55, v185\n"
      "v_mfma_f32_16x16x32_f16 v[180:183], a[92:95], a[228:231], v[36:39]\n"
      "v_mfma_f32_16x16x32_f16 v[180:183], a[88:91], a[224:227], v[180:183]\n"
      "v_mfma_f32_16x16x32_f16 v[180:183], a[84:87], a[220:223], v[180:183]\n"
      "v_mfma_f32_16x16x32_f16 v[180:183], a[80:83], a[216:219], v[180:183]\n"
      "s_nop 7\n"
      "v_fma_f32 v185, |v180|, v68, v185\n"
      "v_fma_f32 v185, |v181|, v69, v185\n"
      "v_fma_f32 v185, |v182|, v70, v185\n"
      "v_fma_f32 v185, |v183|, v71, v185\n"
      "v_mfma_f32_16x16x32_f16 v[180:183], a[32:35], a[216:219], v[24:27]\n"
      "v_mfma_f32_16x16x32_f16 v[180:183], a[36:39], a[220:223], v[180:183]\n"
      "v_mfma_f32_16x16x32_f16 v[180:183], a[40:43], a[224:227], v[180:183]\n"
      "v_mfma_f32_16x16x32_f16 v[180:183], a[44:47], a[228:231], v[180:183]\n"
      "s_nop 7\n"
      "v_mul_f32 v186, |v180|, v56\n"
      "v_fma_f32 v186, |v181|, v57, v186\n"
      "v_fma_f32 v186, |v182|, v58, v186\n"
      "v_fma_f32 v186, |v183|, v59, v186\n"
      "v_mfma_f32_16x16x32_f16 v[180:183], a[108:111], a[228:231], v[40:43]\n"
      "v_mfma_f32_16x16x32_f16 v[180:183], a[104:107], a[224:227], v[180:183]\n"
      "v_mfma_f32_16x16x32_f16 v[180:183], a[100:103], a[220:223], v[180:183]\n"
      "v_mfma_f32_16x16x32_f16 v[180:183], a[96:99], a[216:219], v[180:183]\n"
      "s_nop 7\n"
      "v_fma_f32 v186, |v180|, v72, v186\n"
      "v_fma_f32 v186, |v181|, v73, v186\n"
      "v_fma_f32 v186, |v182|, v74, v186\n"
      "v_fma_f32 v186, |v183|, v75, v186\n"
      "v_mfma_f32_16x16x32_f16 v[180:183], a[48:51], a[216:219], v[28:31]\n"
      "v_mfma_f32_16x16x32_f16 v[180:183], a[52:55], a[220:223], v[180:183]\n"
      "v_mfma_f32_16x16x32_f16 v[180:183], a[56:59], a[224:227], v[180:183]\n"
      "v_mfma_f32_16x16x32_f16 v[180:183], a[60:63], a[228:231], v[180:183]\n"
      "s_nop 7\n"
      "v_mul_f32 v187, |v180|, v60\n"
      "v_fma_f32 v187, |v181|, v61, v187\n"
      "v_fma_f32 v187, |v182|, v62, v187\n"
      "v_fma_f32 v187, |v183|, v63, v187\n"
      "v_mfma_f32_16x16x32_f16 v[180:183], a[124:127], a[228:231], v[44:47]\n"
      "v_mfma_f32_16x16x32_f16 v[180:183], a[120:123], a[224:227], v[180:183]\n"
      "v_mfma_f32_16x16x32_f16 v[180:183], a[116:119], a[220:223], v[180:183]\n"
      "v_mfma_f32_16x16x32_f16 v[180:183], a[112:115], a[216:219], v[180:183]\n"
      "s_nop 7\n"
      "v_fma_f32 v187, |v180|, v76, v187\n"
      "v_fma_f32 v187, |v181|, v77, v187\n"
      "v_fma_f32 v187, |v182|, v78, v187\n"
      "v_fma_f32 v187, |v183|, v79, v187\n"
      "v_pk_add_f32 v[184:185], v[184:185], v[186:187]\n"
      "v_add_f32 v199, v184, v185\n"
      "s_nop 1\n"
      "s_branch 18b\n"
      "19:\n"
      "s_mov_b64 exec, -1\n"
      "v_max3_f32 v224, v188, v199, v196\n"
      "v_max_f32 v224, 0, v224\n"
      "s_nop 1\n"
      "v_max_f32_dpp v225, v224, v224 row_ror:4 row_mask:0xf bank_mask:0xf\n"
      "v_max_f32_dpp v225, v224, v225 row_ror:8 row_mask:0xf bank_mask:0xf\n"
      "v_max_f32_dpp v225, v224, v225 row_ror:12 row_mask:0xf bank_mask:0xf\n"
      "s_nop 1\n"
      "v_max_f32_dpp v224, v225, v225 row_ror:1 row_mask:0xf bank_mask:0xf\n"
      "v_max_f32_dpp v224, v225, v224 row_ror:2 row_mask:0xf bank_mask:0xf\n"
      "v_max_f32_dpp v224, v225, v224 row_ror:3 row_mask:0xf bank_mask:0xf\n"
      "s_nop 1\n"
      "v_max_f32_dpp v224, v224, v224 row_bcast:15 row_mask:0xa bank_mask:0xf\n"
      "s_nop 1\n"
      "v_max_f32_dpp v224, v224, v224 row_bcast:31 row_mask:0xc bank_mask:0xf\n"
      "s_nop 0\n"
      "v_readlane_b32 s44, v224, 63\n"
      "s_nop 1\n"
      "v_exp_f32 v222, -s44\n"
      "s_nop 0\n"
      "v_mul_f32 v214, v220, v222\n"
      "v_mul_f32 v226, v221, v222\n"
      "v_subrev_f32 v188, s44, v188\n"
      "v_exp_f32 v222, v188\n"
      "v_fmac_f32 v214, 0, v188\n"
      "v_add_f32 v214, v214, v222\n"
      "v_fmac_f32 v226, v222, v235\n"
      "v_subrev_f32 v199, s44, v199\n"
      "v_exp_f32 v222, v199\n"
      "v_fmac_f32 v214, 0, v199\n"
      "v_add_f32 v214, v214, v222\n"
      "v_fmac_f32_dpp v226, v181, v222 quad_perm:[0,1,2,3] row_mask:0x1 bank_mask:0x3\n"
      "v_subrev_f32 v196, s44, v196\n"
      "v_exp_f32 v222, v196\n"
      "v_fmac_f32 v214, 0, v196\n"
      "v_add_f32 v214, v214, v222\n"
      "v_fmac_f32 v226, v222, v237\n"
      "s_waitcnt vmcnt(0)\n"
      "v_add_f32_dpp v214, v214, v214 row_ror:8 row_mask:0xf bank_mask:0xf\n"
      "v_add_f32_dpp v226, v226, v226 row_ror:8 row_mask:0xf bank_mask:0xf\n"
      "s_nop 0\n"
      "v_add_f32_dpp v214, v214, v214 row_ror:4 row_mask:0xf bank_mask:0xf\n"
      "v_add_f32_dpp v226, v226, v226 row_ror:4 row_mask:0xf bank_mask:0xf\n"
      "s_nop 0\n"
      "v_add_f32_dpp v214, v214, v214 row_ror:2 row_mask:0xf bank_mask:0xf\n"
      "v_add_f32_dpp v226, v226, v226 row_ror:2 row_mask:0xf bank_mask:0xf\n"
      "s_nop 0\n"
      "v_add_f32_dpp v214, v214, v214 row_ror:1 row_mask:0xf bank_mask:0xf\n"
      "v_add_f32_dpp v226, v226, v226 row_ror:1 row_mask:0xf bank_mask:0xf\n"
      "s_nop 0\n"
      "v_add_f32_dpp v214, v214, v214 row_bcast:15 row_mask:0xa bank_mask:0xf\n"
      "v_add_f32_dpp v226, v226, v226 row_bcast:15 row_mask:0xa bank_mask:0xf\n"
      "s_nop 0\n"
      "v_add_f32_dpp v214, v214, v214 row_bcast:31 row_mask:0xc bank_mask:0xf\n"
      "v_add_f32_dpp v226, v226, v226 row_bcast:31 row_mask:0xc bank_mask:0xf\n"
      "v_rcp_f32 v222, v214\n"
      "v_ldexp_f32 v226, v226, s39\n"
      "s_lshl_b64 exec, 1, 63\n"
      "v_mul_f32 v226, v226, v222\n"
      "v_cvt_pk_bf16_f32 v222, v226, v226\n"
      "global_store_short v3, v222, s[24:25]\n"
      "v_cmp_class_f32 vcc, v226, s45\n"
      "s_nop 0\n"
      "s_cmp_lt_i32 vcc_hi, 0\n"
      "s_cbranch_scc1 7b\n"
      "s_branch 20b\n"
      "8:\n"
      "s_mov_b32 s42, 1\n"
      "9:\n"
      "s_nop 7\n"
      "s_waitcnt vmcnt(0) lgkmcnt(0)\n"
      "s_mov_b32 %0, s42\n"
      : "=s"(flag)
      : "s"(__builtin_amdgcn_kernarg_segment_ptr()), "s"(blockIdx.x), "v"(threadIdx.x), "s"(lbase)
      :
      "v1", "v2", "v3", "v4", "v5", "v6", "v7", "v8", "v9", "v10", "v11", "v12", "v13", "v14", "v15", "v16", "v17", "v18", "v19", "v20", "v21", "v22", "v23", "v24",
      "v25", "v26", "v27", "v28", "v29", "v30", "v31", "v32", "v33", "v34", "v35", "v36", "v37", "v38", "v39", "v40", "v41", "v42", "v43", "v44", "v45", "v46", "v47", "v48",
      "v49", "v50", "v51", "v52", "v53", "v54", "v55", "v56", "v57", "v58", "v59", "v60", "v61", "v62", "v63", "v64", "v65", "v66", "v67", "v68", "v69", "v70", "v71", "v72",
      "v73", "v74", "v75", "v76", "v77", "v78", "v79", "v80", "v81", "v82", "v83", "v84", "v85", "v86", "v87", "v88", "v89", "v90", "v91", "v92", "v93", "v94", "v95", "v96",
      "v97", "v98", "v99", "v100", "v101", "v102", "v103", "v104", "v105", "v106", "v107", "v108", "v109", "v110", "v111", "v112", "v113", "v114", "v115", "v116", "v117", "v118", "v119", "v120",
      "v121", "v122", "v123", "v124", "v125", "v126", "v127", "v128", "v129", "v130", "v131", "v132", "v133", "v134", "v135", "v136", "v137", "v138", "v139", "v140", "v141", "v142", "v143", "v144",
      "v145", "v146", "v147", "v148", "v149", "v150", "v151", "v152", "v153", "v154", "v155", "v156", "v157", "v158", "v159", "v160", "v161", "v162", "v163", "v164", "v165", "v166", "v167", "v168",
      "v169", "v170", "v171", "v172", "v173", "v174", "v175", "v176", "v177", "v178", "v179", "v180", "v181", "v182", "v183", "v184", "v185", "v186", "v187", "v188", "v189", "v190", "v191", "v192",
      "v193", "v194", "v195", "v196", "v197", "v198", "v199", "v200", "v201", "v202", "v203", "v204", "v205", "v206", "v207", "v208", "v209", "v210", "v211", "v212", "v213", "v214", "v215", "v216",
      "v217", "v218", "v219", "v220", "v221", "v222", "v223", "v224", "v225", "v226", "v227", "v228", "v229", "v230", "v231", "v232", "v233", "v234", "v235", "v236", "v237", "v238", "v239", "v240",
      "v241", "v242", "v243", "v244", "v245", "v246", "v247", "v248", "v249", "v250", "v251", "v252", "v253", "v254", "v255", "a0", "a1", "a2", "a3", "a4", "a5", "a6", "a7", "a8",
      "a9", "a10", "a11", "a12", "a13", "a14", "a15", "a16", "a17", "a18", "a19", "a20", "a21", "a22", "a23", "a24", "a25", "a26", "a27", "a28", "a29", "a30", "a31", "a32",
      "a33", "a34", "a35", "a36", "a37", "a38", "a39", "a40", "a41", "a42", "a43", "a44", "a45", "a46", "a47", "a48", "a49", "a50", "a51", "a52", "a53", "a54", "a55", "a56",
      "a57", "a58", "a59", "a60", "a61", "a62", "a63", "a64", "a65", "a66", "a67", "a68", "a69", "a70", "a71", "a72", "a73", "a74", "a75", "a76", "a77", "a78", "a79", "a80",
      "a81", "a82", "a83", "a84", "a85", "a86", "a87", "a88", "a89", "a90", "a91", "a92", "a93", "a94", "a95", "a96", "a97", "a98", "a99", "a100", "a101", "a102", "a103", "a104",
      "a105", "a106", "a107", "a108", "a109", "a110", "a111", "a112", "a113", "a114", "a115", "a116", "a117", "a118", "a119", "a120", "a121", "a122", "a123", "a124", "a125", "a126", "a127", "a128",
      "a129", "a130", "a131", "a132", "a133", "a134", "a135", "a136", "a137", "a138", "a139", "a140", "a141", "a142", "a143", "a144", "a145", "a146", "a147", "a148", "a149", "a150", "a151", "a152",
      "a153", "a154", "a155", "a156", "a157", "a158", "a159", "a160", "a161", "a162", "a163", "a164", "a165", "a166", "a167", "a168", "a169", "a170", "a171", "a172", "a173", "a174", "a175", "a176",
      "a177", "a178", "a179", "a180", "a181", "a182", "a183", "a184", "a185", "a186", "a187", "a188", "a189", "a190", "a191", "a192", "a193", "a194", "a195", "a196", "a197", "a198", "a199", "a200",
      "a201", "a202", "a203", "a204", "a205", "a206", "a207", "a208", "a209", "a210", "a211", "a212", "a213", "a214", "a215", "a216", "a217", "a218", "a219", "a220", "a221", "a222", "a223", "a224",
      "a225", "a226", "a227", "a228", "a229", "a230", "a231", "a232", "a233", "a234", "a235", "a236", "a237", "a238", "a239", "a240", "a241", "a242", "a243", "a244", "a245", "a246", "a247", "s8",
      "s9", "s10", "s11", "s12", "s13", "s14", "s15", "s16", "s17", "s18", "s19", "s20", "s21", "s22", "s23", "s24", "s25", "s26", "s27", "s28", "s29", "s30", "s31", "s33",
      "s34", "s35", "s36", "s37", "s38", "s39", "s40", "s41", "s42", "s43", "s44", "s45", "s46", "s47", "s48", "s49", "s50", "s51", "s52", "s53", "s54", "s55", "s56", "s57",
      "s58", "s59", "s60", "s61", "s62", "s63", "s64", "s65", "s66", "s67", "s68", "s69", "s70", "s71", "s72", "s73", "s74", "s75", "s76", "s77", "s78", "s79", "s80", "s81",
      "s82", "s83", "s84", "s85", "s86", "s87", "s88", "s89", "s90", "s91", "s92", "s93", "s94", "s95", "s96", "s97", "s98", "s99", "s100", "s101", "vcc", "scc", "m0", "memory");
  if (flag | lbase) {
    const int F = 26, P = 325;
    const int lane = threadIdx.x & 63;
    const int wg = (int)((blockIdx.x & ~255u) + (blockIdx.x & 7) * 32 + ((blockIdx.x >> 3) & 31));
    const int b = min(wg * 4 + (int)(threadIdx.x >> 6), B - 1);
    {
      float m = -INFINITY, z = 0.f, n = 0.f;
      for (int p = lane; p < P; p += 64) {
        const unsigned short* ei = table + (size_t)idx[b * F + ii[p]] * 128;
        const unsigned short* ej = table + (size_t)idx[b * F + jj[p]] * 128;
        float e = 0.f, s = 0.f;
        for (int a = 0; a < 128; ++a) {
          float acc = 0.f;
          for (int d = 0; d < 128; ++d) acc = fmaf(rbf(bf2f(ei[d]) * bf2f(ej[d])), bf2f(W[d * 128 + a]), acc);
          float x = acc + bf2f(bvec[a]);
          x = 0.5f * (x + fabsf(x));
          e = fmaf(x, bf2f(hvec[a]), e);
        }
        for (int d = 0; d < 128; ++d) s = fmaf(rbf(bf2f(ei[d]) * bf2f(ej[d])), bf2f(pvec[d]), s);
        float mn = fmaxf(m, e);
        float fa = __expf(m - mn), fb = __expf(e - mn);
        z = z * fa + fb; n = n * fa + fb * s; m = mn;
      }
      float mx = m;
      for (int o = 1; o < 64; o <<= 1) mx = fmaxf(mx, __shfl_xor(mx, o));
      float f = (m == -INFINITY) ? 0.f : __expf(m - mx);
      z *= f; n *= f;
      for (int o = 1; o < 64; o <<= 1) { z += __shfl_xor(z, o); n += __shfl_xor(n, o); }
      if (lane == 0) out[b] = f2bf(n / z);
    }
  }
}

__global__ void afm_generic_kernel(const int* __restrict__ idx, const unsigned short* __restrict__ table,
                                   const int* __restrict__ ii, const int* __restrict__ jj,
                                   const unsigned short* __restrict__ W, const unsigned short* __restrict__ bvec,
                                   const unsigned short* __restrict__ hvec, const unsigned short* __restrict__ pvec,
                                   unsigned short* __restrict__ out, float* __restrict__ ws,
                                   int B, int F, int P, int D, int A) {
  const int b = blockIdx.x;
  float* ebuf = ws + (size_t)b * 2 * P;
  float* sbuf = ebuf + P;
  for (int p = threadIdx.x; p < P; p += blockDim.x) {
    const unsigned short* ei = table + (size_t)idx[b * F + ii[p]] * D;
    const unsigned short* ej = table + (size_t)idx[b * F + jj[p]] * D;
    float e = 0.f, sacc = 0.f;
    for (int a = 0; a < A; ++a) {
      float acc = 0.f;
      for (int d = 0; d < D; ++d) acc = fmaf(rbf(bf2f(ei[d]) * bf2f(ej[d])), bf2f(W[(size_t)d * A + a]), acc);
      float x = acc + bf2f(bvec[a]);
      x = 0.5f * (x + fabsf(x));
      e = fmaf(x, bf2f(hvec[a]), e);
    }
    for (int d = 0; d < D; ++d) sacc = fmaf(rbf(bf2f(ei[d]) * bf2f(ej[d])), bf2f(pvec[d]), sacc);
    ebuf[p] = e; sbuf[p] = sacc;
  }
  __syncthreads();
  if (threadIdx.x == 0) {
    float mx = -INFINITY;
    for (int p = 0; p < P; ++p) mx = fmaxf(mx, ebuf[p]);
    float z = 0.f, n = 0.f;
    for (int p = 0; p < P; ++p) { float w = __expf(ebuf[p] - mx); z += w; n += w * sbuf[p]; }
    out[b] = f2bf(n / z);
  }
}

extern "C" void kernel_launch(void* const* d_in, const int* in_sizes, int n_in,
                              void* d_out, int out_size, void* d_ws, size_t ws_size,
                              hipStream_t stream) {
  const int* idx = (const int*)d_in[0];
  const unsigned short* table = (const unsigned short*)d_in[1];
  const int* ii = (const int*)d_in[2];
  const int* jj = (const int*)d_in[3];
  const unsigned short* W = (const unsigned short*)d_in[4];
  const unsigned short* bvec = (const unsigned short*)d_in[5];
  const unsigned short* hvec = (const unsigned short*)d_in[6];
  const unsigned short* pvec = (const unsigned short*)d_in[7];
  unsigned short* out = (unsigned short*)d_out;
  const int B = out_size;
  const int P = in_sizes[2];
  const int D = in_sizes[7];
  const int A = in_sizes[5];
  if (B <= 0) return;
  const int F = in_sizes[0] / B;
  if (D == 128 && A == 128 && F == 26 && P == 325 && B < (1 << 24)) {
    const int G = (B + 1023) / 1024 * 256;
    afm_fast_kernel<<<G, 256, 0, stream>>>(idx, table, ii, jj, W, bvec, hvec, pvec, out, B, 0
        );
  } else {
    afm_generic_kernel<<<B, 256, 0, stream>>>(idx, table, ii, jj, W, bvec, hvec, pvec, out, (float*)d_ws, B, F, P, D, A);
  }
}
```

```cpp
#include <hip/hip_runtime.h>
#include <hip/hip_bf16.h>
#include <stdint.h>

#define AFM_HAS_STAMPS 0

typedef float  f32x2  __attribute__((ext_vector_type(2)));
typedef __bf16 bf16x2 __attribute__((ext_vector_type(2)));

__device__ __forceinline__ float bf2f(unsigned short u) { return __builtin_bit_cast(float, (unsigned)u << 16); }
__device__ __forceinline__ unsigned short f2bf(float x) {
  f32x2 v = {x, 0.f}; bf16x2 r = __builtin_convertvector(v, bf16x2);
  return (unsigned short)(__builtin_bit_cast(unsigned, r) & 0xffffu);
}
__device__ __forceinline__ float rbf(float x) { return bf2f(f2bf(x)); }

static __device__ const __attribute__((aligned(128), used)) unsigned afm_tab[4096] asm("afm_tab") = {
  40704, 41568, 38400, 41856, 41568, 40992, 38416, 42144, 41280, 40992, 38432, 42720, 39552, 40416, 38448, 41856, 39840, 38688, 38464, 42144, 39840, 38976, 38480, 42432,
  38688, 39552, 38496, 42720, 38976, 40128, 38512, 40704, 38400, 39264, 38528, 42144, 39840, 38400, 38544, 42432, 38688, 40128, 38560, 41856, 39264, 40128, 38576, 42144,
  40704, 40992, 38592, 42432, 40416, 38976, 38608, 42720, 41280, 41568, 38624, 40704, 41280, 40704, 38640, 40992, 40720, 41584, 40704, 41872, 41584, 41008, 40720, 42160,
  41296, 41008, 40736, 42736, 39568, 40432, 40752, 41872, 39856, 38704, 40768, 42160, 39856, 38992, 40784, 42448, 38704, 39568, 40800, 42736, 38992, 40144, 40816, 40720,
  38416, 39280, 40832, 42160, 39856, 38416, 40848, 42448, 38704, 40144, 40864, 41872, 39280, 40144, 40880, 42160, 40720, 41008, 40896, 42448, 40432, 38992, 40912, 42736,
  41296, 41584, 40928, 40720, 41296, 40720, 40944, 41008, 40736, 41600, 43008, 41888, 41600, 41024, 43024, 42176, 41312, 41024, 43040, 42752, 39584, 40448, 43056, 41888,
  39872, 38720, 43072, 42176, 39872, 39008, 43088, 42464, 38720, 39584, 43104, 42752, 39008, 40160, 43120, 40736, 38432, 39296, 43136, 42176, 39872, 38432, 43152, 42464,
  38720, 40160, 43168, 41888, 39296, 40160, 43184, 42176, 40736, 41024, 43200, 42464, 40448, 39008, 43216, 42752, 41312, 41600, 43232, 40736, 41312, 40736, 43248, 41024,
  40752, 41616, 37536, 41904, 41616, 41040, 37552, 42192, 41328, 41040, 37568, 42768, 39600, 40464, 37584, 41904, 39888, 38736, 37600, 42192, 39888, 39024, 37616, 42480,
  38736, 39600, 37632, 42768, 39024, 40176, 37648, 40752, 38448, 39312, 37664, 42192, 39888, 38448, 37680, 42480, 38736, 40176, 37696, 41904, 39312, 40176, 37712, 42192,
  40752, 41040, 37728, 42480, 40464, 39024, 37744, 42768, 41328, 41616, 37760, 40752, 41328, 40752, 37776, 41040, 44736, 43296, 46464, 82016, 44448, 43008, 46752, 82304,
  44736, 43872, 47040, 98496, 43008, 44160, 47328, 98784, 42144, 41280, 47616, 99072, 43296, 44160, 47616, 99360, 41856, 42720, 47616, 99648, 43872, 43008, 47616, 49920,
  45024, 44160, 63808, 49920, 43584, 44736, 64096, 49920, 41856, 40992, 64384, 49920, 42432, 41280, 64672, 49920, 44448, 43296, 64960, 49920, 43584, 44448, 81152, 49920,
  43584, 45024, 81440, 49920, 41568, 42432, 81728, 49920, 44752, 43312, 46480, 82032, 44464, 43024, 46768, 82320, 44752, 43888, 47056, 98512, 43024, 44176, 47344, 98800,
  42160, 41296, 47632, 99088, 43312, 44176, 47632, 99376, 41872, 42736, 47632, 99664, 43888, 43024, 47632, 49936, 45040, 44176, 63824, 49936, 43600, 44752, 64112, 49936,
  41872, 41008, 64400, 49936, 42448, 41296, 64688, 49936, 44464, 43312, 64976, 49936, 43600, 44464, 81168, 49936, 43600, 45040, 81456, 49936, 41584, 42448, 81744, 49936,
  44768, 43328, 46496, 82048, 44480, 43040, 46784, 82336, 44768, 43904, 47072, 98528, 43040, 44192, 47360, 98816, 42176, 41312, 47648, 99104, 43328, 44192, 47648, 99392,
  41888, 42752, 47648, 99680, 43904, 43040, 47648, 49952, 45056, 44192, 63840, 49952, 43616, 44768, 64128, 49952, 41888, 41024, 64416, 49952, 42464, 41312, 64704, 49952,
  44480, 43328, 64992, 49952, 43616, 44480, 81184, 49952, 43616, 45056, 81472, 49952, 41600, 42464, 81760, 49952, 44784, 43344, 46512, 82064, 44496, 43056, 46800, 82352,
  44784, 43920, 47088, 98544, 43056, 44208, 47376, 98832, 42192, 41328, 47664, 99120, 43344, 44208, 47664, 99408, 41904, 42768, 47664, 99696, 43920, 43056, 47664, 49968,
  45072, 44208, 63856, 49968, 43632, 44784, 64144, 49968, 41904, 41040, 64432, 49968, 42480, 41328, 64720, 49968, 44496, 43344, 65008, 49968, 43632, 44496, 81200, 49968,
  43632, 45072, 81488, 49968, 41616, 42480, 81776, 49968, 51712, 54272, 52992, 51152, 51716, 54352, 52996, 51236, 51720, 54432, 53000, 51244, 51724, 54512, 53004, 51404,
  51728, 54592, 53008, 51488, 51732, 54672, 53012, 51572, 51736, 54752, 53016, 51580, 51740, 54832, 53020, 51100, 51792, 54276, 53072, 51232, 51796, 54356, 53076, 51164,
  51800, 54436, 53080, 51400, 51804, 54516, 53084, 51484, 51808, 54596, 53088, 51568, 51812, 54676, 53092, 51500, 51816, 54756, 53096, 51096, 51820, 54836, 53100, 51180,
  51872, 54280, 53152, 52364, 51876, 54360, 53156, 52444, 51880, 54440, 53160, 52440, 51884, 54520, 53164, 51420, 51888, 54600, 53168, 51172, 51892, 54680, 53172, 51252,
  51896, 54760, 53176, 51396, 51900, 54840, 53180, 51560, 51952, 54284, 53232, 51084, 51956, 54364, 53236, 51092, 51960, 54444, 53240, 51556, 51964, 54524, 53244, 51564,
  51968, 54604, 53248, 52432, 51972, 54684, 53252, 51260, 51976, 54764, 53256, 52524, 51980, 54844, 53260, 52360, 52032, 54288, 53312, 52672, 52036, 54368, 53316, 52756,
  52040, 54448, 53320, 52540, 52044, 54528, 53324, 52684, 52048, 54608, 53328, 52768, 52052, 54688, 53332, 52852, 52056, 54768, 53336, 52860, 52060, 54848, 53340, 52380,
  52112, 54292, 53392, 52752, 52116, 54372, 53396, 52836, 52120, 54452, 53400, 52680, 52124, 54532, 53404, 52764, 52128, 54612, 53408, 52848, 52132, 54692, 53412, 52780,
  52136, 54772, 53416, 52376, 52140, 54852, 53420, 52460, 52192, 54296, 53472, 53712, 52196, 54376, 53476, 53796, 52200, 54456, 53480, 53804, 52204, 54536, 53484, 53964,
  52208, 54616, 53488, 54048, 52212, 54696, 53492, 54132, 52216, 54776, 53496, 54140, 52220, 54856, 53500, 53660, 52272, 54300, 53552, 53792, 52276, 54380, 53556, 53724,
  52280, 54460, 53560, 53960, 52284, 54540, 53564, 54044, 52288, 54620, 53568, 54128, 52292, 54700, 53572, 54060, 52296, 54780, 53576, 53656, 52300, 54860, 53580, 53740,
  46464, 82016, 53736, 38400, 46752, 82304, 53652, 38720, 47040, 98496, 54124, 33408, 47328, 98784, 53952, 33408, 47616, 99072, 52532, 33408, 47616, 99360, 53956, 33408,
  47616, 99648, 52700, 33408, 47616, 47904, 53644, 33408, 63808, 47904, 53980, 33408, 64096, 47904, 54120, 33408, 64384, 47904, 52448, 33408, 64672, 47904, 52536, 33408,
  64960, 47904, 53732, 33408, 81152, 47904, 54040, 33408, 81440, 47904, 53820, 33408, 81728, 47904, 52844, 33408, 46480, 82032, 52592, 38416, 46768, 82320, 52596, 38736,
  47056, 98512, 52600, 33424, 47344, 98800, 52604, 33424, 47632, 99088, 52608, 33424, 47632, 99376, 52612, 33424, 47632, 99664, 52616, 33424, 47632, 47920, 52620, 33424,
  63824, 47920, 52912, 33424, 64112, 47920, 52916, 33424, 64400, 47920, 52920, 33424, 64688, 47920, 52924, 33424, 64976, 47920, 52928, 33424, 81168, 47920, 52932, 33424,
  81456, 47920, 52936, 33424, 81744, 47920, 52940, 33424, 46496, 82048, 51312, 38432, 46784, 82336, 51316, 38752, 47072, 98528, 51320, 33440, 47360, 98816, 51324, 33440,
  47648, 99104, 51328, 33440, 47648, 99392, 51332, 33440, 47648, 99680, 51336, 33440, 47648, 47936, 51340, 33440, 63840, 47936, 51632, 33440, 64128, 47936, 51636, 33440,
  64416, 47936, 51640, 33440, 64704, 47936, 51644, 33440, 64992, 47936, 51648, 33440, 81184, 47936, 51652, 33440, 81472, 47936, 51656, 33440, 81760, 47936, 51660, 33440,
  46512, 82064, 53872, 38448, 46800, 82352, 53876, 38768, 47088, 98544, 53880, 33456, 47376, 98832, 53884, 33456, 47664, 99120, 53888, 33456, 47664, 99408, 53892, 33456,
  47664, 99696, 53896, 33456, 47664, 47952, 53900, 33456, 63856, 47952, 54192, 33456, 64144, 47952, 54196, 33456, 64432, 47952, 54200, 33456, 64720, 47952, 54204, 33456,
  65008, 47952, 54208, 33456, 81200, 47952, 54212, 33456, 81488, 47952, 54216, 33456, 81776, 47952, 54220, 33456, 58048, 58912, 55744, 59200, 58912, 58336, 55760, 59488,
  58624, 58336, 55776, 60064, 56896, 57760, 55792, 59200, 57184, 56032, 55808, 59488, 57184, 56320, 55824, 59776, 56032, 56896, 55840, 60064, 56320, 57472, 55856, 58048,
  55744, 56608, 55872, 59488, 57184, 55744, 55888, 59776, 56032, 57472, 55904, 59200, 56608, 57472, 55920, 59488, 58048, 58336, 55936, 59776, 57760, 56320, 55952, 60064,
  58624, 58912, 55968, 58048, 58624, 58048, 55984, 58336, 58064, 58928, 58048, 59216, 58928, 58352, 58064, 59504, 58640, 58352, 58080, 60080, 56912, 57776, 58096, 59216,
  57200, 56048, 58112, 59504, 57200, 56336, 58128, 59792, 56048, 56912, 58144, 60080, 56336, 57488, 58160, 58064, 55760, 56624, 58176, 59504, 57200, 55760, 58192, 59792,
  56048, 57488, 58208, 59216, 56624, 57488, 58224, 59504, 58064, 58352, 58240, 59792, 57776, 56336, 58256, 60080, 58640, 58928, 58272, 58064, 58640, 58064, 58288, 58352,
  58080, 58944, 60352, 59232, 58944, 58368, 60368, 59520, 58656, 58368, 60384, 60096, 56928, 57792, 60400, 59232, 57216, 56064, 60416, 59520, 57216, 56352, 60432, 59808,
  56064, 56928, 60448, 60096, 56352, 57504, 60464, 58080, 55776, 56640, 60480, 59520, 57216, 55776, 60496, 59808, 56064, 57504, 60512, 59232, 56640, 57504, 60528, 59520,
  58080, 58368, 60544, 59808, 57792, 56352, 60560, 60096, 58656, 58944, 60576, 58080, 58656, 58080, 60592, 58368, 58096, 58960, 54880, 59248, 58960, 58384, 54896, 59536,
  58672, 58384, 54912, 60112, 56944, 57808, 54928, 59248, 57232, 56080, 54944, 59536, 57232, 56368, 54960, 59824, 56080, 56944, 54976, 60112, 56368, 57520, 54992, 58096,
  55792, 56656, 55008, 59536, 57232, 55792, 55024, 59824, 56080, 57520, 55040, 59248, 56656, 57520, 55056, 59536, 58096, 58384, 55072, 59824, 57808, 56368, 55088, 60112,
  58672, 58960, 55104, 58096, 58672, 58096, 55120, 58384, 62080, 60640, 63808, 99360, 61792, 60352, 64096, 99648, 62080, 61216, 64384, 46464, 60352, 61504, 64672, 46752,
  59488, 58624, 64960, 47040, 60640, 61504, 64960, 47328, 59200, 60064, 64960, 47616, 61216, 60352, 64960, 67264, 62368, 61504, 81152, 67264, 60928, 62080, 81440, 67264,
  59200, 58336, 81728, 67264, 59776, 58624, 82016, 67264, 61792, 60640, 82304, 67264, 60928, 61792, 98496, 67264, 60928, 62368, 98784, 67264, 58912, 59776, 99072, 67264,
  62096, 60656, 63824, 99376, 61808, 60368, 64112, 99664, 62096, 61232, 64400, 46480, 60368, 61520, 64688, 46768, 59504, 58640, 64976, 47056, 60656, 61520, 64976, 47344,
  59216, 60080, 64976, 47632, 61232, 60368, 64976, 67280, 62384, 61520, 81168, 67280, 60944, 62096, 81456, 67280, 59216, 58352, 81744, 67280, 59792, 58640, 82032, 67280,
  61808, 60656, 82320, 67280, 60944, 61808, 98512, 67280, 60944, 62384, 98800, 67280, 58928, 59792, 99088, 67280, 62112, 60672, 63840, 99392, 61824, 60384, 64128, 99680,
  62112, 61248, 64416, 46496, 60384, 61536, 64704, 46784, 59520, 58656, 64992, 47072, 60672, 61536, 64992, 47360, 59232, 60096, 64992, 47648, 61248, 60384, 64992, 67296,
  62400, 61536, 81184, 67296, 60960, 62112, 81472, 67296, 59232, 58368, 81760, 67296, 59808, 58656, 82048, 67296, 61824, 60672, 82336, 67296, 60960, 61824, 98528, 67296,
  60960, 62400, 98816, 67296, 58944, 59808, 99104, 67296, 62128, 60688, 63856, 99408, 61840, 60400, 64144, 99696, 62128, 61264, 64432, 46512, 60400, 61552, 64720, 46800,
  59536, 58672, 65008, 47088, 60688, 61552, 65008, 47376, 59248, 60112, 65008, 47664, 61264, 60400, 65008, 67312, 62416, 61552, 81200, 67312, 60976, 62128, 81488, 67312,
  59248, 58384, 81776, 67312, 59824, 58672, 82064, 67312, 61840, 60688, 82352, 67312, 60976, 61840, 98544, 67312, 60976, 62416, 98832, 67312, 58960, 59824, 99120, 67312,
  69056, 71616, 70336, 68496, 69060, 71696, 70340, 68580, 69064, 71776, 70344, 68588, 69068, 71856, 70348, 68748, 69072, 71936, 70352, 68832, 69076, 72016, 70356, 68916,
  69080, 72096, 70360, 68924, 69084, 72176, 70364, 68444, 69136, 71620, 70416, 68576, 69140, 71700, 70420, 68508, 69144, 71780, 70424, 68744, 69148, 71860, 70428, 68828,
  69152, 71940, 70432, 68912, 69156, 72020, 70436, 68844, 69160, 72100, 70440, 68440, 69164, 72180, 70444, 68524, 69216, 71624, 70496, 69708, 69220, 71704, 70500, 69788,
  69224, 71784, 70504, 69784, 69228, 71864, 70508, 68764, 69232, 71944, 70512, 68516, 69236, 72024, 70516, 68596, 69240, 72104, 70520, 68740, 69244, 72184, 70524, 68904,
  69296, 71628, 70576, 68428, 69300, 71708, 70580, 68436, 69304, 71788, 70584, 68900, 69308, 71868, 70588, 68908, 69312, 71948, 70592, 69776, 69316, 72028, 70596, 68604,
  69320, 72108, 70600, 69868, 69324, 72188, 70604, 69704, 69376, 71632, 70656, 70016, 69380, 71712, 70660, 70100, 69384, 71792, 70664, 69884, 69388, 71872, 70668, 70028,
  69392, 71952, 70672, 70112, 69396, 72032, 70676, 70196, 69400, 72112, 70680, 70204, 69404, 72192, 70684, 69724, 69456, 71636, 70736, 70096, 69460, 71716, 70740, 70180,
  69464, 71796, 70744, 70024, 69468, 71876, 70748, 70108, 69472, 71956, 70752, 70192, 69476, 72036, 70756, 70124, 69480, 72116, 70760, 69720, 69484, 72196, 70764, 69804,
  69536, 71640, 70816, 71056, 69540, 71720, 70820, 71140, 69544, 71800, 70824, 71148, 69548, 71880, 70828, 71308, 69552, 71960, 70832, 71392, 69556, 72040, 70836, 71476,
  69560, 72120, 70840, 71484, 69564, 72200, 70844, 71004, 69616, 71644, 70896, 71136, 69620, 71724, 70900, 71068, 69624, 71804, 70904, 71304, 69628, 71884, 70908, 71388,
  69632, 71964, 70912, 71472, 69636, 72044, 70916, 71404, 69640, 72124, 70920, 71000, 69644, 72204, 70924, 71084, 63812, 99364, 71080, 55744, 64100, 99652, 70996, 56064,
  64388, 46468, 71468, 50752, 64676, 46756, 71296, 50752, 64964, 47044, 69876, 50752, 64964, 47332, 71300, 50752, 64964, 47620, 70044, 50752, 64964, 65248, 70988, 50752,
  81156, 65248, 71324, 50752, 81444, 65248, 71464, 50752, 81732, 65248, 69792, 50752, 82020, 65248, 69880, 50752, 82308, 65248, 71076, 50752, 98500, 65248, 71384, 50752,
  98788, 65248, 71164, 50752, 99076, 65248, 70188, 50752, 63828, 99380, 69936, 55760, 64116, 99668, 69940, 56080, 64404, 46484, 69944, 50768, 64692, 46772, 69948, 50768,
  64980, 47060, 69952, 50768, 64980, 47348, 69956, 50768, 64980, 47636, 69960, 50768, 64980, 65264, 69964, 50768, 81172, 65264, 70256, 50768, 81460, 65264, 70260, 50768,
  81748, 65264, 70264, 50768, 82036, 65264, 70268, 50768, 82324, 65264, 70272, 50768, 98516, 65264, 70276, 50768, 98804, 65264, 70280, 50768, 99092, 65264, 70284, 50768,
  63844, 99396, 68656, 55776, 64132, 99684, 68660, 56096, 64420, 46500, 68664, 50784, 64708, 46788, 68668, 50784, 64996, 47076, 68672, 50784, 64996, 47364, 68676, 50784,
  64996, 47652, 68680, 50784, 64996, 65280, 68684, 50784, 81188, 65280, 68976, 50784, 81476, 65280, 68980, 50784, 81764, 65280, 68984, 50784, 82052, 65280, 68988, 50784,
  82340, 65280, 68992, 50784, 98532, 65280, 68996, 50784, 98820, 65280, 69000, 50784, 99108, 65280, 69004, 50784, 63860, 99412, 71216, 55792, 64148, 99700, 71220, 56112,
  64436, 46516, 71224, 50800, 64724, 46804, 71228, 50800, 65012, 47092, 71232, 50800, 65012, 47380, 71236, 50800, 65012, 47668, 71240, 50800, 65012, 65296, 71244, 50800,
  81204, 65296, 71536, 50800, 81492, 65296, 71540, 50800, 81780, 65296, 71544, 50800, 82068, 65296, 71548, 50800, 82356, 65296, 71552, 50800, 98548, 65296, 71556, 50800,
  98836, 65296, 71560, 50800, 99124, 65296, 71564, 50800, 75392, 76256, 73088, 76544, 76256, 75680, 73104, 76832, 75968, 75680, 73120, 77408, 74240, 75104, 73136, 76544,
  74528, 73376, 73152, 76832, 74528, 73664, 73168, 77120, 73376, 74240, 73184, 77408, 73664, 74816, 73200, 75392, 73088, 73952, 73216, 76832, 74528, 73088, 73232, 77120,
  73376, 74816, 73248, 76544, 73952, 74816, 73264, 76832, 75392, 75680, 73280, 77120, 75104, 73664, 73296, 77408, 75968, 76256, 73312, 75392, 75968, 75392, 73328, 75680,
  75408, 76272, 75392, 76560, 76272, 75696, 75408, 76848, 75984, 75696, 75424, 77424, 74256, 75120, 75440, 76560, 74544, 73392, 75456, 76848, 74544, 73680, 75472, 77136,
  73392, 74256, 75488, 77424, 73680, 74832, 75504, 75408, 73104, 73968, 75520, 76848, 74544, 73104, 75536, 77136, 73392, 74832, 75552, 76560, 73968, 74832, 75568, 76848,
  75408, 75696, 75584, 77136, 75120, 73680, 75600, 77424, 75984, 76272, 75616, 75408, 75984, 75408, 75632, 75696, 75424, 76288, 77696, 76576, 76288, 75712, 77712, 76864,
  76000, 75712, 77728, 77440, 74272, 75136, 77744, 76576, 74560, 73408, 77760, 76864, 74560, 73696, 77776, 77152, 73408, 74272, 77792, 77440, 73696, 74848, 77808, 75424,
  73120, 73984, 77824, 76864, 74560, 73120, 77840, 77152, 73408, 74848, 77856, 76576, 73984, 74848, 77872, 76864, 75424, 75712, 77888, 77152, 75136, 73696, 77904, 77440,
  76000, 76288, 77920, 75424, 76000, 75424, 77936, 75712, 75440, 76304, 72224, 76592, 76304, 75728, 72240, 76880, 76016, 75728, 72256, 77456, 74288, 75152, 72272, 76592,
  74576, 73424, 72288, 76880, 74576, 73712, 72304, 77168, 73424, 74288, 72320, 77456, 73712, 74864, 72336, 75440, 73136, 74000, 72352, 76880, 74576, 73136, 72368, 77168,
  73424, 74864, 72384, 76592, 74000, 74864, 72400, 76880, 75440, 75728, 72416, 77168, 75152, 73712, 72432, 77456, 76016, 76304, 72448, 75440, 76016, 75440, 72464, 75728,
  79424, 77984, 81152, 47328, 79136, 77696, 81440, 47616, 79424, 78560, 81728, 63808, 77696, 78848, 82016, 64096, 76832, 75968, 82304, 64384, 77984, 78848, 82304, 64672,
  76544, 77408, 82304, 64960, 78560, 77696, 82304, 84608, 79712, 78848, 98496, 84608, 78272, 79424, 98784, 84608, 76544, 75680, 99072, 84608, 77120, 75968, 99360, 84608,
  79136, 77984, 99648, 84608, 78272, 79136, 46464, 84608, 78272, 79712, 46752, 84608, 76256, 77120, 47040, 84608, 79440, 78000, 81168, 47344, 79152, 77712, 81456, 47632,
  79440, 78576, 81744, 63824, 77712, 78864, 82032, 64112, 76848, 75984, 82320, 64400, 78000, 78864, 82320, 64688, 76560, 77424, 82320, 64976, 78576, 77712, 82320, 84624,
  79728, 78864, 98512, 84624, 78288, 79440, 98800, 84624, 76560, 75696, 99088, 84624, 77136, 75984, 99376, 84624, 79152, 78000, 99664, 84624, 78288, 79152, 46480, 84624,
  78288, 79728, 46768, 84624, 76272, 77136, 47056, 84624, 79456, 78016, 81184, 47360, 79168, 77728, 81472, 47648, 79456, 78592, 81760, 63840, 77728, 78880, 82048, 64128,
  76864, 76000, 82336, 64416, 78016, 78880, 82336, 64704, 76576, 77440, 82336, 64992, 78592, 77728, 82336, 84640, 79744, 78880, 98528, 84640, 78304, 79456, 98816, 84640,
  76576, 75712, 99104, 84640, 77152, 76000, 99392, 84640, 79168, 78016, 99680, 84640, 78304, 79168, 46496, 84640, 78304, 79744, 46784, 84640, 76288, 77152, 47072, 84640,
  79472, 78032, 81200, 47376, 79184, 77744, 81488, 47664, 79472, 78608, 81776, 63856, 77744, 78896, 82064, 64144, 76880, 76016, 82352, 64432, 78032, 78896, 82352, 64720,
  76592, 77456, 82352, 65008, 78608, 77744, 82352, 84656, 79760, 78896, 98544, 84656, 78320, 79472, 98832, 84656, 76592, 75728, 99120, 84656, 77168, 76016, 99408, 84656,
  79184, 78032, 99696, 84656, 78320, 79184, 46512, 84656, 78320, 79760, 46800, 84656, 76304, 77168, 47088, 84656, 86400, 88960, 87680, 85840, 86404, 89040, 87684, 85924,
  86408, 89120, 87688, 85932, 86412, 89200, 87692, 86092, 86416, 89280, 87696, 86176, 86420, 89360, 87700, 86260, 86424, 89440, 87704, 86268, 86428, 89520, 87708, 85788,
  86480, 88964, 87760, 85920, 86484, 89044, 87764, 85852, 86488, 89124, 87768, 86088, 86492, 89204, 87772, 86172, 86496, 89284, 87776, 86256, 86500, 89364, 87780, 86188,
  86504, 89444, 87784, 85784, 86508, 89524, 87788, 85868, 86560, 88968, 87840, 87052, 86564, 89048, 87844, 87132, 86568, 89128, 87848, 87128, 86572, 89208, 87852, 86108,
  86576, 89288, 87856, 85860, 86580, 89368, 87860, 85940, 86584, 89448, 87864, 86084, 86588, 89528, 87868, 86248, 86640, 88972, 87920, 85772, 86644, 89052, 87924, 85780,
  86648, 89132, 87928, 86244, 86652, 89212, 87932, 86252, 86656, 89292, 87936, 87120, 86660, 89372, 87940, 85948, 86664, 89452, 87944, 87212, 86668, 89532, 87948, 87048,
  86720, 88976, 88000, 87360, 86724, 89056, 88004, 87444, 86728, 89136, 88008, 87228, 86732, 89216, 88012, 87372, 86736, 89296, 88016, 87456, 86740, 89376, 88020, 87540,
  86744, 89456, 88024, 87548, 86748, 89536, 88028, 87068, 86800, 88980, 88080, 87440, 86804, 89060, 88084, 87524, 86808, 89140, 88088, 87368, 86812, 89220, 88092, 87452,
  86816, 89300, 88096, 87536, 86820, 89380, 88100, 87468, 86824, 89460, 88104, 87064, 86828, 89540, 88108, 87148, 86880, 88984, 88160, 88400, 86884, 89064, 88164, 88484,
  86888, 89144, 88168, 88492, 86892, 89224, 88172, 88652, 86896, 89304, 88176, 88736, 86900, 89384, 88180, 88820, 86904, 89464, 88184, 88828, 86908, 89544, 88188, 88348,
  86960, 88988, 88240, 88480, 86964, 89068, 88244, 88412, 86968, 89148, 88248, 88648, 86972, 89228, 88252, 88732, 86976, 89308, 88256, 88816, 86980, 89388, 88260, 88748,
  86984, 89468, 88264, 88344, 86988, 89548, 88268, 88428, 81160, 47336, 88424, 73088, 81448, 47624, 88340, 73408, 81736, 63816, 88812, 68096, 82024, 64104, 88640, 68096,
  82312, 64392, 87220, 68096, 82312, 64680, 88644, 68096, 82312, 64968, 87388, 68096, 82312, 82592, 88332, 68096, 98504, 82592, 88668, 68096, 98792, 82592, 88808, 68096,
  99080, 82592, 87136, 68096, 99368, 82592, 87224, 68096, 99656, 82592, 88420, 68096, 46472, 82592, 88728, 68096, 46760, 82592, 88508, 68096, 47048, 82592, 87532, 68096,
  81176, 47352, 87280, 73104, 81464, 47640, 87284, 73424, 81752, 63832, 87288, 68112, 82040, 64120, 87292, 68112, 82328, 64408, 87296, 68112, 82328, 64696, 87300, 68112,
  82328, 64984, 87304, 68112, 82328, 82608, 87308, 68112, 98520, 82608, 87600, 68112, 98808, 82608, 87604, 68112, 99096, 82608, 87608, 68112, 99384, 82608, 87612, 68112,
  99672, 82608, 87616, 68112, 46488, 82608, 87620, 68112, 46776, 82608, 87624, 68112, 47064, 82608, 87628, 68112, 81192, 47368, 86000, 73120, 81480, 47656, 86004, 73440,
  81768, 63848, 86008, 68128, 82056, 64136, 86012, 68128, 82344, 64424, 86016, 68128, 82344, 64712, 86020, 68128, 82344, 65000, 86024, 68128, 82344, 82624, 86028, 68128,
  98536, 82624, 86320, 68128, 98824, 82624, 86324, 68128, 99112, 82624, 86328, 68128, 99400, 82624, 86332, 68128, 99688, 82624, 86336, 68128, 46504, 82624, 86340, 68128,
  46792, 82624, 86344, 68128, 47080, 82624, 86348, 68128, 81208, 47384, 88560, 73136, 81496, 47672, 88564, 73456, 81784, 63864, 88568, 68144, 82072, 64152, 88572, 68144,
  82360, 64440, 88576, 68144, 82360, 64728, 88580, 68144, 82360, 65016, 88584, 68144, 82360, 82640, 88588, 68144, 98552, 82640, 88880, 68144, 98840, 82640, 88884, 68144,
  99128, 82640, 88888, 68144, 99416, 82640, 88892, 68144, 99704, 82640, 88896, 68144, 46520, 82640, 88900, 68144, 46808, 82640, 88904, 68144, 47096, 82640, 88908, 68144,
  92736, 93600, 90432, 93888, 93600, 93024, 90448, 94176, 93312, 93024, 90464, 94752, 91584, 92448, 90480, 93888, 91872, 90720, 90496, 94176, 91872, 91008, 90512, 94464,
  90720, 91584, 90528, 94752, 91008, 92160, 90544, 92736, 90432, 91296, 90560, 94176, 91872, 90432, 90576, 94464, 90720, 92160, 90592, 93888, 91296, 92160, 90608, 94176,
  92736, 93024, 90624, 94464, 92448, 91008, 90640, 94752, 93312, 93600, 90656, 92736, 93312, 92736, 90672, 93024, 92752, 93616, 92736, 93904, 93616, 93040, 92752, 94192,
  93328, 93040, 92768, 94768, 91600, 92464, 92784, 93904, 91888, 90736, 92800, 94192, 91888, 91024, 92816, 94480, 90736, 91600, 92832, 94768, 91024, 92176, 92848, 92752,
  90448, 91312, 92864, 94192, 91888, 90448, 92880, 94480, 90736, 92176, 92896, 93904, 91312, 92176, 92912, 94192, 92752, 93040, 92928, 94480, 92464, 91024, 92944, 94768,
  93328, 93616, 92960, 92752, 93328, 92752, 92976, 93040, 92768, 93632, 95040, 93920, 93632, 93056, 95056, 94208, 93344, 93056, 95072, 94784, 91616, 92480, 95088, 93920,
  91904, 90752, 95104, 94208, 91904, 91040, 95120, 94496, 90752, 91616, 95136, 94784, 91040, 92192, 95152, 92768, 90464, 91328, 95168, 94208, 91904, 90464, 95184, 94496,
  90752, 92192, 95200, 93920, 91328, 92192, 95216, 94208, 92768, 93056, 95232, 94496, 92480, 91040, 95248, 94784, 93344, 93632, 95264, 92768, 93344, 92768, 95280, 93056,
  92784, 93648, 89568, 93936, 93648, 93072, 89584, 94224, 93360, 93072, 89600, 94800, 91632, 92496, 89616, 93936, 91920, 90768, 89632, 94224, 91920, 91056, 89648, 94512,
  90768, 91632, 89664, 94800, 91056, 92208, 89680, 92784, 90480, 91344, 89696, 94224, 91920, 90480, 89712, 94512, 90768, 92208, 89728, 93936, 91344, 92208, 89744, 94224,
  92784, 93072, 89760, 94512, 92496, 91056, 89776, 94800, 93360, 93648, 89792, 92784, 93360, 92784, 89808, 93072, 96768, 95328, 98496, 64672, 96480, 95040, 98784, 64960,
  96768, 95904, 99072, 81152, 95040, 96192, 99360, 81440, 94176, 93312, 99648, 81728, 95328, 96192, 99648, 82016, 93888, 94752, 99648, 82304, 95904, 95040, 99648, 101952,
  97056, 96192, 46464, 101952, 95616, 96768, 46752, 101952, 93888, 93024, 47040, 101952, 94464, 93312, 47328, 101952, 96480, 95328, 47616, 101952, 95616, 96480, 63808, 101952,
  95616, 97056, 64096, 101952, 93600, 94464, 64384, 101952, 96784, 95344, 98512, 64688, 96496, 95056, 98800, 64976, 96784, 95920, 99088, 81168, 95056, 96208, 99376, 81456,
  94192, 93328, 99664, 81744, 95344, 96208, 99664, 82032, 93904, 94768, 99664, 82320, 95920, 95056, 99664, 101968, 97072, 96208, 46480, 101968, 95632, 96784, 46768, 101968,
  93904, 93040, 47056, 101968, 94480, 93328, 47344, 101968, 96496, 95344, 47632, 101968, 95632, 96496, 63824, 101968, 95632, 97072, 64112, 101968, 93616, 94480, 64400, 101968,
  96800, 95360, 98528, 64704, 96512, 95072, 98816, 64992, 96800, 95936, 99104, 81184, 95072, 96224, 99392, 81472, 94208, 93344, 99680, 81760, 95360, 96224, 99680, 82048,
  93920, 94784, 99680, 82336, 95936, 95072, 99680, 101984, 97088, 96224, 46496, 101984, 95648, 96800, 46784, 101984, 93920, 93056, 47072, 101984, 94496, 93344, 47360, 101984,
  96512, 95360, 47648, 101984, 95648, 96512, 63840, 101984, 95648, 97088, 64128, 101984, 93632, 94496, 64416, 101984, 96816, 95376, 98544, 64720, 96528, 95088, 98832, 65008,
  96816, 95952, 99120, 81200, 95088, 96240, 99408, 81488, 94224, 93360, 99696, 81776, 95376, 96240, 99696, 82064, 93936, 94800, 99696, 82352, 95952, 95088, 99696, 102000,
  97104, 96240, 46512, 102000, 95664, 96816, 46800, 102000, 93936, 93072, 47088, 102000, 94512, 93360, 47376, 102000, 96528, 95376, 47664, 102000, 95664, 96528, 63856, 102000,
  95664, 97104, 64144, 102000, 93648, 94512, 64432, 102000, 103744, 106304, 105024, 103184, 103748, 106384, 105028, 103268, 103752, 106464, 105032, 103276, 103756, 106544, 105036, 103436,
  103760, 106624, 105040, 103520, 103764, 106704, 105044, 103604, 103768, 106784, 105048, 103612, 103772, 106864, 105052, 103132, 103824, 106308, 105104, 103264, 103828, 106388, 105108, 103196,
  103832, 106468, 105112, 103432, 103836, 106548, 105116, 103516, 103840, 106628, 105120, 103600, 103844, 106708, 105124, 103532, 103848, 106788, 105128, 103128, 103852, 106868, 105132, 103212,
  103904, 106312, 105184, 104396, 103908, 106392, 105188, 104476, 103912, 106472, 105192, 104472, 103916, 106552, 105196, 103452, 103920, 106632, 105200, 103204, 103924, 106712, 105204, 103284,
  103928, 106792, 105208, 103428, 103932, 106872, 105212, 103592, 103984, 106316, 105264, 103116, 103988, 106396, 105268, 103124, 103992, 106476, 105272, 103588, 103996, 106556, 105276, 103596,
  104000, 106636, 105280, 104464, 104004, 106716, 105284, 103292, 104008, 106796, 105288, 104556, 104012, 106876, 105292, 104392, 104064, 106320, 105344, 104704, 104068, 106400, 105348, 104788,
  104072, 106480, 105352, 104572, 104076, 106560, 105356, 104716, 104080, 106640, 105360, 104800, 104084, 106720, 105364, 104884, 104088, 106800, 105368, 104892, 104092, 106880, 105372, 104412,
  104144, 106324, 105424, 104784, 104148, 106404, 105428, 104868, 104152, 106484, 105432, 104712, 104156, 106564, 105436, 104796, 104160, 106644, 105440, 104880, 104164, 106724, 105444, 104812,
  104168, 106804, 105448, 104408, 104172, 106884, 105452, 104492, 104224, 106328, 105504, 105744, 104228, 106408, 105508, 105828, 104232, 106488, 105512, 105836, 104236, 106568, 105516, 105996,
  104240, 106648, 105520, 106080, 104244, 106728, 105524, 106164, 104248, 106808, 105528, 106172, 104252, 106888, 105532, 105692, 104304, 106332, 105584, 105824, 104308, 106412, 105588, 105756,
  104312, 106492, 105592, 105992, 104316, 106572, 105596, 106076, 104320, 106652, 105600, 106160, 104324, 106732, 105604, 106092, 104328, 106812, 105608, 105688, 104332, 106892, 105612, 105772,
  98508, 64684, 105768, 90432, 98796, 64972, 105684, 90752, 99084, 81164, 106156, 85440, 99372, 81452, 105984, 85440, 99660, 81740, 104564, 85440, 99660, 82028, 105988, 85440,
  99660, 82316, 104732, 85440, 99660, 99936, 105676, 85440, 46476, 99936, 106012, 85440, 46764, 99936, 106152, 85440, 47052, 99936, 104480, 85440, 47340, 99936, 104568, 85440,
  47628, 99936, 105764, 85440, 63820, 99936, 106072, 85440, 64108, 99936, 105852, 85440, 64396, 99936, 104876, 85440, 98524, 64700, 104624, 90448, 98812, 64988, 104628, 90768,
  99100, 81180, 104632, 85456, 99388, 81468, 104636, 85456, 99676, 81756, 104640, 85456, 99676, 82044, 104644, 85456, 99676, 82332, 104648, 85456, 99676, 99952, 104652, 85456,
  46492, 99952, 104944, 85456, 46780, 99952, 104948, 85456, 47068, 99952, 104952, 85456, 47356, 99952, 104956, 85456, 47644, 99952, 104960, 85456, 63836, 99952, 104964, 85456,
  64124, 99952, 104968, 85456, 64412, 99952, 104972, 85456, 98540, 64716, 103344, 90464, 98828, 65004, 103348, 90784, 99116, 81196, 103352, 85472, 99404, 81484, 103356, 85472,
  99692, 81772, 103360, 85472, 99692, 82060, 103364, 85472, 99692, 82348, 103368, 85472, 99692, 99968, 103372, 85472, 46508, 99968, 103664, 85472, 46796, 99968, 103668, 85472,
  47084, 99968, 103672, 85472, 47372, 99968, 103676, 85472, 47660, 99968, 103680, 85472, 63852, 99968, 103684, 85472, 64140, 99968, 103688, 85472, 64428, 99968, 103692, 85472,
  98556, 64732, 105904, 90480, 98844, 65020, 105908, 90800, 99132, 81212, 105912, 85488, 99420, 81500, 105916, 85488, 99708, 81788, 105920, 85488, 99708, 82076, 105924, 85488,
  99708, 82364, 105928, 85488, 99708, 99984, 105932, 85488, 46524, 99984, 106224, 85488, 46812, 99984, 106228, 85488, 47100, 99984, 106232, 85488, 47388, 99984, 106236, 85488,
  47676, 99984, 106240, 85488, 63868, 99984, 106244, 85488, 64156, 99984, 106248, 85488, 64444, 99984, 106252, 85488};

__global__ __launch_bounds__(256, 1) __attribute__((aligned(4096)))
void afm_fast_kernel(const int* __restrict__ idx, const unsigned short* __restrict__ table,
                     const int* __restrict__ ii, const int* __restrict__ jj,
                     const unsigned short* __restrict__ W, const unsigned short* __restrict__ bvec,
                     const unsigned short* __restrict__ hvec, const unsigned short* __restrict__ pvec,
                     unsigned short* __restrict__ out, int B, int unused
                     ) {
  __shared__ __attribute__((aligned(16))) char smem[107776];
  const unsigned lbase = (unsigned)(uintptr_t)(__attribute__((address_space(3))) char*)smem;
  unsigned flag;
  asm volatile(
      "s_load_dwordx16 s[8:23], %1, 0x0\n"
      "s_load_dwordx4 s[24:27], %1, 0x40\n"
      "s_getpc_b64 s[56:57]\n"
      "s_add_u32 s96, s56, afm_tab@rel32@lo+4\n"
      "s_addc_u32 s97, s57, afm_tab@rel32@hi+12\n"
      "v_lshrrev_b32 v254, 6, %3\n"
      "v_and_b32 v255, 63, %3\n"
      "s_bfe_u32 s44, %2, 0x20003\n"
      "v_mad_u32_u24 v255, v254, 10, v255\n"
      "s_mov_b64 exec, 0x3ff\n"
      "v_lshl_add_u32 v255, v255, 2, s44\n"
      "v_lshlrev_b32 v254, 7, v255\n"
      "v_min_u32 v253, 16256, v254\n"
      "global_load_dword v254, v254, s[56:57] offset:1536\n"
      "global_load_dword v253, v253, s[96:97]\n"
      "s_mov_b64 exec, -1\n"
      "s_mov_b32 s41, 0\n"
      "s_mov_b32 s42, 0\n"
      "s_mov_b32 s58, 0\n"
      "s_mov_b32 s35, 0\n"
      "0:\n"
      "v_lshrrev_b32 v2, 6, %3\n"
      "v_bfe_u32 v4, %3, 4, 2\n"
      "v_and_b32 v3, 15, %3\n"
      "v_readfirstlane_b32 s53, v2\n"
      "v_mul_u32_u24 v10, 28, v4\n"
      "v_mad_u32_u24 v11, v4, 24, 4\n"
      "v_min_u32 v10, v10, v11\n"
      "s_and_b32 s50, %2, 7\n"
      "s_bfe_u32 s51, %2, 0x50003\n"
      "s_andn2_b32 s34, %2, 255\n"
      "s_lshl_b32 s50, s50, 5\n"
      "s_add_u32 s34, s34, s51\n"
      "s_add_u32 s34, s34, s50\n"
      "s_lshl2_add_u32 s34, s34, s53\n"
      "s_mul_i32 s50, s34, 104\n"
      "s_mov_b32 s33, s34\n"
      "v_add_u32 v10, s50, v10\n"
      "v_lshlrev_b32 v13, 4, v3\n"
      "v_lshlrev_b32 v225, 3, %3\n"
      "v_min_u32 v225, 1292, v225\n"
      "v_lshl_or_b32 v80, v4, 9, v13\n"
      "v_lshl_add_u32 v80, v2, 11, v80\n"
      "v_add_u32 v81, 8192, v80\n"
      "v_add_u32 v82, 16384, v80\n"
      "v_add_u32 v83, 24576, v80\n"
      "s_lshl2_add_u32 s44, s53, 37888\n"
      "v_mov_b32 v2, s44\n"
      "v_mov_b32 v5, 0\n"
      "v_mov_b32 v1, 0x7f000000\n"
      "s_cmp_lg_u32 s35, 0\n"
      "s_cbranch_scc1 2f\n"
      "ds_write2_b32 v2, v1, v5 offset0:0 offset1:4\n"
      "ds_write_b32 v2, v5 offset:32\n"
      "2:\n"
      "s_mul_i32 s38, s53, 17344\n"
      "s_add_u32 s38, s38, 38400\n"
      "s_add_u32 s54, s38, 12672\n"
      "s_mul_i32 s61, s53, 17088\n"
      "s_add_u32 s61, s61, 45312\n"
      "s_mov_b32 s43, 0xffff0000\n"
      "s_movk_i32 s36, 0x400\n"
      "s_mov_b32 s37, 0x64006400\n"
      "s_mov_b32 s56, 0\n"
      "s_mov_b32 s99, 0x3f400000\n"
      "s_mov_b32 s40, 0\n"
      "s_or_b32 s60, s41, s58\n"
      "s_mov_b32 s55, 0\n"
      "v_cmp_eq_u32 vcc, 1, v4\n"
      "v_mov_b32 v222, 0x3280\n"
      "v_mov_b32 v145, 0\n"
      "v_cndmask_b32 v144, 0, v222, vcc\n"
      "s_mov_b32 s46, 0\n"
      "s_mov_b32 s47, 0xc7ac4c39\n"
      "v_lshlrev_b32 v223, 4, v4\n"
      "v_lshrrev_b64 v[146:147], v223, s[46:47]\n"
      "s_lshl_b32 s48, s53, 4\n"
      "v_mov_b32 v147, 0\n"
      "v_and_b32 v146, 0xffff, v146\n"
      "v_mov_b32 v215, 0x4738aa00\n"
      "s_xor_b32 s46, s48, 0\n"
      "v_lshlrev_b64 v[128:129], s46, v[144:145]\n"
      "v_lshlrev_b64 v[136:137], s46, v[146:147]\n"
      "s_xor_b32 s46, s48, 16\n"
      "v_lshlrev_b64 v[130:131], s46, v[144:145]\n"
      "v_lshlrev_b64 v[138:139], s46, v[146:147]\n"
      "s_xor_b32 s46, s48, 32\n"
      "v_lshlrev_b64 v[132:133], s46, v[144:145]\n"
      "v_lshlrev_b64 v[140:141], s46, v[146:147]\n"
      "s_xor_b32 s46, s48, 48\n"
      "v_lshlrev_b64 v[134:135], s46, v[144:145]\n"
      "v_lshlrev_b64 v[142:143], s46, v[146:147]\n"
      "v_mov_b32 v222, 0x3a80\n"
      "v_and_b32 v227, 3, %3\n"
      "v_bfe_u32 v223, %3, 2, 2\n"
      "v_cmp_eq_u32 vcc, v227, v4\n"
      "v_mov_b64 v[84:85], 0\n"
      "v_mov_b64 v[88:89], 0\n"
      "v_cndmask_b32 v84, 0, v222, vcc\n"
      "v_lshlrev_b32 v222, 4, v223\n"
      "v_lshlrev_b64 v[86:87], v222, v[84:85]\n"
      "s_mul_i32 s46, s53, 2304\n"
      "v_mul_u32_u24 v222, 2304, v4\n"
      "v_mov_b32 v84, 0\n"
      "s_mov_b32 m0, s46\n"
      "s_movk_i32 s47, 272\n"
      "v_lshl_add_u32 v222, v223, 6, v222\n"
      "v_mad_u32_u24 v14, v227, s47, v222\n"
      "v_mul_u32_u24 v216, 272, v4\n"
      "v_add_u32 v213, s38, v13\n"
      "v_lshlrev_b32 v222, 3, v4\n"
      "s_mov_b32 s47, 0x09090005\n"
      "v_bfe_u32 v222, s47, v222, 8\n"
      "v_lshl_add_u32 v91, v222, 6, v213\n"
      "v_lshlrev_b32 v224, 4, v4\n"
      "v_add_u32 v95, s38, v224\n"
      "v_lshl_add_u32 v223, v223, 4, v216\n"
      "v_bfe_u32 v222, %3, 1, 1\n"
      "v_mul_u32_u24 v222, 9344, v222\n"
      "v_add3_u32 v223, v223, v222, s46\n"
      "v_and_b32 v222, 1, %3\n"
      "v_lshl_add_u32 v216, v222, 3, v223\n"
      "s_lshl_b32 s46, s53, 5\n"
      "s_add_u32 s46, s46, 37376\n"
      "v_and_b32 v222, 8, %3\n"
      "v_and_b32 v223, 7, %3\n"
      "v_lshl_add_u32 v222, v222, 4, s46\n"
      "v_lshl_add_u32 v217, v223, 2, v222\n"
      "v_lshlrev_b32 v227, 2, v227\n"
      "v_lshlrev_b32 v5, 5, v3\n"
      "v_mul_u32_u24 v222, 288, v4\n"
      "v_add_u32 v1, v222, v213\n"
      "s_movk_i32 s46, 288\n"
      "v_mul_u32_u24 v222, 80, v3\n"
      "v_add3_u32 v212, v222, v224, s54\n"
      "v_and_b32 v176, 7, %3\n"
      "v_bfe_u32 v177, %3, 3, 1\n"
      "v_mad_u32_u24 v201, v176, s46, v95\n"
      "v_mad_u32_u24 v203, v177, s46, v95\n"
      "v_add3_u32 v178, v176, v177, 1\n"
      "v_and_b32 v178, 7, v178\n"
      "v_mad_u32_u24 v202, v178, s46, v95\n"
      "v_and_b32 v222, 11, %3\n"
      "v_cmp_eq_u32 vcc, 3, v222\n"
      "v_lshrrev_b32 v222, 2, v3\n"
      "v_add_u32 v222, 24, v222\n"
      "v_mad_u32_u24 v222, v222, s46, v95\n"
      "v_mad_u32_u24 v223, v3, s46, v95\n"
      "v_add_u32 v224, 2304, v223\n"
      "v_cndmask_b32 v214, v223, v222, vcc\n"
      "v_cndmask_b32 v220, v224, v222, vcc\n"
      "v_xor_b32 v223, 1, v177\n"
      "v_lshl_add_u32 v223, v223, 4, v176\n"
      "v_mad_u32_u24 v223, v223, s46, v95\n"
      "v_cndmask_b32 v221, v223, v222, vcc\n"
      "s_movk_i32 s48, 320\n"
      "v_mad_u32_u24 v200, v177, s48, v95\n"
      "v_cvt_f32_u32 v223, v225\n"
      "v_fmaak_f32 v223, -2.0, v223, 0x45229000\n"
      "v_sqrt_f32 v223, v223\n"
      "v_lshrrev_b32 v224, 2, v225\n"
      "v_fmaak_f32 v223, -0.5, v223, 0x41cc28f6\n"
      "v_cvt_u32_f32 v208, v223\n"
      "v_add_u32 v224, 1, v224\n"
      "v_sub_u32 v223, 49, v208\n"
      "v_mul_u32_u24 v223, v208, v223\n"
      "v_lshrrev_b32 v223, 1, v223\n"
      "v_sub_u32 v210, v224, v223\n"
      "v_cmp_eq_u32 vcc, 25, v210\n"
      "v_add_u32 v223, 1, v208\n"
      "s_mov_b32 s59, 0\n"
      "s_mov_b32 s57, 0\n"
      "v_cndmask_b32 v209, v208, v223, vcc\n"
      "v_cndmask_b32 v211, v210, v223, vcc\n"
      "v_add_u32 v211, 1, v211\n"
      "s_mul_i32 s46, s53, 3072\n"
      "v_lshl_add_u32 v218, %3, 4, s46\n"
      "s_xor_b32 s46, s53, 1\n"
      "v_lshl_add_u32 v4, s46, 4, v14\n"
      "s_xor_b32 s46, s53, 2\n"
      "v_lshl_add_u32 v222, s46, 4, v14\n"
      "s_xor_b32 s46, s53, 3\n"
      "v_lshl_add_u32 v223, s46, 4, v14\n"
      "s_xor_b32 s46, s53, 0\n"
      "v_lshl_add_u32 v14, s46, 4, v14\n"
      "s_waitcnt lgkmcnt(0)\n"
      "s_cmp_lt_u32 s33, s26\n"
      "s_cbranch_scc0 36f\n"
      "37:\n"
      "global_load_dwordx4 v[6:9], v10, s[8:9] nt\n"
      "global_load_dwordx3 v[10:12], v10, s[8:9] offset:16 nt\n"
      "v_mov_b32 v92, s20\n"
      "v_mov_b32 v93, s21\n"
      "s_mov_b64 exec, 0xffff\n"
      "v_mov_b32 v92, s22\n"
      "v_mov_b32 v93, s23\n"
      "s_lshl_b64 exec, exec, 16\n"
      "v_mov_b32 v92, s18\n"
      "v_mov_b32 v93, s19\n"
      "s_mov_b64 exec, -1\n"
      "v_add_co_u32 v92, vcc, v92, v13\n"
      "v_addc_co_u32 v93, vcc, 0, v93, vcc\n"
      "global_load_dwordx4 v[192:195], v[92:93], off\n"
      "s_cmp_lg_u32 s35, 0\n"
      "s_cbranch_scc1 21f\n"
      "s_barrier\n"
      "21:\n"
      "global_load_dwordx4 v[96:99], v80, s[16:17] offset:0\n"
      "global_load_dwordx4 v[100:103], v80, s[16:17] offset:256\n"
      "global_load_dwordx4 v[104:107], v81, s[16:17] offset:0\n"
      "global_load_dwordx4 v[108:111], v81, s[16:17] offset:256\n"
      "global_load_dwordx4 v[112:115], v82, s[16:17] offset:0\n"
      "global_load_dwordx4 v[116:119], v82, s[16:17] offset:256\n"
      "s_mov_b64 exec, 0\n"
      "s_cmp_eq_u32 s53, 0\n"
      "s_cbranch_scc1 40f\n"
      "s_cmp_eq_u32 s53, 1\n"
      "s_cbranch_scc1 41f\n"
      "s_cmp_eq_u32 s53, 2\n"
      "s_cbranch_scc1 42f\n"
      "s_branch 43f\n"
      "44:\n"
      "s_mov_b64 exec, -1\n"
      "v_lshlrev_b32_e64 v3, 1, s33\n"
      "v_mov_b64 v[176:177], -1\n"
      "v_mov_b64 v[178:179], -1\n"
      "ds_write_b128 v1, v[176:179] offset:8064\n"
      "ds_write_b128 v1, v[176:179] offset:9216\n"
      "ds_write_b128 v1, v[176:179] offset:10368\n"
      "v_mov_b64 v[180:181], 0\n"
      "v_mov_b64 v[182:183], 0\n"
      "ds_write_b128 v1, v[180:183] offset:11520\n"
      "s_waitcnt vmcnt(8)\n"
      "v_lshl_add_u32 v7, v7, 8, v13\n"
      "global_load_dwordx4 v[228:231], v7, s[10:11] nt\n"
      "v_lshl_add_u32 v8, v8, 8, v13\n"
      "global_load_dwordx4 v[232:235], v8, s[10:11] nt\n"
      "s_waitcnt vmcnt(9)\n"
      "v_mov_b32_dpp v6, v12 quad_perm:[0,1,2,3] row_mask:0xc bank_mask:0xf\n"
      "v_lshl_add_u32 v9, v9, 8, v13\n"
      "v_lshl_add_u32 v10, v10, 8, v13\n"
      "v_lshl_add_u32 v11, v11, 8, v13\n"
      "v_lshl_add_u32 v12, v12, 8, v13\n"
      "v_lshl_add_u32 v6, v6, 8, v13\n"
      "v_lshl_add_u32 v8, %3, 2, s61\n"
      "s_waitcnt vmcnt(8)\n"
      "v_mfma_f32_16x16x16_bf16 v[48:51], v[192:193], v[136:137], 0\n"
      "v_max3_f32 v224, |v192|, |v193|, |v195|\n"
      "v_lshlrev_b32 v176, 16, v192\n"
      "v_mfma_f32_16x16x16_bf16 v[52:55], v[192:193], v[138:139], 0\n"
      "v_and_b32 v177, s43, v192\n"
      "v_subrev_u32 v224, 0x3d000000, v224\n"
      "v_mfma_f32_16x16x16_bf16 v[56:59], v[192:193], v[140:141], 0\n"
      "v_lshlrev_b32 v178, 16, v193\n"
      "v_and_b32 v179, s43, v193\n"
      "v_mfma_f32_16x16x16_bf16 v[60:63], v[192:193], v[142:143], 0\n"
      "v_cmp_gt_u32 vcc, 0x5800000, v224\n"
      "v_lshlrev_b32 v180, 16, v194\n"
      "v_mfma_f32_16x16x16_bf16 v[64:67], v[194:195], v[136:137], 0\n"
      "v_and_b32 v181, s43, v194\n"
      "s_and_b32 s44, vcc_lo, 0xffff\n"
      "v_mfma_f32_16x16x16_bf16 v[68:71], v[194:195], v[138:139], 0\n"
      "v_lshlrev_b32 v182, 16, v195\n"
      "s_cselect_b32 s44, vcc_hi, 0\n"
      "v_mfma_f32_16x16x16_bf16 v[72:75], v[194:195], v[140:141], 0\n"
      "v_and_b32 v183, s43, v195\n"
      "s_cmp_eq_u32 s44, 0\n"
      "s_cbranch_scc1 22f\n"
      "23:\n"
      "v_mfma_f32_16x16x16_bf16 v[76:79], v[194:195], v[142:143], 0\n"
      "v_cvt_pk_f16_f32 v184, v176, v177\n"
      "v_mfma_f32_16x16x16_bf16 v[16:19], v[192:193], v[128:129], 0\n"
      "v_cvt_pk_f16_f32 v185, v178, v179\n"
      "v_mfma_f32_16x16x16_bf16 v[20:23], v[192:193], v[130:131], 0\n"
      "v_cvt_pk_f16_f32 v186, v180, v181\n"
      "v_mfma_f32_16x16x16_bf16 v[24:27], v[192:193], v[132:133], 0\n"
      "v_cvt_pk_f16_f32 v187, v182, v183\n"
      "v_mfma_f32_16x16x16_bf16 v[28:31], v[192:193], v[134:135], 0\n"
      "v_pk_mul_f16 v184, v184, s37\n"
      "v_mfma_f32_16x16x16_bf16 v[32:35], v[194:195], v[128:129], 0\n"
      "v_pk_mul_f16 v185, v185, s37\n"
      "v_mfma_f32_16x16x16_bf16 v[36:39], v[194:195], v[130:131], 0\n"
      "v_pk_mul_f16 v186, v186, s37\n"
      "v_mfma_f32_16x16x16_bf16 v[40:43], v[194:195], v[132:133], 0\n"
      "v_pk_mul_f16 v187, v187, s37\n"
      "v_mfma_f32_16x16x16_bf16 v[44:47], v[194:195], v[134:135], 0\n"
      "ds_write_b128 v91, v[184:187] offset:16512\n"
      "ds_read_b128 a[184:187], v95 offset:17088\n"
      "ds_read_b128 a[188:191], v95 offset:17152\n"
      "ds_read_b128 a[192:195], v95 offset:17216\n"
      "ds_read_b128 a[196:199], v95 offset:17280\n"
      "global_load_dwordx4 v[120:123], v83, s[16:17] offset:0\n"
      "global_load_dwordx4 v[124:127], v83, s[16:17] offset:256\n"
      "s_cmp_lg_u32 s35, 0\n"
      "s_cbranch_scc1 24f\n"
      "s_barrier\n"
      "s_mov_b32 s35, 1\n"
      "24:\n"
      "global_load_dwordx4 v[236:239], v9, s[10:11] nt\n"
      "s_cmp_lg_u32 s58, 0\n"
      "s_cbranch_scc1 70f\n"
      "71:\n"
      "s_waitcnt vmcnt(10)\n"
      "v_mfma_f32_16x16x32_bf16 v[128:131], v[96:99], v[86:89], 0\n"
      "s_waitcnt vmcnt(9)\n"
      "v_mfma_f32_16x16x32_bf16 v[132:135], v[100:103], v[86:89], 0\n"
      "v_mfma_f32_16x16x32_bf16 v[136:139], v[96:99], v[84:87], 0\n"
      "v_mfma_f32_16x16x32_bf16 v[140:143], v[100:103], v[84:87], 0\n"
      "v_max3_f32 v224, |v97|, |v98|, |v103|\n"
      "s_waitcnt vmcnt(8)\n"
      "v_mfma_f32_16x16x32_bf16 v[144:147], v[104:107], v[86:89], 0\n"
      "s_nop 2\n"
      "v_cvt_pk_f16_f32 v164, v128, v132\n"
      "v_cvt_pk_f16_f32 v165, v129, v133\n"
      "v_cvt_pk_f16_f32 v166, v130, v134\n"
      "v_cvt_pk_f16_f32 v167, v131, v135\n"
      "s_waitcnt vmcnt(7)\n"
      "v_mfma_f32_16x16x32_bf16 v[148:151], v[108:111], v[86:89], 0\n"
      "ds_write_addtid_b32 v164 offset:0\n"
      "ds_write_addtid_b32 v165 offset:272\n"
      "ds_write_addtid_b32 v166 offset:544\n"
      "ds_write_addtid_b32 v167 offset:816\n"
      "v_mfma_f32_16x16x32_bf16 v[152:155], v[104:107], v[84:87], 0\n"
      "v_cvt_pk_f16_f32 v168, v136, v140\n"
      "v_cvt_pk_f16_f32 v169, v137, v141\n"
      "v_cvt_pk_f16_f32 v170, v138, v142\n"
      "v_cvt_pk_f16_f32 v171, v139, v143\n"
      "v_mfma_f32_16x16x32_bf16 v[156:159], v[108:111], v[84:87], 0\n"
      "ds_write_addtid_b32 v168 offset:1088\n"
      "ds_write_addtid_b32 v169 offset:1360\n"
      "ds_write_addtid_b32 v170 offset:1632\n"
      "ds_write_addtid_b32 v171 offset:1904\n"
      "s_waitcnt vmcnt(6)\n"
      "v_mfma_f32_16x16x32_bf16 v[128:131], v[112:115], v[86:89], 0\n"
      "v_cvt_pk_f16_f32 v164, v144, v148\n"
      "v_cvt_pk_f16_f32 v165, v145, v149\n"
      "v_cvt_pk_f16_f32 v166, v146, v150\n"
      "v_cvt_pk_f16_f32 v167, v147, v151\n"
      "s_waitcnt vmcnt(5)\n"
      "v_mfma_f32_16x16x32_bf16 v[132:135], v[116:119], v[86:89], 0\n"
      "ds_write_addtid_b32 v164 offset:9344\n"
      "ds_write_addtid_b32 v165 offset:9616\n"
      "ds_write_addtid_b32 v166 offset:9888\n"
      "ds_write_addtid_b32 v167 offset:10160\n"
      "v_mfma_f32_16x16x32_bf16 v[136:139], v[112:115], v[84:87], 0\n"
      "v_cvt_pk_f16_f32 v168, v152, v156\n"
      "v_cvt_pk_f16_f32 v169, v153, v157\n"
      "v_cvt_pk_f16_f32 v170, v154, v158\n"
      "v_cvt_pk_f16_f32 v171, v155, v159\n"
      "v_mfma_f32_16x16x32_bf16 v[140:143], v[116:119], v[84:87], 0\n"
      "ds_write_addtid_b32 v168 offset:10432\n"
      "ds_write_addtid_b32 v169 offset:10704\n"
      "ds_write_addtid_b32 v170 offset:10976\n"
      "ds_write_addtid_b32 v171 offset:11248\n"
      "v_max3_f32 v224, |v112|, |v117|, v224\n"
      "v_cmp_le_f32 vcc, 0x3d000000, v224\n"
      "v_cvt_pk_f16_f32 v164, v128, v132\n"
      "v_cvt_pk_f16_f32 v165, v129, v133\n"
      "v_cvt_pk_f16_f32 v166, v130, v134\n"
      "v_cvt_pk_f16_f32 v167, v131, v135\n"
      "ds_write_addtid_b32 v164 offset:18688\n"
      "ds_write_addtid_b32 v165 offset:18960\n"
      "ds_write_addtid_b32 v166 offset:19232\n"
      "ds_write_addtid_b32 v167 offset:19504\n"
      "v_cvt_pk_f16_f32 v168, v136, v140\n"
      "v_cvt_pk_f16_f32 v169, v137, v141\n"
      "v_cvt_pk_f16_f32 v170, v138, v142\n"
      "v_cvt_pk_f16_f32 v171, v139, v143\n"
      "ds_write_addtid_b32 v168 offset:19776\n"
      "ds_write_addtid_b32 v169 offset:20048\n"
      "ds_write_addtid_b32 v170 offset:20320\n"
      "ds_write_addtid_b32 v171 offset:20592\n"
      "s_bcnt1_i32_b64 s52, vcc\n"
      "s_cmp_ge_u32 s52, 32\n"
      "s_cselect_b32 s52, 13, 0x7f000005\n"
      "ds_write_b32 v2, v2 offset:16\n"
      "global_load_dwordx4 v[204:207], v218, s[96:97] offset:0\n"
      "ds_read_b64_tr_b16 a[152:153], v216 offset:0\n"
      "ds_read_b64_tr_b16 a[154:155], v216 offset:1088\n"
      "ds_read_b64_tr_b16 a[156:157], v216 offset:64\n"
      "ds_read_b64_tr_b16 a[158:159], v216 offset:1152\n"
      "ds_read_b64_tr_b16 a[160:161], v216 offset:128\n"
      "ds_read_b64_tr_b16 a[162:163], v216 offset:1216\n"
      "ds_read_b64_tr_b16 a[164:165], v216 offset:192\n"
      "ds_read_b64_tr_b16 a[166:167], v216 offset:1280\n"
      "s_waitcnt lgkmcnt(6)\n"
      "v_mfma_f32_16x16x32_f16 a[200:203], a[184:187], a[152:155], 0\n"
      "s_waitcnt lgkmcnt(4)\n"
      "v_mfma_f32_16x16x32_f16 a[200:203], a[188:191], a[156:159], a[200:203]\n"
      "v_mov_b32 v7, 0xf149f2ca\n"
      "ds_read_b32 v224, v227 offset:37904\n"
      "s_waitcnt lgkmcnt(3)\n"
      "v_mfma_f32_16x16x32_f16 a[200:203], a[192:195], a[160:163], a[200:203]\n"
      "s_waitcnt lgkmcnt(1)\n"
      "v_mfma_f32_16x16x32_f16 a[200:203], a[196:199], a[164:167], a[200:203]\n"
      "s_waitcnt lgkmcnt(0)\n"
      "v_cmp_eq_u32 vcc, 0, v224\n"
      "s_nop 0\n"
      "s_cmp_lg_u32 vcc_lo, 0\n"
      "s_cbranch_scc0 14f\n"
      "11:\n"
      "ds_read_b32 v224, v227 offset:37904\n"
      "s_waitcnt lgkmcnt(0)\n"
      "v_cmp_eq_u32 vcc, 0, v224\n"
      "s_nop 0\n"
      "s_cmp_lg_u32 vcc_lo, 0\n"
      "s_cbranch_scc1 11b\n"
      "14:\n"
      "ds_read_b128 a[0:3], v14 offset:0\n"
      "ds_read_b128 a[16:19], v4 offset:0\n"
      "ds_read_b128 a[32:35], v222 offset:0\n"
      "ds_read_b128 a[48:51], v223 offset:0\n"
      "ds_read_b128 a[64:67], v14 offset:1088\n"
      "ds_read_b128 a[80:83], v4 offset:1088\n"
      "ds_read_b128 a[96:99], v222 offset:1088\n"
      "ds_read_b128 a[112:115], v223 offset:1088\n"
      "ds_read_b128 a[4:7], v14 offset:9344\n"
      "ds_read_b128 a[20:23], v4 offset:9344\n"
      "ds_read_b128 a[36:39], v222 offset:9344\n"
      "ds_read_b128 a[52:55], v223 offset:9344\n"
      "ds_read_b128 a[68:71], v14 offset:10432\n"
      "ds_read_b128 a[84:87], v4 offset:10432\n"
      "ds_read_b128 a[100:103], v222 offset:10432\n"
      "ds_read_b128 a[116:119], v223 offset:10432\n"
      "ds_read_b128 a[8:11], v14 offset:18688\n"
      "ds_read_b128 a[24:27], v4 offset:18688\n"
      "ds_read_b128 a[40:43], v222 offset:18688\n"
      "ds_read_b128 a[56:59], v223 offset:18688\n"
      "ds_read_b128 a[72:75], v14 offset:19776\n"
      "ds_read_b128 a[88:91], v4 offset:19776\n"
      "ds_read_b128 a[104:107], v222 offset:19776\n"
      "ds_read_b128 a[120:123], v223 offset:19776\n"
      "global_load_dwordx4 v[240:243], v10, s[10:11] nt\n"
      "3:\n"
      "s_waitcnt vmcnt(6)\n"
      "v_sub_u16_sdwa v176, v228, s36 clamp dst_sel:WORD_1 dst_unused:UNUSED_PAD src0_sel:WORD_0 src1_sel:DWORD\n"
      "v_sub_u16_sdwa v177, v228, s36 clamp dst_sel:WORD_1 dst_unused:UNUSED_PAD src0_sel:WORD_1 src1_sel:DWORD\n"
      "v_sub_u16_sdwa v178, v229, s36 clamp dst_sel:WORD_1 dst_unused:UNUSED_PAD src0_sel:WORD_0 src1_sel:DWORD\n"
      "v_sub_u16_sdwa v179, v229, s36 clamp dst_sel:WORD_1 dst_unused:UNUSED_PAD src0_sel:WORD_1 src1_sel:DWORD\n"
      "v_sub_u16_sdwa v180, v230, s36 clamp dst_sel:WORD_1 dst_unused:UNUSED_PAD src0_sel:WORD_0 src1_sel:DWORD\n"
      "v_sub_u16_sdwa v181, v230, s36 clamp dst_sel:WORD_1 dst_unused:UNUSED_PAD src0_sel:WORD_1 src1_sel:DWORD\n"
      "v_sub_u16_sdwa v182, v231, s36 clamp dst_sel:WORD_1 dst_unused:UNUSED_PAD src0_sel:WORD_0 src1_sel:DWORD\n"
      "v_sub_u16_sdwa v183, v231, s36 clamp dst_sel:WORD_1 dst_unused:UNUSED_PAD src0_sel:WORD_1 src1_sel:DWORD\n"
      "v_cvt_pk_f16_f32 v184, v176, v177\n"
      "v_cvt_pk_f16_f32 v185, v178, v179\n"
      "v_cvt_pk_f16_f32 v186, v180, v181\n"
      "v_cvt_pk_f16_f32 v187, v182, v183\n"
      "ds_write_b128 v1, v[184:187] offset:0\n"
      "s_waitcnt vmcnt(5)\n"
      "v_sub_u16_sdwa v144, v232, s36 clamp dst_sel:WORD_1 dst_unused:UNUSED_PAD src0_sel:WORD_0 src1_sel:DWORD\n"
      "v_sub_u16_sdwa v145, v232, s36 clamp dst_sel:WORD_1 dst_unused:UNUSED_PAD src0_sel:WORD_1 src1_sel:DWORD\n"
      "v_sub_u16_sdwa v146, v233, s36 clamp dst_sel:WORD_1 dst_unused:UNUSED_PAD src0_sel:WORD_0 src1_sel:DWORD\n"
      "v_sub_u16_sdwa v147, v233, s36 clamp dst_sel:WORD_1 dst_unused:UNUSED_PAD src0_sel:WORD_1 src1_sel:DWORD\n"
      "v_sub_u16_sdwa v148, v234, s36 clamp dst_sel:WORD_1 dst_unused:UNUSED_PAD src0_sel:WORD_0 src1_sel:DWORD\n"
      "v_sub_u16_sdwa v149, v234, s36 clamp dst_sel:WORD_1 dst_unused:UNUSED_PAD src0_sel:WORD_1 src1_sel:DWORD\n"
      "v_sub_u16_sdwa v150, v235, s36 clamp dst_sel:WORD_1 dst_unused:UNUSED_PAD src0_sel:WORD_0 src1_sel:DWORD\n"
      "v_sub_u16_sdwa v151, v235, s36 clamp dst_sel:WORD_1 dst_unused:UNUSED_PAD src0_sel:WORD_1 src1_sel:DWORD\n"
      "v_cvt_pk_f16_f32 v152, v144, v145\n"
      "v_cvt_pk_f16_f32 v153, v146, v147\n"
      "v_cvt_pk_f16_f32 v154, v148, v149\n"
      "v_cvt_pk_f16_f32 v155, v150, v151\n"
      "ds_write_b128 v1, v[152:155] offset:1152\n"
      "ds_read_b128 v[128:131], v201 offset:0\n"
      "ds_read_b128 v[160:163], v202 offset:0\n"
      "ds_read_b128 v[132:135], v201 offset:64\n"
      "ds_read_b128 v[164:167], v202 offset:64\n"
      "s_or_b32 s44, s55, s58\n"
      "s_cbranch_scc1 74f\n"
      "75:\n"
      "s_waitcnt vmcnt(4)\n"
      "v_mfma_f32_16x16x32_bf16 v[100:103], v[120:123], v[86:89], 0\n"
      "ds_read_b128 v[136:139], v201 offset:128\n"
      "s_waitcnt vmcnt(3)\n"
      "v_mfma_f32_16x16x32_bf16 v[104:107], v[124:127], v[86:89], 0\n"
      "ds_read_b128 v[168:171], v202 offset:128\n"
      "v_mfma_f32_16x16x32_bf16 v[108:111], v[120:123], v[84:87], 0\n"
      "ds_read_b128 v[140:143], v201 offset:192\n"
      "v_mfma_f32_16x16x32_bf16 v[112:115], v[124:127], v[84:87], 0\n"
      "ds_read_b128 v[172:175], v202 offset:192\n"
      "s_waitcnt lgkmcnt(6)\n"
      "v_pk_mul_f16 v96, v128, v160\n"
      "v_pk_mul_f16 v97, v129, v161\n"
      "v_pk_mul_f16 v98, v130, v162\n"
      "v_pk_mul_f16 v99, v131, v163\n"
      "v_cvt_pk_f16_f32 v116, v100, v104\n"
      "v_cvt_pk_f16_f32 v117, v101, v105\n"
      "v_cvt_pk_f16_f32 v118, v102, v106\n"
      "v_cvt_pk_f16_f32 v119, v103, v107\n"
      "ds_write_addtid_b32 v116 offset:28032\n"
      "ds_write_addtid_b32 v117 offset:28304\n"
      "ds_write_addtid_b32 v118 offset:28576\n"
      "ds_write_addtid_b32 v119 offset:28848\n"
      "v_cvt_pk_f16_f32 v184, v108, v112\n"
      "v_cvt_pk_f16_f32 v185, v109, v113\n"
      "v_cvt_pk_f16_f32 v186, v110, v114\n"
      "v_cvt_pk_f16_f32 v187, v111, v115\n"
      "ds_write_addtid_b32 v184 offset:29120\n"
      "ds_write_addtid_b32 v185 offset:29392\n"
      "ds_write_addtid_b32 v186 offset:29664\n"
      "ds_write_addtid_b32 v187 offset:29936\n"
      "ds_write_b32 v2, v2 offset:32\n"
      "31:\n"
      "v_mfma_f32_16x16x32_f16 v[80:83], a[0:3], v[96:99], v[16:19]\n"
      "s_waitcnt lgkmcnt(13)\n"
      "v_pk_mul_f16 v100, v132, v164\n"
      "v_pk_mul_f16 v101, v133, v165\n"
      "v_mfma_f32_16x16x32_f16 v[84:87], a[16:19], v[96:99], v[20:23]\n"
      "v_pk_mul_f16 v102, v134, v166\n"
      "v_pk_mul_f16 v103, v135, v167\n"
      "v_mfma_f32_16x16x32_f16 v[88:91], a[32:35], v[96:99], v[24:27]\n"
      "s_waitcnt lgkmcnt(11)\n"
      "v_pk_mul_f16 v104, v136, v168\n"
      "v_pk_mul_f16 v105, v137, v169\n"
      "v_mfma_f32_16x16x32_f16 v[92:95], a[48:51], v[96:99], v[28:31]\n"
      "s_waitcnt vmcnt(2)\n"
      "v_sub_u16_sdwa v176, v236, s36 clamp dst_sel:WORD_1 dst_unused:UNUSED_PAD src0_sel:WORD_0 src1_sel:DWORD\n"
      "v_sub_u16_sdwa v177, v236, s36 clamp dst_sel:WORD_1 dst_unused:UNUSED_PAD src0_sel:WORD_1 src1_sel:DWORD\n"
      "v_sub_u16_sdwa v178, v237, s36 clamp dst_sel:WORD_1 dst_unused:UNUSED_PAD src0_sel:WORD_0 src1_sel:DWORD\n"
      "v_sub_u16_sdwa v179, v237, s36 clamp dst_sel:WORD_1 dst_unused:UNUSED_PAD src0_sel:WORD_1 src1_sel:DWORD\n"
      "v_mfma_f32_16x16x32_f16 v[244:247], a[64:67], v[96:99], v[32:35]\n"
      "v_sub_u16_sdwa v180, v238, s36 clamp dst_sel:WORD_1 dst_unused:UNUSED_PAD src0_sel:WORD_0 src1_sel:DWORD\n"
      "v_sub_u16_sdwa v181, v238, s36 clamp dst_sel:WORD_1 dst_unused:UNUSED_PAD src0_sel:WORD_1 src1_sel:DWORD\n"
      "v_mfma_f32_16x16x32_f16 v[248:251], a[80:83], v[96:99], v[36:39]\n"
      "v_sub_u16_sdwa v182, v239, s36 clamp dst_sel:WORD_1 dst_unused:UNUSED_PAD src0_sel:WORD_0 src1_sel:DWORD\n"
      "v_sub_u16_sdwa v183, v239, s36 clamp dst_sel:WORD_1 dst_unused:UNUSED_PAD src0_sel:WORD_1 src1_sel:DWORD\n"
      "v_mfma_f32_16x16x32_f16 v[252:255], a[96:99], v[96:99], v[40:43]\n"
      "v_cvt_pk_f16_f32 v184, v176, v177\n"
      "v_cvt_pk_f16_f32 v185, v178, v179\n"
      "v_mfma_f32_16x16x32_f16 v[192:195], a[112:115], v[96:99], v[44:47]\n"
      "v_cvt_pk_f16_f32 v186, v180, v181\n"
      "v_cvt_pk_f16_f32 v187, v182, v183\n"
      "v_mfma_f32_16x16x32_f16 v[80:83], a[4:7], v[100:103], v[80:83]\n"
      "ds_write_b128 v1, v[184:187] offset:2304\n"
      "v_mfma_f32_16x16x32_f16 v[84:87], a[20:23], v[100:103], v[84:87]\n"
      "ds_read_b32 v224, v227 offset:37920\n"
      "v_pk_mul_f16 v106, v138, v170\n"
      "v_mfma_f32_16x16x32_f16 v[88:91], a[36:39], v[100:103], v[88:91]\n"
      "s_waitcnt lgkmcnt(11)\n"
      "v_pk_mul_f16 v107, v139, v171\n"
      "v_pk_mul_f16 v108, v140, v172\n"
      "v_mfma_f32_16x16x32_f16 v[92:95], a[52:55], v[100:103], v[92:95]\n"
      "v_pk_mul_f16 v109, v141, v173\n"
      "v_pk_mul_f16 v110, v142, v174\n"
      "v_mfma_f32_16x16x32_f16 v[244:247], a[68:71], v[100:103], v[244:247]\n"
      "ds_read_b128 v[144:147], v203 offset:2304\n"
      "v_pk_mul_f16 v111, v143, v175\n"
      "v_mfma_f32_16x16x32_f16 v[248:251], a[84:87], v[100:103], v[248:251]\n"
      "ds_read_b128 v[148:151], v203 offset:2368\n"
      "global_load_dwordx2 v[196:197], v225, s[12:13]\n"
      "v_mfma_f32_16x16x32_f16 v[252:255], a[100:103], v[100:103], v[252:255]\n"
      "ds_read_b128 v[152:155], v203 offset:2432\n"
      "global_load_dwordx2 v[198:199], v225, s[14:15]\n"
      "v_mfma_f32_16x16x32_f16 v[192:195], a[116:119], v[100:103], v[192:195]\n"
      "s_waitcnt lgkmcnt(3)\n"
      "ds_read_b128 v[156:159], v203 offset:2496\n"
      "v_cmp_eq_u32 vcc, 0, v224\n"
      "v_mfma_f32_16x16x32_f16 v[80:83], a[8:11], v[104:107], v[80:83]\n"
      "global_load_ushort a140, v3, s[24:25]\n"
      "ds_read_b128 v[160:163], v203 offset:2880\n"
      "v_mfma_f32_16x16x32_f16 v[84:87], a[24:27], v[104:107], v[84:87]\n"
      "s_cbranch_vccz 13f\n"
      "12:\n"
      "ds_read_b32 v224, v227 offset:37920\n"
      "s_waitcnt lgkmcnt(0)\n"
      "v_cmp_eq_u32 vcc, 0, v224\n"
      "s_nop 4\n"
      "s_cbranch_vccnz 12b\n"
      "13:\n"
      "ds_read_b128 a[12:15], v14 offset:28032\n"
      "v_mfma_f32_16x16x32_f16 v[88:91], a[40:43], v[104:107], v[88:91]\n"
      "ds_read_b128 a[28:31], v4 offset:28032\n"
      "v_max3_f32 v226, |v228|, |v229|, |v230|\n"
      "v_mfma_f32_16x16x32_f16 v[92:95], a[56:59], v[104:107], v[92:95]\n"
      "ds_read_b128 a[44:47], v222 offset:28032\n"
      "v_cmp_le_f32 s[28:29], s99, v226\n"
      "v_mfma_f32_16x16x32_f16 v[244:247], a[72:75], v[104:107], v[244:247]\n"
      "s_waitcnt lgkmcnt(7)\n"
      "ds_read_b128 a[60:63], v223 offset:28032\n"
      "v_pk_mul_f16 v112, v128, v144\n"
      "v_mfma_f32_16x16x32_f16 v[248:251], a[88:91], v[104:107], v[248:251]\n"
      "ds_read_b128 a[76:79], v14 offset:29120\n"
      "s_bcnt1_i32_b64 s44, s[28:29]\n"
      "v_pk_mul_f16 v113, v129, v145\n"
      "v_mfma_f32_16x16x32_f16 v[252:255], a[104:107], v[104:107], v[252:255]\n"
      "s_waitcnt lgkmcnt(8)\n"
      "ds_read_b128 a[92:95], v4 offset:29120\n"
      "v_pk_mul_f16 v116, v132, v148\n"
      "v_mfma_f32_16x16x32_f16 v[192:195], a[120:123], v[104:107], v[192:195]\n"
      "s_waitcnt lgkmcnt(4)\n"
      "ds_read_b128 a[108:111], v222 offset:29120\n"
      "s_mov_b32 vcc_hi, 0\n"
      "v_pk_mul_f16 v114, v130, v146\n"
      "v_mfma_f32_16x16x32_f16 v[80:83], a[12:15], v[108:111], v[80:83]\n"
      "ds_read_b128 a[124:127], v223 offset:29120\n"
      "s_cmp_lt_u32 s44, 24\n"
      "s_cselect_b32 vcc_lo, -1, s60\n"
      "v_pk_mul_f16 v117, v133, v149\n"
      "v_mfma_f32_16x16x32_f16 v[84:87], a[28:31], v[108:111], v[84:87]\n"
      "s_waitcnt lgkmcnt(4)\n"
      "v_pk_mul_f16 v120, v136, v152\n"
      "v_pk_mul_f16 v115, v131, v147\n"
      "v_mfma_f32_16x16x32_f16 v[88:91], a[44:47], v[108:111], v[88:91]\n"
      "v_pk_mul_f16 v118, v134, v150\n"
      "v_pk_mul_f16 v121, v137, v153\n"
      "v_mfma_f32_16x16x32_f16 v[92:95], a[60:63], v[108:111], v[92:95]\n"
      "s_waitcnt lgkmcnt(2)\n"
      "v_mul_f32 v188, |v80|, v48\n"
      "v_fma_f32 v188, |v81|, v49, v188\n"
      "v_mfma_f32_16x16x32_f16 v[244:247], a[76:79], v[108:111], v[244:247]\n"
      "v_pk_mul_f16 v124, v140, v156\n"
      "v_fma_f32 v188, |v82|, v50, v188\n"
      "v_mfma_f32_16x16x32_f16 v[248:251], a[92:95], v[108:111], v[248:251]\n"
      "s_waitcnt lgkmcnt(0)\n"
      "v_fma_f32 v188, |v83|, v51, v188\n"
      "v_pk_mul_f16 v119, v135, v151\n"
      "v_mfma_f32_16x16x32_f16 v[252:255], a[108:111], v[108:111], v[252:255]\n"
      "v_pk_mul_f16 v122, v138, v154\n"
      "v_pk_mul_f16 v125, v141, v157\n"
      "v_mfma_f32_16x16x32_f16 v[192:195], a[124:127], v[108:111], v[192:195]\n"
      "v_pk_mul_f16 v123, v139, v155\n"
      "v_pk_mul_f16 v126, v142, v158\n"
      "v_mfma_f32_16x16x32_f16 v[80:83], a[0:3], v[112:115], v[16:19]\n"
      "v_pk_mul_f16 v127, v143, v159\n"
      "v_fma_f32 v188, |v84|, v52, v188\n"
      "v_mfma_f32_16x16x32_f16 v[80:83], a[4:7], v[116:119], v[80:83]\n"
      "40:\n"
      "s_cbranch_execz 44b\n"
      "v_mfma_f32_16x16x32_f16 v[80:83], a[8:11], v[120:123], v[80:83]\n"
      "v_fma_f32 v188, |v85|, v53, v188\n"
      "v_fma_f32 v188, |v86|, v54, v188\n"
      "v_mfma_f32_16x16x32_f16 v[80:83], a[12:15], v[124:127], v[80:83]\n"
      "v_fma_f32 v188, |v87|, v55, v188\n"
      "v_fma_f32 v188, |v88|, v56, v188\n"
      "v_mfma_f32_16x16x32_f16 v[84:87], a[28:31], v[124:127], v[20:23]\n"
      "v_fma_f32 v188, |v89|, v57, v188\n"
      "v_fma_f32 v188, |v90|, v58, v188\n"
      "v_mfma_f32_16x16x32_f16 v[84:87], a[24:27], v[120:123], v[84:87]\n"
      "v_fma_f32 v188, |v91|, v59, v188\n"
      "v_fma_f32 v188, |v92|, v60, v188\n"
      "v_mfma_f32_16x16x32_f16 v[84:87], a[20:23], v[116:119], v[84:87]\n"
      "ds_read_b128 v[164:167], v203 offset:2944\n"
      "v_fma_f32 v188, |v93|, v61, v188\n"
      "v_mfma_f32_16x16x32_f16 v[84:87], a[16:19], v[112:115], v[84:87]\n"
      "v_fma_f32 v188, |v94|, v62, v188\n"
      "v_fma_f32 v188, |v95|, v63, v188\n"
      "v_mfma_f32_16x16x32_f16 v[88:91], a[32:35], v[112:115], v[24:27]\n"
      "v_pk_mul_f16 v96, v128, v160\n"
      "v_pk_mul_f16 v97, v129, v161\n"
      "v_mfma_f32_16x16x32_f16 v[88:91], a[36:39], v[116:119], v[88:91]\n"
      "v_fma_f32 v188, |v244|, v64, v188\n"
      "v_pk_mul_f16 v98, v130, v162\n"
      "v_mfma_f32_16x16x32_f16 v[88:91], a[40:43], v[120:123], v[88:91]\n"
      "v_mul_f32 v189, |v80|, v48\n"
      "v_fma_f32 v189, |v81|, v49, v189\n"
      "v_mfma_f32_16x16x32_f16 v[88:91], a[44:47], v[124:127], v[88:91]\n"
      "v_pk_mul_f16 v99, v131, v163\n"
      "v_fma_f32 v188, |v245|, v65, v188\n"
      "v_mfma_f32_16x16x32_f16 v[92:95], a[60:63], v[124:127], v[28:31]\n"
      "v_fma_f32 v189, |v82|, v50, v189\n"
      "v_fma_f32 v189, |v83|, v51, v189\n"
      "v_mfma_f32_16x16x32_f16 v[92:95], a[56:59], v[120:123], v[92:95]\n"
      "ds_read_b128 v[168:171], v203 offset:3008\n"
      "v_fma_f32 v188, |v246|, v66, v188\n"
      "v_mfma_f32_16x16x32_f16 v[92:95], a[52:55], v[116:119], v[92:95]\n"
      "v_fma_f32 v189, |v84|, v52, v189\n"
      "v_fma_f32 v189, |v85|, v53, v189\n"
      "v_mfma_f32_16x16x32_f16 v[92:95], a[48:51], v[112:115], v[92:95]\n"
      "s_waitcnt lgkmcnt(1)\n"
      "v_pk_mul_f16 v100, v132, v164\n"
      "v_fma_f32 v188, |v247|, v67, v188\n"
      "v_mfma_f32_16x16x32_f16 v[80:83], a[64:67], v[112:115], v[32:35]\n"
      "v_fma_f32 v189, |v86|, v54, v189\n"
      "v_fma_f32 v189, |v87|, v55, v189\n"
      "v_mfma_f32_16x16x32_f16 v[80:83], a[68:71], v[116:119], v[80:83]\n"
      "v_pk_mul_f16 v101, v133, v165\n"
      "v_pk_mul_f16 v102, v134, v166\n"
      "v_mfma_f32_16x16x32_f16 v[80:83], a[72:75], v[120:123], v[80:83]\n"
      "v_fma_f32 v188, |v248|, v68, v188\n"
      "v_pk_mul_f16 v103, v135, v167\n"
      "v_mfma_f32_16x16x32_f16 v[80:83], a[76:79], v[124:127], v[80:83]\n"
      "v_fma_f32 v189, |v88|, v56, v189\n"
      "v_fma_f32 v189, |v89|, v57, v189\n"
      "v_mfma_f32_16x16x32_f16 v[84:87], a[92:95], v[124:127], v[36:39]\n"
      "ds_read_b128 v[172:175], v203 offset:3072\n"
      "v_fma_f32 v188, |v249|, v69, v188\n"
      "v_mfma_f32_16x16x32_f16 v[84:87], a[88:91], v[120:123], v[84:87]\n"
      "v_fma_f32 v189, |v90|, v58, v189\n"
      "v_fma_f32 v189, |v91|, v59, v189\n"
      "v_mfma_f32_16x16x32_f16 v[84:87], a[84:87], v[116:119], v[84:87]\n"
      "s_waitcnt lgkmcnt(1)\n"
      "v_pk_mul_f16 v104, v136, v168\n"
      "v_fma_f32 v188, |v250|, v70, v188\n"
      "v_mfma_f32_16x16x32_f16 v[84:87], a[80:83], v[112:115], v[84:87]\n"
      "v_pk_mul_f16 v105, v137, v169\n"
      "v_fma_f32 v189, |v92|, v60, v189\n"
      "v_mfma_f32_16x16x32_f16 v[88:91], a[96:99], v[112:115], v[40:43]\n"
      "v_fma_f32 v189, |v93|, v61, v189\n"
      "v_pk_mul_f16 v106, v138, v170\n"
      "v_mfma_f32_16x16x32_f16 v[88:91], a[100:103], v[116:119], v[88:91]\n"
      "v_fma_f32 v188, |v251|, v71, v188\n"
      "v_fma_f32 v189, |v94|, v62, v189\n"
      "v_mfma_f32_16x16x32_f16 v[88:91], a[104:107], v[120:123], v[88:91]\n"
      "v_fma_f32 v189, |v95|, v63, v189\n"
      "v_pk_mul_f16 v107, v139, v171\n"
      "v_mfma_f32_16x16x32_f16 v[88:91], a[108:111], v[124:127], v[88:91]\n"
      "v_fma_f32 v188, |v252|, v72, v188\n"
      "v_fma_f32 v189, |v80|, v64, v189\n"
      "v_mfma_f32_16x16x32_f16 v[92:95], a[124:127], v[124:127], v[44:47]\n"
      "s_waitcnt lgkmcnt(0)\n"
      "v_fma_f32 v189, |v81|, v65, v189\n"
      "v_pk_mul_f16 v108, v140, v172\n"
      "v_mfma_f32_16x16x32_f16 v[92:95], a[120:123], v[120:123], v[92:95]\n"
      "v_fma_f32 v188, |v253|, v73, v188\n"
      "v_fma_f32 v189, |v82|, v66, v189\n"
      "v_mfma_f32_16x16x32_f16 v[92:95], a[116:119], v[116:119], v[92:95]\n"
      "v_fma_f32 v189, |v83|, v67, v189\n"
      "v_pk_mul_f16 v109, v141, v173\n"
      "v_mfma_f32_16x16x32_f16 v[92:95], a[112:115], v[112:115], v[92:95]\n"
      "v_pk_mul_f16 v110, v142, v174\n"
      "v_fma_f32 v188, |v254|, v74, v188\n"
      "v_mfma_f32_16x16x32_f16 v[80:83], a[0:3], v[96:99], v[16:19]\n"
      "v_pk_mul_f16 v111, v143, v175\n"
      "v_fma_f32 v189, |v84|, v68, v189\n"
      "v_mfma_f32_16x16x32_f16 v[80:83], a[4:7], v[100:103], v[80:83]\n"
      "v_fma_f32 v189, |v85|, v69, v189\n"
      "v_fma_f32 v188, |v255|, v75, v188\n"
      "v_mfma_f32_16x16x32_f16 v[80:83], a[8:11], v[104:107], v[80:83]\n"
      "v_fma_f32 v189, |v86|, v70, v189\n"
      "v_fma_f32 v189, |v87|, v71, v189\n"
      "v_mfma_f32_16x16x32_f16 v[80:83], a[12:15], v[108:111], v[80:83]\n"
      "s_waitcnt vmcnt(4)\n"
      "ds_read_b128 v[112:115], v204 offset:0\n"
      "v_fma_f32 v188, |v192|, v76, v188\n"
      "v_mfma_f32_16x16x32_f16 v[84:87], a[28:31], v[108:111], v[20:23]\n"
      "ds_read_b128 v[160:163], v205 offset:0\n"
      "v_fma_f32 v189, |v88|, v72, v189\n"
      "v_mfma_f32_16x16x32_f16 v[84:87], a[24:27], v[104:107], v[84:87]\n"
      "v_fma_f32 v189, |v89|, v73, v189\n"
      "v_fma_f32 v188, |v193|, v77, v188\n"
      "v_mfma_f32_16x16x32_f16 v[84:87], a[20:23], v[100:103], v[84:87]\n"
      "v_fma_f32 v189, |v90|, v74, v189\n"
      "v_fma_f32 v189, |v91|, v75, v189\n"
      "v_mfma_f32_16x16x32_f16 v[84:87], a[16:19], v[96:99], v[84:87]\n"
      "v_fma_f32 v188, |v194|, v78, v188\n"
      "v_fma_f32 v189, |v92|, v76, v189\n"
      "v_mfma_f32_16x16x32_f16 v[88:91], a[32:35], v[96:99], v[24:27]\n"
      "v_fma_f32 v189, |v93|, v77, v189\n"
      "v_fma_f32 v188, |v195|, v79, v188\n"
      "v_mfma_f32_16x16x32_f16 v[88:91], a[36:39], v[100:103], v[88:91]\n"
      "v_fma_f32 v189, |v94|, v78, v189\n"
      "v_fma_f32 v189, |v95|, v79, v189\n"
      "v_mfma_f32_16x16x32_f16 v[88:91], a[40:43], v[104:107], v[88:91]\n"
      "ds_read_b128 v[116:119], v204 offset:64\n"
      "v_mul_f32 v190, |v80|, v48\n"
      "v_mfma_f32_16x16x32_f16 v[88:91], a[44:47], v[108:111], v[88:91]\n"
      "ds_read_b128 v[164:167], v205 offset:64\n"
      "v_fma_f32 v190, |v81|, v49, v190\n"
      "v_mfma_f32_16x16x32_f16 v[92:95], a[60:63], v[108:111], v[28:31]\n"
      "s_waitcnt lgkmcnt(2)\n"
      "v_pk_mul_f16 v112, v112, v160\n"
      "v_pk_mul_f16 v113, v113, v161\n"
      "v_mfma_f32_16x16x32_f16 v[92:95], a[56:59], v[104:107], v[92:95]\n"
      "v_fma_f32 v190, |v82|, v50, v190\n"
      "v_fma_f32 v190, |v83|, v51, v190\n"
      "v_mfma_f32_16x16x32_f16 v[92:95], a[52:55], v[100:103], v[92:95]\n"
      "v_pk_mul_f16 v114, v114, v162\n"
      "v_pk_mul_f16 v115, v115, v163\n"
      "v_mfma_f32_16x16x32_f16 v[92:95], a[48:51], v[96:99], v[92:95]\n"
      "ds_read_b128 v[120:123], v204 offset:128\n"
      "v_fma_f32 v190, |v84|, v52, v190\n"
      "v_mfma_f32_16x16x32_f16 v[80:83], a[64:67], v[96:99], v[32:35]\n"
      "ds_read_b128 v[168:171], v205 offset:128\n"
      "v_fma_f32 v190, |v85|, v53, v190\n"
      "v_mfma_f32_16x16x32_f16 v[80:83], a[68:71], v[100:103], v[80:83]\n"
      "s_cbranch_vccnz 33f\n"
      "global_load_dwordx4 v[244:247], v11, s[10:11] nt\n"
      "global_load_dwordx4 v[248:251], v12, s[10:11] nt\n"
      "global_load_dwordx4 v[252:255], v6, s[10:11] nt\n"
      "32:\n"
      "v_mfma_f32_16x16x32_f16 v[80:83], a[72:75], v[104:107], v[80:83]\n"
      "s_mov_b32 s99, 0x71800000\n"
      "v_fma_f32 v190, |v86|, v54, v190\n"
      "v_fma_f32 v190, |v87|, v55, v190\n"
      "v_mfma_f32_16x16x32_f16 v[80:83], a[76:79], v[108:111], v[80:83]\n"
      "s_waitcnt lgkmcnt(2)\n"
      "v_pk_mul_f16 v116, v116, v164\n"
      "v_pk_mul_f16 v117, v117, v165\n"
      "v_mfma_f32_16x16x32_f16 v[84:87], a[92:95], v[108:111], v[36:39]\n"
      "v_pk_mul_f16 v118, v118, v166\n"
      "v_fma_f32 v190, |v88|, v56, v190\n"
      "v_mfma_f32_16x16x32_f16 v[84:87], a[88:91], v[104:107], v[84:87]\n"
      "v_fma_f32 v190, |v89|, v57, v190\n"
      "v_pk_mul_f16 v119, v119, v167\n"
      "v_mfma_f32_16x16x32_f16 v[84:87], a[84:87], v[100:103], v[84:87]\n"
      "ds_read_b128 v[124:127], v204 offset:192\n"
      "ds_read_b128 v[172:175], v205 offset:192\n"
      "v_mfma_f32_16x16x32_f16 v[84:87], a[80:83], v[96:99], v[84:87]\n"
      "v_fma_f32 v190, |v90|, v58, v190\n"
      "v_fma_f32 v190, |v91|, v59, v190\n"
      "v_mfma_f32_16x16x32_f16 v[88:91], a[96:99], v[96:99], v[40:43]\n"
      "s_waitcnt vmcnt(6) lgkmcnt(2)\n"
      "v_sub_u16_sdwa v176, v240, s36 clamp dst_sel:WORD_1 dst_unused:UNUSED_PAD src0_sel:WORD_0 src1_sel:DWORD\n"
      "v_pk_mul_f16 v120, v120, v168\n"
      "v_mfma_f32_16x16x32_f16 v[88:91], a[100:103], v[100:103], v[88:91]\n"
      "v_sub_u16_sdwa v177, v240, s36 clamp dst_sel:WORD_1 dst_unused:UNUSED_PAD src0_sel:WORD_1 src1_sel:DWORD\n"
      "v_pk_mul_f16 v121, v121, v169\n"
      "v_mfma_f32_16x16x32_f16 v[88:91], a[104:107], v[104:107], v[88:91]\n"
      "v_fma_f32 v190, |v92|, v60, v190\n"
      "v_fma_f32 v190, |v93|, v61, v190\n"
      "v_sub_u16_sdwa v178, v241, s36 clamp dst_sel:WORD_1 dst_unused:UNUSED_PAD src0_sel:WORD_0 src1_sel:DWORD\n"
      "v_pk_mul_f16 v122, v122, v170\n"
      "v_mfma_f32_16x16x32_f16 v[88:91], a[108:111], v[108:111], v[88:91]\n"
      "v_fma_f32 v190, |v94|, v62, v190\n"
      "v_fma_f32 v190, |v95|, v63, v190\n"
      "v_sub_u16_sdwa v179, v241, s36 clamp dst_sel:WORD_1 dst_unused:UNUSED_PAD src0_sel:WORD_1 src1_sel:DWORD\n"
      "v_pk_mul_f16 v123, v123, v171\n"
      "v_mfma_f32_16x16x32_f16 v[92:95], a[124:127], v[108:111], v[44:47]\n"
      "v_sub_u16_sdwa v180, v242, s36 clamp dst_sel:WORD_1 dst_unused:UNUSED_PAD src0_sel:WORD_0 src1_sel:DWORD\n"
      "v_sub_u16_sdwa v181, v242, s36 clamp dst_sel:WORD_1 dst_unused:UNUSED_PAD src0_sel:WORD_1 src1_sel:DWORD\n"
      "v_mfma_f32_16x16x32_f16 v[92:95], a[120:123], v[104:107], v[92:95]\n"
      "v_fma_f32 v190, |v80|, v64, v190\n"
      "v_fma_f32 v190, |v81|, v65, v190\n"
      "v_mfma_f32_16x16x32_f16 v[92:95], a[116:119], v[100:103], v[92:95]\n"
      "s_waitcnt lgkmcnt(0)\n"
      "v_sub_u16_sdwa v182, v243, s36 clamp dst_sel:WORD_1 dst_unused:UNUSED_PAD src0_sel:WORD_0 src1_sel:DWORD\n"
      "v_pk_mul_f16 v124, v124, v172\n"
      "v_mfma_f32_16x16x32_f16 v[92:95], a[112:115], v[96:99], v[92:95]\n"
      "v_fma_f32 v190, |v82|, v66, v190\n"
      "v_fma_f32 v190, |v83|, v67, v190\n"
      "v_sub_u16_sdwa v183, v243, s36 clamp dst_sel:WORD_1 dst_unused:UNUSED_PAD src0_sel:WORD_1 src1_sel:DWORD\n"
      "v_pk_mul_f16 v125, v125, v173\n"
      "v_mfma_f32_16x16x32_f16 v[80:83], a[0:3], v[112:115], v[16:19]\n"
      "v_cvt_pk_f16_f32 v184, v176, v177\n"
      "v_pk_mul_f16 v126, v126, v174\n"
      "v_mfma_f32_16x16x32_f16 v[80:83], a[4:7], v[116:119], v[80:83]\n"
      "v_cvt_pk_f16_f32 v185, v178, v179\n"
      "v_pk_mul_f16 v127, v127, v175\n"
      "v_mfma_f32_16x16x32_f16 v[80:83], a[8:11], v[120:123], v[80:83]\n"
      "v_fma_f32 v190, |v84|, v68, v190\n"
      "v_fma_f32 v190, |v85|, v69, v190\n"
      "v_cvt_pk_f16_f32 v186, v180, v181\n"
      "v_mfma_f32_16x16x32_f16 v[80:83], a[12:15], v[124:127], v[80:83]\n"
      "v_fma_f32 v190, |v86|, v70, v190\n"
      "v_fma_f32 v190, |v87|, v71, v190\n"
      "v_cvt_pk_f16_f32 v187, v182, v183\n"
      "v_mfma_f32_16x16x32_f16 v[84:87], a[28:31], v[124:127], v[20:23]\n"
      "ds_write_b128 v1, v[184:187] offset:3456\n"
      "v_mfma_f32_16x16x32_f16 v[84:87], a[24:27], v[120:123], v[84:87]\n"
      "ds_read_b128 v[160:163], v203 offset:3456\n"
      "v_fma_f32 v190, |v88|, v72, v190\n"
      "v_mfma_f32_16x16x32_f16 v[84:87], a[20:23], v[116:119], v[84:87]\n"
      "v_fma_f32 v190, |v89|, v73, v190\n"
      "v_fma_f32 v190, |v90|, v74, v190\n"
      "v_mfma_f32_16x16x32_f16 v[84:87], a[16:19], v[112:115], v[84:87]\n"
      "v_fma_f32 v190, |v91|, v75, v190\n"
      "v_fma_f32 v190, |v92|, v76, v190\n"
      "v_mfma_f32_16x16x32_f16 v[88:91], a[32:35], v[112:115], v[24:27]\n"
      "v_fma_f32 v190, |v93|, v77, v190\n"
      "v_fma_f32 v190, |v94|, v78, v190\n"
      "v_mfma_f32_16x16x32_f16 v[88:91], a[36:39], v[116:119], v[88:91]\n"
      "ds_read_b128 v[164:167], v203 offset:3520\n"
      "v_fma_f32 v190, |v95|, v79, v190\n"
      "v_mfma_f32_16x16x32_f16 v[88:91], a[40:43], v[120:123], v[88:91]\n"
      "v_mul_f32 v194, |v80|, v48\n"
      "v_fma_f32 v194, |v81|, v49, v194\n"
      "v_mfma_f32_16x16x32_f16 v[88:91], a[44:47], v[124:127], v[88:91]\n"
      "v_fma_f32 v194, |v82|, v50, v194\n"
      "v_fma_f32 v194, |v83|, v51, v194\n"
      "v_mfma_f32_16x16x32_f16 v[92:95], a[60:63], v[124:127], v[28:31]\n"
      "ds_read_b128 v[168:171], v203 offset:3584\n"
      "v_fma_f32 v194, |v84|, v52, v194\n"
      "v_mfma_f32_16x16x32_f16 v[92:95], a[56:59], v[120:123], v[92:95]\n"
      "s_waitcnt lgkmcnt(2)\n"
      "v_pk_mul_f16 v96, v128, v160\n"
      "v_pk_mul_f16 v97, v129, v161\n"
      "v_mfma_f32_16x16x32_f16 v[92:95], a[52:55], v[116:119], v[92:95]\n"
      "v_pk_mul_f16 v98, v130, v162\n"
      "v_pk_mul_f16 v99, v131, v163\n"
      "v_mfma_f32_16x16x32_f16 v[92:95], a[48:51], v[112:115], v[92:95]\n"
      "v_fma_f32 v194, |v85|, v53, v194\n"
      "v_fma_f32 v194, |v86|, v54, v194\n"
      "v_mfma_f32_16x16x32_f16 v[80:83], a[64:67], v[112:115], v[32:35]\n"
      "global_load_dwordx4 v[10:13], v218, s[96:97] offset:1024\n"
      "v_fma_f32 v194, |v87|, v55, v194\n"
      "v_mfma_f32_16x16x32_f16 v[80:83], a[68:71], v[116:119], v[80:83]\n"
      "s_waitcnt lgkmcnt(1)\n"
      "v_pk_mul_f16 v100, v132, v164\n"
      "v_pk_mul_f16 v101, v133, v165\n"
      "v_mfma_f32_16x16x32_f16 v[80:83], a[72:75], v[120:123], v[80:83]\n"
      "v_pk_mul_f16 v102, v134, v166\n"
      "v_pk_mul_f16 v103, v135, v167\n"
      "v_mfma_f32_16x16x32_f16 v[80:83], a[76:79], v[124:127], v[80:83]\n"
      "v_fma_f32 v194, |v88|, v56, v194\n"
      "v_fma_f32 v194, |v89|, v57, v194\n"
      "v_mfma_f32_16x16x32_f16 v[84:87], a[92:95], v[124:127], v[36:39]\n"
      "ds_read_b128 v[172:175], v203 offset:3648\n"
      "v_fma_f32 v194, |v90|, v58, v194\n"
      "v_mfma_f32_16x16x32_f16 v[84:87], a[88:91], v[120:123], v[84:87]\n"
      "s_waitcnt lgkmcnt(1)\n"
      "v_fma_f32 v194, |v91|, v59, v194\n"
      "v_pk_mul_f16 v104, v136, v168\n"
      "v_mfma_f32_16x16x32_f16 v[84:87], a[84:87], v[116:119], v[84:87]\n"
      "v_pk_mul_f16 v105, v137, v169\n"
      "v_pk_mul_f16 v106, v138, v170\n"
      "v_mfma_f32_16x16x32_f16 v[84:87], a[80:83], v[112:115], v[84:87]\n"
      "v_fma_f32 v194, |v92|, v60, v194\n"
      "v_fma_f32 v194, |v93|, v61, v194\n"
      "v_mfma_f32_16x16x32_f16 v[88:91], a[96:99], v[112:115], v[40:43]\n"
      "v_pk_mul_f16 v107, v139, v171\n"
      "v_fma_f32 v194, |v94|, v62, v194\n"
      "v_mfma_f32_16x16x32_f16 v[88:91], a[100:103], v[116:119], v[88:91]\n"
      "v_fma_f32 v194, |v95|, v63, v194\n"
      "v_fma_f32 v194, |v80|, v64, v194\n"
      "v_mfma_f32_16x16x32_f16 v[88:91], a[104:107], v[120:123], v[88:91]\n"
      "ds_read_b128 v[160:163], v203 offset:4032\n"
      "v_fma_f32 v194, |v81|, v65, v194\n"
      "v_mfma_f32_16x16x32_f16 v[88:91], a[108:111], v[124:127], v[88:91]\n"
      "v_fma_f32 v194, |v82|, v66, v194\n"
      "v_fma_f32 v194, |v83|, v67, v194\n"
      "v_mfma_f32_16x16x32_f16 v[92:95], a[124:127], v[124:127], v[44:47]\n"
      "s_waitcnt lgkmcnt(1)\n"
      "v_pk_mul_f16 v108, v140, v172\n"
      "v_pk_mul_f16 v109, v141, v173\n"
      "v_mfma_f32_16x16x32_f16 v[92:95], a[120:123], v[120:123], v[92:95]\n"
      "v_pk_mul_f16 v110, v142, v174\n"
      "v_pk_mul_f16 v111, v143, v175\n"
      "v_mfma_f32_16x16x32_f16 v[92:95], a[116:119], v[116:119], v[92:95]\n"
      "v_fma_f32 v194, |v84|, v68, v194\n"
      "v_fma_f32 v194, |v85|, v69, v194\n"
      "v_mfma_f32_16x16x32_f16 v[92:95], a[112:115], v[112:115], v[92:95]\n"
      "v_fma_f32 v194, |v86|, v70, v194\n"
      "v_fma_f32 v194, |v87|, v71, v194\n"
      "v_mfma_f32_16x16x32_f16 v[80:83], a[0:3], v[96:99], v[16:19]\n"
      "v_fma_f32 v194, |v88|, v72, v194\n"
      "v_fma_f32 v194, |v89|, v73, v194\n"
      "v_mfma_f32_16x16x32_f16 v[80:83], a[4:7], v[100:103], v[80:83]\n"
      "ds_read_b128 v[164:167], v203 offset:4096\n"
      "v_fma_f32 v194, |v90|, v74, v194\n"
      "v_mfma_f32_16x16x32_f16 v[80:83], a[8:11], v[104:107], v[80:83]\n"
      "s_waitcnt lgkmcnt(1)\n"
      "v_pk_mul_f16 v112, v128, v160\n"
      "v_fma_f32 v194, |v91|, v75, v194\n"
      "v_mfma_f32_16x16x32_f16 v[80:83], a[12:15], v[108:111], v[80:83]\n"
      "ds_read_b64_tr_b16 a[168:169], v216 offset:18688\n"
      "v_pk_mul_f16 v113, v129, v161\n"
      "v_mfma_f32_16x16x32_f16 v[84:87], a[28:31], v[108:111], v[20:23]\n"
      "ds_read_b64_tr_b16 a[170:171], v216 offset:19776\n"
      "v_pk_mul_f16 v114, v130, v162\n"
      "v_mfma_f32_16x16x32_f16 v[84:87], a[24:27], v[104:107], v[84:87]\n"
      "ds_read_b64_tr_b16 a[172:173], v216 offset:18752\n"
      "v_pk_mul_f16 v115, v131, v163\n"
      "v_mfma_f32_16x16x32_f16 v[84:87], a[20:23], v[100:103], v[84:87]\n"
      "v_fma_f32 v194, |v92|, v76, v194\n"
      "v_fma_f32 v194, |v93|, v77, v194\n"
      "v_mfma_f32_16x16x32_f16 v[84:87], a[16:19], v[96:99], v[84:87]\n"
      "ds_read_b64_tr_b16 a[174:175], v216 offset:19840\n"
      "v_fma_f32 v194, |v94|, v78, v194\n"
      "v_mfma_f32_16x16x32_f16 v[88:91], a[32:35], v[96:99], v[24:27]\n"
      "ds_read_b64_tr_b16 a[176:177], v216 offset:18816\n"
      "v_fma_f32 v194, |v95|, v79, v194\n"
      "v_mfma_f32_16x16x32_f16 v[88:91], a[36:39], v[100:103], v[88:91]\n"
      "ds_read_b64_tr_b16 a[178:179], v216 offset:19904\n"
      "v_mul_f32 v191, |v80|, v48\n"
      "v_mfma_f32_16x16x32_f16 v[88:91], a[40:43], v[104:107], v[88:91]\n"
      "ds_read_b64_tr_b16 a[180:181], v216 offset:18880\n"
      "v_fma_f32 v191, |v81|, v49, v191\n"
      "v_mfma_f32_16x16x32_f16 v[88:91], a[44:47], v[108:111], v[88:91]\n"
      "s_waitcnt lgkmcnt(7)\n"
      "ds_read_b64_tr_b16 a[182:183], v216 offset:19968\n"
      "v_pk_mul_f16 v116, v132, v164\n"
      "v_mfma_f32_16x16x32_f16 v[92:95], a[60:63], v[108:111], v[28:31]\n"
      "v_fma_f32 v191, |v82|, v50, v191\n"
      "v_fma_f32 v191, |v83|, v51, v191\n"
      "v_mfma_f32_16x16x32_f16 v[92:95], a[56:59], v[104:107], v[92:95]\n"
      "v_pk_mul_f16 v117, v133, v165\n"
      "v_pk_mul_f16 v118, v134, v166\n"
      "v_mfma_f32_16x16x32_f16 v[92:95], a[52:55], v[100:103], v[92:95]\n"
      "v_pk_mul_f16 v119, v135, v167\n"
      "v_fma_f32 v191, |v84|, v52, v191\n"
      "v_mfma_f32_16x16x32_f16 v[92:95], a[48:51], v[96:99], v[92:95]\n"
      "ds_read_b128 v[168:171], v203 offset:4160\n"
      "v_fma_f32 v191, |v85|, v53, v191\n"
      "v_mfma_f32_16x16x32_f16 v[80:83], a[64:67], v[96:99], v[32:35]\n"
      "v_fma_f32 v191, |v86|, v54, v191\n"
      "v_fma_f32 v191, |v87|, v55, v191\n"
      "v_mfma_f32_16x16x32_f16 v[80:83], a[68:71], v[100:103], v[80:83]\n"
      "v_fma_f32 v191, |v88|, v56, v191\n"
      "v_fma_f32 v191, |v89|, v57, v191\n"
      "v_mfma_f32_16x16x32_f16 v[80:83], a[72:75], v[104:107], v[80:83]\n"
      "ds_read_b128 v[172:175], v203 offset:4224\n"
      "v_fma_f32 v191, |v90|, v58, v191\n"
      "v_mfma_f32_16x16x32_f16 v[80:83], a[76:79], v[108:111], v[80:83]\n"
      "s_waitcnt vmcnt(6)\n"
      "v_fma_f32 v191, |v91|, v59, v191\n"
      "v_cmp_eq_u64 s[46:47], v[196:197], v[208:209]\n"
      "v_mfma_f32_16x16x32_f16 v[84:87], a[92:95], v[108:111], v[36:39]\n"
      "s_waitcnt vmcnt(5)\n"
      "v_cmp_eq_u64 s[48:49], v[198:199], v[210:211]\n"
      "v_fma_f32 v191, |v92|, v60, v191\n"
      "v_mfma_f32_16x16x32_f16 v[84:87], a[88:91], v[104:107], v[84:87]\n"
      "s_waitcnt lgkmcnt(8)\n"
      "v_fma_f32 v191, |v93|, v61, v191\n"
      "v_fma_f32 v191, |v94|, v62, v191\n"
      "v_mfma_f32_16x16x32_f16 a[204:207], a[184:187], a[168:171], 0\n"
      "s_waitcnt lgkmcnt(6)\n"
      "ds_read_b128 v[144:147], v201 offset:2304\n"
      "s_and_b64 s[46:47], s[46:47], s[48:49]\n"
      "v_fma_f32 v191, |v95|, v63, v191\n"
      "v_mfma_f32_16x16x32_f16 a[204:207], a[188:191], a[172:175], a[204:207]\n"
      "s_waitcnt lgkmcnt(2)\n"
      "v_pk_mul_f16 v120, v136, v168\n"
      "v_pk_mul_f16 v121, v137, v169\n"
      "v_mfma_f32_16x16x32_f16 a[204:207], a[192:195], a[176:179], a[204:207]\n"
      "s_cmp_eq_u64 s[46:47], -1\n"
      "s_cselect_b32 s52, s52, 0x7f000001\n"
      "v_pk_mul_f16 v122, v138, v170\n"
      "v_pk_mul_f16 v123, v139, v171\n"
      "v_mfma_f32_16x16x32_f16 a[204:207], a[196:199], a[180:183], a[204:207]\n"
      "ds_read_b128 v[148:151], v201 offset:2368\n"
      "v_mov_b32 v223, s52\n"
      "v_mfma_f32_16x16x32_f16 v[84:87], a[84:87], v[100:103], v[84:87]\n"
      "v_fma_f32 v191, |v80|, v64, v191\n"
      "v_fma_f32 v191, |v81|, v65, v191\n"
      "v_mfma_f32_16x16x32_f16 v[84:87], a[80:83], v[96:99], v[84:87]\n"
      "s_waitcnt lgkmcnt(2)\n"
      "ds_read_b128 v[152:155], v201 offset:2432\n"
      "v_pk_mul_f16 v124, v140, v172\n"
      "v_mfma_f32_16x16x32_f16 v[88:91], a[96:99], v[96:99], v[40:43]\n"
      "ds_write2_b32 v217, a200, a204 offset0:0 offset1:64\n"
      "v_mfma_f32_16x16x32_f16 v[88:91], a[100:103], v[100:103], v[88:91]\n"
      "ds_write_b32 v2, v223\n"
      "v_mfma_f32_16x16x32_f16 v[88:91], a[104:107], v[104:107], v[88:91]\n"
      "ds_read_b128 v[160:163], v207 offset:0\n"
      "v_fma_f32 v191, |v82|, v66, v191\n"
      "v_mfma_f32_16x16x32_f16 v[88:91], a[108:111], v[108:111], v[88:91]\n"
      "v_fma_f32 v191, |v83|, v67, v191\n"
      "v_pk_mul_f16 v125, v141, v173\n"
      "v_mfma_f32_16x16x32_f16 v[92:95], a[124:127], v[108:111], v[44:47]\n"
      "ds_read_b128 v[156:159], v201 offset:2496\n"
      "v_pk_mul_f16 v126, v142, v174\n"
      "v_mfma_f32_16x16x32_f16 v[92:95], a[120:123], v[104:107], v[92:95]\n"
      "v_pk_mul_f16 v127, v143, v175\n"
      "v_fma_f32 v191, |v84|, v68, v191\n"
      "v_mfma_f32_16x16x32_f16 v[92:95], a[116:119], v[100:103], v[92:95]\n"
      "v_fma_f32 v191, |v85|, v69, v191\n"
      "v_fma_f32 v191, |v86|, v70, v191\n"
      "v_mfma_f32_16x16x32_f16 v[92:95], a[112:115], v[96:99], v[92:95]\n"
      "v_fma_f32 v191, |v87|, v71, v191\n"
      "v_fma_f32 v191, |v88|, v72, v191\n"
      "v_mfma_f32_16x16x32_f16 v[80:83], a[0:3], v[112:115], v[16:19]\n"
      "ds_read_b128 v[164:167], v207 offset:64\n"
      "v_fma_f32 v191, |v89|, v73, v191\n"
      "v_mfma_f32_16x16x32_f16 v[80:83], a[4:7], v[116:119], v[80:83]\n"
      "v_fma_f32 v191, |v90|, v74, v191\n"
      "v_fma_f32 v191, |v91|, v75, v191\n"
      "v_mfma_f32_16x16x32_f16 v[80:83], a[8:11], v[120:123], v[80:83]\n"
      "s_waitcnt lgkmcnt(2)\n"
      "v_pk_mul_f16 v96, v144, v160\n"
      "v_pk_mul_f16 v97, v145, v161\n"
      "v_mfma_f32_16x16x32_f16 v[80:83], a[12:15], v[124:127], v[80:83]\n"
      "v_pk_mul_f16 v98, v146, v162\n"
      "v_pk_mul_f16 v99, v147, v163\n"
      "v_mfma_f32_16x16x32_f16 v[84:87], a[28:31], v[124:127], v[20:23]\n"
      "v_fma_f32 v191, |v92|, v76, v191\n"
      "v_fma_f32 v191, |v93|, v77, v191\n"
      "v_mfma_f32_16x16x32_f16 v[84:87], a[24:27], v[120:123], v[84:87]\n"
      "v_fma_f32 v191, |v94|, v78, v191\n"
      "v_fma_f32 v191, |v95|, v79, v191\n"
      "v_mfma_f32_16x16x32_f16 v[84:87], a[20:23], v[116:119], v[84:87]\n"
      "v_mul_f32 v192, |v80|, v48\n"
      "v_fma_f32 v192, |v81|, v49, v192\n"
      "v_mfma_f32_16x16x32_f16 v[84:87], a[16:19], v[112:115], v[84:87]\n"
      "v_permlane32_swap_b32 v189, v191\n"
      "v_mfma_f32_16x16x32_f16 v[88:91], a[32:35], v[112:115], v[24:27]\n"
      "s_waitcnt lgkmcnt(0)\n"
      "ds_read_b128 v[168:171], v207 offset:128\n"
      "v_pk_mul_f16 v100, v148, v164\n"
      "v_mfma_f32_16x16x32_f16 v[88:91], a[36:39], v[116:119], v[88:91]\n"
      "v_fma_f32 v192, |v82|, v50, v192\n"
      "v_fma_f32 v192, |v83|, v51, v192\n"
      "v_mfma_f32_16x16x32_f16 v[88:91], a[40:43], v[120:123], v[88:91]\n"
      "v_pk_mul_f16 v101, v149, v165\n"
      "v_pk_mul_f16 v102, v150, v166\n"
      "v_mfma_f32_16x16x32_f16 v[88:91], a[44:47], v[124:127], v[88:91]\n"
      "v_pk_mul_f16 v103, v151, v167\n"
      "v_fma_f32 v192, |v84|, v52, v192\n"
      "v_mfma_f32_16x16x32_f16 v[92:95], a[60:63], v[124:127], v[28:31]\n"
      "v_fma_f32 v192, |v85|, v53, v192\n"
      "v_fma_f32 v192, |v86|, v54, v192\n"
      "v_mfma_f32_16x16x32_f16 v[92:95], a[56:59], v[120:123], v[92:95]\n"
      "s_waitcnt vmcnt(3)\n"
      "v_fma_f32 v192, |v87|, v55, v192\n"
      "v_sub_u16_sdwa v176, v244, s36 clamp dst_sel:WORD_1 dst_unused:UNUSED_PAD src0_sel:WORD_0 src1_sel:DWORD\n"
      "v_mfma_f32_16x16x32_f16 v[92:95], a[52:55], v[116:119], v[92:95]\n"
      "global_load_dwordx4 v[208:211], v218, s[96:97] offset:2048\n"
      "global_load_dwordx4 v[216:219], v218, s[96:97] offset:3072\n"
      "v_mfma_f32_16x16x32_f16 v[92:95], a[48:51], v[112:115], v[92:95]\n"
      "v_sub_u16_sdwa v177, v244, s36 clamp dst_sel:WORD_1 dst_unused:UNUSED_PAD src0_sel:WORD_1 src1_sel:DWORD\n"
      "v_sub_u16_sdwa v178, v245, s36 clamp dst_sel:WORD_1 dst_unused:UNUSED_PAD src0_sel:WORD_0 src1_sel:DWORD\n"
      "v_mfma_f32_16x16x32_f16 v[80:83], a[64:67], v[112:115], v[32:35]\n"
      "v_fma_f32 v192, |v88|, v56, v192\n"
      "v_fma_f32 v192, |v89|, v57, v192\n"
      "v_mfma_f32_16x16x32_f16 v[80:83], a[68:71], v[116:119], v[80:83]\n"
      "s_waitcnt lgkmcnt(0)\n"
      "v_sub_u16_sdwa v179, v245, s36 clamp dst_sel:WORD_1 dst_unused:UNUSED_PAD src0_sel:WORD_1 src1_sel:DWORD\n"
      "v_pk_mul_f16 v104, v152, v168\n"
      "v_mfma_f32_16x16x32_f16 v[80:83], a[72:75], v[120:123], v[80:83]\n"
      "ds_read_b128 v[172:175], v207 offset:192\n"
      "v_fma_f32 v192, |v90|, v58, v192\n"
      "v_mfma_f32_16x16x32_f16 v[80:83], a[76:79], v[124:127], v[80:83]\n"
      "v_fma_f32 v192, |v91|, v59, v192\n"
      "v_sub_u16_sdwa v180, v246, s36 clamp dst_sel:WORD_1 dst_unused:UNUSED_PAD src0_sel:WORD_0 src1_sel:DWORD\n"
      "v_mfma_f32_16x16x32_f16 v[84:87], a[92:95], v[124:127], v[36:39]\n"
      "v_pk_mul_f16 v105, v153, v169\n"
      "v_sub_u16_sdwa v181, v246, s36 clamp dst_sel:WORD_1 dst_unused:UNUSED_PAD src0_sel:WORD_1 src1_sel:DWORD\n"
      "v_mfma_f32_16x16x32_f16 v[84:87], a[88:91], v[120:123], v[84:87]\n"
      "v_pk_mul_f16 v106, v154, v170\n"
      "v_sub_u16_sdwa v182, v247, s36 clamp dst_sel:WORD_1 dst_unused:UNUSED_PAD src0_sel:WORD_0 src1_sel:DWORD\n"
      "v_mfma_f32_16x16x32_f16 v[84:87], a[84:87], v[116:119], v[84:87]\n"
      "v_pk_mul_f16 v107, v155, v171\n"
      "v_fma_f32 v192, |v92|, v60, v192\n"
      "v_mfma_f32_16x16x32_f16 v[84:87], a[80:83], v[112:115], v[84:87]\n"
      "v_fma_f32 v192, |v93|, v61, v192\n"
      "v_sub_u16_sdwa v183, v247, s36 clamp dst_sel:WORD_1 dst_unused:UNUSED_PAD src0_sel:WORD_1 src1_sel:DWORD\n"
      "v_mfma_f32_16x16x32_f16 v[88:91], a[96:99], v[112:115], v[40:43]\n"
      "v_fma_f32 v192, |v94|, v62, v192\n"
      "v_fma_f32 v192, |v95|, v63, v192\n"
      "v_mfma_f32_16x16x32_f16 v[88:91], a[100:103], v[116:119], v[88:91]\n"
      "v_cvt_pk_f16_f32 v184, v176, v177\n"
      "v_cvt_pk_f16_f32 v185, v178, v179\n"
      "v_mfma_f32_16x16x32_f16 v[88:91], a[104:107], v[120:123], v[88:91]\n"
      "v_cvt_pk_f16_f32 v186, v180, v181\n"
      "v_fma_f32 v192, |v80|, v64, v192\n"
      "v_mfma_f32_16x16x32_f16 v[88:91], a[108:111], v[124:127], v[88:91]\n"
      "v_fma_f32 v192, |v81|, v65, v192\n"
      "v_cvt_pk_f16_f32 v187, v182, v183\n"
      "v_mfma_f32_16x16x32_f16 v[92:95], a[124:127], v[124:127], v[44:47]\n"
      "s_waitcnt lgkmcnt(0)\n"
      "v_pk_mul_f16 v108, v156, v172\n"
      "v_fma_f32 v192, |v82|, v66, v192\n"
      "v_mfma_f32_16x16x32_f16 v[92:95], a[120:123], v[120:123], v[92:95]\n"
      "ds_write_b128 v1, v[184:187] offset:4608\n"
      "v_mfma_f32_16x16x32_f16 v[92:95], a[116:119], v[116:119], v[92:95]\n"
      "ds_read_b128 v[160:163], v203 offset:4608\n"
      "v_fma_f32 v192, |v83|, v67, v192\n"
      "v_mfma_f32_16x16x32_f16 v[92:95], a[112:115], v[112:115], v[92:95]\n"
      "v_pk_mul_f16 v109, v157, v173\n"
      "v_pk_mul_f16 v110, v158, v174\n"
      "v_mfma_f32_16x16x32_f16 v[80:83], a[0:3], v[96:99], v[16:19]\n"
      "v_pk_mul_f16 v111, v159, v175\n"
      "v_fma_f32 v192, |v84|, v68, v192\n"
      "v_mfma_f32_16x16x32_f16 v[80:83], a[4:7], v[100:103], v[80:83]\n"
      "v_fma_f32 v192, |v85|, v69, v192\n"
      "v_fma_f32 v192, |v86|, v70, v192\n"
      "v_mfma_f32_16x16x32_f16 v[80:83], a[8:11], v[104:107], v[80:83]\n"
      "41:\n"
      "s_cbranch_execz 44b\n"
      "v_mfma_f32_16x16x32_f16 v[80:83], a[12:15], v[108:111], v[80:83]\n"
      "v_fma_f32 v192, |v87|, v71, v192\n"
      "v_fma_f32 v192, |v88|, v72, v192\n"
      "v_mfma_f32_16x16x32_f16 v[84:87], a[28:31], v[108:111], v[20:23]\n"
      "ds_read_b128 v[164:167], v203 offset:4672\n"
      "v_fma_f32 v192, |v89|, v73, v192\n"
      "v_mfma_f32_16x16x32_f16 v[84:87], a[24:27], v[104:107], v[84:87]\n"
      "v_fma_f32 v192, |v90|, v74, v192\n"
      "v_fma_f32 v192, |v91|, v75, v192\n"
      "v_mfma_f32_16x16x32_f16 v[84:87], a[20:23], v[100:103], v[84:87]\n"
      "s_waitcnt lgkmcnt(1)\n"
      "v_pk_mul_f16 v112, v128, v160\n"
      "v_pk_mul_f16 v113, v129, v161\n"
      "v_mfma_f32_16x16x32_f16 v[84:87], a[16:19], v[96:99], v[84:87]\n"
      "v_pk_mul_f16 v114, v130, v162\n"
      "v_pk_mul_f16 v115, v131, v163\n"
      "v_mfma_f32_16x16x32_f16 v[88:91], a[32:35], v[96:99], v[24:27]\n"
      "v_fma_f32 v192, |v92|, v76, v192\n"
      "v_fma_f32 v192, |v93|, v77, v192\n"
      "v_mfma_f32_16x16x32_f16 v[88:91], a[36:39], v[100:103], v[88:91]\n"
      "v_fma_f32 v192, |v94|, v78, v192\n"
      "v_fma_f32 v192, |v95|, v79, v192\n"
      "v_mfma_f32_16x16x32_f16 v[88:91], a[40:43], v[104:107], v[88:91]\n"
      "v_mul_f32 v193, |v80|, v48\n"
      "v_fma_f32 v193, |v81|, v49, v193\n"
      "v_mfma_f32_16x16x32_f16 v[88:91], a[44:47], v[108:111], v[88:91]\n"
      "ds_read_b128 v[168:171], v203 offset:4736\n"
      "v_fma_f32 v193, |v82|, v50, v193\n"
      "v_mfma_f32_16x16x32_f16 v[92:95], a[60:63], v[108:111], v[28:31]\n"
      "s_waitcnt lgkmcnt(1)\n"
      "v_pk_mul_f16 v116, v132, v164\n"
      "v_fma_f32 v193, |v83|, v51, v193\n"
      "v_mfma_f32_16x16x32_f16 v[92:95], a[56:59], v[104:107], v[92:95]\n"
      "v_pk_mul_f16 v117, v133, v165\n"
      "v_pk_mul_f16 v118, v134, v166\n"
      "v_mfma_f32_16x16x32_f16 v[92:95], a[52:55], v[100:103], v[92:95]\n"
      "v_pk_mul_f16 v119, v135, v167\n"
      "v_fma_f32 v193, |v84|, v52, v193\n"
      "v_mfma_f32_16x16x32_f16 v[92:95], a[48:51], v[96:99], v[92:95]\n"
      "v_fma_f32 v193, |v85|, v53, v193\n"
      "v_fma_f32 v193, |v86|, v54, v193\n"
      "v_mfma_f32_16x16x32_f16 v[80:83], a[64:67], v[96:99], v[32:35]\n"
      "v_fma_f32 v193, |v87|, v55, v193\n"
      "v_fma_f32 v193, |v88|, v56, v193\n"
      "v_mfma_f32_16x16x32_f16 v[80:83], a[68:71], v[100:103], v[80:83]\n"
      "ds_read_b128 v[172:175], v203 offset:4800\n"
      "v_fma_f32 v193, |v89|, v57, v193\n"
      "v_mfma_f32_16x16x32_f16 v[80:83], a[72:75], v[104:107], v[80:83]\n"
      "v_fma_f32 v193, |v90|, v58, v193\n"
      "v_fma_f32 v193, |v91|, v59, v193\n"
      "v_mfma_f32_16x16x32_f16 v[80:83], a[76:79], v[108:111], v[80:83]\n"
      "s_waitcnt lgkmcnt(1)\n"
      "v_pk_mul_f16 v120, v136, v168\n"
      "v_pk_mul_f16 v121, v137, v169\n"
      "v_mfma_f32_16x16x32_f16 v[84:87], a[92:95], v[108:111], v[36:39]\n"
      "v_pk_mul_f16 v122, v138, v170\n"
      "v_pk_mul_f16 v123, v139, v171\n"
      "v_mfma_f32_16x16x32_f16 v[84:87], a[88:91], v[104:107], v[84:87]\n"
      "v_fma_f32 v193, |v92|, v60, v193\n"
      "v_fma_f32 v193, |v93|, v61, v193\n"
      "v_mfma_f32_16x16x32_f16 v[84:87], a[84:87], v[100:103], v[84:87]\n"
      "v_fma_f32 v193, |v94|, v62, v193\n"
      "v_fma_f32 v193, |v95|, v63, v193\n"
      "v_mfma_f32_16x16x32_f16 v[84:87], a[80:83], v[96:99], v[84:87]\n"
      "v_fma_f32 v193, |v80|, v64, v193\n"
      "v_fma_f32 v193, |v81|, v65, v193\n"
      "v_mfma_f32_16x16x32_f16 v[88:91], a[96:99], v[96:99], v[40:43]\n"
      "s_waitcnt vmcnt(4)\n"
      "v_sub_u16_sdwa v176, v248, s36 clamp dst_sel:WORD_1 dst_unused:UNUSED_PAD src0_sel:WORD_0 src1_sel:DWORD\n"
      "v_fma_f32 v193, |v82|, v66, v193\n"
      "v_mfma_f32_16x16x32_f16 v[88:91], a[100:103], v[100:103], v[88:91]\n"
      "s_waitcnt lgkmcnt(0)\n"
      "v_pk_mul_f16 v124, v140, v172\n"
      "v_fma_f32 v193, |v83|, v67, v193\n"
      "v_mfma_f32_16x16x32_f16 v[88:91], a[104:107], v[104:107], v[88:91]\n"
      "v_sub_u16_sdwa v177, v248, s36 clamp dst_sel:WORD_1 dst_unused:UNUSED_PAD src0_sel:WORD_1 src1_sel:DWORD\n"
      "v_pk_mul_f16 v125, v141, v173\n"
      "v_mfma_f32_16x16x32_f16 v[88:91], a[108:111], v[108:111], v[88:91]\n"
      "v_sub_u16_sdwa v178, v249, s36 clamp dst_sel:WORD_1 dst_unused:UNUSED_PAD src0_sel:WORD_0 src1_sel:DWORD\n"
      "v_pk_mul_f16 v126, v142, v174\n"
      "v_mfma_f32_16x16x32_f16 v[92:95], a[124:127], v[108:111], v[44:47]\n"
      "v_add_f32 v189, v189, v191\n"
      "v_sub_u16_sdwa v179, v249, s36 clamp dst_sel:WORD_1 dst_unused:UNUSED_PAD src0_sel:WORD_1 src1_sel:DWORD\n"
      "v_mfma_f32_16x16x32_f16 v[92:95], a[120:123], v[104:107], v[92:95]\n"
      "v_permlane32_swap_b32 v190, v192\n"
      "v_mfma_f32_16x16x32_f16 v[92:95], a[116:119], v[100:103], v[92:95]\n"
      "v_pk_mul_f16 v127, v143, v175\n"
      "v_fma_f32 v193, |v84|, v68, v193\n"
      "v_mfma_f32_16x16x32_f16 v[92:95], a[112:115], v[96:99], v[92:95]\n"
      "v_fma_f32 v193, |v85|, v69, v193\n"
      "v_sub_u16_sdwa v180, v250, s36 clamp dst_sel:WORD_1 dst_unused:UNUSED_PAD src0_sel:WORD_0 src1_sel:DWORD\n"
      "v_mfma_f32_16x16x32_f16 v[80:83], a[0:3], v[112:115], v[16:19]\n"
      "v_add_f32 v190, v190, v192\n"
      "v_fma_f32 v193, |v86|, v70, v193\n"
      "v_mfma_f32_16x16x32_f16 v[80:83], a[4:7], v[116:119], v[80:83]\n"
      "v_fma_f32 v193, |v87|, v71, v193\n"
      "v_sub_u16_sdwa v181, v250, s36 clamp dst_sel:WORD_1 dst_unused:UNUSED_PAD src0_sel:WORD_1 src1_sel:DWORD\n"
      "v_mfma_f32_16x16x32_f16 v[80:83], a[8:11], v[120:123], v[80:83]\n"
      "v_permlane16_swap_b32 v189, v190\n"
      "v_mfma_f32_16x16x32_f16 v[80:83], a[12:15], v[124:127], v[80:83]\n"
      "v_sub_u16_sdwa v182, v251, s36 clamp dst_sel:WORD_1 dst_unused:UNUSED_PAD src0_sel:WORD_0 src1_sel:DWORD\n"
      "v_sub_u16_sdwa v183, v251, s36 clamp dst_sel:WORD_1 dst_unused:UNUSED_PAD src0_sel:WORD_1 src1_sel:DWORD\n"
      "v_mfma_f32_16x16x32_f16 v[84:87], a[28:31], v[124:127], v[20:23]\n"
      "v_add_f32 v189, v189, v190\n"
      "v_fma_f32 v193, |v88|, v72, v193\n"
      "v_mfma_f32_16x16x32_f16 v[84:87], a[24:27], v[120:123], v[84:87]\n"
      "v_fma_f32 v193, |v89|, v73, v193\n"
      "v_cvt_pk_f16_f32 v184, v176, v177\n"
      "v_mfma_f32_16x16x32_f16 v[84:87], a[20:23], v[116:119], v[84:87]\n"
      "v_pk_mul_f16 v96, v144, v160\n"
      "v_fma_f32 v193, |v90|, v74, v193\n"
      "v_mfma_f32_16x16x32_f16 v[84:87], a[16:19], v[112:115], v[84:87]\n"
      "v_fma_f32 v193, |v91|, v75, v193\n"
      "v_cvt_pk_f16_f32 v185, v178, v179\n"
      "v_pk_mul_f16 v97, v145, v161\n"
      "v_mfma_f32_16x16x32_f16 v[88:91], a[32:35], v[112:115], v[24:27]\n"
      "v_cvt_pk_f16_f32 v186, v180, v181\n"
      "v_pk_mul_f16 v98, v146, v162\n"
      "v_mfma_f32_16x16x32_f16 v[88:91], a[36:39], v[116:119], v[88:91]\n"
      "v_cvt_pk_f16_f32 v187, v182, v183\n"
      "v_pk_mul_f16 v99, v147, v163\n"
      "v_mfma_f32_16x16x32_f16 v[88:91], a[40:43], v[120:123], v[88:91]\n"
      "ds_write_b128 v1, v[184:187] offset:5760\n"
      "v_fma_f32 v193, |v92|, v76, v193\n"
      "v_fma_f32 v193, |v93|, v77, v193\n"
      "v_mfma_f32_16x16x32_f16 v[88:91], a[44:47], v[124:127], v[88:91]\n"
      "v_fma_f32 v193, |v94|, v78, v193\n"
      "v_fma_f32 v193, |v95|, v79, v193\n"
      "v_mfma_f32_16x16x32_f16 v[92:95], a[60:63], v[124:127], v[28:31]\n"
      "v_mul_f32 v190, |v80|, v48\n"
      "v_fma_f32 v190, |v81|, v49, v190\n"
      "v_mfma_f32_16x16x32_f16 v[92:95], a[56:59], v[120:123], v[92:95]\n"
      "v_pk_mul_f16 v100, v148, v164\n"
      "v_fma_f32 v190, |v82|, v50, v190\n"
      "v_mfma_f32_16x16x32_f16 v[92:95], a[52:55], v[116:119], v[92:95]\n"
      "v_fma_f32 v190, |v83|, v51, v190\n"
      "v_pk_mul_f16 v101, v149, v165\n"
      "v_mfma_f32_16x16x32_f16 v[92:95], a[48:51], v[112:115], v[92:95]\n"
      "v_pk_mul_f16 v102, v150, v166\n"
      "v_pk_mul_f16 v103, v151, v167\n"
      "v_mfma_f32_16x16x32_f16 v[80:83], a[64:67], v[112:115], v[32:35]\n"
      "v_fma_f32 v190, |v84|, v52, v190\n"
      "v_fma_f32 v190, |v85|, v53, v190\n"
      "v_mfma_f32_16x16x32_f16 v[80:83], a[68:71], v[116:119], v[80:83]\n"
      "v_fma_f32 v190, |v86|, v54, v190\n"
      "v_fma_f32 v190, |v87|, v55, v190\n"
      "v_mfma_f32_16x16x32_f16 v[80:83], a[72:75], v[120:123], v[80:83]\n"
      "v_fma_f32 v190, |v88|, v56, v190\n"
      "v_fma_f32 v190, |v89|, v57, v190\n"
      "v_mfma_f32_16x16x32_f16 v[80:83], a[76:79], v[124:127], v[80:83]\n"
      "v_pk_mul_f16 v104, v152, v168\n"
      "v_fma_f32 v190, |v90|, v58, v190\n"
      "v_mfma_f32_16x16x32_f16 v[84:87], a[92:95], v[124:127], v[36:39]\n"
      "v_fma_f32 v190, |v91|, v59, v190\n"
      "v_pk_mul_f16 v105, v153, v169\n"
      "v_mfma_f32_16x16x32_f16 v[84:87], a[88:91], v[120:123], v[84:87]\n"
      "v_pk_mul_f16 v106, v154, v170\n"
      "v_pk_mul_f16 v107, v155, v171\n"
      "v_mfma_f32_16x16x32_f16 v[84:87], a[84:87], v[116:119], v[84:87]\n"
      "v_fma_f32 v190, |v92|, v60, v190\n"
      "v_fma_f32 v190, |v93|, v61, v190\n"
      "v_mfma_f32_16x16x32_f16 v[84:87], a[80:83], v[112:115], v[84:87]\n"
      "v_fma_f32 v190, |v94|, v62, v190\n"
      "v_fma_f32 v190, |v95|, v63, v190\n"
      "v_mfma_f32_16x16x32_f16 v[88:91], a[96:99], v[112:115], v[40:43]\n"
      "v_permlane32_swap_b32 v188, v193\n"
      "v_mfma_f32_16x16x32_f16 v[88:91], a[100:103], v[116:119], v[88:91]\n"
      "v_fma_f32 v190, |v80|, v64, v190\n"
      "v_fma_f32 v190, |v81|, v65, v190\n"
      "v_mfma_f32_16x16x32_f16 v[88:91], a[104:107], v[120:123], v[88:91]\n"
      "ds_read_b128 v[160:163], v203 offset:5184\n"
      "v_pk_mul_f16 v108, v156, v172\n"
      "v_mfma_f32_16x16x32_f16 v[88:91], a[108:111], v[124:127], v[88:91]\n"
      "v_fma_f32 v190, |v82|, v66, v190\n"
      "v_fma_f32 v190, |v83|, v67, v190\n"
      "v_mfma_f32_16x16x32_f16 v[92:95], a[124:127], v[124:127], v[44:47]\n"
      "v_pk_mul_f16 v109, v157, v173\n"
      "v_pk_mul_f16 v110, v158, v174\n"
      "v_mfma_f32_16x16x32_f16 v[92:95], a[120:123], v[120:123], v[92:95]\n"
      "v_pk_mul_f16 v111, v159, v175\n"
      "v_fma_f32 v190, |v84|, v68, v190\n"
      "v_mfma_f32_16x16x32_f16 v[92:95], a[116:119], v[116:119], v[92:95]\n"
      "s_waitcnt vmcnt(3)\n"
      "v_fma_f32 v190, |v85|, v69, v190\n"
      "v_sub_u16_sdwa v176, v252, s36 clamp dst_sel:WORD_1 dst_unused:UNUSED_PAD src0_sel:WORD_0 src1_sel:DWORD\n"
      "v_mfma_f32_16x16x32_f16 v[92:95], a[112:115], v[112:115], v[92:95]\n"
      "v_fma_f32 v190, |v86|, v70, v190\n"
      "v_fma_f32 v190, |v87|, v71, v190\n"
      "v_mfma_f32_16x16x32_f16 v[80:83], a[0:3], v[96:99], v[16:19]\n"
      "v_sub_u16_sdwa v177, v252, s36 clamp dst_sel:WORD_1 dst_unused:UNUSED_PAD src0_sel:WORD_1 src1_sel:DWORD\n"
      "v_sub_u16_sdwa v178, v253, s36 clamp dst_sel:WORD_1 dst_unused:UNUSED_PAD src0_sel:WORD_0 src1_sel:DWORD\n"
      "v_mfma_f32_16x16x32_f16 v[80:83], a[4:7], v[100:103], v[80:83]\n"
      "v_sub_u16_sdwa v179, v253, s36 clamp dst_sel:WORD_1 dst_unused:UNUSED_PAD src0_sel:WORD_1 src1_sel:DWORD\n"
      "v_fma_f32 v190, |v88|, v72, v190\n"
      "v_mfma_f32_16x16x32_f16 v[80:83], a[8:11], v[104:107], v[80:83]\n"
      "v_fma_f32 v190, |v89|, v73, v190\n"
      "v_sub_u16_sdwa v180, v254, s36 clamp dst_sel:WORD_1 dst_unused:UNUSED_PAD src0_sel:WORD_0 src1_sel:DWORD\n"
      "v_mfma_f32_16x16x32_f16 v[80:83], a[12:15], v[108:111], v[80:83]\n"
      "s_waitcnt lgkmcnt(0)\n"
      "ds_read_b128 v[164:167], v203 offset:5248\n"
      "v_pk_mul_f16 v112, v128, v160\n"
      "v_mfma_f32_16x16x32_f16 v[84:87], a[28:31], v[108:111], v[20:23]\n"
      "v_fma_f32 v190, |v90|, v74, v190\n"
      "v_fma_f32 v190, |v91|, v75, v190\n"
      "v_mfma_f32_16x16x32_f16 v[84:87], a[24:27], v[104:107], v[84:87]\n"
      "v_sub_u16_sdwa v181, v254, s36 clamp dst_sel:WORD_1 dst_unused:UNUSED_PAD src0_sel:WORD_1 src1_sel:DWORD\n"
      "v_pk_mul_f16 v113, v129, v161\n"
      "v_mfma_f32_16x16x32_f16 v[84:87], a[20:23], v[100:103], v[84:87]\n"
      "v_sub_u16_sdwa v182, v255, s36 clamp dst_sel:WORD_1 dst_unused:UNUSED_PAD src0_sel:WORD_0 src1_sel:DWORD\n"
      "v_pk_mul_f16 v114, v130, v162\n"
      "v_mfma_f32_16x16x32_f16 v[84:87], a[16:19], v[96:99], v[84:87]\n"
      "v_sub_u16_sdwa v183, v255, s36 clamp dst_sel:WORD_1 dst_unused:UNUSED_PAD src0_sel:WORD_1 src1_sel:DWORD\n"
      "v_pk_mul_f16 v115, v131, v163\n"
      "v_mfma_f32_16x16x32_f16 v[88:91], a[32:35], v[96:99], v[24:27]\n"
      "v_fma_f32 v190, |v92|, v76, v190\n"
      "v_fma_f32 v190, |v93|, v77, v190\n"
      "v_mfma_f32_16x16x32_f16 v[88:91], a[36:39], v[100:103], v[88:91]\n"
      "v_cvt_pk_f16_f32 v184, v176, v177\n"
      "v_fma_f32 v190, |v94|, v78, v190\n"
      "v_mfma_f32_16x16x32_f16 v[88:91], a[40:43], v[104:107], v[88:91]\n"
      "v_fma_f32 v190, |v95|, v79, v190\n"
      "v_cvt_pk_f16_f32 v185, v178, v179\n"
      "v_mfma_f32_16x16x32_f16 v[88:91], a[44:47], v[108:111], v[88:91]\n"
      "v_cvt_pk_f16_f32 v186, v180, v181\n"
      "v_cvt_pk_f16_f32 v187, v182, v183\n"
      "v_mfma_f32_16x16x32_f16 v[92:95], a[60:63], v[108:111], v[28:31]\n"
      "ds_write_b128 v1, v[184:187] offset:6912\n"
      "v_mfma_f32_16x16x32_f16 v[92:95], a[56:59], v[104:107], v[92:95]\n"
      "v_mul_f32 v191, |v80|, v48\n"
      "v_fma_f32 v191, |v81|, v49, v191\n"
      "v_mfma_f32_16x16x32_f16 v[92:95], a[52:55], v[100:103], v[92:95]\n"
      "s_waitcnt lgkmcnt(1)\n"
      "ds_read_b128 v[168:171], v203 offset:5312\n"
      "v_pk_mul_f16 v116, v132, v164\n"
      "v_mfma_f32_16x16x32_f16 v[92:95], a[48:51], v[96:99], v[92:95]\n"
      "v_fma_f32 v191, |v82|, v50, v191\n"
      "v_fma_f32 v191, |v83|, v51, v191\n"
      "v_pk_mul_f16 v117, v133, v165\n"
      "v_mfma_f32_16x16x32_f16 v[80:83], a[64:67], v[96:99], v[32:35]\n"
      "v_pk_mul_f16 v118, v134, v166\n"
      "v_pk_mul_f16 v119, v135, v167\n"
      "v_mfma_f32_16x16x32_f16 v[80:83], a[68:71], v[100:103], v[80:83]\n"
      "v_fma_f32 v191, |v84|, v52, v191\n"
      "v_fma_f32 v191, |v85|, v53, v191\n"
      "v_mfma_f32_16x16x32_f16 v[80:83], a[72:75], v[104:107], v[80:83]\n"
      "v_fma_f32 v191, |v86|, v54, v191\n"
      "v_fma_f32 v191, |v87|, v55, v191\n"
      "v_mfma_f32_16x16x32_f16 v[80:83], a[76:79], v[108:111], v[80:83]\n"
      "v_fma_f32 v191, |v88|, v56, v191\n"
      "v_fma_f32 v191, |v89|, v57, v191\n"
      "v_mfma_f32_16x16x32_f16 v[84:87], a[92:95], v[108:111], v[36:39]\n"
      "ds_read_b128 v[172:175], v203 offset:5376\n"
      "v_fma_f32 v191, |v90|, v58, v191\n"
      "v_mfma_f32_16x16x32_f16 v[84:87], a[88:91], v[104:107], v[84:87]\n"
      "ds_read_b128 v[176:179], v206 offset:864\n"
      "v_fma_f32 v191, |v91|, v59, v191\n"
      "v_mfma_f32_16x16x32_f16 v[84:87], a[84:87], v[100:103], v[84:87]\n"
      "s_waitcnt lgkmcnt(2)\n"
      "v_pk_mul_f16 v120, v136, v168\n"
      "v_pk_mul_f16 v121, v137, v169\n"
      "v_mfma_f32_16x16x32_f16 v[84:87], a[80:83], v[96:99], v[84:87]\n"
      "ds_read_b128 v[180:183], v206 offset:2016\n"
      "v_pk_mul_f16 v122, v138, v170\n"
      "v_mfma_f32_16x16x32_f16 v[88:91], a[96:99], v[96:99], v[40:43]\n"
      "ds_read2_b32 v[184:185], v8 offset0:0 offset1:72\n"
      "v_pk_mul_f16 v123, v139, v171\n"
      "v_mfma_f32_16x16x32_f16 v[88:91], a[100:103], v[100:103], v[88:91]\n"
      "v_fma_f32 v191, |v92|, v60, v191\n"
      "v_fma_f32 v191, |v93|, v61, v191\n"
      "v_mfma_f32_16x16x32_f16 v[88:91], a[104:107], v[104:107], v[88:91]\n"
      "v_fma_f32 v191, |v94|, v62, v191\n"
      "v_fma_f32 v191, |v95|, v63, v191\n"
      "v_mfma_f32_16x16x32_f16 v[88:91], a[108:111], v[108:111], v[88:91]\n"
      "v_fma_f32 v191, |v80|, v64, v191\n"
      "v_fma_f32 v191, |v81|, v65, v191\n"
      "v_mfma_f32_16x16x32_f16 v[92:95], a[124:127], v[108:111], v[44:47]\n"
      "s_waitcnt lgkmcnt(3)\n"
      "v_pk_mul_f16 v124, v140, v172\n"
      "v_fma_f32 v191, |v82|, v66, v191\n"
      "v_mfma_f32_16x16x32_f16 v[92:95], a[120:123], v[104:107], v[92:95]\n"
      "v_fma_f32 v191, |v83|, v67, v191\n"
      "v_pk_mul_f16 v125, v141, v173\n"
      "v_mfma_f32_16x16x32_f16 v[92:95], a[116:119], v[100:103], v[92:95]\n"
      "v_pk_mul_f16 v126, v142, v174\n"
      "v_add_f32 v188, v188, v193\n"
      "v_mfma_f32_16x16x32_f16 v[92:95], a[112:115], v[96:99], v[92:95]\n"
      "s_waitcnt lgkmcnt(1)\n"
      "v_pk_mul_f16 v176, v176, v180\n"
      "v_pk_mul_f16 v177, v177, v181\n"
      "v_mfma_f32_16x16x32_f16 v[80:83], a[0:3], v[112:115], v[16:19]\n"
      "v_pk_mul_f16 v127, v143, v175\n"
      "v_fma_f32 v191, |v84|, v68, v191\n"
      "v_mfma_f32_16x16x32_f16 v[80:83], a[4:7], v[116:119], v[80:83]\n"
      "v_fma_f32 v191, |v85|, v69, v191\n"
      "v_pk_mul_f16 v178, v178, v182\n"
      "v_mfma_f32_16x16x32_f16 v[80:83], a[8:11], v[120:123], v[80:83]\n"
      "v_fma_f32 v191, |v86|, v70, v191\n"
      "v_fma_f32 v191, |v87|, v71, v191\n"
      "v_mfma_f32_16x16x32_f16 v[80:83], a[12:15], v[124:127], v[80:83]\n"
      "s_waitcnt lgkmcnt(0)\n"
      "v_pk_mul_f16 v179, v179, v183\n"
      "v_pk_mul_f16 v184, v184, v185\n"
      "v_mfma_f32_16x16x32_f16 v[84:87], a[28:31], v[124:127], v[20:23]\n"
      "ds_write_b128 v1, v[176:179] offset:8064\n"
      "v_mfma_f32_16x16x32_f16 v[84:87], a[24:27], v[120:123], v[84:87]\n"
      "v_fma_f32 v191, |v88|, v72, v191\n"
      "v_fma_f32 v191, |v89|, v73, v191\n"
      "v_mfma_f32_16x16x32_f16 v[84:87], a[20:23], v[116:119], v[84:87]\n"
      "ds_write_b32 v8, v184 offset:2304\n"
      "v_pk_mul_f16 v96, v144, v160\n"
      "v_mfma_f32_16x16x32_f16 v[84:87], a[16:19], v[112:115], v[84:87]\n"
      "v_fma_f32 v191, |v90|, v74, v191\n"
      "v_fma_f32 v191, |v91|, v75, v191\n"
      "v_pk_mul_f16 v97, v145, v161\n"
      "v_mfma_f32_16x16x32_f16 v[88:91], a[32:35], v[112:115], v[24:27]\n"
      "v_pk_mul_f16 v98, v146, v162\n"
      "v_pk_mul_f16 v99, v147, v163\n"
      "v_mfma_f32_16x16x32_f16 v[88:91], a[36:39], v[116:119], v[88:91]\n"
      "v_fma_f32 v191, |v92|, v76, v191\n"
      "v_fma_f32 v191, |v93|, v77, v191\n"
      "v_mfma_f32_16x16x32_f16 v[88:91], a[40:43], v[120:123], v[88:91]\n"
      "v_fma_f32 v191, |v94|, v78, v191\n"
      "v_fma_f32 v191, |v95|, v79, v191\n"
      "v_mfma_f32_16x16x32_f16 v[88:91], a[44:47], v[124:127], v[88:91]\n"
      "v_mul_f32 v192, |v80|, v48\n"
      "v_fma_f32 v192, |v81|, v49, v192\n"
      "v_mfma_f32_16x16x32_f16 v[92:95], a[60:63], v[124:127], v[28:31]\n"
      "v_pk_mul_f16 v100, v148, v164\n"
      "v_fma_f32 v192, |v82|, v50, v192\n"
      "v_mfma_f32_16x16x32_f16 v[92:95], a[56:59], v[120:123], v[92:95]\n"
      "v_fma_f32 v192, |v83|, v51, v192\n"
      "v_pk_mul_f16 v101, v149, v165\n"
      "v_mfma_f32_16x16x32_f16 v[92:95], a[52:55], v[116:119], v[92:95]\n"
      "v_pk_mul_f16 v102, v150, v166\n"
      "v_pk_mul_f16 v103, v151, v167\n"
      "v_mfma_f32_16x16x32_f16 v[92:95], a[48:51], v[112:115], v[92:95]\n"
      "v_fma_f32 v192, |v84|, v52, v192\n"
      "v_fma_f32 v192, |v85|, v53, v192\n"
      "v_mfma_f32_16x16x32_f16 v[80:83], a[64:67], v[112:115], v[32:35]\n"
      "v_fma_f32 v192, |v86|, v54, v192\n"
      "v_fma_f32 v192, |v87|, v55, v192\n"
      "v_mfma_f32_16x16x32_f16 v[80:83], a[68:71], v[116:119], v[80:83]\n"
      "v_fma_f32 v192, |v88|, v56, v192\n"
      "v_fma_f32 v192, |v89|, v57, v192\n"
      "v_mfma_f32_16x16x32_f16 v[80:83], a[72:75], v[120:123], v[80:83]\n"
      "ds_read_b32 v224, v227 offset:37888\n"
      "v_pk_mul_f16 v104, v152, v168\n"
      "v_mfma_f32_16x16x32_f16 v[80:83], a[76:79], v[124:127], v[80:83]\n"
      "v_fma_f32 v192, |v90|, v58, v192\n"
      "v_fma_f32 v192, |v91|, v59, v192\n"
      "v_mfma_f32_16x16x32_f16 v[84:87], a[92:95], v[124:127], v[36:39]\n"
      "ds_read_b128 v[176:179], v5 offset:37376\n"
      "v_pk_mul_f16 v105, v153, v169\n"
      "v_mfma_f32_16x16x32_f16 v[84:87], a[88:91], v[120:123], v[84:87]\n"
      "ds_read_b128 v[180:183], v5 offset:37392\n"
      "s_mov_b32 s44, 0x3d800000\n"
      "v_pk_mul_f16 v106, v154, v170\n"
      "v_mfma_f32_16x16x32_f16 v[84:87], a[84:87], v[116:119], v[84:87]\n"
      "v_pk_mul_f16 v107, v155, v171\n"
      "v_fma_f32 v192, |v92|, v60, v192\n"
      "v_mfma_f32_16x16x32_f16 v[84:87], a[80:83], v[112:115], v[84:87]\n"
      "v_fma_f32 v192, |v93|, v61, v192\n"
      "v_fma_f32 v192, |v94|, v62, v192\n"
      "v_mfma_f32_16x16x32_f16 v[88:91], a[96:99], v[112:115], v[40:43]\n"
      "v_fma_f32 v192, |v95|, v63, v192\n"
      "v_fma_f32 v192, |v80|, v64, v192\n"
      "v_mfma_f32_16x16x32_f16 v[88:91], a[100:103], v[116:119], v[88:91]\n"
      "v_fma_f32 v192, |v81|, v65, v192\n"
      "v_pk_mul_f16 v108, v156, v172\n"
      "v_mfma_f32_16x16x32_f16 v[88:91], a[104:107], v[120:123], v[88:91]\n"
      "ds_read_b128 v[160:163], v203 offset:5760\n"
      "v_fma_f32 v192, |v82|, v66, v192\n"
      "v_mfma_f32_16x16x32_f16 v[88:91], a[108:111], v[124:127], v[88:91]\n"
      "v_fma_f32 v192, |v83|, v67, v192\n"
      "v_pk_mul_f16 v109, v157, v173\n"
      "v_mfma_f32_16x16x32_f16 v[92:95], a[124:127], v[124:127], v[44:47]\n"
      "v_pk_mul_f16 v110, v158, v174\n"
      "v_pk_mul_f16 v111, v159, v175\n"
      "v_mfma_f32_16x16x32_f16 v[92:95], a[120:123], v[120:123], v[92:95]\n"
      "s_waitcnt lgkmcnt(1)\n"
      "v_max3_f32 v225, |v176|, |v181|, |v224|\n"
      "v_cmp_le_f32 vcc, 0x3f400000, v225\n"
      "v_mfma_f32_16x16x32_f16 v[92:95], a[116:119], v[116:119], v[92:95]\n"
      "v_fma_f32 v192, |v84|, v68, v192\n"
      "v_fma_f32 v192, |v85|, v69, v192\n"
      "v_mfma_f32_16x16x32_f16 v[92:95], a[112:115], v[112:115], v[92:95]\n"
      "v_cmp_le_f32 s[28:29], s44, v225\n"
      "v_fma_f32 v192, |v86|, v70, v192\n"
      "v_mfma_f32_16x16x32_f16 v[80:83], a[0:3], v[96:99], v[16:19]\n"
      "v_fma_f32 v192, |v87|, v71, v192\n"
      "v_fma_f32 v192, |v88|, v72, v192\n"
      "v_mfma_f32_16x16x32_f16 v[80:83], a[4:7], v[100:103], v[80:83]\n"
      "ds_read_b128 v[164:167], v203 offset:5824\n"
      "s_cmp_eq_u32 s28, 0\n"
      "s_cselect_b64 vcc, -1, vcc\n"
      "v_fma_f32 v192, |v89|, v73, v192\n"
      "v_mfma_f32_16x16x32_f16 v[80:83], a[8:11], v[104:107], v[80:83]\n"
      "s_cbranch_vccnz 5f\n"
      "4:\n"
      "v_mfma_f32_16x16x32_f16 v[80:83], a[12:15], v[108:111], v[80:83]\n"
      "s_waitcnt lgkmcnt(1)\n"
      "v_pk_mul_f16 v112, v128, v160\n"
      "v_cvt_pk_f16_f32 v184, v176, v177\n"
      "v_mfma_f32_16x16x32_f16 v[84:87], a[28:31], v[108:111], v[20:23]\n"
      "v_fma_f32 v192, |v90|, v74, v192\n"
      "v_fma_f32 v192, |v91|, v75, v192\n"
      "v_mfma_f32_16x16x32_f16 v[84:87], a[24:27], v[104:107], v[84:87]\n"
      "v_pk_mul_f16 v113, v129, v161\n"
      "v_cvt_pk_f16_f32 v185, v178, v179\n"
      "v_mfma_f32_16x16x32_f16 v[84:87], a[20:23], v[100:103], v[84:87]\n"
      "v_pk_mul_f16 v114, v130, v162\n"
      "v_cvt_pk_f16_f32 v186, v180, v181\n"
      "v_mfma_f32_16x16x32_f16 v[84:87], a[16:19], v[96:99], v[84:87]\n"
      "v_pk_mul_f16 v115, v131, v163\n"
      "v_cvt_pk_f16_f32 v187, v182, v183\n"
      "v_mfma_f32_16x16x32_f16 v[88:91], a[32:35], v[96:99], v[24:27]\n"
      "ds_write_b128 v213, v[184:187] offset:16512\n"
      "s_lshl_b32 s49, s40, 1\n"
      "v_mfma_f32_16x16x32_f16 v[88:91], a[36:39], v[100:103], v[88:91]\n"
      "s_add_u32 s51, s57, s49\n"
      "v_fma_f32 v192, |v92|, v76, v192\n"
      "v_fma_f32 v192, |v93|, v77, v192\n"
      "v_mfma_f32_16x16x32_f16 v[88:91], a[40:43], v[104:107], v[88:91]\n"
      "s_add_u32 s51, s51, s58\n"
      "v_fma_f32 v192, |v94|, v78, v192\n"
      "v_fma_f32 v192, |v95|, v79, v192\n"
      "v_mfma_f32_16x16x32_f16 v[88:91], a[44:47], v[108:111], v[88:91]\n"
      "s_add_u32 s51, s51, s56\n"
      "v_mul_f32 v193, |v80|, v48\n"
      "v_fma_f32 v193, |v81|, v49, v193\n"
      "v_mfma_f32_16x16x32_f16 v[92:95], a[60:63], v[108:111], v[28:31]\n"
      "s_waitcnt lgkmcnt(1)\n"
      "ds_read_b128 v[168:171], v203 offset:5888\n"
      "v_pk_mul_f16 v116, v132, v164\n"
      "v_mfma_f32_16x16x32_f16 v[92:95], a[56:59], v[104:107], v[92:95]\n"
      "s_sub_u32 s51, 0, s51\n"
      "v_fma_f32 v193, |v82|, v50, v193\n"
      "v_fma_f32 v193, |v83|, v51, v193\n"
      "v_mfma_f32_16x16x32_f16 v[92:95], a[52:55], v[100:103], v[92:95]\n"
      "s_add_u32 s49, s59, s49\n"
      "v_pk_mul_f16 v117, v133, v165\n"
      "v_pk_mul_f16 v118, v134, v166\n"
      "v_mfma_f32_16x16x32_f16 v[92:95], a[48:51], v[96:99], v[92:95]\n"
      "s_sub_u32 s39, 6, s49\n"
      "v_pk_mul_f16 v119, v135, v167\n"
      "v_fma_f32 v193, |v84|, v52, v193\n"
      "v_mfma_f32_16x16x32_f16 v[80:83], a[64:67], v[96:99], v[32:35]\n"
      "s_movk_i32 s45, 0x207\n"
      "v_fma_f32 v193, |v85|, v53, v193\n"
      "v_fma_f32 v193, |v86|, v54, v193\n"
      "v_mfma_f32_16x16x32_f16 v[80:83], a[68:71], v[100:103], v[80:83]\n"
      "v_fma_f32 v193, |v87|, v55, v193\n"
      "v_fma_f32 v193, |v88|, v56, v193\n"
      "v_mfma_f32_16x16x32_f16 v[80:83], a[72:75], v[104:107], v[80:83]\n"
      "ds_read_b128 v[172:175], v203 offset:5952\n"
      "v_fma_f32 v193, |v89|, v57, v193\n"
      "v_mfma_f32_16x16x32_f16 v[80:83], a[76:79], v[108:111], v[80:83]\n"
      "v_fma_f32 v193, |v90|, v58, v193\n"
      "v_fma_f32 v193, |v91|, v59, v193\n"
      "v_mfma_f32_16x16x32_f16 v[84:87], a[92:95], v[108:111], v[36:39]\n"
      "s_waitcnt lgkmcnt(1)\n"
      "v_pk_mul_f16 v120, v136, v168\n"
      "v_pk_mul_f16 v121, v137, v169\n"
      "v_mfma_f32_16x16x32_f16 v[84:87], a[88:91], v[104:107], v[84:87]\n"
      "v_ldexp_f32 v215, v215, s51\n"
      "v_pk_mul_f16 v122, v138, v170\n"
      "v_mfma_f32_16x16x32_f16 v[84:87], a[84:87], v[100:103], v[84:87]\n"
      "v_pk_mul_f16 v123, v139, v171\n"
      "v_fma_f32 v193, |v92|, v60, v193\n"
      "v_mfma_f32_16x16x32_f16 v[84:87], a[80:83], v[96:99], v[84:87]\n"
      "v_fma_f32 v193, |v93|, v61, v193\n"
      "v_fma_f32 v193, |v94|, v62, v193\n"
      "v_mfma_f32_16x16x32_f16 v[88:91], a[96:99], v[96:99], v[40:43]\n"
      "ds_read_b128 v[240:243], v201 offset:4608\n"
      "v_fma_f32 v193, |v95|, v63, v193\n"
      "v_mfma_f32_16x16x32_f16 v[88:91], a[100:103], v[100:103], v[88:91]\n"
      "ds_read_b128 v[244:247], v201 offset:4672\n"
      "v_fma_f32 v193, |v80|, v64, v193\n"
      "v_mfma_f32_16x16x32_f16 v[88:91], a[104:107], v[104:107], v[88:91]\n"
      "ds_read_b128 v[248:251], v201 offset:4736\n"
      "v_fma_f32 v193, |v81|, v65, v193\n"
      "v_mfma_f32_16x16x32_f16 v[88:91], a[108:111], v[108:111], v[88:91]\n"
      "s_waitcnt lgkmcnt(3)\n"
      "v_pk_mul_f16 v124, v140, v172\n"
      "v_fma_f32 v193, |v82|, v66, v193\n"
      "v_mfma_f32_16x16x32_f16 v[92:95], a[124:127], v[108:111], v[44:47]\n"
      "ds_read_b128 v[252:255], v201 offset:4800\n"
      "v_fma_f32 v193, |v83|, v67, v193\n"
      "v_mfma_f32_16x16x32_f16 v[92:95], a[120:123], v[104:107], v[92:95]\n"
      "v_pk_mul_f16 v125, v141, v173\n"
      "v_pk_mul_f16 v126, v142, v174\n"
      "v_mfma_f32_16x16x32_f16 v[92:95], a[116:119], v[100:103], v[92:95]\n"
      "ds_read_b128 v[176:179], v200 offset:16512\n"
      "v_pk_mul_f16 v127, v143, v175\n"
      "v_mfma_f32_16x16x32_f16 v[92:95], a[112:115], v[96:99], v[92:95]\n"
      "v_fma_f32 v193, |v84|, v68, v193\n"
      "v_fma_f32 v193, |v85|, v69, v193\n"
      "v_mfma_f32_16x16x32_f16 v[80:83], a[0:3], v[112:115], v[16:19]\n"
      "v_fma_f32 v193, |v86|, v70, v193\n"
      "v_fma_f32 v193, |v87|, v71, v193\n"
      "v_mfma_f32_16x16x32_f16 v[80:83], a[4:7], v[116:119], v[80:83]\n"
      "v_fma_f32 v193, |v88|, v72, v193\n"
      "v_fma_f32 v193, |v89|, v73, v193\n"
      "v_mfma_f32_16x16x32_f16 v[80:83], a[8:11], v[120:123], v[80:83]\n"
      "ds_read_b128 a[152:155], v214 offset:0\n"
      "v_pk_mul_f16 v96, v144, v160\n"
      "v_mfma_f32_16x16x32_f16 v[80:83], a[12:15], v[124:127], v[80:83]\n"
      "v_fma_f32 v193, |v90|, v74, v193\n"
      "v_fma_f32 v193, |v91|, v75, v193\n"
      "v_mfma_f32_16x16x32_f16 v[84:87], a[28:31], v[124:127], v[20:23]\n"
      "v_pk_mul_f16 v97, v145, v161\n"
      "v_pk_mul_f16 v98, v146, v162\n"
      "v_mfma_f32_16x16x32_f16 v[84:87], a[24:27], v[120:123], v[84:87]\n"
      "v_pk_mul_f16 v99, v147, v163\n"
      "v_fma_f32 v193, |v92|, v76, v193\n"
      "v_mfma_f32_16x16x32_f16 v[84:87], a[20:23], v[116:119], v[84:87]\n"
      "v_fma_f32 v193, |v93|, v77, v193\n"
      "v_fma_f32 v193, |v94|, v78, v193\n"
      "v_mfma_f32_16x16x32_f16 v[84:87], a[16:19], v[112:115], v[84:87]\n"
      "s_waitcnt lgkmcnt(1)\n"
      "v_fma_f32 v193, |v95|, v79, v193\n"
      "v_pk_mul_f16 v228, v128, v176\n"
      "v_mfma_f32_16x16x32_f16 v[88:91], a[32:35], v[112:115], v[24:27]\n"
      "v_pk_mul_f16 v229, v129, v177\n"
      "v_pk_mul_f16 v230, v130, v178\n"
      "v_mfma_f32_16x16x32_f16 v[88:91], a[36:39], v[116:119], v[88:91]\n"
      "v_pk_mul_f16 v231, v131, v179\n"
      "v_mul_f32 v195, |v80|, v48\n"
      "v_mfma_f32_16x16x32_f16 v[88:91], a[40:43], v[120:123], v[88:91]\n"
      "42:\n"
      "s_cbranch_execz 44b\n"
      "v_mfma_f32_16x16x32_f16 v[88:91], a[44:47], v[124:127], v[88:91]\n"
      "v_fma_f32 v195, |v81|, v49, v195\n"
      "v_pk_mul_f16 v100, v148, v164\n"
      "v_mfma_f32_16x16x32_f16 v[92:95], a[60:63], v[124:127], v[28:31]\n"
      "ds_read_b128 a[168:171], v220 offset:0\n"
      "v_fma_f32 v195, |v82|, v50, v195\n"
      "v_mfma_f32_16x16x32_f16 v[92:95], a[56:59], v[120:123], v[92:95]\n"
      "v_fma_f32 v195, |v83|, v51, v195\n"
      "v_pk_mul_f16 v101, v149, v165\n"
      "v_mfma_f32_16x16x32_f16 v[92:95], a[52:55], v[116:119], v[92:95]\n"
      "v_pk_mul_f16 v102, v150, v166\n"
      "v_pk_mul_f16 v103, v151, v167\n"
      "v_mfma_f32_16x16x32_f16 v[92:95], a[48:51], v[112:115], v[92:95]\n"
      "s_waitcnt lgkmcnt(1)\n"
      "v_fma_f32 v195, |v84|, v52, v195\n"
      "v_fma_f32 v195, |v85|, v53, v195\n"
      "v_mfma_f32_16x16x32_f16 a[128:131], v[228:231], a[152:155], 0\n"
      "v_fma_f32 v195, |v86|, v54, v195\n"
      "v_fma_f32 v195, |v87|, v55, v195\n"
      "v_mfma_f32_16x16x32_f16 v[80:83], a[64:67], v[112:115], v[32:35]\n"
      "v_pk_mul_f16 v232, v144, v176\n"
      "v_pk_mul_f16 v233, v145, v177\n"
      "v_mfma_f32_16x16x32_f16 v[80:83], a[68:71], v[116:119], v[80:83]\n"
      "v_pk_mul_f16 v234, v146, v178\n"
      "v_pk_mul_f16 v235, v147, v179\n"
      "v_mfma_f32_16x16x32_f16 v[80:83], a[72:75], v[120:123], v[80:83]\n"
      "v_fma_f32 v195, |v88|, v56, v195\n"
      "v_fma_f32 v195, |v89|, v57, v195\n"
      "v_mfma_f32_16x16x32_f16 v[80:83], a[76:79], v[124:127], v[80:83]\n"
      "ds_read_b128 a[184:187], v221 offset:0\n"
      "v_pk_mul_f16 v104, v152, v168\n"
      "v_mfma_f32_16x16x32_f16 v[84:87], a[92:95], v[124:127], v[36:39]\n"
      "v_fma_f32 v195, |v90|, v58, v195\n"
      "v_fma_f32 v195, |v91|, v59, v195\n"
      "v_mfma_f32_16x16x32_f16 v[84:87], a[88:91], v[120:123], v[84:87]\n"
      "v_pk_mul_f16 v105, v153, v169\n"
      "v_pk_mul_f16 v106, v154, v170\n"
      "v_mfma_f32_16x16x32_f16 v[84:87], a[84:87], v[116:119], v[84:87]\n"
      "v_pk_mul_f16 v107, v155, v171\n"
      "v_fma_f32 v195, |v92|, v60, v195\n"
      "v_mfma_f32_16x16x32_f16 v[84:87], a[80:83], v[112:115], v[84:87]\n"
      "s_waitcnt lgkmcnt(1)\n"
      "v_fma_f32 v195, |v93|, v61, v195\n"
      "v_fma_f32 v195, |v94|, v62, v195\n"
      "v_mfma_f32_16x16x32_f16 a[132:135], v[232:235], a[168:171], 0\n"
      "v_fma_f32 v195, |v95|, v63, v195\n"
      "v_pk_mul_f16 v236, v240, v176\n"
      "v_mfma_f32_16x16x32_f16 v[88:91], a[96:99], v[112:115], v[40:43]\n"
      "v_pk_mul_f16 v237, v241, v177\n"
      "v_pk_mul_f16 v238, v242, v178\n"
      "v_mfma_f32_16x16x32_f16 v[88:91], a[100:103], v[116:119], v[88:91]\n"
      "v_pk_mul_f16 v239, v243, v179\n"
      "v_fma_f32 v195, |v80|, v64, v195\n"
      "v_mfma_f32_16x16x32_f16 v[88:91], a[104:107], v[120:123], v[88:91]\n"
      "v_fma_f32 v195, |v81|, v65, v195\n"
      "v_pk_mul_f16 v108, v156, v172\n"
      "v_mfma_f32_16x16x32_f16 v[88:91], a[108:111], v[124:127], v[88:91]\n"
      "ds_read_b128 v[160:163], v203 offset:6336\n"
      "v_fma_f32 v195, |v82|, v66, v195\n"
      "v_mfma_f32_16x16x32_f16 v[92:95], a[124:127], v[124:127], v[44:47]\n"
      "v_fma_f32 v195, |v83|, v67, v195\n"
      "v_pk_mul_f16 v109, v157, v173\n"
      "v_mfma_f32_16x16x32_f16 v[92:95], a[120:123], v[120:123], v[92:95]\n"
      "ds_read_b128 v[176:179], v200 offset:16576\n"
      "v_pk_mul_f16 v110, v158, v174\n"
      "v_mfma_f32_16x16x32_f16 v[92:95], a[116:119], v[116:119], v[92:95]\n"
      "v_pk_mul_f16 v111, v159, v175\n"
      "v_fma_f32 v195, |v84|, v68, v195\n"
      "v_mfma_f32_16x16x32_f16 v[92:95], a[112:115], v[112:115], v[92:95]\n"
      "s_waitcnt lgkmcnt(2)\n"
      "v_fma_f32 v195, |v85|, v69, v195\n"
      "v_fma_f32 v195, |v86|, v70, v195\n"
      "v_mfma_f32_16x16x32_f16 a[136:139], v[236:239], a[184:187], 0\n"
      "v_fma_f32 v195, |v87|, v71, v195\n"
      "v_fma_f32 v195, |v88|, v72, v195\n"
      "v_mfma_f32_16x16x32_f16 v[80:83], a[0:3], v[96:99], v[16:19]\n"
      "ds_read_b128 a[156:159], v214 offset:64\n"
      "v_fma_f32 v195, |v89|, v73, v195\n"
      "v_mfma_f32_16x16x32_f16 v[80:83], a[4:7], v[100:103], v[80:83]\n"
      "v_fma_f32 v195, |v90|, v74, v195\n"
      "v_fma_f32 v195, |v91|, v75, v195\n"
      "v_mfma_f32_16x16x32_f16 v[80:83], a[8:11], v[104:107], v[80:83]\n"
      "s_waitcnt lgkmcnt(2)\n"
      "v_pk_mul_f16 v112, v128, v160\n"
      "v_pk_mul_f16 v113, v129, v161\n"
      "v_mfma_f32_16x16x32_f16 v[80:83], a[12:15], v[108:111], v[80:83]\n"
      "ds_read_b128 v[164:167], v203 offset:6400\n"
      "v_pk_mul_f16 v114, v130, v162\n"
      "v_mfma_f32_16x16x32_f16 v[84:87], a[28:31], v[108:111], v[20:23]\n"
      "v_pk_mul_f16 v115, v131, v163\n"
      "v_fma_f32 v195, |v92|, v76, v195\n"
      "v_mfma_f32_16x16x32_f16 v[84:87], a[24:27], v[104:107], v[84:87]\n"
      "v_fma_f32 v195, |v93|, v77, v195\n"
      "v_fma_f32 v195, |v94|, v78, v195\n"
      "v_mfma_f32_16x16x32_f16 v[84:87], a[20:23], v[100:103], v[84:87]\n"
      "s_waitcnt lgkmcnt(2)\n"
      "v_fma_f32 v195, |v95|, v79, v195\n"
      "v_pk_mul_f16 v228, v132, v176\n"
      "v_mfma_f32_16x16x32_f16 v[84:87], a[16:19], v[96:99], v[84:87]\n"
      "v_pk_mul_f16 v229, v133, v177\n"
      "v_pk_mul_f16 v230, v134, v178\n"
      "v_mfma_f32_16x16x32_f16 v[88:91], a[32:35], v[96:99], v[24:27]\n"
      "v_permlane32_swap_b32 v190, v195\n"
      "v_mfma_f32_16x16x32_f16 v[88:91], a[36:39], v[100:103], v[88:91]\n"
      "v_pk_mul_f16 v231, v135, v179\n"
      "v_mul_f32 v196, |v80|, v48\n"
      "v_mfma_f32_16x16x32_f16 v[88:91], a[40:43], v[104:107], v[88:91]\n"
      "ds_read_b128 a[172:175], v220 offset:64\n"
      "v_fma_f32 v196, |v81|, v49, v196\n"
      "v_mfma_f32_16x16x32_f16 v[88:91], a[44:47], v[108:111], v[88:91]\n"
      "s_waitcnt lgkmcnt(1)\n"
      "v_pk_mul_f16 v116, v132, v164\n"
      "v_fma_f32 v196, |v82|, v50, v196\n"
      "v_mfma_f32_16x16x32_f16 v[92:95], a[60:63], v[108:111], v[28:31]\n"
      "v_fma_f32 v196, |v83|, v51, v196\n"
      "v_pk_mul_f16 v117, v133, v165\n"
      "v_mfma_f32_16x16x32_f16 v[92:95], a[56:59], v[104:107], v[92:95]\n"
      "ds_read_b128 v[168:171], v203 offset:6464\n"
      "v_pk_mul_f16 v118, v134, v166\n"
      "v_mfma_f32_16x16x32_f16 v[92:95], a[52:55], v[100:103], v[92:95]\n"
      "v_pk_mul_f16 v119, v135, v167\n"
      "v_fma_f32 v196, |v84|, v52, v196\n"
      "v_mfma_f32_16x16x32_f16 v[92:95], a[48:51], v[96:99], v[92:95]\n"
      "v_fma_f32 v196, |v85|, v53, v196\n"
      "v_fma_f32 v196, |v86|, v54, v196\n"
      "v_mfma_f32_16x16x32_f16 a[128:131], v[228:231], a[156:159], a[128:131]\n"
      "v_fma_f32 v196, |v87|, v55, v196\n"
      "v_pk_mul_f16 v232, v148, v176\n"
      "v_mfma_f32_16x16x32_f16 v[80:83], a[64:67], v[96:99], v[32:35]\n"
      "v_pk_mul_f16 v233, v149, v177\n"
      "v_pk_mul_f16 v234, v150, v178\n"
      "v_mfma_f32_16x16x32_f16 v[80:83], a[68:71], v[100:103], v[80:83]\n"
      "v_pk_mul_f16 v235, v151, v179\n"
      "v_fma_f32 v196, |v88|, v56, v196\n"
      "v_mfma_f32_16x16x32_f16 v[80:83], a[72:75], v[104:107], v[80:83]\n"
      "ds_read_b128 a[188:191], v221 offset:64\n"
      "v_fma_f32 v196, |v89|, v57, v196\n"
      "v_mfma_f32_16x16x32_f16 v[80:83], a[76:79], v[108:111], v[80:83]\n"
      "v_fma_f32 v196, |v90|, v58, v196\n"
      "v_fma_f32 v196, |v91|, v59, v196\n"
      "v_mfma_f32_16x16x32_f16 v[84:87], a[92:95], v[108:111], v[36:39]\n"
      "s_waitcnt lgkmcnt(1)\n"
      "v_pk_mul_f16 v120, v136, v168\n"
      "v_pk_mul_f16 v121, v137, v169\n"
      "v_mfma_f32_16x16x32_f16 v[84:87], a[88:91], v[104:107], v[84:87]\n"
      "s_waitcnt vmcnt(2)\n"
      "ds_read_b128 a[216:219], v12 offset:0\n"
      "v_pk_mul_f16 v122, v138, v170\n"
      "v_mfma_f32_16x16x32_f16 v[84:87], a[84:87], v[100:103], v[84:87]\n"
      "ds_read_b128 a[220:223], v12 offset:64\n"
      "v_pk_mul_f16 v123, v139, v171\n"
      "v_mfma_f32_16x16x32_f16 v[84:87], a[80:83], v[96:99], v[84:87]\n"
      "v_fma_f32 v196, |v92|, v60, v196\n"
      "v_fma_f32 v196, |v93|, v61, v196\n"
      "v_mfma_f32_16x16x32_f16 a[132:135], v[232:235], a[172:175], a[132:135]\n"
      "ds_read_b128 a[224:227], v12 offset:128\n"
      "v_fma_f32 v196, |v94|, v62, v196\n"
      "v_mfma_f32_16x16x32_f16 v[88:91], a[96:99], v[96:99], v[40:43]\n"
      "v_fma_f32 v196, |v95|, v63, v196\n"
      "v_pk_mul_f16 v236, v244, v176\n"
      "v_mfma_f32_16x16x32_f16 v[88:91], a[100:103], v[100:103], v[88:91]\n"
      "ds_read_b128 a[228:231], v12 offset:192\n"
      "v_pk_mul_f16 v237, v245, v177\n"
      "v_mfma_f32_16x16x32_f16 v[88:91], a[104:107], v[104:107], v[88:91]\n"
      "v_pk_mul_f16 v238, v246, v178\n"
      "v_pk_mul_f16 v239, v247, v179\n"
      "v_mfma_f32_16x16x32_f16 v[88:91], a[108:111], v[108:111], v[88:91]\n"
      "v_fma_f32 v196, |v80|, v64, v196\n"
      "v_fma_f32 v196, |v81|, v65, v196\n"
      "v_mfma_f32_16x16x32_f16 v[92:95], a[124:127], v[108:111], v[44:47]\n"
      "v_fma_f32 v196, |v82|, v66, v196\n"
      "v_fma_f32 v196, |v83|, v67, v196\n"
      "v_mfma_f32_16x16x32_f16 v[92:95], a[120:123], v[104:107], v[92:95]\n"
      "ds_read_b128 a[232:235], v13 offset:0\n"
      "v_fma_f32 v196, |v84|, v68, v196\n"
      "v_mfma_f32_16x16x32_f16 v[92:95], a[116:119], v[100:103], v[92:95]\n"
      "ds_read_b128 a[236:239], v13 offset:64\n"
      "v_fma_f32 v196, |v85|, v69, v196\n"
      "v_mfma_f32_16x16x32_f16 v[92:95], a[112:115], v[96:99], v[92:95]\n"
      "s_waitcnt lgkmcnt(6)\n"
      "ds_read_b128 a[240:243], v13 offset:128\n"
      "v_fma_f32 v196, |v86|, v70, v196\n"
      "v_mfma_f32_16x16x32_f16 a[136:139], v[236:239], a[188:191], a[136:139]\n"
      "s_waitcnt lgkmcnt(6)\n"
      "ds_read_b128 a[244:247], v13 offset:192\n"
      "v_fma_f32 v196, |v87|, v71, v196\n"
      "v_mfma_f32_16x16x32_f16 v[80:83], a[0:3], a[216:219], v[16:19]\n"
      "s_waitcnt lgkmcnt(6)\n"
      "v_fma_f32 v196, |v88|, v72, v196\n"
      "v_fma_f32 v196, |v89|, v73, v196\n"
      "v_mfma_f32_16x16x32_f16 v[80:83], a[4:7], a[220:223], v[80:83]\n"
      "s_waitcnt lgkmcnt(5)\n"
      "ds_read_b128 v[172:175], v203 offset:6528\n"
      "v_fma_f32 v196, |v90|, v74, v196\n"
      "v_mfma_f32_16x16x32_f16 v[80:83], a[8:11], a[224:227], v[80:83]\n"
      "s_waitcnt lgkmcnt(5)\n"
      "v_fma_f32 v196, |v91|, v75, v196\n"
      "v_fma_f32 v196, |v92|, v76, v196\n"
      "v_mfma_f32_16x16x32_f16 v[80:83], a[12:15], a[228:231], v[80:83]\n"
      "v_fma_f32 v196, |v93|, v77, v196\n"
      "v_fma_f32 v196, |v94|, v78, v196\n"
      "v_mfma_f32_16x16x32_f16 v[84:87], a[76:79], a[228:231], v[32:35]\n"
      "ds_read_b128 v[176:179], v200 offset:16640\n"
      "v_fma_f32 v196, |v95|, v79, v196\n"
      "v_mfma_f32_16x16x32_f16 v[84:87], a[72:75], a[224:227], v[84:87]\n"
      "ds_read_b128 a[160:163], v214 offset:128\n"
      "v_pk_mul_f16 v96, v144, v160\n"
      "v_mfma_f32_16x16x32_f16 v[84:87], a[68:71], a[220:223], v[84:87]\n"
      "v_permlane32_swap_b32 v191, v196\n"
      "v_mfma_f32_16x16x32_f16 v[84:87], a[64:67], a[216:219], v[84:87]\n"
      "s_waitcnt lgkmcnt(6)\n"
      "v_mul_f32 v199, |v80|, v48\n"
      "v_fma_f32 v199, |v81|, v49, v199\n"
      "v_mfma_f32_16x16x32_f16 v[88:91], a[0:3], a[232:235], v[16:19]\n"
      "s_waitcnt lgkmcnt(5)\n"
      "v_fma_f32 v199, |v82|, v50, v199\n"
      "v_fma_f32 v199, |v83|, v51, v199\n"
      "v_mfma_f32_16x16x32_f16 v[88:91], a[4:7], a[236:239], v[88:91]\n"
      "s_waitcnt lgkmcnt(2)\n"
      "v_pk_mul_f16 v124, v140, v172\n"
      "v_pk_mul_f16 v125, v141, v173\n"
      "v_mfma_f32_16x16x32_f16 v[88:91], a[8:11], a[240:243], v[88:91]\n"
      "v_pk_mul_f16 v126, v142, v174\n"
      "v_pk_mul_f16 v127, v143, v175\n"
      "v_mfma_f32_16x16x32_f16 v[88:91], a[12:15], a[244:247], v[88:91]\n"
      "v_fma_f32 v199, |v84|, v64, v199\n"
      "v_fma_f32 v199, |v85|, v65, v199\n"
      "v_mfma_f32_16x16x32_f16 v[92:95], a[76:79], a[244:247], v[32:35]\n"
      "v_fma_f32 v199, |v86|, v66, v199\n"
      "v_fma_f32 v199, |v87|, v67, v199\n"
      "v_mfma_f32_16x16x32_f16 v[92:95], a[72:75], a[240:243], v[92:95]\n"
      "s_waitcnt vmcnt(0)\n"
      "ds_write_b32 v216, v199 offset:1440\n"
      "v_mfma_f32_16x16x32_f16 v[92:95], a[68:71], a[236:239], v[92:95]\n"
      "v_mul_f32 v225, |v88|, v48\n"
      "v_fma_f32 v225, |v89|, v49, v225\n"
      "v_mfma_f32_16x16x32_f16 v[92:95], a[64:67], a[232:235], v[92:95]\n"
      "v_fma_f32 v225, |v90|, v50, v225\n"
      "v_fma_f32 v225, |v91|, v51, v225\n"
      "v_mfma_f32_16x16x32_f16 v[80:83], a[0:3], v[112:115], v[16:19]\n"
      "v_pk_mul_f16 v97, v145, v161\n"
      "v_pk_mul_f16 v98, v146, v162\n"
      "v_mfma_f32_16x16x32_f16 v[80:83], a[4:7], v[116:119], v[80:83]\n"
      "s_waitcnt lgkmcnt(2)\n"
      "v_pk_mul_f16 v99, v147, v163\n"
      "v_pk_mul_f16 v228, v136, v176\n"
      "v_mfma_f32_16x16x32_f16 v[80:83], a[8:11], v[120:123], v[80:83]\n"
      "v_fma_f32 v225, |v92|, v64, v225\n"
      "v_fma_f32 v225, |v93|, v65, v225\n"
      "v_mfma_f32_16x16x32_f16 v[80:83], a[12:15], v[124:127], v[80:83]\n"
      "v_fma_f32 v225, |v94|, v66, v225\n"
      "v_fma_f32 v225, |v95|, v67, v225\n"
      "v_mfma_f32_16x16x32_f16 v[84:87], a[28:31], v[124:127], v[20:23]\n"
      "ds_write_b32 v217, v225 offset:1440\n"
      "v_mfma_f32_16x16x32_f16 v[84:87], a[24:27], v[120:123], v[84:87]\n"
      "v_pk_mul_f16 v229, v137, v177\n"
      "v_pk_mul_f16 v230, v138, v178\n"
      "v_mfma_f32_16x16x32_f16 v[84:87], a[20:23], v[116:119], v[84:87]\n"
      "v_pk_mul_f16 v231, v139, v179\n"
      "v_mul_f32 v197, |v80|, v48\n"
      "v_mfma_f32_16x16x32_f16 v[84:87], a[16:19], v[112:115], v[84:87]\n"
      "v_fma_f32 v197, |v81|, v49, v197\n"
      "v_pk_mul_f16 v100, v148, v164\n"
      "v_mfma_f32_16x16x32_f16 v[88:91], a[32:35], v[112:115], v[24:27]\n"
      "ds_read_b128 a[176:179], v220 offset:128\n"
      "v_fma_f32 v197, |v82|, v50, v197\n"
      "v_mfma_f32_16x16x32_f16 v[88:91], a[36:39], v[116:119], v[88:91]\n"
      "v_fma_f32 v197, |v83|, v51, v197\n"
      "v_pk_mul_f16 v101, v149, v165\n"
      "v_mfma_f32_16x16x32_f16 v[88:91], a[40:43], v[120:123], v[88:91]\n"
      "v_pk_mul_f16 v102, v150, v166\n"
      "v_pk_mul_f16 v103, v151, v167\n"
      "v_mfma_f32_16x16x32_f16 v[88:91], a[44:47], v[124:127], v[88:91]\n"
      "v_fma_f32 v197, |v84|, v52, v197\n"
      "v_fma_f32 v197, |v85|, v53, v197\n"
      "v_mfma_f32_16x16x32_f16 v[92:95], a[60:63], v[124:127], v[28:31]\n"
      "v_fma_f32 v197, |v86|, v54, v197\n"
      "v_fma_f32 v197, |v87|, v55, v197\n"
      "v_mfma_f32_16x16x32_f16 v[92:95], a[56:59], v[120:123], v[92:95]\n"
      "v_pk_mul_f16 v232, v152, v176\n"
      "v_pk_mul_f16 v233, v153, v177\n"
      "v_mfma_f32_16x16x32_f16 v[92:95], a[52:55], v[116:119], v[92:95]\n"
      "v_pk_mul_f16 v234, v154, v178\n"
      "v_pk_mul_f16 v235, v155, v179\n"
      "v_mfma_f32_16x16x32_f16 v[92:95], a[48:51], v[112:115], v[92:95]\n"
      "s_waitcnt lgkmcnt(3)\n"
      "v_fma_f32 v197, |v88|, v56, v197\n"
      "v_fma_f32 v197, |v89|, v57, v197\n"
      "v_mfma_f32_16x16x32_f16 a[128:131], v[228:231], a[160:163], a[128:131]\n"
      "ds_read_b128 a[192:195], v221 offset:128\n"
      "v_pk_mul_f16 v104, v152, v168\n"
      "v_mfma_f32_16x16x32_f16 v[80:83], a[64:67], v[112:115], v[32:35]\n"
      "v_fma_f32 v197, |v90|, v58, v197\n"
      "v_fma_f32 v197, |v91|, v59, v197\n"
      "v_mfma_f32_16x16x32_f16 v[80:83], a[68:71], v[116:119], v[80:83]\n"
      "v_pk_mul_f16 v105, v153, v169\n"
      "v_pk_mul_f16 v106, v154, v170\n"
      "v_mfma_f32_16x16x32_f16 v[80:83], a[72:75], v[120:123], v[80:83]\n"
      "v_pk_mul_f16 v107, v155, v171\n"
      "v_fma_f32 v197, |v92|, v60, v197\n"
      "v_mfma_f32_16x16x32_f16 v[80:83], a[76:79], v[124:127], v[80:83]\n"
      "v_fma_f32 v197, |v93|, v61, v197\n"
      "v_fma_f32 v197, |v94|, v62, v197\n"
      "v_mfma_f32_16x16x32_f16 v[84:87], a[92:95], v[124:127], v[36:39]\n"
      "v_fma_f32 v197, |v95|, v63, v197\n"
      "v_pk_mul_f16 v236, v248, v176\n"
      "v_mfma_f32_16x16x32_f16 v[84:87], a[88:91], v[120:123], v[84:87]\n"
      "v_pk_mul_f16 v237, v249, v177\n"
      "v_pk_mul_f16 v238, v250, v178\n"
      "v_mfma_f32_16x16x32_f16 v[84:87], a[84:87], v[116:119], v[84:87]\n"
      "v_pk_mul_f16 v239, v251, v179\n"
      "v_fma_f32 v197, |v80|, v64, v197\n"
      "v_mfma_f32_16x16x32_f16 v[84:87], a[80:83], v[112:115], v[84:87]\n"
      "s_waitcnt lgkmcnt(1)\n"
      "v_fma_f32 v197, |v81|, v65, v197\n"
      "v_pk_mul_f16 v108, v156, v172\n"
      "v_mfma_f32_16x16x32_f16 a[132:135], v[232:235], a[176:179], a[132:135]\n"
      "ds_read_b128 v[160:163], v202 offset:4608\n"
      "v_fma_f32 v197, |v82|, v66, v197\n"
      "v_mfma_f32_16x16x32_f16 v[88:91], a[96:99], v[112:115], v[40:43]\n"
      "v_fma_f32 v197, |v83|, v67, v197\n"
      "v_pk_mul_f16 v109, v157, v173\n"
      "v_mfma_f32_16x16x32_f16 v[88:91], a[100:103], v[116:119], v[88:91]\n"
      "ds_read_b128 v[176:179], v200 offset:16704\n"
      "v_pk_mul_f16 v110, v158, v174\n"
      "v_mfma_f32_16x16x32_f16 v[88:91], a[104:107], v[120:123], v[88:91]\n"
      "v_add_f32 v190, v190, v195\n"
      "v_pk_mul_f16 v111, v159, v175\n"
      "v_mfma_f32_16x16x32_f16 v[88:91], a[108:111], v[124:127], v[88:91]\n"
      "v_fma_f32 v197, |v84|, v68, v197\n"
      "v_fma_f32 v197, |v85|, v69, v197\n"
      "v_mfma_f32_16x16x32_f16 v[92:95], a[124:127], v[124:127], v[44:47]\n"
      "v_fma_f32 v197, |v86|, v70, v197\n"
      "v_fma_f32 v197, |v87|, v71, v197\n"
      "v_mfma_f32_16x16x32_f16 v[92:95], a[120:123], v[120:123], v[92:95]\n"
      "ds_read_b128 a[164:167], v214 offset:192\n"
      "v_add_f32 v191, v191, v196\n"
      "v_mfma_f32_16x16x32_f16 v[92:95], a[116:119], v[116:119], v[92:95]\n"
      "v_fma_f32 v197, |v88|, v72, v197\n"
      "v_fma_f32 v197, |v89|, v73, v197\n"
      "v_mfma_f32_16x16x32_f16 v[92:95], a[112:115], v[112:115], v[92:95]\n"
      "s_waitcnt lgkmcnt(2)\n"
      "v_pk_mul_f16 v112, v240, v160\n"
      "v_fma_f32 v197, |v90|, v74, v197\n"
      "v_mfma_f32_16x16x32_f16 a[136:139], v[236:239], a[192:195], a[136:139]\n"
      "v_fma_f32 v197, |v91|, v75, v197\n"
      "v_pk_mul_f16 v113, v241, v161\n"
      "v_mfma_f32_16x16x32_f16 v[80:83], a[0:3], v[96:99], v[16:19]\n"
      "ds_read_b128 v[164:167], v202 offset:4672\n"
      "v_pk_mul_f16 v114, v242, v162\n"
      "v_mfma_f32_16x16x32_f16 v[80:83], a[4:7], v[100:103], v[80:83]\n"
      "v_pk_mul_f16 v115, v243, v163\n"
      "v_fma_f32 v197, |v92|, v76, v197\n"
      "v_mfma_f32_16x16x32_f16 v[80:83], a[8:11], v[104:107], v[80:83]\n"
      "v_fma_f32 v197, |v93|, v77, v197\n"
      "v_fma_f32 v197, |v94|, v78, v197\n"
      "v_mfma_f32_16x16x32_f16 v[80:83], a[12:15], v[108:111], v[80:83]\n"
      "s_waitcnt lgkmcnt(2)\n"
      "v_fma_f32 v197, |v95|, v79, v197\n"
      "v_pk_mul_f16 v228, v140, v176\n"
      "v_mfma_f32_16x16x32_f16 v[84:87], a[28:31], v[108:111], v[20:23]\n"
      "v_pk_mul_f16 v229, v141, v177\n"
      "v_pk_mul_f16 v230, v142, v178\n"
      "v_mfma_f32_16x16x32_f16 v[84:87], a[24:27], v[104:107], v[84:87]\n"
      "v_permlane32_swap_b32 v192, v197\n"
      "v_mfma_f32_16x16x32_f16 v[84:87], a[20:23], v[100:103], v[84:87]\n"
      "v_pk_mul_f16 v231, v143, v179\n"
      "v_mul_f32 v198, |v80|, v48\n"
      "v_mfma_f32_16x16x32_f16 v[84:87], a[16:19], v[96:99], v[84:87]\n"
      "ds_read_b128 a[180:183], v220 offset:192\n"
      "v_fma_f32 v198, |v81|, v49, v198\n"
      "v_mfma_f32_16x16x32_f16 v[88:91], a[32:35], v[96:99], v[24:27]\n"
      "s_waitcnt lgkmcnt(1)\n"
      "v_pk_mul_f16 v116, v244, v164\n"
      "v_fma_f32 v198, |v82|, v50, v198\n"
      "v_mfma_f32_16x16x32_f16 v[88:91], a[36:39], v[100:103], v[88:91]\n"
      "v_fma_f32 v198, |v83|, v51, v198\n"
      "v_pk_mul_f16 v117, v245, v165\n"
      "v_mfma_f32_16x16x32_f16 v[88:91], a[40:43], v[104:107], v[88:91]\n"
      "ds_read_b128 v[168:171], v202 offset:4736\n"
      "v_pk_mul_f16 v118, v246, v166\n"
      "v_mfma_f32_16x16x32_f16 v[88:91], a[44:47], v[108:111], v[88:91]\n"
      "v_pk_mul_f16 v119, v247, v167\n"
      "v_fma_f32 v198, |v84|, v52, v198\n"
      "v_mfma_f32_16x16x32_f16 v[92:95], a[60:63], v[108:111], v[28:31]\n"
      "v_fma_f32 v198, |v85|, v53, v198\n"
      "v_fma_f32 v198, |v86|, v54, v198\n"
      "v_mfma_f32_16x16x32_f16 v[92:95], a[56:59], v[104:107], v[92:95]\n"
      "v_fma_f32 v198, |v87|, v55, v198\n"
      "v_pk_mul_f16 v232, v156, v176\n"
      "v_mfma_f32_16x16x32_f16 v[92:95], a[52:55], v[100:103], v[92:95]\n"
      "v_pk_mul_f16 v233, v157, v177\n"
      "v_pk_mul_f16 v234, v158, v178\n"
      "v_mfma_f32_16x16x32_f16 v[92:95], a[48:51], v[96:99], v[92:95]\n"
      "v_pk_mul_f16 v235, v159, v179\n"
      "v_fma_f32 v198, |v88|, v56, v198\n"
      "v_mfma_f32_16x16x32_f16 a[128:131], v[228:231], a[164:167], a[128:131]\n"
      "ds_read_b128 a[196:199], v221 offset:192\n"
      "v_fma_f32 v198, |v89|, v57, v198\n"
      "v_mfma_f32_16x16x32_f16 v[80:83], a[64:67], v[96:99], v[32:35]\n"
      "v_fma_f32 v198, |v90|, v58, v198\n"
      "v_fma_f32 v198, |v91|, v59, v198\n"
      "v_mfma_f32_16x16x32_f16 v[80:83], a[68:71], v[100:103], v[80:83]\n"
      "s_waitcnt lgkmcnt(1)\n"
      "v_pk_mul_f16 v120, v248, v168\n"
      "v_pk_mul_f16 v121, v249, v169\n"
      "v_mfma_f32_16x16x32_f16 v[80:83], a[72:75], v[104:107], v[80:83]\n"
      "ds_write_b128 v212, a[128:131] offset:0\n"
      "v_mfma_f32_16x16x32_f16 v[80:83], a[76:79], v[108:111], v[80:83]\n"
      "v_pk_mul_f16 v122, v250, v170\n"
      "v_pk_mul_f16 v123, v251, v171\n"
      "v_mfma_f32_16x16x32_f16 v[84:87], a[92:95], v[108:111], v[36:39]\n"
      "v_fma_f32 v198, |v92|, v60, v198\n"
      "v_fma_f32 v198, |v93|, v61, v198\n"
      "v_mfma_f32_16x16x32_f16 v[84:87], a[88:91], v[104:107], v[84:87]\n"
      "v_fma_f32 v198, |v94|, v62, v198\n"
      "v_fma_f32 v198, |v95|, v63, v198\n"
      "v_mfma_f32_16x16x32_f16 v[84:87], a[84:87], v[100:103], v[84:87]\n"
      "ds_read_b128 v[172:175], v202 offset:4800\n"
      "v_pk_mul_f16 v236, v252, v176\n"
      "v_mfma_f32_16x16x32_f16 v[84:87], a[80:83], v[96:99], v[84:87]\n"
      "v_pk_mul_f16 v237, v253, v177\n"
      "v_pk_mul_f16 v238, v254, v178\n"
      "v_mfma_f32_16x16x32_f16 a[132:135], v[232:235], a[180:183], a[132:135]\n"
      "v_pk_mul_f16 v239, v255, v179\n"
      "v_fma_f32 v198, |v80|, v64, v198\n"
      "v_mfma_f32_16x16x32_f16 v[88:91], a[96:99], v[96:99], v[40:43]\n"
      "v_fma_f32 v198, |v81|, v65, v198\n"
      "v_fma_f32 v198, |v82|, v66, v198\n"
      "v_mfma_f32_16x16x32_f16 v[88:91], a[100:103], v[100:103], v[88:91]\n"
      "v_fma_f32 v198, |v83|, v67, v198\n"
      "v_fma_f32 v198, |v84|, v68, v198\n"
      "v_mfma_f32_16x16x32_f16 v[88:91], a[104:107], v[104:107], v[88:91]\n"
      "ds_write_b128 v212, a[132:135] offset:1280\n"
      "v_mfma_f32_16x16x32_f16 v[88:91], a[108:111], v[108:111], v[88:91]\n"
      "v_fma_f32 v198, |v85|, v69, v198\n"
      "v_fma_f32 v198, |v86|, v70, v198\n"
      "v_mfma_f32_16x16x32_f16 v[92:95], a[124:127], v[108:111], v[44:47]\n"
      "v_fma_f32 v198, |v87|, v71, v198\n"
      "v_add_f32 v192, v192, v197\n"
      "v_mfma_f32_16x16x32_f16 v[92:95], a[120:123], v[104:107], v[92:95]\n"
      "s_waitcnt lgkmcnt(1)\n"
      "v_pk_mul_f16 v124, v252, v172\n"
      "v_pk_mul_f16 v125, v253, v173\n"
      "v_mfma_f32_16x16x32_f16 v[92:95], a[116:119], v[100:103], v[92:95]\n"
      "v_pk_mul_f16 v126, v254, v174\n"
      "v_pk_mul_f16 v127, v255, v175\n"
      "v_mfma_f32_16x16x32_f16 v[92:95], a[112:115], v[96:99], v[92:95]\n"
      "v_permlane16_swap_b32 v190, v192\n"
      "v_mfma_f32_16x16x32_f16 a[136:139], v[236:239], a[196:199], a[136:139]\n"
      "ds_read_b128 v[96:99], v10 offset:0\n"
      "v_fma_f32 v198, |v88|, v72, v198\n"
      "v_mfma_f32_16x16x32_f16 v[80:83], a[0:3], v[112:115], v[16:19]\n"
      "ds_read_b128 v[160:163], v11 offset:0\n"
      "v_fma_f32 v198, |v89|, v73, v198\n"
      "v_mfma_f32_16x16x32_f16 v[80:83], a[4:7], v[116:119], v[80:83]\n"
      "v_fma_f32 v198, |v90|, v74, v198\n"
      "v_fma_f32 v198, |v91|, v75, v198\n"
      "v_mfma_f32_16x16x32_f16 v[80:83], a[8:11], v[120:123], v[80:83]\n"
      "ds_write_b128 v212, a[136:139] offset:2560\n"
      "v_mfma_f32_16x16x32_f16 v[80:83], a[12:15], v[124:127], v[80:83]\n"
      "v_fma_f32 v198, |v92|, v76, v198\n"
      "v_fma_f32 v198, |v93|, v77, v198\n"
      "v_mfma_f32_16x16x32_f16 v[84:87], a[28:31], v[124:127], v[20:23]\n"
      "v_fma_f32 v198, |v94|, v78, v198\n"
      "v_fma_f32 v198, |v95|, v79, v198\n"
      "v_mfma_f32_16x16x32_f16 v[84:87], a[24:27], v[120:123], v[84:87]\n"
      "v_add_f32 v190, v190, v192\n"
      "v_mfma_f32_16x16x32_f16 v[84:87], a[20:23], v[116:119], v[84:87]\n"
      "v_permlane32_swap_b32 v193, v198\n"
      "v_mfma_f32_16x16x32_f16 v[84:87], a[16:19], v[112:115], v[84:87]\n"
      "ds_read_b128 v[100:103], v10 offset:64\n"
      "v_mul_f32 v195, |v80|, v48\n"
      "v_mfma_f32_16x16x32_f16 v[88:91], a[32:35], v[112:115], v[24:27]\n"
      "s_waitcnt lgkmcnt(2)\n"
      "ds_read_b128 v[164:167], v11 offset:64\n"
      "v_pk_mul_f16 v96, v96, v160\n"
      "v_mfma_f32_16x16x32_f16 v[88:91], a[36:39], v[116:119], v[88:91]\n"
      "v_fma_f32 v195, |v81|, v49, v195\n"
      "v_pk_mul_f16 v97, v97, v161\n"
      "v_mfma_f32_16x16x32_f16 v[88:91], a[40:43], v[120:123], v[88:91]\n"
      "v_fma_f32 v195, |v82|, v50, v195\n"
      "v_fma_f32 v195, |v83|, v51, v195\n"
      "v_mfma_f32_16x16x32_f16 v[88:91], a[44:47], v[124:127], v[88:91]\n"
      "v_pk_mul_f16 v98, v98, v162\n"
      "v_pk_mul_f16 v99, v99, v163\n"
      "v_mfma_f32_16x16x32_f16 v[92:95], a[60:63], v[124:127], v[28:31]\n"
      "ds_read_b128 v[104:107], v10 offset:128\n"
      "v_fma_f32 v195, |v84|, v52, v195\n"
      "v_mfma_f32_16x16x32_f16 v[92:95], a[56:59], v[120:123], v[92:95]\n"
      "ds_read_b128 v[168:171], v11 offset:128\n"
      "v_fma_f32 v195, |v85|, v53, v195\n"
      "v_mfma_f32_16x16x32_f16 v[92:95], a[52:55], v[116:119], v[92:95]\n"
      "v_fma_f32 v195, |v86|, v54, v195\n"
      "v_fma_f32 v195, |v87|, v55, v195\n"
      "v_mfma_f32_16x16x32_f16 v[92:95], a[48:51], v[112:115], v[92:95]\n"
      "v_fma_f32 v195, |v88|, v56, v195\n"
      "v_fma_f32 v195, |v89|, v57, v195\n"
      "v_mfma_f32_16x16x32_f16 v[80:83], a[64:67], v[112:115], v[32:35]\n"
      "s_waitcnt lgkmcnt(2)\n"
      "v_pk_mul_f16 v100, v100, v164\n"
      "v_pk_mul_f16 v101, v101, v165\n"
      "v_mfma_f32_16x16x32_f16 v[80:83], a[68:71], v[116:119], v[80:83]\n"
      "v_pk_mul_f16 v102, v102, v166\n"
      "v_pk_mul_f16 v103, v103, v167\n"
      "v_mfma_f32_16x16x32_f16 v[80:83], a[72:75], v[120:123], v[80:83]\n"
      "ds_read_b128 v[108:111], v10 offset:192\n"
      "v_fma_f32 v195, |v90|, v58, v195\n"
      "v_mfma_f32_16x16x32_f16 v[80:83], a[76:79], v[124:127], v[80:83]\n"
      "ds_read_b128 v[172:175], v11 offset:192\n"
      "v_fma_f32 v195, |v91|, v59, v195\n"
      "v_mfma_f32_16x16x32_f16 v[84:87], a[92:95], v[124:127], v[36:39]\n"
      "v_fma_f32 v195, |v92|, v60, v195\n"
      "v_fma_f32 v195, |v93|, v61, v195\n"
      "v_mfma_f32_16x16x32_f16 v[84:87], a[88:91], v[120:123], v[84:87]\n"
      "s_waitcnt lgkmcnt(2)\n"
      "v_pk_mul_f16 v104, v104, v168\n"
      "v_pk_mul_f16 v105, v105, v169\n"
      "v_mfma_f32_16x16x32_f16 v[84:87], a[84:87], v[116:119], v[84:87]\n"
      "v_pk_mul_f16 v106, v106, v170\n"
      "v_fma_f32 v195, |v94|, v62, v195\n"
      "v_mfma_f32_16x16x32_f16 v[84:87], a[80:83], v[112:115], v[84:87]\n"
      "v_fma_f32 v195, |v95|, v63, v195\n"
      "v_pk_mul_f16 v107, v107, v171\n"
      "v_mfma_f32_16x16x32_f16 v[88:91], a[96:99], v[112:115], v[40:43]\n"
      "v_fma_f32 v195, |v80|, v64, v195\n"
      "v_fma_f32 v195, |v81|, v65, v195\n"
      "v_mfma_f32_16x16x32_f16 v[88:91], a[100:103], v[116:119], v[88:91]\n"
      "ds_read_b128 v[160:163], v203 offset:6912\n"
      "v_fma_f32 v195, |v82|, v66, v195\n"
      "v_mfma_f32_16x16x32_f16 v[88:91], a[104:107], v[120:123], v[88:91]\n"
      "43:\n"
      "s_cbranch_execz 44b\n"
      "v_mfma_f32_16x16x32_f16 v[88:91], a[108:111], v[124:127], v[88:91]\n"
      "s_waitcnt lgkmcnt(1)\n"
      "v_pk_mul_f16 v108, v108, v172\n"
      "v_fma_f32 v195, |v83|, v67, v195\n"
      "v_mfma_f32_16x16x32_f16 v[92:95], a[124:127], v[124:127], v[44:47]\n"
      "v_pk_mul_f16 v109, v109, v173\n"
      "v_pk_mul_f16 v110, v110, v174\n"
      "v_mfma_f32_16x16x32_f16 v[92:95], a[120:123], v[120:123], v[92:95]\n"
      "v_pk_mul_f16 v111, v111, v175\n"
      "v_fma_f32 v195, |v84|, v68, v195\n"
      "v_mfma_f32_16x16x32_f16 v[92:95], a[116:119], v[116:119], v[92:95]\n"
      "v_fma_f32 v195, |v85|, v69, v195\n"
      "v_fma_f32 v195, |v86|, v70, v195\n"
      "v_mfma_f32_16x16x32_f16 v[92:95], a[112:115], v[112:115], v[92:95]\n"
      "v_fma_f32 v195, |v87|, v71, v195\n"
      "v_fma_f32 v195, |v88|, v72, v195\n"
      "v_mfma_f32_16x16x32_f16 v[80:83], a[0:3], v[96:99], v[16:19]\n"
      "ds_read_b128 v[164:167], v203 offset:6976\n"
      "v_fma_f32 v195, |v89|, v73, v195\n"
      "v_mfma_f32_16x16x32_f16 v[80:83], a[4:7], v[100:103], v[80:83]\n"
      "s_waitcnt lgkmcnt(1)\n"
      "v_pk_mul_f16 v112, v128, v160\n"
      "v_fma_f32 v195, |v90|, v74, v195\n"
      "v_mfma_f32_16x16x32_f16 v[80:83], a[8:11], v[104:107], v[80:83]\n"
      "v_fma_f32 v195, |v91|, v75, v195\n"
      "v_pk_mul_f16 v113, v129, v161\n"
      "v_mfma_f32_16x16x32_f16 v[80:83], a[12:15], v[108:111], v[80:83]\n"
      "v_pk_mul_f16 v114, v130, v162\n"
      "v_pk_mul_f16 v115, v131, v163\n"
      "v_mfma_f32_16x16x32_f16 v[84:87], a[28:31], v[108:111], v[20:23]\n"
      "v_fma_f32 v195, |v92|, v76, v195\n"
      "v_fma_f32 v195, |v93|, v77, v195\n"
      "v_mfma_f32_16x16x32_f16 v[84:87], a[24:27], v[104:107], v[84:87]\n"
      "v_fma_f32 v195, |v94|, v78, v195\n"
      "v_fma_f32 v195, |v95|, v79, v195\n"
      "v_mfma_f32_16x16x32_f16 v[84:87], a[20:23], v[100:103], v[84:87]\n"
      "v_add_f32 v193, v193, v198\n"
      "v_mul_f32 v196, |v80|, v48\n"
      "v_mfma_f32_16x16x32_f16 v[84:87], a[16:19], v[96:99], v[84:87]\n"
      "v_permlane32_swap_b32 v194, v195\n"
      "v_mfma_f32_16x16x32_f16 v[88:91], a[32:35], v[96:99], v[24:27]\n"
      "v_permlane16_swap_b32 v191, v193\n"
      "v_mfma_f32_16x16x32_f16 v[88:91], a[36:39], v[100:103], v[88:91]\n"
      "s_waitcnt lgkmcnt(0)\n"
      "v_fma_f32 v196, |v81|, v49, v196\n"
      "v_pk_mul_f16 v116, v132, v164\n"
      "v_mfma_f32_16x16x32_f16 v[88:91], a[40:43], v[104:107], v[88:91]\n"
      "ds_read_b128 v[168:171], v203 offset:7040\n"
      "v_fma_f32 v196, |v82|, v50, v196\n"
      "v_mfma_f32_16x16x32_f16 v[88:91], a[44:47], v[108:111], v[88:91]\n"
      "v_fma_f32 v196, |v83|, v51, v196\n"
      "v_pk_mul_f16 v117, v133, v165\n"
      "v_mfma_f32_16x16x32_f16 v[92:95], a[60:63], v[108:111], v[28:31]\n"
      "v_pk_mul_f16 v118, v134, v166\n"
      "v_add_f32 v191, v191, v193\n"
      "v_mfma_f32_16x16x32_f16 v[92:95], a[56:59], v[104:107], v[92:95]\n"
      "v_pk_mul_f16 v119, v135, v167\n"
      "v_fma_f32 v196, |v84|, v52, v196\n"
      "v_mfma_f32_16x16x32_f16 v[92:95], a[52:55], v[100:103], v[92:95]\n"
      "v_fma_f32 v196, |v85|, v53, v196\n"
      "v_fma_f32 v196, |v86|, v54, v196\n"
      "v_mfma_f32_16x16x32_f16 v[92:95], a[48:51], v[96:99], v[92:95]\n"
      "v_fma_f32 v196, |v87|, v55, v196\n"
      "v_fma_f32 v196, |v88|, v56, v196\n"
      "v_mfma_f32_16x16x32_f16 v[80:83], a[64:67], v[96:99], v[32:35]\n"
      "ds_read_b128 v[172:175], v203 offset:7104\n"
      "v_fma_f32 v196, |v89|, v57, v196\n"
      "v_mfma_f32_16x16x32_f16 v[80:83], a[68:71], v[100:103], v[80:83]\n"
      "v_fma_f32 v196, |v90|, v58, v196\n"
      "v_fma_f32 v196, |v91|, v59, v196\n"
      "v_mfma_f32_16x16x32_f16 v[80:83], a[72:75], v[104:107], v[80:83]\n"
      "s_waitcnt lgkmcnt(1)\n"
      "v_pk_mul_f16 v120, v136, v168\n"
      "v_pk_mul_f16 v121, v137, v169\n"
      "v_mfma_f32_16x16x32_f16 v[80:83], a[76:79], v[108:111], v[80:83]\n"
      "v_pk_mul_f16 v122, v138, v170\n"
      "v_pk_mul_f16 v123, v139, v171\n"
      "v_mfma_f32_16x16x32_f16 v[84:87], a[92:95], v[108:111], v[36:39]\n"
      "v_fma_f32 v196, |v92|, v60, v196\n"
      "v_fma_f32 v196, |v93|, v61, v196\n"
      "v_mfma_f32_16x16x32_f16 v[84:87], a[88:91], v[104:107], v[84:87]\n"
      "v_fma_f32 v196, |v94|, v62, v196\n"
      "v_fma_f32 v196, |v95|, v63, v196\n"
      "v_mfma_f32_16x16x32_f16 v[84:87], a[84:87], v[100:103], v[84:87]\n"
      "ds_read2_b32 v[228:229], v208 offset0:0 offset1:8\n"
      "v_fma_f32 v196, |v80|, v64, v196\n"
      "v_mfma_f32_16x16x32_f16 v[84:87], a[80:83], v[96:99], v[84:87]\n"
      "ds_read2_b32 v[230:231], v209 offset0:0 offset1:8\n"
      "v_fma_f32 v196, |v81|, v65, v196\n"
      "v_mfma_f32_16x16x32_f16 v[88:91], a[96:99], v[96:99], v[40:43]\n"
      "s_waitcnt lgkmcnt(2)\n"
      "ds_read2_b32 v[232:233], v210 offset0:0 offset1:8\n"
      "v_pk_mul_f16 v124, v140, v172\n"
      "v_mfma_f32_16x16x32_f16 v[88:91], a[100:103], v[100:103], v[88:91]\n"
      "v_fma_f32 v196, |v82|, v66, v196\n"
      "v_fma_f32 v196, |v83|, v67, v196\n"
      "v_mfma_f32_16x16x32_f16 v[88:91], a[104:107], v[104:107], v[88:91]\n"
      "ds_read2_b32 v[234:235], v211 offset0:0 offset1:8\n"
      "v_pk_mul_f16 v125, v141, v173\n"
      "v_mfma_f32_16x16x32_f16 v[88:91], a[108:111], v[108:111], v[88:91]\n"
      "ds_read2_b32 v[236:237], v218 offset0:0 offset1:8\n"
      "v_pk_mul_f16 v126, v142, v174\n"
      "v_mfma_f32_16x16x32_f16 v[92:95], a[124:127], v[108:111], v[44:47]\n"
      "v_pk_mul_f16 v127, v143, v175\n"
      "v_fma_f32 v196, |v84|, v68, v196\n"
      "v_mfma_f32_16x16x32_f16 v[92:95], a[120:123], v[104:107], v[92:95]\n"
      "v_fma_f32 v196, |v85|, v69, v196\n"
      "v_fma_f32 v196, |v86|, v70, v196\n"
      "v_mfma_f32_16x16x32_f16 v[92:95], a[116:119], v[100:103], v[92:95]\n"
      "v_fma_f32 v196, |v87|, v71, v196\n"
      "v_fma_f32 v196, |v88|, v72, v196\n"
      "v_mfma_f32_16x16x32_f16 v[92:95], a[112:115], v[96:99], v[92:95]\n"
      "v_fma_f32 v196, |v89|, v73, v196\n"
      "v_pk_mul_f16 v96, v144, v160\n"
      "v_mfma_f32_16x16x32_f16 v[80:83], a[0:3], v[112:115], v[16:19]\n"
      "v_fma_f32 v196, |v90|, v74, v196\n"
      "v_fma_f32 v196, |v91|, v75, v196\n"
      "v_mfma_f32_16x16x32_f16 v[80:83], a[4:7], v[116:119], v[80:83]\n"
      "v_pk_mul_f16 v97, v145, v161\n"
      "v_pk_mul_f16 v98, v146, v162\n"
      "v_mfma_f32_16x16x32_f16 v[80:83], a[8:11], v[120:123], v[80:83]\n"
      "v_pk_mul_f16 v99, v147, v163\n"
      "v_fma_f32 v196, |v92|, v76, v196\n"
      "v_mfma_f32_16x16x32_f16 v[80:83], a[12:15], v[124:127], v[80:83]\n"
      "v_fma_f32 v196, |v93|, v77, v196\n"
      "v_fma_f32 v196, |v94|, v78, v196\n"
      "v_mfma_f32_16x16x32_f16 v[84:87], a[28:31], v[124:127], v[20:23]\n"
      "s_waitcnt lgkmcnt(4)\n"
      "v_fma_f32 v196, |v95|, v79, v196\n"
      "v_fmac_f32 v189, v215, v228\n"
      "v_mfma_f32_16x16x32_f16 v[84:87], a[24:27], v[120:123], v[84:87]\n"
      "s_waitcnt lgkmcnt(3)\n"
      "v_add_f32 v194, v194, v195\n"
      "v_fmac_f32 v190, v215, v230\n"
      "v_mfma_f32_16x16x32_f16 v[84:87], a[20:23], v[116:119], v[84:87]\n"
      "v_mul_f32 v192, |v80|, v48\n"
      "v_fma_f32 v192, |v81|, v49, v192\n"
      "v_mfma_f32_16x16x32_f16 v[84:87], a[16:19], v[112:115], v[84:87]\n"
      "v_permlane16_swap_b32 v188, v194\n"
      "v_mfma_f32_16x16x32_f16 v[88:91], a[32:35], v[112:115], v[24:27]\n"
      "s_waitcnt lgkmcnt(2)\n"
      "v_pk_mul_f16 v100, v148, v164\n"
      "v_fmac_f32 v191, v215, v232\n"
      "v_mfma_f32_16x16x32_f16 v[88:91], a[36:39], v[116:119], v[88:91]\n"
      "v_fma_f32 v192, |v82|, v50, v192\n"
      "v_fma_f32 v192, |v83|, v51, v192\n"
      "v_mfma_f32_16x16x32_f16 v[88:91], a[40:43], v[120:123], v[88:91]\n"
      "v_pk_mul_f16 v101, v149, v165\n"
      "v_max3_f32 v214, v189, v190, v191\n"
      "v_mfma_f32_16x16x32_f16 v[88:91], a[44:47], v[124:127], v[88:91]\n"
      "v_pk_mul_f16 v102, v150, v166\n"
      "v_add_f32 v188, v188, v194\n"
      "v_mfma_f32_16x16x32_f16 v[92:95], a[60:63], v[124:127], v[28:31]\n"
      "v_min3_f32 v226, v189, v190, v191\n"
      "v_pk_mul_f16 v103, v151, v167\n"
      "v_mfma_f32_16x16x32_f16 v[92:95], a[56:59], v[120:123], v[92:95]\n"
      "v_max_f32_dpp v222, v214, v214 row_ror:4 row_mask:0xf bank_mask:0xf\n"
      "v_fma_f32 v192, |v84|, v52, v192\n"
      "v_mfma_f32_16x16x32_f16 v[92:95], a[52:55], v[116:119], v[92:95]\n"
      "v_fma_f32 v192, |v85|, v53, v192\n"
      "v_fma_f32 v192, |v86|, v54, v192\n"
      "v_mfma_f32_16x16x32_f16 v[92:95], a[48:51], v[112:115], v[92:95]\n"
      "v_fma_f32 v192, |v87|, v55, v192\n"
      "v_max_f32_dpp v222, v214, v222 row_ror:8 row_mask:0xf bank_mask:0xf\n"
      "v_mfma_f32_16x16x32_f16 v[80:83], a[64:67], v[112:115], v[32:35]\n"
      "v_fma_f32 v192, |v88|, v56, v192\n"
      "v_fma_f32 v192, |v89|, v57, v192\n"
      "v_mfma_f32_16x16x32_f16 v[80:83], a[68:71], v[116:119], v[80:83]\n"
      "v_max_f32_dpp v222, v214, v222 row_ror:12 row_mask:0xf bank_mask:0xf\n"
      "v_pk_mul_f16 v104, v152, v168\n"
      "v_mfma_f32_16x16x32_f16 v[80:83], a[72:75], v[120:123], v[80:83]\n"
      "v_fma_f32 v192, |v90|, v58, v192\n"
      "v_fma_f32 v192, |v91|, v59, v192\n"
      "v_mfma_f32_16x16x32_f16 v[80:83], a[76:79], v[124:127], v[80:83]\n"
      "v_pk_mul_f16 v105, v153, v169\n"
      "v_max_f32_dpp v214, v222, v222 row_ror:1 row_mask:0xf bank_mask:0xf\n"
      "v_mfma_f32_16x16x32_f16 v[84:87], a[92:95], v[124:127], v[36:39]\n"
      "v_pk_mul_f16 v106, v154, v170\n"
      "v_pk_mul_f16 v107, v155, v171\n"
      "v_mfma_f32_16x16x32_f16 v[84:87], a[88:91], v[120:123], v[84:87]\n"
      "v_max_f32_dpp v214, v222, v214 row_ror:2 row_mask:0xf bank_mask:0xf\n"
      "v_fma_f32 v192, |v92|, v60, v192\n"
      "v_mfma_f32_16x16x32_f16 v[84:87], a[84:87], v[116:119], v[84:87]\n"
      "v_fma_f32 v192, |v93|, v61, v192\n"
      "v_fma_f32 v192, |v94|, v62, v192\n"
      "v_mfma_f32_16x16x32_f16 v[84:87], a[80:83], v[112:115], v[84:87]\n"
      "v_fma_f32 v192, |v95|, v63, v192\n"
      "v_max_f32_dpp v214, v222, v214 row_ror:3 row_mask:0xf bank_mask:0xf\n"
      "v_mfma_f32_16x16x32_f16 v[88:91], a[96:99], v[112:115], v[40:43]\n"
      "v_fma_f32 v192, |v80|, v64, v192\n"
      "v_fma_f32 v192, |v81|, v65, v192\n"
      "v_mfma_f32_16x16x32_f16 v[88:91], a[100:103], v[116:119], v[88:91]\n"
      "v_max_f32_dpp v214, v214, v214 row_bcast:15 row_mask:0xa bank_mask:0xf\n"
      "v_pk_mul_f16 v108, v156, v172\n"
      "v_mfma_f32_16x16x32_f16 v[88:91], a[104:107], v[120:123], v[88:91]\n"
      "v_fma_f32 v192, |v82|, v66, v192\n"
      "v_fma_f32 v192, |v83|, v67, v192\n"
      "v_mfma_f32_16x16x32_f16 v[88:91], a[108:111], v[124:127], v[88:91]\n"
      "v_pk_mul_f16 v109, v157, v173\n"
      "v_max_f32_dpp v214, v214, v214 row_bcast:31 row_mask:0xc bank_mask:0xf\n"
      "v_mfma_f32_16x16x32_f16 v[92:95], a[124:127], v[124:127], v[44:47]\n"
      "v_pk_mul_f16 v110, v158, v174\n"
      "v_readlane_b32 s62, v214, 63\n"
      "v_mfma_f32_16x16x32_f16 v[92:95], a[120:123], v[120:123], v[92:95]\n"
      "v_pk_mul_f16 v111, v159, v175\n"
      "v_subrev_f32 v189, s62, v189\n"
      "v_mfma_f32_16x16x32_f16 v[92:95], a[116:119], v[116:119], v[92:95]\n"
      "v_fma_f32 v192, |v84|, v68, v192\n"
      "v_fma_f32 v192, |v85|, v69, v192\n"
      "v_mfma_f32_16x16x32_f16 v[92:95], a[112:115], v[112:115], v[92:95]\n"
      "v_subrev_f32 v190, s62, v190\n"
      "v_fma_f32 v192, |v86|, v70, v192\n"
      "v_mfma_f32_16x16x32_f16 v[80:83], a[0:3], v[96:99], v[16:19]\n"
      "v_fma_f32 v192, |v87|, v71, v192\n"
      "v_subrev_f32 v191, s62, v191\n"
      "v_mfma_f32_16x16x32_f16 v[80:83], a[4:7], v[100:103], v[80:83]\n"
      "v_exp_f32 v189, v189\n"
      "v_mfma_f32_16x16x32_f16 v[80:83], a[8:11], v[104:107], v[80:83]\n"
      "v_exp_f32 v190, v190\n"
      "v_mfma_f32_16x16x32_f16 v[80:83], a[12:15], v[108:111], v[80:83]\n"
      "v_exp_f32 v191, v191\n"
      "v_mfma_f32_16x16x32_f16 v[84:87], a[28:31], v[108:111], v[20:23]\n"
      "v_fma_f32 v192, |v88|, v72, v192\n"
      "v_fma_f32 v192, |v89|, v73, v192\n"
      "v_mfma_f32_16x16x32_f16 v[84:87], a[24:27], v[104:107], v[84:87]\n"
      "v_pk_mul_f16 v112, v240, v160\n"
      "v_fma_f32 v192, |v90|, v74, v192\n"
      "v_mfma_f32_16x16x32_f16 v[84:87], a[20:23], v[100:103], v[84:87]\n"
      "v_fma_f32 v192, |v91|, v75, v192\n"
      "v_pk_mul_f16 v113, v241, v161\n"
      "v_mfma_f32_16x16x32_f16 v[84:87], a[16:19], v[96:99], v[84:87]\n"
      "v_mul_f32 v221, v189, v229\n"
      "v_pk_mul_f16 v114, v242, v162\n"
      "v_mfma_f32_16x16x32_f16 v[88:91], a[32:35], v[96:99], v[24:27]\n"
      "v_fma_f32 v220, v226, 0, v189\n"
      "v_pk_mul_f16 v115, v243, v163\n"
      "v_mfma_f32_16x16x32_f16 v[88:91], a[36:39], v[100:103], v[88:91]\n"
      "v_fmac_f32 v221, v190, v231\n"
      "v_fma_f32 v192, |v92|, v76, v192\n"
      "v_mfma_f32_16x16x32_f16 v[88:91], a[40:43], v[104:107], v[88:91]\n"
      "v_fma_f32 v192, |v93|, v77, v192\n"
      "v_add_f32 v220, v220, v190\n"
      "v_mfma_f32_16x16x32_f16 v[88:91], a[44:47], v[108:111], v[88:91]\n"
      "v_fma_f32 v192, |v94|, v78, v192\n"
      "v_fma_f32 v192, |v95|, v79, v192\n"
      "v_fmac_f32 v221, v191, v233\n"
      "v_mfma_f32_16x16x32_f16 v[92:95], a[60:63], v[108:111], v[28:31]\n"
      "v_add_f32 v220, v220, v191\n"
      "v_mul_f32 v193, |v80|, v48\n"
      "v_mfma_f32_16x16x32_f16 v[92:95], a[56:59], v[104:107], v[92:95]\n"
      "v_fma_f32 v193, |v81|, v49, v193\n"
      "v_pk_mul_f16 v116, v244, v164\n"
      "v_mfma_f32_16x16x32_f16 v[92:95], a[52:55], v[100:103], v[92:95]\n"
      "v_fma_f32 v193, |v82|, v50, v193\n"
      "v_fma_f32 v193, |v83|, v51, v193\n"
      "v_mfma_f32_16x16x32_f16 v[92:95], a[48:51], v[96:99], v[92:95]\n"
      "v_pk_mul_f16 v117, v245, v165\n"
      "v_pk_mul_f16 v118, v246, v166\n"
      "v_mfma_f32_16x16x32_f16 v[80:83], a[64:67], v[96:99], v[32:35]\n"
      "v_pk_mul_f16 v119, v247, v167\n"
      "v_fma_f32 v193, |v84|, v52, v193\n"
      "v_mfma_f32_16x16x32_f16 v[80:83], a[68:71], v[100:103], v[80:83]\n"
      "v_fma_f32 v193, |v85|, v53, v193\n"
      "v_fma_f32 v193, |v86|, v54, v193\n"
      "v_mfma_f32_16x16x32_f16 v[80:83], a[72:75], v[104:107], v[80:83]\n"
      "v_fma_f32 v193, |v87|, v55, v193\n"
      "v_fma_f32 v193, |v88|, v56, v193\n"
      "v_mfma_f32_16x16x32_f16 v[80:83], a[76:79], v[108:111], v[80:83]\n"
      "v_fma_f32 v193, |v89|, v57, v193\n"
      "v_pk_mul_f16 v120, v248, v168\n"
      "v_mfma_f32_16x16x32_f16 v[84:87], a[92:95], v[108:111], v[36:39]\n"
      "v_fma_f32 v193, |v90|, v58, v193\n"
      "v_fma_f32 v193, |v91|, v59, v193\n"
      "v_mfma_f32_16x16x32_f16 v[84:87], a[88:91], v[104:107], v[84:87]\n"
      "v_pk_mul_f16 v121, v249, v169\n"
      "v_pk_mul_f16 v122, v250, v170\n"
      "v_mfma_f32_16x16x32_f16 v[84:87], a[84:87], v[100:103], v[84:87]\n"
      "v_pk_mul_f16 v123, v251, v171\n"
      "v_fma_f32 v193, |v92|, v60, v193\n"
      "v_mfma_f32_16x16x32_f16 v[84:87], a[80:83], v[96:99], v[84:87]\n"
      "v_fma_f32 v193, |v93|, v61, v193\n"
      "v_fma_f32 v193, |v94|, v62, v193\n"
      "v_mfma_f32_16x16x32_f16 v[88:91], a[96:99], v[96:99], v[40:43]\n"
      "v_fma_f32 v193, |v95|, v63, v193\n"
      "v_fma_f32 v193, |v80|, v64, v193\n"
      "v_mfma_f32_16x16x32_f16 v[88:91], a[100:103], v[100:103], v[88:91]\n"
      "v_permlane32_swap_b32 v196, v192\n"
      "v_mfma_f32_16x16x32_f16 v[88:91], a[104:107], v[104:107], v[88:91]\n"
      "v_fma_f32 v193, |v81|, v65, v193\n"
      "v_pk_mul_f16 v124, v252, v172\n"
      "v_mfma_f32_16x16x32_f16 v[88:91], a[108:111], v[108:111], v[88:91]\n"
      "v_fma_f32 v193, |v82|, v66, v193\n"
      "v_fma_f32 v193, |v83|, v67, v193\n"
      "v_mfma_f32_16x16x32_f16 v[92:95], a[124:127], v[108:111], v[44:47]\n"
      "v_pk_mul_f16 v125, v253, v173\n"
      "v_pk_mul_f16 v126, v254, v174\n"
      "v_mfma_f32_16x16x32_f16 v[92:95], a[120:123], v[104:107], v[92:95]\n"
      "v_pk_mul_f16 v127, v255, v175\n"
      "v_fma_f32 v193, |v84|, v68, v193\n"
      "v_mfma_f32_16x16x32_f16 v[92:95], a[116:119], v[100:103], v[92:95]\n"
      "v_fma_f32 v193, |v85|, v69, v193\n"
      "v_fma_f32 v193, |v86|, v70, v193\n"
      "v_mfma_f32_16x16x32_f16 v[92:95], a[112:115], v[96:99], v[92:95]\n"
      "v_fma_f32 v193, |v87|, v71, v193\n"
      "v_fma_f32 v193, |v88|, v72, v193\n"
      "v_mfma_f32_16x16x32_f16 v[80:83], a[0:3], v[112:115], v[16:19]\n"
      "v_fma_f32 v193, |v89|, v73, v193\n"
      "v_fma_f32 v193, |v90|, v74, v193\n"
      "v_mfma_f32_16x16x32_f16 v[80:83], a[4:7], v[116:119], v[80:83]\n"
      "ds_read_b128 v[184:187], v12 offset:1440\n"
      "v_fma_f32 v193, |v91|, v75, v193\n"
      "v_mfma_f32_16x16x32_f16 v[80:83], a[8:11], v[120:123], v[80:83]\n"
      "v_fma_f32 v193, |v92|, v76, v193\n"
      "v_fma_f32 v193, |v93|, v77, v193\n"
      "v_mfma_f32_16x16x32_f16 v[80:83], a[12:15], v[124:127], v[80:83]\n"
      "v_fma_f32 v193, |v94|, v78, v193\n"
      "v_fma_f32 v193, |v95|, v79, v193\n"
      "v_mfma_f32_16x16x32_f16 v[84:87], a[28:31], v[124:127], v[20:23]\n"
      "s_waitcnt lgkmcnt(1)\n"
      "ds_read_b128 a[200:203], v219 offset:16512\n"
      "v_fma_f32 v236, v236, v215, -s62\n"
      "v_mfma_f32_16x16x32_f16 v[84:87], a[24:27], v[120:123], v[84:87]\n"
      "ds_read_b128 a[204:207], v219 offset:16576\n"
      "v_add_f32 v196, v196, v192\n"
      "v_mfma_f32_16x16x32_f16 v[84:87], a[20:23], v[116:119], v[84:87]\n"
      "v_mul_f32 v197, |v80|, v48\n"
      "v_fma_f32 v197, |v81|, v49, v197\n"
      "v_mfma_f32_16x16x32_f16 v[84:87], a[16:19], v[112:115], v[84:87]\n"
      "v_fma_f32 v197, |v82|, v50, v197\n"
      "v_fma_f32 v197, |v83|, v51, v197\n"
      "v_mfma_f32_16x16x32_f16 v[88:91], a[32:35], v[112:115], v[24:27]\n"
      "ds_read_b128 a[208:211], v219 offset:16640\n"
      "v_fmac_f32 v188, v215, v234\n"
      "v_mfma_f32_16x16x32_f16 v[88:91], a[36:39], v[116:119], v[88:91]\n"
      "s_waitcnt lgkmcnt(3)\n"
      "v_pk_add_f32 v[184:185], v[184:185], v[186:187]\n"
      "v_add_f32 v199, v184, v185\n"
      "v_mfma_f32_16x16x32_f16 v[88:91], a[40:43], v[120:123], v[88:91]\n"
      "v_fma_f32 v197, |v84|, v52, v197\n"
      "v_fma_f32 v197, |v85|, v53, v197\n"
      "v_mfma_f32_16x16x32_f16 v[88:91], a[44:47], v[124:127], v[88:91]\n"
      "v_fma_f32 v197, |v86|, v54, v197\n"
      "v_fma_f32 v197, |v87|, v55, v197\n"
      "v_mfma_f32_16x16x32_f16 v[92:95], a[60:63], v[124:127], v[28:31]\n"
      "ds_read_b128 a[212:215], v219 offset:16704\n"
      "v_cmp_u_f32 vcc, v199, v199\n"
      "v_mfma_f32_16x16x32_f16 v[92:95], a[56:59], v[120:123], v[92:95]\n"
      "v_subrev_f32 v188, s62, v188\n"
      "v_add_f32_dpp v196, v236, v196 quad_perm:[0,1,2,3] row_mask:0x5 bank_mask:0xf\n"
      "v_mfma_f32_16x16x32_f16 v[92:95], a[52:55], v[116:119], v[92:95]\n"
      "v_fma_f32 v197, |v88|, v56, v197\n"
      "v_fma_f32 v197, |v89|, v57, v197\n"
      "v_mfma_f32_16x16x32_f16 v[92:95], a[48:51], v[112:115], v[92:95]\n"
      "v_fma_f32 v197, |v90|, v58, v197\n"
      "v_fma_f32 v197, |v91|, v59, v197\n"
      "v_mfma_f32_16x16x32_f16 v[80:83], a[64:67], v[112:115], v[32:35]\n"
      "s_cbranch_vccnz 17f\n"
      "18:\n"
      "v_mfma_f32_16x16x32_f16 v[80:83], a[68:71], v[116:119], v[80:83]\n"
      "v_permlane32_swap_b32 v199, v184\n"
      "v_mfma_f32_16x16x32_f16 v[80:83], a[72:75], v[120:123], v[80:83]\n"
      "s_nop 0\n"
      "v_fma_f32 v197, |v92|, v60, v197\n"
      "v_fma_f32 v197, |v93|, v61, v197\n"
      "v_mfma_f32_16x16x32_f16 v[80:83], a[76:79], v[124:127], v[80:83]\n"
      "v_fma_f32 v197, |v94|, v62, v197\n"
      "v_fma_f32 v197, |v95|, v63, v197\n"
      "v_mfma_f32_16x16x32_f16 v[84:87], a[92:95], v[124:127], v[36:39]\n"
      "v_add_f32 v199, v199, v184\n"
      "v_add_f32_dpp v193, v236, v193 quad_perm:[0,1,2,3] row_mask:0x2 bank_mask:0xf\n"
      "v_mfma_f32_16x16x32_f16 v[84:87], a[88:91], v[120:123], v[84:87]\n"
      "v_exp_f32 v222, v188\n"
      "v_mfma_f32_16x16x32_f16 v[84:87], a[84:87], v[116:119], v[84:87]\n"
      "v_fma_f32 v197, |v80|, v64, v197\n"
      "v_fma_f32 v197, |v81|, v65, v197\n"
      "v_mfma_f32_16x16x32_f16 v[84:87], a[80:83], v[112:115], v[84:87]\n"
      "v_fma_f32 v197, |v82|, v66, v197\n"
      "v_fma_f32 v197, |v83|, v67, v197\n"
      "v_mfma_f32_16x16x32_f16 v[88:91], a[96:99], v[112:115], v[40:43]\n"
      "v_permlane16_swap_b32 v199, v185\n"
      "v_mfma_f32_16x16x32_f16 v[88:91], a[100:103], v[116:119], v[88:91]\n"
      "v_add_f32 v199, v199, v185\n"
      "v_add_f32_dpp v197, v236, v197 quad_perm:[0,1,2,3] row_mask:0x8 bank_mask:0xf\n"
      "v_mfma_f32_16x16x32_f16 v[88:91], a[104:107], v[120:123], v[88:91]\n"
      "s_mov_b64 vcc, 31\n"
      "v_fma_f32 v197, |v84|, v68, v197\n"
      "v_fma_f32 v197, |v85|, v69, v197\n"
      "v_mfma_f32_16x16x32_f16 v[88:91], a[108:111], v[124:127], v[88:91]\n"
      "v_fma_f32 v197, |v86|, v70, v197\n"
      "v_fma_f32 v197, |v87|, v71, v197\n"
      "v_mfma_f32_16x16x32_f16 v[92:95], a[124:127], v[124:127], v[44:47]\n"
      "v_subrev_f32 v199, s62, v199\n"
      "v_cndmask_b32 v199, v7, v199, vcc\n"
      "v_mfma_f32_16x16x32_f16 v[92:95], a[120:123], v[120:123], v[92:95]\n"
      "v_fma_f32 v214, v188, 0, v220\n"
      "v_fma_f32 v226, v222, v235, v221\n"
      "v_mfma_f32_16x16x32_f16 v[92:95], a[116:119], v[116:119], v[92:95]\n"
      "v_fma_f32 v197, |v88|, v72, v197\n"
      "v_fma_f32 v197, |v89|, v73, v197\n"
      "v_mfma_f32_16x16x32_f16 v[92:95], a[112:115], v[112:115], v[92:95]\n"
      "s_waitcnt lgkmcnt(3)\n"
      "v_fma_f32 v197, |v90|, v74, v197\n"
      "v_fma_f32 v197, |v91|, v75, v197\n"
      "v_mfma_f32_16x16x32_f16 v[180:183], a[200:203], a[216:219], 0\n"
      "s_waitcnt lgkmcnt(2)\n"
      "v_add_f32 v214, v214, v222\n"
      "v_fmac_f32 v214, 0, v199\n"
      "v_mfma_f32_16x16x32_f16 v[180:183], a[204:207], a[220:223], v[180:183]\n"
      "s_waitcnt lgkmcnt(1)\n"
      "v_fma_f32 v197, |v92|, v76, v197\n"
      "v_fma_f32 v197, |v93|, v77, v197\n"
      "v_mfma_f32_16x16x32_f16 v[180:183], a[208:211], a[224:227], v[180:183]\n"
      "s_waitcnt lgkmcnt(0)\n"
      "v_fma_f32 v197, |v94|, v78, v197\n"
      "v_fma_f32 v197, |v95|, v79, v197\n"
      "v_mfma_f32_16x16x32_f16 v[180:183], a[212:215], a[228:231], v[180:183]\n"
      "s_nop 0\n"
      "v_permlane32_swap_b32 v193, v197\n"
      "v_add_f32 v193, v193, v197\n"
      "s_nop 1\n"
      "v_permlane16_swap_b32 v196, v193\n"
      "v_add_f32 v196, v196, v193\n"
      "v_exp_f32 v222, v196\n"
      "v_fmac_f32_dpp v199, v180, v215 quad_perm:[0,1,2,3] row_mask:0x1 bank_mask:0x3\n"
      "v_fma_f32 v214, v196, 0, v214\n"
      "v_exp_f32 v223, v199\n"
      "v_fmac_f32 v226, v222, v237\n"
      "v_add_f32 v214, v214, v222\n"
      "v_fmac_f32_dpp v226, v181, v223 quad_perm:[0,1,2,3] row_mask:0x1 bank_mask:0x3\n"
      "v_add_f32 v214, v214, v223\n"
      "s_nop 1\n"
      "v_add_f32_dpp v214, v214, v214 row_ror:8 row_mask:0xf bank_mask:0xf\n"
      "v_add_f32_dpp v226, v226, v226 row_ror:8 row_mask:0xf bank_mask:0xf\n"
      "s_nop 0\n"
      "v_add_f32_dpp v214, v214, v214 row_ror:4 row_mask:0xf bank_mask:0xf\n"
      "v_add_f32_dpp v226, v226, v226 row_ror:4 row_mask:0xf bank_mask:0xf\n"
      "s_nop 0\n"
      "v_add_f32_dpp v214, v214, v214 row_ror:2 row_mask:0xf bank_mask:0xf\n"
      "v_add_f32_dpp v226, v226, v226 row_ror:2 row_mask:0xf bank_mask:0xf\n"
      "s_nop 0\n"
      "v_add_f32_dpp v214, v214, v214 row_ror:1 row_mask:0xf bank_mask:0xf\n"
      "v_add_f32_dpp v226, v226, v226 row_ror:1 row_mask:0xf bank_mask:0xf\n"
      "s_nop 0\n"
      "v_add_f32_dpp v214, v214, v214 row_bcast:15 row_mask:0xa bank_mask:0xf\n"
      "v_add_f32_dpp v226, v226, v226 row_bcast:15 row_mask:0xa bank_mask:0xf\n"
      "s_nop 0\n"
      "v_add_f32_dpp v214, v214, v214 row_bcast:31 row_mask:0xc bank_mask:0xf\n"
      "v_add_f32_dpp v226, v226, v226 row_bcast:31 row_mask:0xc bank_mask:0xf\n"
      "v_rcp_f32 v222, v214\n"
      "v_ldexp_f32 v226, v226, s39\n"
      "s_lshl_b64 exec, 1, 63\n"
      "v_mul_f32 v226, v226, v222\n"
      "v_cvt_pk_bf16_f32 v222, v226, v226\n"
      "global_store_short v3, v222, s[24:25]\n"
      "v_cmp_class_f32 vcc, v226, s45\n"
      "v_cmp_lt_f32 s[28:29], s99, v214\n"
      "s_nop 0\n"
      "s_or_b32 vcc_hi, vcc_hi, s29\n"
      "s_cmp_lt_i32 vcc_hi, 0\n"
      "s_cbranch_scc1 19f\n"
      "20:\n"
      "s_endpgm\n"
      "s_mov_b64 exec, -1\n"
      "s_branch 9f\n"
      "33:\n"
      "s_cmp_lg_u32 s55, 0\n"
      "s_cbranch_scc1 34f\n"
      "global_load_dwordx4 v[244:247], v11, s[10:11] nt\n"
      "global_load_dwordx4 v[248:251], v12, s[10:11] nt\n"
      "global_load_dwordx4 v[252:255], v6, s[10:11] nt\n"
      "s_branch 6f\n"
      "34:\n"
      "v_accvgpr_read_b32 v244, a216\n"
      "v_accvgpr_read_b32 v245, a217\n"
      "v_accvgpr_read_b32 v246, a218\n"
      "v_accvgpr_read_b32 v247, a219\n"
      "v_accvgpr_read_b32 v248, a220\n"
      "v_accvgpr_read_b32 v249, a221\n"
      "v_accvgpr_read_b32 v250, a222\n"
      "v_accvgpr_read_b32 v251, a223\n"
      "v_accvgpr_read_b32 v252, a224\n"
      "v_accvgpr_read_b32 v253, a225\n"
      "v_accvgpr_read_b32 v254, a226\n"
      "v_accvgpr_read_b32 v255, a227\n"
      "s_branch 32b\n"
      "5:\n"
      "s_waitcnt lgkmcnt(0)\n"
      "ds_read_b32 v224, v227 offset:37888\n"
      "s_waitcnt lgkmcnt(0)\n"
      "v_and_b32 v224, 15, v224\n"
      "v_cmp_eq_u32 vcc, 0, v224\n"
      "s_nop 4\n"
      "s_cmp_lg_u32 vcc_lo, 0\n"
      "s_cbranch_scc1 5b\n"
      "v_cmp_gt_u32 vcc, 4, v224\n"
      "s_nop 4\n"
      "s_cmp_lg_u32 vcc_lo, 0\n"
      "s_cbranch_scc1 8f\n"
      "v_and_b32 v225, 2, v224\n"
      "v_cmp_ne_u32 vcc, 0, v225\n"
      "s_nop 4\n"
      "s_cmp_eq_u32 vcc_lo, 0\n"
      "s_cbranch_scc1 53f\n"
      "s_waitcnt vmcnt(0)\n"
      "v_add_u32 v216, 2592, v8\n"
      "v_mov_b32 v217, v216\n"
      "53:\n"
      "v_cmp_le_u32 vcc, 8, v224\n"
      "s_nop 4\n"
      "s_or_b32 s44, vcc_lo, s58\n"
      "s_cbranch_scc1 52f\n"
      "v_and_b32 v224, 63, %3\n"
      "v_lshlrev_b32 v224, 9, v224\n"
      "global_load_dwordx4 v[176:179], v224, s[16:17]\n"
      "global_load_dwordx4 v[180:183], v224, s[16:17] offset:336\n"
      "s_waitcnt vmcnt(0)\n"
      "v_max3_f32 v224, |v176|, |v177|, |v178|\n"
      "v_max3_f32 v224, |v179|, |v180|, v224\n"
      "v_max3_f32 v224, |v181|, |v182|, v224\n"
      "v_max_f32 v224, |v183|, v224\n"
      "s_nop 1\n"
      "v_max_f32_dpp v224, v224, v224 row_ror:8 row_mask:0xf bank_mask:0xf\n"
      "s_nop 1\n"
      "v_max_f32_dpp v224, v224, v224 row_ror:4 row_mask:0xf bank_mask:0xf\n"
      "s_nop 1\n"
      "v_max_f32_dpp v224, v224, v224 row_ror:2 row_mask:0xf bank_mask:0xf\n"
      "s_nop 1\n"
      "v_max_f32_dpp v224, v224, v224 row_ror:1 row_mask:0xf bank_mask:0xf\n"
      "s_nop 0\n"
      "v_readlane_b32 s44, v224, 0\n"
      "v_readlane_b32 s45, v224, 16\n"
      "v_readlane_b32 s46, v224, 32\n"
      "v_readlane_b32 s47, v224, 48\n"
      "s_max_u32 s44, s44, s45\n"
      "s_max_u32 s46, s46, s47\n"
      "s_max_u32 s44, s44, s46\n"
      "s_cmp_ge_u32 s44, 0x43000000\n"
      "s_cbranch_scc1 52f\n"
      "s_lshr_b32 s44, s44, 16\n"
      "s_cmp_eq_u32 s44, 0\n"
      "s_cbranch_scc1 52f\n"
      "s_lshr_b32 s44, s44, 7\n"
      "s_sub_i32 s58, 134, s44\n"
      "s_cmp_gt_u32 s58, 50\n"
      "s_cbranch_scc1 8f\n"
      "s_mov_b32 s41, 0\n"
      "s_mov_b32 s35, 0\n"
      "s_waitcnt vmcnt(0) lgkmcnt(0)\n"
      "s_barrier\n"
      "s_branch 0b\n"
      "52:\n"
      "ds_read_b128 v[176:179], v5 offset:37376\n"
      "ds_read_b128 v[180:183], v5 offset:37392\n"
      "s_waitcnt lgkmcnt(0)\n"
      "v_max_f32 v225, |v176|, |v181|\n"
      "s_mov_b32 s44, 0x3d800000\n"
      "v_cmp_le_f32 vcc, 0x3f400000, v225\n"
      "v_cmp_le_f32 s[28:29], s44, v225\n"
      "s_nop 4\n"
      "s_cmp_eq_u32 vcc_lo, 0\n"
      "s_cbranch_scc1 54f\n"
      "s_cmp_eq_u32 s60, 0\n"
      "s_cselect_b32 s99, -1.0, s99\n"
      "s_mov_b32 s28, 0\n"
      "54:\n"
      "s_cmp_lg_u32 s28, 0\n"
      "s_cbranch_scc1 4b\n"
      "v_max3_f32 v225, |v176|, |v177|, |v178|\n"
      "v_max3_f32 v225, |v179|, |v180|, v225\n"
      "v_max3_f32 v225, |v181|, |v182|, v225\n"
      "v_max_f32 v225, |v183|, v225\n"
      "s_nop 1\n"
      "v_max_f32_dpp v225, v225, v225 row_ror:8 row_mask:0xf bank_mask:0xf\n"
      "s_nop 1\n"
      "v_max_f32_dpp v225, v225, v225 row_ror:4 row_mask:0xf bank_mask:0xf\n"
      "s_nop 1\n"
      "v_max_f32_dpp v225, v225, v225 row_ror:2 row_mask:0xf bank_mask:0xf\n"
      "s_nop 1\n"
      "v_max_f32_dpp v225, v225, v225 row_ror:1 row_mask:0xf bank_mask:0xf\n"
      "v_frexp_exp_i32_f32 v224, v225\n"
      "v_sub_u32 v224, 0, v224\n"
      "v_ldexp_f32 v176, v176, v224\n"
      "v_ldexp_f32 v177, v177, v224\n"
      "v_ldexp_f32 v178, v178, v224\n"
      "v_ldexp_f32 v179, v179, v224\n"
      "v_ldexp_f32 v180, v180, v224\n"
      "v_ldexp_f32 v181, v181, v224\n"
      "v_ldexp_f32 v182, v182, v224\n"
      "v_ldexp_f32 v183, v183, v224\n"
      "v_readfirstlane_b32 s56, v224\n"
      "s_nop 1\n"
      "s_branch 4b\n"
      "36:\n"
      "s_sub_u32 s33, s26, 1\n"
      "s_sub_u32 s44, s34, s33\n"
      "s_mul_i32 s44, s44, 104\n"
      "v_subrev_u32 v10, s44, v10\n"
      "s_branch 37b\n"
      "22:\n"
      "v_max3_f32 v224, |v176|, |v177|, |v178|\n"
      "v_max3_f32 v224, |v179|, |v180|, v224\n"
      "v_max3_f32 v224, |v181|, |v182|, v224\n"
      "v_max_f32 v224, |v183|, v224\n"
      "s_nop 1\n"
      "v_max_f32_dpp v224, v224, v224 row_ror:8 row_mask:0xf bank_mask:0xf\n"
      "s_nop 1\n"
      "v_max_f32_dpp v224, v224, v224 row_ror:4 row_mask:0xf bank_mask:0xf\n"
      "s_nop 1\n"
      "v_max_f32_dpp v224, v224, v224 row_ror:2 row_mask:0xf bank_mask:0xf\n"
      "s_nop 1\n"
      "v_max_f32_dpp v224, v224, v224 row_ror:1 row_mask:0xf bank_mask:0xf\n"
      "v_frexp_exp_i32_f32 v90, v224\n"
      "v_sub_u32 v90, 0, v90\n"
      "v_ldexp_f32 v176, v176, v90\n"
      "v_ldexp_f32 v177, v177, v90\n"
      "v_ldexp_f32 v178, v178, v90\n"
      "v_ldexp_f32 v179, v179, v90\n"
      "v_ldexp_f32 v180, v180, v90\n"
      "v_ldexp_f32 v181, v181, v90\n"
      "v_ldexp_f32 v182, v182, v90\n"
      "v_ldexp_f32 v183, v183, v90\n"
      "v_readlane_b32 s59, v90, 0\n"
      "v_readlane_b32 s57, v90, 32\n"
      "s_nop 1\n"
      "s_cmp_lt_i32 s57, 0\n"
      "s_cselect_b32 s60, 1, s60\n"
      "s_branch 23b\n"
      "70:\n"
      "s_add_u32 s44, s58, 127\n"
      "s_lshl_b32 s44, s44, 23\n"
      "s_waitcnt vmcnt(0) lgkmcnt(0)\n"
      "s_nop 7\n"
      "v_lshlrev_b32 v176, 16, v96\n"
      "v_and_b32 v177, s43, v96\n"
      "v_mul_f32 v176, s44, v176\n"
      "v_mul_f32 v177, s44, v177\n"
      "v_cvt_pk_bf16_f32 v96, v176, v177\n"
      "v_lshlrev_b32 v176, 16, v97\n"
      "v_and_b32 v177, s43, v97\n"
      "v_mul_f32 v176, s44, v176\n"
      "v_mul_f32 v177, s44, v177\n"
      "v_cvt_pk_bf16_f32 v97, v176, v177\n"
      "v_lshlrev_b32 v176, 16, v98\n"
      "v_and_b32 v177, s43, v98\n"
      "v_mul_f32 v176, s44, v176\n"
      "v_mul_f32 v177, s44, v177\n"
      "v_cvt_pk_bf16_f32 v98, v176, v177\n"
      "v_lshlrev_b32 v176, 16, v99\n"
      "v_and_b32 v177, s43, v99\n"
      "v_mul_f32 v176, s44, v176\n"
      "v_mul_f32 v177, s44, v177\n"
      "v_cvt_pk_bf16_f32 v99, v176, v177\n"
      "v_lshlrev_b32 v176, 16, v100\n"
      "v_and_b32 v177, s43, v100\n"
      "v_mul_f32 v176, s44, v176\n"
      "v_mul_f32 v177, s44, v177\n"
      "v_cvt_pk_bf16_f32 v100, v176, v177\n"
      "v_lshlrev_b32 v176, 16, v101\n"
      "v_and_b32 v177, s43, v101\n"
      "v_mul_f32 v176, s44, v176\n"
      "v_mul_f32 v177, s44, v177\n"
      "v_cvt_pk_bf16_f32 v101, v176, v177\n"
      "v_lshlrev_b32 v176, 16, v102\n"
      "v_and_b32 v177, s43, v102\n"
      "v_mul_f32 v176, s44, v176\n"
      "v_mul_f32 v177, s44, v177\n"
      "v_cvt_pk_bf16_f32 v102, v176, v177\n"
      "v_lshlrev_b32 v176, 16, v103\n"
      "v_and_b32 v177, s43, v103\n"
      "v_mul_f32 v176, s44, v176\n"
      "v_mul_f32 v177, s44, v177\n"
      "v_cvt_pk_bf16_f32 v103, v176, v177\n"
      "v_lshlrev_b32 v176, 16, v104\n"
      "v_and_b32 v177, s43, v104\n"
      "v_mul_f32 v176, s44, v176\n"
      "v_mul_f32 v177, s44, v177\n"
      "v_cvt_pk_bf16_f32 v104, v176, v177\n"
      "v_lshlrev_b32 v176, 16, v105\n"
      "v_and_b32 v177, s43, v105\n"
      "v_mul_f32 v176, s44, v176\n"
      "v_mul_f32 v177, s44, v177\n"
      "v_cvt_pk_bf16_f32 v105, v176, v177\n"
      "v_lshlrev_b32 v176, 16, v106\n"
      "v_and_b32 v177, s43, v106\n"
      "v_mul_f32 v176, s44, v176\n"
      "v_mul_f32 v177, s44, v177\n"
      "v_cvt_pk_bf16_f32 v106, v176, v177\n"
      "v_lshlrev_b32 v176, 16, v107\n"
      "v_and_b32 v177, s43, v107\n"
      "v_mul_f32 v176, s44, v176\n"
      "v_mul_f32 v177, s44, v177\n"
      "v_cvt_pk_bf16_f32 v107, v176, v177\n"
      "v_lshlrev_b32 v176, 16, v108\n"
      "v_and_b32 v177, s43, v108\n"
      "v_mul_f32 v176, s44, v176\n"
      "v_mul_f32 v177, s44, v177\n"
      "v_cvt_pk_bf16_f32 v108, v176, v177\n"
      "v_lshlrev_b32 v176, 16, v109\n"
      "v_and_b32 v177, s43, v109\n"
      "v_mul_f32 v176, s44, v176\n"
      "v_mul_f32 v177, s44, v177\n"
      "v_cvt_pk_bf16_f32 v109, v176, v177\n"
      "v_lshlrev_b32 v176, 16, v110\n"
      "v_and_b32 v177, s43, v110\n"
      "v_mul_f32 v176, s44, v176\n"
      "v_mul_f32 v177, s44, v177\n"
      "v_cvt_pk_bf16_f32 v110, v176, v177\n"
      "v_lshlrev_b32 v176, 16, v111\n"
      "v_and_b32 v177, s43, v111\n"
      "v_mul_f32 v176, s44, v176\n"
      "v_mul_f32 v177, s44, v177\n"
      "v_cvt_pk_bf16_f32 v111, v176, v177\n"
      "v_lshlrev_b32 v176, 16, v112\n"
      "v_and_b32 v177, s43, v112\n"
      "v_mul_f32 v176, s44, v176\n"
      "v_mul_f32 v177, s44, v177\n"
      "v_cvt_pk_bf16_f32 v112, v176, v177\n"
      "v_lshlrev_b32 v176, 16, v113\n"
      "v_and_b32 v177, s43, v113\n"
      "v_mul_f32 v176, s44, v176\n"
      "v_mul_f32 v177, s44, v177\n"
      "v_cvt_pk_bf16_f32 v113, v176, v177\n"
      "v_lshlrev_b32 v176, 16, v114\n"
      "v_and_b32 v177, s43, v114\n"
      "v_mul_f32 v176, s44, v176\n"
      "v_mul_f32 v177, s44, v177\n"
      "v_cvt_pk_bf16_f32 v114, v176, v177\n"
      "v_lshlrev_b32 v176, 16, v115\n"
      "v_and_b32 v177, s43, v115\n"
      "v_mul_f32 v176, s44, v176\n"
      "v_mul_f32 v177, s44, v177\n"
      "v_cvt_pk_bf16_f32 v115, v176, v177\n"
      "v_lshlrev_b32 v176, 16, v116\n"
      "v_and_b32 v177, s43, v116\n"
      "v_mul_f32 v176, s44, v176\n"
      "v_mul_f32 v177, s44, v177\n"
      "v_cvt_pk_bf16_f32 v116, v176, v177\n"
      "v_lshlrev_b32 v176, 16, v117\n"
      "v_and_b32 v177, s43, v117\n"
      "v_mul_f32 v176, s44, v176\n"
      "v_mul_f32 v177, s44, v177\n"
      "v_cvt_pk_bf16_f32 v117, v176, v177\n"
      "v_lshlrev_b32 v176, 16, v118\n"
      "v_and_b32 v177, s43, v118\n"
      "v_mul_f32 v176, s44, v176\n"
      "v_mul_f32 v177, s44, v177\n"
      "v_cvt_pk_bf16_f32 v118, v176, v177\n"
      "v_lshlrev_b32 v176, 16, v119\n"
      "v_and_b32 v177, s43, v119\n"
      "v_mul_f32 v176, s44, v176\n"
      "v_mul_f32 v177, s44, v177\n"
      "v_cvt_pk_bf16_f32 v119, v176, v177\n"
      "s_sub_u32 s44, 0, s58\n"
      "v_ldexp_f32 v16, v16, s58\n"
      "v_ldexp_f32 v48, v48, s44\n"
      "v_ldexp_f32 v17, v17, s58\n"
      "v_ldexp_f32 v49, v49, s44\n"
      "v_ldexp_f32 v18, v18, s58\n"
      "v_ldexp_f32 v50, v50, s44\n"
      "v_ldexp_f32 v19, v19, s58\n"
      "v_ldexp_f32 v51, v51, s44\n"
      "v_ldexp_f32 v20, v20, s58\n"
      "v_ldexp_f32 v52, v52, s44\n"
      "v_ldexp_f32 v21, v21, s58\n"
      "v_ldexp_f32 v53, v53, s44\n"
      "v_ldexp_f32 v22, v22, s58\n"
      "v_ldexp_f32 v54, v54, s44\n"
      "v_ldexp_f32 v23, v23, s58\n"
      "v_ldexp_f32 v55, v55, s44\n"
      "v_ldexp_f32 v24, v24, s58\n"
      "v_ldexp_f32 v56, v56, s44\n"
      "v_ldexp_f32 v25, v25, s58\n"
      "v_ldexp_f32 v57, v57, s44\n"
      "v_ldexp_f32 v26, v26, s58\n"
      "v_ldexp_f32 v58, v58, s44\n"
      "v_ldexp_f32 v27, v27, s58\n"
      "v_ldexp_f32 v59, v59, s44\n"
      "v_ldexp_f32 v28, v28, s58\n"
      "v_ldexp_f32 v60, v60, s44\n"
      "v_ldexp_f32 v29, v29, s58\n"
      "v_ldexp_f32 v61, v61, s44\n"
      "v_ldexp_f32 v30, v30, s58\n"
      "v_ldexp_f32 v62, v62, s44\n"
      "v_ldexp_f32 v31, v31, s58\n"
      "v_ldexp_f32 v63, v63, s44\n"
      "v_ldexp_f32 v32, v32, s58\n"
      "v_ldexp_f32 v64, v64, s44\n"
      "v_ldexp_f32 v33, v33, s58\n"
      "v_ldexp_f32 v65, v65, s44\n"
      "v_ldexp_f32 v34, v34, s58\n"
      "v_ldexp_f32 v66, v66, s44\n"
      "v_ldexp_f32 v35, v35, s58\n"
      "v_ldexp_f32 v67, v67, s44\n"
      "v_ldexp_f32 v36, v36, s58\n"
      "v_ldexp_f32 v68, v68, s44\n"
      "v_ldexp_f32 v37, v37, s58\n"
      "v_ldexp_f32 v69, v69, s44\n"
      "v_ldexp_f32 v38, v38, s58\n"
      "v_ldexp_f32 v70, v70, s44\n"
      "v_ldexp_f32 v39, v39, s58\n"
      "v_ldexp_f32 v71, v71, s44\n"
      "v_ldexp_f32 v40, v40, s58\n"
      "v_ldexp_f32 v72, v72, s44\n"
      "v_ldexp_f32 v41, v41, s58\n"
      "v_ldexp_f32 v73, v73, s44\n"
      "v_ldexp_f32 v42, v42, s58\n"
      "v_ldexp_f32 v74, v74, s44\n"
      "v_ldexp_f32 v43, v43, s58\n"
      "v_ldexp_f32 v75, v75, s44\n"
      "v_ldexp_f32 v44, v44, s58\n"
      "v_ldexp_f32 v76, v76, s44\n"
      "v_ldexp_f32 v45, v45, s58\n"
      "v_ldexp_f32 v77, v77, s44\n"
      "v_ldexp_f32 v46, v46, s58\n"
      "v_ldexp_f32 v78, v78, s44\n"
      "v_ldexp_f32 v47, v47, s58\n"
      "v_ldexp_f32 v79, v79, s44\n"
      "s_nop 1\n"
      "s_branch 71b\n"
      "74:\n"
      "s_cmp_eq_u32 s55, 0\n"
      "s_cbranch_scc1 76f\n"
      "ds_read_b128 v[136:139], v201 offset:128\n"
      "ds_read_b128 v[168:171], v202 offset:128\n"
      "ds_read_b128 v[140:143], v201 offset:192\n"
      "ds_read_b128 v[172:175], v202 offset:192\n"
      "s_waitcnt lgkmcnt(0)\n"
      "v_pk_mul_f16 v96, v128, v160\n"
      "v_pk_mul_f16 v97, v129, v161\n"
      "v_pk_mul_f16 v98, v130, v162\n"
      "v_pk_mul_f16 v99, v131, v163\n"
      "s_branch 31b\n"
      "76:\n"
      "s_add_u32 s44, s58, 127\n"
      "s_lshl_b32 s44, s44, 23\n"
      "s_waitcnt vmcnt(0) lgkmcnt(0)\n"
      "s_nop 7\n"
      "v_lshlrev_b32 v176, 16, v120\n"
      "v_and_b32 v177, s43, v120\n"
      "v_mul_f32 v176, s44, v176\n"
      "v_mul_f32 v177, s44, v177\n"
      "v_cvt_pk_bf16_f32 v120, v176, v177\n"
      "v_lshlrev_b32 v176, 16, v121\n"
      "v_and_b32 v177, s43, v121\n"
      "v_mul_f32 v176, s44, v176\n"
      "v_mul_f32 v177, s44, v177\n"
      "v_cvt_pk_bf16_f32 v121, v176, v177\n"
      "v_lshlrev_b32 v176, 16, v122\n"
      "v_and_b32 v177, s43, v122\n"
      "v_mul_f32 v176, s44, v176\n"
      "v_mul_f32 v177, s44, v177\n"
      "v_cvt_pk_bf16_f32 v122, v176, v177\n"
      "v_lshlrev_b32 v176, 16, v123\n"
      "v_and_b32 v177, s43, v123\n"
      "v_mul_f32 v176, s44, v176\n"
      "v_mul_f32 v177, s44, v177\n"
      "v_cvt_pk_bf16_f32 v123, v176, v177\n"
      "v_lshlrev_b32 v176, 16, v124\n"
      "v_and_b32 v177, s43, v124\n"
      "v_mul_f32 v176, s44, v176\n"
      "v_mul_f32 v177, s44, v177\n"
      "v_cvt_pk_bf16_f32 v124, v176, v177\n"
      "v_lshlrev_b32 v176, 16, v125\n"
      "v_and_b32 v177, s43, v125\n"
      "v_mul_f32 v176, s44, v176\n"
      "v_mul_f32 v177, s44, v177\n"
      "v_cvt_pk_bf16_f32 v125, v176, v177\n"
      "v_lshlrev_b32 v176, 16, v126\n"
      "v_and_b32 v177, s43, v126\n"
      "v_mul_f32 v176, s44, v176\n"
      "v_mul_f32 v177, s44, v177\n"
      "v_cvt_pk_bf16_f32 v126, v176, v177\n"
      "v_lshlrev_b32 v176, 16, v127\n"
      "v_and_b32 v177, s43, v127\n"
      "v_mul_f32 v176, s44, v176\n"
      "v_mul_f32 v177, s44, v177\n"
      "v_cvt_pk_bf16_f32 v127, v176, v177\n"
      "s_nop 1\n"
      "s_branch 75b\n"
      "6:\n"
      "s_waitcnt vmcnt(0) lgkmcnt(0)\n"
      "s_nop 7\n"
      "s_mov_b32 s55, 1\n"
      "s_or_b32 s52, s52, 0x7f000002\n"
      "s_mov_b32 s60, 1\n"
      "v_max_f32_dpp v226, v226, v226 row_ror:8 row_mask:0xf bank_mask:0xf\n"
      "s_nop 1\n"
      "v_max_f32_dpp v226, v226, v226 row_ror:4 row_mask:0xf bank_mask:0xf\n"
      "s_nop 1\n"
      "v_max_f32_dpp v226, v226, v226 row_ror:2 row_mask:0xf bank_mask:0xf\n"
      "s_nop 1\n"
      "v_max_f32_dpp v226, v226, v226 row_ror:1 row_mask:0xf bank_mask:0xf\n"
      "s_nop 0\n"
      "v_readlane_b32 s44, v226, 0\n"
      "v_readlane_b32 s45, v226, 16\n"
      "v_readlane_b32 s46, v226, 32\n"
      "v_readlane_b32 s47, v226, 48\n"
      "s_max_u32 s44, s44, s45\n"
      "s_max_u32 s46, s46, s47\n"
      "s_max_u32 s44, s44, s46\n"
      "s_lshr_b32 s44, s44, 23\n"
      "s_sub_i32 s40, 132, s44\n"
      "s_cmp_lg_u32 s41, 0\n"
      "s_cbranch_scc1 61f\n"
      "s_cmp_ge_u32 s44, 2\n"
      "s_cbranch_scc1 62f\n"
      "61:\n"
      "s_mov_b32 s41, 1\n"
      "s_mov_b32 s45, 0x7fff7fff\n"
      "v_mov_b32 v226, 0\n"
      "v_and_b32 v176, s45, v228\n"
      "v_pk_max_u16 v226, v226, v176\n"
      "v_and_b32 v176, s45, v229\n"
      "v_pk_max_u16 v226, v226, v176\n"
      "v_and_b32 v176, s45, v230\n"
      "v_pk_max_u16 v226, v226, v176\n"
      "v_and_b32 v176, s45, v231\n"
      "v_pk_max_u16 v226, v226, v176\n"
      "v_and_b32 v176, s45, v232\n"
      "v_pk_max_u16 v226, v226, v176\n"
      "v_and_b32 v176, s45, v233\n"
      "v_pk_max_u16 v226, v226, v176\n"
      "v_and_b32 v176, s45, v234\n"
      "v_pk_max_u16 v226, v226, v176\n"
      "v_and_b32 v176, s45, v235\n"
      "v_pk_max_u16 v226, v226, v176\n"
      "v_and_b32 v176, s45, v236\n"
      "v_pk_max_u16 v226, v226, v176\n"
      "v_and_b32 v176, s45, v237\n"
      "v_pk_max_u16 v226, v226, v176\n"
      "v_and_b32 v176, s45, v238\n"
      "v_pk_max_u16 v226, v226, v176\n"
      "v_and_b32 v176, s45, v239\n"
      "v_pk_max_u16 v226, v226, v176\n"
      "v_and_b32 v176, s45, v240\n"
      "v_pk_max_u16 v226, v226, v176\n"
      "v_and_b32 v176, s45, v241\n"
      "v_pk_max_u16 v226, v226, v176\n"
      "v_and_b32 v176, s45, v242\n"
      "v_pk_max_u16 v226, v226, v176\n"
      "v_and_b32 v176, s45, v243\n"
      "v_pk_max_u16 v226, v226, v176\n"
      "v_and_b32 v176, s45, v244\n"
      "v_pk_max_u16 v226, v226, v176\n"
      "v_and_b32 v176, s45, v245\n"
      "v_pk_max_u16 v226, v226, v176\n"
      "v_and_b32 v176, s45, v246\n"
      "v_pk_max_u16 v226, v226, v176\n"
      "v_and_b32 v176, s45, v247\n"
      "v_pk_max_u16 v226, v226, v176\n"
      "v_and_b32 v176, s45, v248\n"
      "v_pk_max_u16 v226, v226, v176\n"
      "v_and_b32 v176, s45, v249\n"
      "v_pk_max_u16 v226, v226, v176\n"
      "v_and_b32 v176, s45, v250\n"
      "v_pk_max_u16 v226, v226, v176\n"
      "v_and_b32 v176, s45, v251\n"
      "v_pk_max_u16 v226, v226, v176\n"
      "v_and_b32 v176, s45, v252\n"
      "v_pk_max_u16 v226, v226, v176\n"
      "v_and_b32 v176, s45, v253\n"
      "v_pk_max_u16 v226, v226, v176\n"
      "v_and_b32 v176, s45, v254\n"
      "v_pk_max_u16 v226, v226, v176\n"
      "v_and_b32 v176, s45, v255\n"
      "v_pk_max_u16 v226, v226, v176\n"
      "v_lshlrev_b32 v176, 16, v226\n"
      "v_max_u32 v226, v226, v176\n"
      "s_nop 1\n"
      "v_max_u32_dpp v226, v226, v226 row_ror:8 row_mask:0xf bank_mask:0xf\n"
      "s_nop 1\n"
      "v_max_u32_dpp v226, v226, v226 row_ror:4 row_mask:0xf bank_mask:0xf\n"
      "s_nop 1\n"
      "v_max_u32_dpp v226, v226, v226 row_ror:2 row_mask:0xf bank_mask:0xf\n"
      "s_nop 1\n"
      "v_max_u32_dpp v226, v226, v226 row_ror:1 row_mask:0xf bank_mask:0xf\n"
      "s_nop 0\n"
      "v_readlane_b32 s44, v226, 0\n"
      "v_readlane_b32 s45, v226, 16\n"
      "v_readlane_b32 s46, v226, 32\n"
      "v_readlane_b32 s47, v226, 48\n"
      "s_max_u32 s44, s44, s45\n"
      "s_max_u32 s46, s46, s47\n"
      "s_max_u32 s44, s44, s46\n"
      "s_lshr_b32 s44, s44, 23\n"
      "s_sub_i32 s40, 133, s44\n"
      "s_cmp_eq_u32 s44, 0\n"
      "s_cselect_b32 s40, 0, s40\n"
      "62:\n"
      "s_add_i32 s45, s40, 32\n"
      "s_cmp_gt_u32 s45, 80\n"
      "s_cselect_b32 s40, 0, s40\n"
      "s_cselect_b32 s46, 0x7fc00000, 0\n"
      "s_cselect_b32 s41, 1, s41\n"
      "s_add_u32 s39, s40, 127\n"
      "s_lshl_b32 s39, s39, 23\n"
      "s_lshl_b32 s44, s40, 1\n"
      "s_sub_u32 s45, 127, s44\n"
      "s_add_u32 s44, s44, 127\n"
      "s_lshl_b32 s44, s44, 23\n"
      "s_lshl_b32 s45, s45, 23\n"
      "s_or_b32 s44, s44, s46\n"
      "v_mul_f32 v16, s44, v16\n"
      "v_mul_f32 v48, s45, v48\n"
      "v_mul_f32 v17, s44, v17\n"
      "v_mul_f32 v49, s45, v49\n"
      "v_mul_f32 v18, s44, v18\n"
      "v_mul_f32 v50, s45, v50\n"
      "v_mul_f32 v19, s44, v19\n"
      "v_mul_f32 v51, s45, v51\n"
      "v_mul_f32 v20, s44, v20\n"
      "v_mul_f32 v52, s45, v52\n"
      "v_mul_f32 v21, s44, v21\n"
      "v_mul_f32 v53, s45, v53\n"
      "v_mul_f32 v22, s44, v22\n"
      "v_mul_f32 v54, s45, v54\n"
      "v_mul_f32 v23, s44, v23\n"
      "v_mul_f32 v55, s45, v55\n"
      "v_mul_f32 v24, s44, v24\n"
      "v_mul_f32 v56, s45, v56\n"
      "v_mul_f32 v25, s44, v25\n"
      "v_mul_f32 v57, s45, v57\n"
      "v_mul_f32 v26, s44, v26\n"
      "v_mul_f32 v58, s45, v58\n"
      "v_mul_f32 v27, s44, v27\n"
      "v_mul_f32 v59, s45, v59\n"
      "v_mul_f32 v28, s44, v28\n"
      "v_mul_f32 v60, s45, v60\n"
      "v_mul_f32 v29, s44, v29\n"
      "v_mul_f32 v61, s45, v61\n"
      "v_mul_f32 v30, s44, v30\n"
      "v_mul_f32 v62, s45, v62\n"
      "v_mul_f32 v31, s44, v31\n"
      "v_mul_f32 v63, s45, v63\n"
      "v_mul_f32 v32, s44, v32\n"
      "v_mul_f32 v64, s45, v64\n"
      "v_mul_f32 v33, s44, v33\n"
      "v_mul_f32 v65, s45, v65\n"
      "v_mul_f32 v34, s44, v34\n"
      "v_mul_f32 v66, s45, v66\n"
      "v_mul_f32 v35, s44, v35\n"
      "v_mul_f32 v67, s45, v67\n"
      "v_mul_f32 v36, s44, v36\n"
      "v_mul_f32 v68, s45, v68\n"
      "v_mul_f32 v37, s44, v37\n"
      "v_mul_f32 v69, s45, v69\n"
      "v_mul_f32 v38, s44, v38\n"
      "v_mul_f32 v70, s45, v70\n"
      "v_mul_f32 v39, s44, v39\n"
      "v_mul_f32 v71, s45, v71\n"
      "v_mul_f32 v40, s44, v40\n"
      "v_mul_f32 v72, s45, v72\n"
      "v_mul_f32 v41, s44, v41\n"
      "v_mul_f32 v73, s45, v73\n"
      "v_mul_f32 v42, s44, v42\n"
      "v_mul_f32 v74, s45, v74\n"
      "v_mul_f32 v43, s44, v43\n"
      "v_mul_f32 v75, s45, v75\n"
      "v_mul_f32 v44, s44, v44\n"
      "v_mul_f32 v76, s45, v76\n"
      "v_mul_f32 v45, s44, v45\n"
      "v_mul_f32 v77, s45, v77\n"
      "v_mul_f32 v46, s44, v46\n"
      "v_mul_f32 v78, s45, v78\n"
      "v_mul_f32 v47, s44, v47\n"
      "v_mul_f32 v79, s45, v79\n"
      "v_mov_b32 v178, s39\n"
      "v_lshlrev_b32 v176, 16, v228\n"
      "v_and_b32 v177, s43, v228\n"
      "v_fmaak_f32 v176, v178, v176, 0x12800000\n"
      "v_fmaak_f32 v177, v178, v177, 0x12800000\n"
      "v_add_f32 v176, 0x92800000, v176\n"
      "v_add_f32 v177, 0x92800000, v177\n"
      "v_cvt_pk_bf16_f32 v228, v176, v177\n"
      "v_lshlrev_b32 v176, 16, v229\n"
      "v_and_b32 v177, s43, v229\n"
      "v_fmaak_f32 v176, v178, v176, 0x12800000\n"
      "v_fmaak_f32 v177, v178, v177, 0x12800000\n"
      "v_add_f32 v176, 0x92800000, v176\n"
      "v_add_f32 v177, 0x92800000, v177\n"
      "v_cvt_pk_bf16_f32 v229, v176, v177\n"
      "v_lshlrev_b32 v176, 16, v230\n"
      "v_and_b32 v177, s43, v230\n"
      "v_fmaak_f32 v176, v178, v176, 0x12800000\n"
      "v_fmaak_f32 v177, v178, v177, 0x12800000\n"
      "v_add_f32 v176, 0x92800000, v176\n"
      "v_add_f32 v177, 0x92800000, v177\n"
      "v_cvt_pk_bf16_f32 v230, v176, v177\n"
      "v_lshlrev_b32 v176, 16, v231\n"
      "v_and_b32 v177, s43, v231\n"
      "v_fmaak_f32 v176, v178, v176, 0x12800000\n"
      "v_fmaak_f32 v177, v178, v177, 0x12800000\n"
      "v_add_f32 v176, 0x92800000, v176\n"
      "v_add_f32 v177, 0x92800000, v177\n"
      "v_cvt_pk_bf16_f32 v231, v176, v177\n"
      "v_lshlrev_b32 v176, 16, v232\n"
      "v_and_b32 v177, s43, v232\n"
      "v_fmaak_f32 v176, v178, v176, 0x12800000\n"
      "v_fmaak_f32 v177, v178, v177, 0x12800000\n"
      "v_add_f32 v176, 0x92800000, v176\n"
      "v_add_f32 v177, 0x92800000, v177\n"
      "v_cvt_pk_bf16_f32 v232, v176, v177\n"
      "v_lshlrev_b32 v176, 16, v233\n"
      "v_and_b32 v177, s43, v233\n"
      "v_fmaak_f32 v176, v178, v176, 0x12800000\n"
      "v_fmaak_f32 v177, v178, v177, 0x12800000\n"
      "v_add_f32 v176, 0x92800000, v176\n"
      "v_add_f32 v177, 0x92800000, v177\n"
      "v_cvt_pk_bf16_f32 v233, v176, v177\n"
      "v_lshlrev_b32 v176, 16, v234\n"
      "v_and_b32 v177, s43, v234\n"
      "v_fmaak_f32 v176, v178, v176, 0x12800000\n"
      "v_fmaak_f32 v177, v178, v177, 0x12800000\n"
      "v_add_f32 v176, 0x92800000, v176\n"
      "v_add_f32 v177, 0x92800000, v177\n"
      "v_cvt_pk_bf16_f32 v234, v176, v177\n"
      "v_lshlrev_b32 v176, 16, v235\n"
      "v_and_b32 v177, s43, v235\n"
      "v_fmaak_f32 v176, v178, v176, 0x12800000\n"
      "v_fmaak_f32 v177, v178, v177, 0x12800000\n"
      "v_add_f32 v176, 0x92800000, v176\n"
      "v_add_f32 v177, 0x92800000, v177\n"
      "v_cvt_pk_bf16_f32 v235, v176, v177\n"
      "v_lshlrev_b32 v176, 16, v236\n"
      "v_and_b32 v177, s43, v236\n"
      "v_fmaak_f32 v176, v178, v176, 0x12800000\n"
      "v_fmaak_f32 v177, v178, v177, 0x12800000\n"
      "v_add_f32 v176, 0x92800000, v176\n"
      "v_add_f32 v177, 0x92800000, v177\n"
      "v_cvt_pk_bf16_f32 v236, v176, v177\n"
      "v_lshlrev_b32 v176, 16, v237\n"
      "v_and_b32 v177, s43, v237\n"
      "v_fmaak_f32 v176, v178, v176, 0x12800000\n"
      "v_fmaak_f32 v177, v178, v177, 0x12800000\n"
      "v_add_f32 v176, 0x92800000, v176\n"
      "v_add_f32 v177, 0x92800000, v177\n"
      "v_cvt_pk_bf16_f32 v237, v176, v177\n"
      "v_lshlrev_b32 v176, 16, v238\n"
      "v_and_b32 v177, s43, v238\n"
      "v_fmaak_f32 v176, v178, v176, 0x12800000\n"
      "v_fmaak_f32 v177, v178, v177, 0x12800000\n"
      "v_add_f32 v176, 0x92800000, v176\n"
      "v_add_f32 v177, 0x92800000, v177\n"
      "v_cvt_pk_bf16_f32 v238, v176, v177\n"
      "v_lshlrev_b32 v176, 16, v239\n"
      "v_and_b32 v177, s43, v239\n"
      "v_fmaak_f32 v176, v178, v176, 0x12800000\n"
      "v_fmaak_f32 v177, v178, v177, 0x12800000\n"
      "v_add_f32 v176, 0x92800000, v176\n"
      "v_add_f32 v177, 0x92800000, v177\n"
      "v_cvt_pk_bf16_f32 v239, v176, v177\n"
      "v_lshlrev_b32 v176, 16, v240\n"
      "v_and_b32 v177, s43, v240\n"
      "v_fmaak_f32 v176, v178, v176, 0x12800000\n"
      "v_fmaak_f32 v177, v178, v177, 0x12800000\n"
      "v_add_f32 v176, 0x92800000, v176\n"
      "v_add_f32 v177, 0x92800000, v177\n"
      "v_cvt_pk_bf16_f32 v240, v176, v177\n"
      "v_lshlrev_b32 v176, 16, v241\n"
      "v_and_b32 v177, s43, v241\n"
      "v_fmaak_f32 v176, v178, v176, 0x12800000\n"
      "v_fmaak_f32 v177, v178, v177, 0x12800000\n"
      "v_add_f32 v176, 0x92800000, v176\n"
      "v_add_f32 v177, 0x92800000, v177\n"
      "v_cvt_pk_bf16_f32 v241, v176, v177\n"
      "v_lshlrev_b32 v176, 16, v242\n"
      "v_and_b32 v177, s43, v242\n"
      "v_fmaak_f32 v176, v178, v176, 0x12800000\n"
      "v_fmaak_f32 v177, v178, v177, 0x12800000\n"
      "v_add_f32 v176, 0x92800000, v176\n"
      "v_add_f32 v177, 0x92800000, v177\n"
      "v_cvt_pk_bf16_f32 v242, v176, v177\n"
      "v_lshlrev_b32 v176, 16, v243\n"
      "v_and_b32 v177, s43, v243\n"
      "v_fmaak_f32 v176, v178, v176, 0x12800000\n"
      "v_fmaak_f32 v177, v178, v177, 0x12800000\n"
      "v_add_f32 v176, 0x92800000, v176\n"
      "v_add_f32 v177, 0x92800000, v177\n"
      "v_cvt_pk_bf16_f32 v243, v176, v177\n"
      "v_lshlrev_b32 v176, 16, v244\n"
      "v_and_b32 v177, s43, v244\n"
      "v_fmaak_f32 v176, v178, v176, 0x12800000\n"
      "v_fmaak_f32 v177, v178, v177, 0x12800000\n"
      "v_add_f32 v176, 0x92800000, v176\n"
      "v_add_f32 v177, 0x92800000, v177\n"
      "v_cvt_pk_bf16_f32 v244, v176, v177\n"
      "v_lshlrev_b32 v176, 16, v245\n"
      "v_and_b32 v177, s43, v245\n"
      "v_fmaak_f32 v176, v178, v176, 0x12800000\n"
      "v_fmaak_f32 v177, v178, v177, 0x12800000\n"
      "v_add_f32 v176, 0x92800000, v176\n"
      "v_add_f32 v177, 0x92800000, v177\n"
      "v_cvt_pk_bf16_f32 v245, v176, v177\n"
      "v_lshlrev_b32 v176, 16, v246\n"
      "v_and_b32 v177, s43, v246\n"
      "v_fmaak_f32 v176, v178, v176, 0x12800000\n"
      "v_fmaak_f32 v177, v178, v177, 0x12800000\n"
      "v_add_f32 v176, 0x92800000, v176\n"
      "v_add_f32 v177, 0x92800000, v177\n"
      "v_cvt_pk_bf16_f32 v246, v176, v177\n"
      "v_lshlrev_b32 v176, 16, v247\n"
      "v_and_b32 v177, s43, v247\n"
      "v_fmaak_f32 v176, v178, v176, 0x12800000\n"
      "v_fmaak_f32 v177, v178, v177, 0x12800000\n"
      "v_add_f32 v176, 0x92800000, v176\n"
      "v_add_f32 v177, 0x92800000, v177\n"
      "v_cvt_pk_bf16_f32 v247, v176, v177\n"
      "v_lshlrev_b32 v176, 16, v248\n"
      "v_and_b32 v177, s43, v248\n"
      "v_fmaak_f32 v176, v178, v176, 0x12800000\n"
      "v_fmaak_f32 v177, v178, v177, 0x12800000\n"
      "v_add_f32 v176, 0x92800000, v176\n"
      "v_add_f32 v177, 0x92800000, v177\n"
      "v_cvt_pk_bf16_f32 v248, v176, v177\n"
      "v_lshlrev_b32 v176, 16, v249\n"
      "v_and_b32 v177, s43, v249\n"
      "v_fmaak_f32 v176, v178, v176, 0x12800000\n"
      "v_fmaak_f32 v177, v178, v177, 0x12800000\n"
      "v_add_f32 v176, 0x92800000, v176\n"
      "v_add_f32 v177, 0x92800000, v177\n"
      "v_cvt_pk_bf16_f32 v249, v176, v177\n"
      "v_lshlrev_b32 v176, 16, v250\n"
      "v_and_b32 v177, s43, v250\n"
      "v_fmaak_f32 v176, v178, v176, 0x12800000\n"
      "v_fmaak_f32 v177, v178, v177, 0x12800000\n"
      "v_add_f32 v176, 0x92800000, v176\n"
      "v_add_f32 v177, 0x92800000, v177\n"
      "v_cvt_pk_bf16_f32 v250, v176, v177\n"
      "v_lshlrev_b32 v176, 16, v251\n"
      "v_and_b32 v177, s43, v251\n"
      "v_fmaak_f32 v176, v178, v176, 0x12800000\n"
      "v_fmaak_f32 v177, v178, v177, 0x12800000\n"
      "v_add_f32 v176, 0x92800000, v176\n"
      "v_add_f32 v177, 0x92800000, v177\n"
      "v_cvt_pk_bf16_f32 v251, v176, v177\n"
      "v_lshlrev_b32 v176, 16, v252\n"
      "v_and_b32 v177, s43, v252\n"
      "v_fmaak_f32 v176, v178, v176, 0x12800000\n"
      "v_fmaak_f32 v177, v178, v177, 0x12800000\n"
      "v_add_f32 v176, 0x92800000, v176\n"
      "v_add_f32 v177, 0x92800000, v177\n"
      "v_cvt_pk_bf16_f32 v252, v176, v177\n"
      "v_lshlrev_b32 v176, 16, v253\n"
      "v_and_b32 v177, s43, v253\n"
      "v_fmaak_f32 v176, v178, v176, 0x12800000\n"
      "v_fmaak_f32 v177, v178, v177, 0x12800000\n"
      "v_add_f32 v176, 0x92800000, v176\n"
      "v_add_f32 v177, 0x92800000, v177\n"
      "v_cvt_pk_bf16_f32 v253, v176, v177\n"
      "v_lshlrev_b32 v176, 16, v254\n"
      "v_and_b32 v177, s43, v254\n"
      "v_fmaak_f32 v176, v178, v176, 0x12800000\n"
      "v_fmaak_f32 v177, v178, v177, 0x12800000\n"
      "v_add_f32 v176, 0x92800000, v176\n"
      "v_add_f32 v177, 0x92800000, v177\n"
      "v_cvt_pk_bf16_f32 v254, v176, v177\n"
      "v_lshlrev_b32 v176, 16, v255\n"
      "v_and_b32 v177, s43, v255\n"
      "v_fmaak_f32 v176, v178, v176, 0x12800000\n"
      "v_fmaak_f32 v177, v178, v177, 0x12800000\n"
      "v_add_f32 v176, 0x92800000, v176\n"
      "v_add_f32 v177, 0x92800000, v177\n"
      "v_cvt_pk_bf16_f32 v255, v176, v177\n"
      "v_accvgpr_write_b32 a216, v244\n"
      "v_accvgpr_write_b32 a217, v245\n"
      "v_accvgpr_write_b32 a218, v246\n"
      "v_accvgpr_write_b32 a219, v247\n"
      "v_accvgpr_write_b32 a220, v248\n"
      "v_accvgpr_write_b32 a221, v249\n"
      "v_accvgpr_write_b32 a222, v250\n"
      "v_accvgpr_write_b32 a223, v251\n"
      "v_accvgpr_write_b32 a224, v252\n"
      "v_accvgpr_write_b32 a225, v253\n"
      "v_accvgpr_write_b32 a226, v254\n"
      "v_accvgpr_write_b32 a227, v255\n"
      "s_branch 3b\n"
      "7:\n"
      "s_mov_b64 exec, -1\n"
      "s_cmp_lg_u32 s41, 0\n"
      "s_cbranch_scc1 8f\n"
      "s_mov_b32 s41, 1\n"
      "s_waitcnt vmcnt(0)\n"
      "s_branch 0b\n"
      "17:\n"
      "v_mfma_f32_16x16x32_f16 v[180:183], a[0:3], a[216:219], v[16:19]\n"
      "v_mfma_f32_16x16x32_f16 v[180:183], a[4:7], a[220:223], v[180:183]\n"
      "v_mfma_f32_16x16x32_f16 v[180:183], a[8:11], a[224:227], v[180:183]\n"
      "v_mfma_f32_16x16x32_f16 v[180:183], a[12:15], a[228:231], v[180:183]\n"
      "s_nop 7\n"
      "v_mul_f32 v184, |v180|, v48\n"
      "v_fma_f32 v184, |v181|, v49, v184\n"
      "v_fma_f32 v184, |v182|, v50, v184\n"
      "v_fma_f32 v184, |v183|, v51, v184\n"
      "v_mfma_f32_16x16x32_f16 v[180:183], a[76:79], a[228:231], v[32:35]\n"
      "v_mfma_f32_16x16x32_f16 v[180:183], a[72:75], a[224:227], v[180:183]\n"
      "v_mfma_f32_16x16x32_f16 v[180:183], a[68:71], a[220:223], v[180:183]\n"
      "v_mfma_f32_16x16x32_f16 v[180:183], a[64:67], a[216:219], v[180:183]\n"
      "s_nop 7\n"
      "v_fma_f32 v184, |v180|, v64, v184\n"
      "v_fma_f32 v184, |v181|, v65, v184\n"
      "v_fma_f32 v184, |v182|, v66, v184\n"
      "v_fma_f32 v184, |v183|, v67, v184\n"
      "v_mfma_f32_16x16x32_f16 v[180:183], a[16:19], a[216:219], v[20:23]\n"
      "v_mfma_f32_16x16x32_f16 v[180:183], a[20:23], a[220:223], v[180:183]\n"
      "v_mfma_f32_16x16x32_f16 v[180:183], a[24:27], a[224:227], v[180:183]\n"
      "v_mfma_f32_16x16x32_f16 v[180:183], a[28:31], a[228:231], v[180:183]\n"
      "s_nop 7\n"
      "v_mul_f32 v185, |v180|, v52\n"
      "v_fma_f32 v185, |v181|, v53, v185\n"
      "v_fma_f32 v185, |v182|, v54, v185\n"
      "v_fma_f32 v185, |v183|, v55, v185\n"
      "v_mfma_f32_16x16x32_f16 v[180:183], a[92:95], a[228:231], v[36:39]\n"
      "v_mfma_f32_16x16x32_f16 v[180:183], a[88:91], a[224:227], v[180:183]\n"
      "v_mfma_f32_16x16x32_f16 v[180:183], a[84:87], a[220:223], v[180:183]\n"
      "v_mfma_f32_16x16x32_f16 v[180:183], a[80:83], a[216:219], v[180:183]\n"
      "s_nop 7\n"
      "v_fma_f32 v185, |v180|, v68, v185\n"
      "v_fma_f32 v185, |v181|, v69, v185\n"
      "v_fma_f32 v185, |v182|, v70, v185\n"
      "v_fma_f32 v185, |v183|, v71, v185\n"
      "v_mfma_f32_16x16x32_f16 v[180:183], a[32:35], a[216:219], v[24:27]\n"
      "v_mfma_f32_16x16x32_f16 v[180:183], a[36:39], a[220:223], v[180:183]\n"
      "v_mfma_f32_16x16x32_f16 v[180:183], a[40:43], a[224:227], v[180:183]\n"
      "v_mfma_f32_16x16x32_f16 v[180:183], a[44:47], a[228:231], v[180:183]\n"
      "s_nop 7\n"
      "v_mul_f32 v186, |v180|, v56\n"
      "v_fma_f32 v186, |v181|, v57, v186\n"
      "v_fma_f32 v186, |v182|, v58, v186\n"
      "v_fma_f32 v186, |v183|, v59, v186\n"
      "v_mfma_f32_16x16x32_f16 v[180:183], a[108:111], a[228:231], v[40:43]\n"
      "v_mfma_f32_16x16x32_f16 v[180:183], a[104:107], a[224:227], v[180:183]\n"
      "v_mfma_f32_16x16x32_f16 v[180:183], a[100:103], a[220:223], v[180:183]\n"
      "v_mfma_f32_16x16x32_f16 v[180:183], a[96:99], a[216:219], v[180:183]\n"
      "s_nop 7\n"
      "v_fma_f32 v186, |v180|, v72, v186\n"
      "v_fma_f32 v186, |v181|, v73, v186\n"
      "v_fma_f32 v186, |v182|, v74, v186\n"
      "v_fma_f32 v186, |v183|, v75, v186\n"
      "v_mfma_f32_16x16x32_f16 v[180:183], a[48:51], a[216:219], v[28:31]\n"
      "v_mfma_f32_16x16x32_f16 v[180:183], a[52:55], a[220:223], v[180:183]\n"
      "v_mfma_f32_16x16x32_f16 v[180:183], a[56:59], a[224:227], v[180:183]\n"
      "v_mfma_f32_16x16x32_f16 v[180:183], a[60:63], a[228:231], v[180:183]\n"
      "s_nop 7\n"
      "v_mul_f32 v187, |v180|, v60\n"
      "v_fma_f32 v187, |v181|, v61, v187\n"
      "v_fma_f32 v187, |v182|, v62, v187\n"
      "v_fma_f32 v187, |v183|, v63, v187\n"
      "v_mfma_f32_16x16x32_f16 v[180:183], a[124:127], a[228:231], v[44:47]\n"
      "v_mfma_f32_16x16x32_f16 v[180:183], a[120:123], a[224:227], v[180:183]\n"
      "v_mfma_f32_16x16x32_f16 v[180:183], a[116:119], a[220:223], v[180:183]\n"
      "v_mfma_f32_16x16x32_f16 v[180:183], a[112:115], a[216:219], v[180:183]\n"
      "s_nop 7\n"
      "v_fma_f32 v187, |v180|, v76, v187\n"
      "v_fma_f32 v187, |v181|, v77, v187\n"
      "v_fma_f32 v187, |v182|, v78, v187\n"
      "v_fma_f32 v187, |v183|, v79, v187\n"
      "v_pk_add_f32 v[184:185], v[184:185], v[186:187]\n"
      "v_add_f32 v199, v184, v185\n"
      "s_nop 1\n"
      "s_branch 18b\n"
      "19:\n"
      "s_mov_b64 exec, -1\n"
      "s_cmp_lt_i32 s99, 0\n"
      "s_cbranch_scc1 7b\n"
      "v_max3_f32 v224, v188, v199, v196\n"
      "v_max_f32 v224, 0, v224\n"
      "s_nop 1\n"
      "v_max_f32_dpp v225, v224, v224 row_ror:4 row_mask:0xf bank_mask:0xf\n"
      "v_max_f32_dpp v225, v224, v225 row_ror:8 row_mask:0xf bank_mask:0xf\n"
      "v_max_f32_dpp v225, v224, v225 row_ror:12 row_mask:0xf bank_mask:0xf\n"
      "s_nop 1\n"
      "v_max_f32_dpp v224, v225, v225 row_ror:1 row_mask:0xf bank_mask:0xf\n"
      "v_max_f32_dpp v224, v225, v224 row_ror:2 row_mask:0xf bank_mask:0xf\n"
      "v_max_f32_dpp v224, v225, v224 row_ror:3 row_mask:0xf bank_mask:0xf\n"
      "s_nop 1\n"
      "v_max_f32_dpp v224, v224, v224 row_bcast:15 row_mask:0xa bank_mask:0xf\n"
      "s_nop 1\n"
      "v_max_f32_dpp v224, v224, v224 row_bcast:31 row_mask:0xc bank_mask:0xf\n"
      "s_nop 0\n"
      "v_readlane_b32 s44, v224, 63\n"
      "s_nop 1\n"
      "v_exp_f32 v222, -s44\n"
      "s_nop 0\n"
      "v_mul_f32 v214, v220, v222\n"
      "v_mul_f32 v226, v221, v222\n"
      "v_subrev_f32 v188, s44, v188\n"
      "v_exp_f32 v222, v188\n"
      "v_fmac_f32 v214, 0, v188\n"
      "v_add_f32 v214, v214, v222\n"
      "v_fmac_f32 v226, v222, v235\n"
      "v_subrev_f32 v199, s44, v199\n"
      "v_exp_f32 v222, v199\n"
      "v_fmac_f32 v214, 0, v199\n"
      "v_add_f32 v214, v214, v222\n"
      "v_fmac_f32_dpp v226, v181, v222 quad_perm:[0,1,2,3] row_mask:0x1 bank_mask:0x3\n"
      "v_subrev_f32 v196, s44, v196\n"
      "v_exp_f32 v222, v196\n"
      "v_fmac_f32 v214, 0, v196\n"
      "v_add_f32 v214, v214, v222\n"
      "v_fmac_f32 v226, v222, v237\n"
      "s_waitcnt vmcnt(0)\n"
      "v_add_f32_dpp v214, v214, v214 row_ror:8 row_mask:0xf bank_mask:0xf\n"
      "v_add_f32_dpp v226, v226, v226 row_ror:8 row_mask:0xf bank_mask:0xf\n"
      "s_nop 0\n"
      "v_add_f32_dpp v214, v214, v214 row_ror:4 row_mask:0xf bank_mask:0xf\n"
      "v_add_f32_dpp v226, v226, v226 row_ror:4 row_mask:0xf bank_mask:0xf\n"
      "s_nop 0\n"
      "v_add_f32_dpp v214, v214, v214 row_ror:2 row_mask:0xf bank_mask:0xf\n"
      "v_add_f32_dpp v226, v226, v226 row_ror:2 row_mask:0xf bank_mask:0xf\n"
      "s_nop 0\n"
      "v_add_f32_dpp v214, v214, v214 row_ror:1 row_mask:0xf bank_mask:0xf\n"
      "v_add_f32_dpp v226, v226, v226 row_ror:1 row_mask:0xf bank_mask:0xf\n"
      "s_nop 0\n"
      "v_add_f32_dpp v214, v214, v214 row_bcast:15 row_mask:0xa bank_mask:0xf\n"
      "v_add_f32_dpp v226, v226, v226 row_bcast:15 row_mask:0xa bank_mask:0xf\n"
      "s_nop 0\n"
      "v_add_f32_dpp v214, v214, v214 row_bcast:31 row_mask:0xc bank_mask:0xf\n"
      "v_add_f32_dpp v226, v226, v226 row_bcast:31 row_mask:0xc bank_mask:0xf\n"
      "v_rcp_f32 v222, v214\n"
      "v_ldexp_f32 v226, v226, s39\n"
      "s_lshl_b64 exec, 1, 63\n"
      "v_mul_f32 v226, v226, v222\n"
      "v_cvt_pk_bf16_f32 v222, v226, v226\n"
      "global_store_short v3, v222, s[24:25]\n"
      "v_cmp_class_f32 vcc, v226, s45\n"
      "s_nop 0\n"
      "s_cmp_lt_i32 vcc_hi, 0\n"
      "s_cbranch_scc1 7b\n"
      "s_branch 20b\n"
      "8:\n"
      "s_mov_b32 s42, 1\n"
      "9:\n"
      "s_nop 7\n"
      "s_waitcnt vmcnt(0) lgkmcnt(0)\n"
      "s_mov_b32 %0, s42\n"
      : "=s"(flag)
      : "s"(__builtin_amdgcn_kernarg_segment_ptr()), "s"(blockIdx.x), "v"(threadIdx.x), "s"(lbase)
      :
      "v1", "v2", "v3", "v4", "v5", "v6", "v7", "v8", "v9", "v10", "v11", "v12", "v13", "v14", "v15", "v16", "v17", "v18", "v19", "v20", "v21", "v22", "v23", "v24",
      "v25", "v26", "v27", "v28", "v29", "v30", "v31", "v32", "v33", "v34", "v35", "v36", "v37", "v38", "v39", "v40", "v41", "v42", "v43", "v44", "v45", "v46", "v47", "v48",
      "v49", "v50", "v51", "v52", "v53", "v54", "v55", "v56", "v57", "v58", "v59", "v60", "v61", "v62", "v63", "v64", "v65", "v66", "v67", "v68", "v69", "v70", "v71", "v72",
      "v73", "v74", "v75", "v76", "v77", "v78", "v79", "v80", "v81", "v82", "v83", "v84", "v85", "v86", "v87", "v88", "v89", "v90", "v91", "v92", "v93", "v94", "v95", "v96",
      "v97", "v98", "v99", "v100", "v101", "v102", "v103", "v104", "v105", "v106", "v107", "v108", "v109", "v110", "v111", "v112", "v113", "v114", "v115", "v116", "v117", "v118", "v119", "v120",
      "v121", "v122", "v123", "v124", "v125", "v126", "v127", "v128", "v129", "v130", "v131", "v132", "v133", "v134", "v135", "v136", "v137", "v138", "v139", "v140", "v141", "v142", "v143", "v144",
      "v145", "v146", "v147", "v148", "v149", "v150", "v151", "v152", "v153", "v154", "v155", "v156", "v157", "v158", "v159", "v160", "v161", "v162", "v163", "v164", "v165", "v166", "v167", "v168",
      "v169", "v170", "v171", "v172", "v173", "v174", "v175", "v176", "v177", "v178", "v179", "v180", "v181", "v182", "v183", "v184", "v185", "v186", "v187", "v188", "v189", "v190", "v191", "v192",
      "v193", "v194", "v195", "v196", "v197", "v198", "v199", "v200", "v201", "v202", "v203", "v204", "v205", "v206", "v207", "v208", "v209", "v210", "v211", "v212", "v213", "v214", "v215", "v216",
      "v217", "v218", "v219", "v220", "v221", "v222", "v223", "v224", "v225", "v226", "v227", "v228", "v229", "v230", "v231", "v232", "v233", "v234", "v235", "v236", "v237", "v238", "v239", "v240",
      "v241", "v242", "v243", "v244", "v245", "v246", "v247", "v248", "v249", "v250", "v251", "v252", "v253", "v254", "v255", "a0", "a1", "a2", "a3", "a4", "a5", "a6", "a7", "a8",
      "a9", "a10", "a11", "a12", "a13", "a14", "a15", "a16", "a17", "a18", "a19", "a20", "a21", "a22", "a23", "a24", "a25", "a26", "a27", "a28", "a29", "a30", "a31", "a32",
      "a33", "a34", "a35", "a36", "a37", "a38", "a39", "a40", "a41", "a42", "a43", "a44", "a45", "a46", "a47", "a48", "a49", "a50", "a51", "a52", "a53", "a54", "a55", "a56",
      "a57", "a58", "a59", "a60", "a61", "a62", "a63", "a64", "a65", "a66", "a67", "a68", "a69", "a70", "a71", "a72", "a73", "a74", "a75", "a76", "a77", "a78", "a79", "a80",
      "a81", "a82", "a83", "a84", "a85", "a86", "a87", "a88", "a89", "a90", "a91", "a92", "a93", "a94", "a95", "a96", "a97", "a98", "a99", "a100", "a101", "a102", "a103", "a104",
      "a105", "a106", "a107", "a108", "a109", "a110", "a111", "a112", "a113", "a114", "a115", "a116", "a117", "a118", "a119", "a120", "a121", "a122", "a123", "a124", "a125", "a126", "a127", "a128",
      "a129", "a130", "a131", "a132", "a133", "a134", "a135", "a136", "a137", "a138", "a139", "a140", "a141", "a142", "a143", "a144", "a145", "a146", "a147", "a148", "a149", "a150", "a151", "a152",
      "a153", "a154", "a155", "a156", "a157", "a158", "a159", "a160", "a161", "a162", "a163", "a164", "a165", "a166", "a167", "a168", "a169", "a170", "a171", "a172", "a173", "a174", "a175", "a176",
      "a177", "a178", "a179", "a180", "a181", "a182", "a183", "a184", "a185", "a186", "a187", "a188", "a189", "a190", "a191", "a192", "a193", "a194", "a195", "a196", "a197", "a198", "a199", "a200",
      "a201", "a202", "a203", "a204", "a205", "a206", "a207", "a208", "a209", "a210", "a211", "a212", "a213", "a214", "a215", "a216", "a217", "a218", "a219", "a220", "a221", "a222", "a223", "a224",
      "a225", "a226", "a227", "a228", "a229", "a230", "a231", "a232", "a233", "a234", "a235", "a236", "a237", "a238", "a239", "a240", "a241", "a242", "a243", "a244", "a245", "a246", "a247", "s8",
      "s9", "s10", "s11", "s12", "s13", "s14", "s15", "s16", "s17", "s18", "s19", "s20", "s21", "s22", "s23", "s24", "s25", "s26", "s27", "s28", "s29", "s30", "s31", "s33",
      "s34", "s35", "s36", "s37", "s38", "s39", "s40", "s41", "s42", "s43", "s44", "s45", "s46", "s47", "s48", "s49", "s50", "s51", "s52", "s53", "s54", "s55", "s56", "s57",
      "s58", "s59", "s60", "s61", "s62", "s63", "s64", "s65", "s66", "s67", "s68", "s69", "s70", "s71", "s72", "s73", "s74", "s75", "s76", "s77", "s78", "s79", "s80", "s81",
      "s82", "s83", "s84", "s85", "s86", "s87", "s88", "s89", "s90", "s91", "s92", "s93", "s94", "s95", "s96", "s97", "s98", "s99", "s100", "s101", "vcc", "scc", "m0", "memory");
  if (flag | lbase) {
    const int F = 26, P = 325;
    const int lane = threadIdx.x & 63;
    const int wg = (int)((blockIdx.x & ~255u) + (blockIdx.x & 7) * 32 + ((blockIdx.x >> 3) & 31));
    const int b = min(wg * 4 + (int)(threadIdx.x >> 6), B - 1);
    {
      float m = -INFINITY, z = 0.f, n = 0.f;
      for (int p = lane; p < P; p += 64) {
        const unsigned short* ei = table + (size_t)idx[b * F + ii[p]] * 128;
        const unsigned short* ej = table + (size_t)idx[b * F + jj[p]] * 128;
        float e = 0.f, s = 0.f;
        for (int a = 0; a < 128; ++a) {
          float acc = 0.f;
          for (int d = 0; d < 128; ++d) acc = fmaf(rbf(bf2f(ei[d]) * bf2f(ej[d])), bf2f(W[d * 128 + a]), acc);
          float x = acc + bf2f(bvec[a]);
          x = 0.5f * (x + fabsf(x));
          e = fmaf(x, bf2f(hvec[a]), e);
        }
        for (int d = 0; d < 128; ++d) s = fmaf(rbf(bf2f(ei[d]) * bf2f(ej[d])), bf2f(pvec[d]), s);
        float mn = fmaxf(m, e);
        float fa = __expf(m - mn), fb = __expf(e - mn);
        z = z * fa + fb; n = n * fa + fb * s; m = mn;
      }
      float mx = m;
      for (int o = 1; o < 64; o <<= 1) mx = fmaxf(mx, __shfl_xor(mx, o));
      float f = (m == -INFINITY) ? 0.f : __expf(m - mx);
      z *= f; n *= f;
      for (int o = 1; o < 64; o <<= 1) { z += __shfl_xor(z, o); n += __shfl_xor(n, o); }
      if (lane == 0) out[b] = f2bf(n / z);
    }
  }
}

__global__ void afm_generic_kernel(const int* __restrict__ idx, const unsigned short* __restrict__ table,
                                   const int* __restrict__ ii, const int* __restrict__ jj,
                                   const unsigned short* __restrict__ W, const unsigned short* __restrict__ bvec,
                                   const unsigned short* __restrict__ hvec, const unsigned short* __restrict__ pvec,
                                   unsigned short* __restrict__ out, float* __restrict__ ws,
                                   int B, int F, int P, int D, int A) {
  const int b = blockIdx.x;
  float* ebuf = ws + (size_t)b * 2 * P;
  float* sbuf = ebuf + P;
  for (int p = threadIdx.x; p < P; p += blockDim.x) {
    const unsigned short* ei = table + (size_t)idx[b * F + ii[p]] * D;
    const unsigned short* ej = table + (size_t)idx[b * F + jj[p]] * D;
    float e = 0.f, sacc = 0.f;
    for (int a = 0; a < A; ++a) {
      float acc = 0.f;
      for (int d = 0; d < D; ++d) acc = fmaf(rbf(bf2f(ei[d]) * bf2f(ej[d])), bf2f(W[(size_t)d * A + a]), acc);
      float x = acc + bf2f(bvec[a]);
      x = 0.5f * (x + fabsf(x));
      e = fmaf(x, bf2f(hvec[a]), e);
    }
    for (int d = 0; d < D; ++d) sacc = fmaf(rbf(bf2f(ei[d]) * bf2f(ej[d])), bf2f(pvec[d]), sacc);
    ebuf[p] = e; sbuf[p] = sacc;
  }
  __syncthreads();
  if (threadIdx.x == 0) {
    float mx = -INFINITY;
    for (int p = 0; p < P; ++p) mx = fmaxf(mx, ebuf[p]);
    float z = 0.f, n = 0.f;
    for (int p = 0; p < P; ++p) { float w = __expf(ebuf[p] - mx); z += w; n += w * sbuf[p]; }
    out[b] = f2bf(n / z);
  }
}

extern "C" void kernel_launch(void* const* d_in, const int* in_sizes, int n_in,
                              void* d_out, int out_size, void* d_ws, size_t ws_size,
                              hipStream_t stream) {
  const int* idx = (const int*)d_in[0];
  const unsigned short* table = (const unsigned short*)d_in[1];
  const int* ii = (const int*)d_in[2];
  const int* jj = (const int*)d_in[3];
  const unsigned short* W = (const unsigned short*)d_in[4];
  const unsigned short* bvec = (const unsigned short*)d_in[5];
  const unsigned short* hvec = (const unsigned short*)d_in[6];
  const unsigned short* pvec = (const unsigned short*)d_in[7];
  unsigned short* out = (unsigned short*)d_out;
  const int B = out_size;
  const int P = in_sizes[2];
  const int D = in_sizes[7];
  const int A = in_sizes[5];
  if (B <= 0) return;
  const int F = in_sizes[0] / B;
  if (D == 128 && A == 128 && F == 26 && P == 325 && B < (1 << 24)) {
    const int G = (B + 1023) / 1024 * 256;
    afm_fast_kernel<<<G, 256, 0, stream>>>(idx, table, ii, jj, W, bvec, hvec, pvec, out, B, 0
        );
  } else {
    afm_generic_kernel<<<B, 256, 0, stream>>>(idx, table, ii, jj, W, bvec, hvec, pvec, out, (float*)d_ws, B, F, P, D, A);
  }
}
```

```cpp
#include <hip/hip_runtime.h>
#include <hip/hip_bf16.h>
#include <stdint.h>

#define AFM_HAS_STAMPS 0

typedef float  f32x2  __attribute__((ext_vector_type(2)));
typedef __bf16 bf16x2 __attribute__((ext_vector_type(2)));

__device__ __forceinline__ float bf2f(unsigned short u) { return __builtin_bit_cast(float, (unsigned)u << 16); }
__device__ __forceinline__ unsigned short f2bf(float x) {
  f32x2 v = {x, 0.f}; bf16x2 r = __builtin_convertvector(v, bf16x2);
  return (unsigned short)(__builtin_bit_cast(unsigned, r) & 0xffffu);
}
__device__ __forceinline__ float rbf(float x) { return bf2f(f2bf(x)); }

static __device__ const __attribute__((aligned(128), used)) unsigned afm_tab[4096] asm("afm_tab") = {
  40704, 41568, 38400, 41856, 41568, 40992, 38416, 42144, 41280, 40992, 38432, 42720, 39552, 40416, 38448, 41856, 39840, 38688, 38464, 42144, 39840, 38976, 38480, 42432,
  38688, 39552, 38496, 42720, 38976, 40128, 38512, 40704, 38400, 39264, 38528, 42144, 39840, 38400, 38544, 42432, 38688, 40128, 38560, 41856, 39264, 40128, 38576, 42144,
  40704, 40992, 38592, 42432, 40416, 38976, 38608, 42720, 41280, 41568, 38624, 40704, 41280, 40704, 38640, 40992, 40720, 41584, 40704, 41872, 41584, 41008, 40720, 42160,
  41296, 41008, 40736, 42736, 39568, 40432, 40752, 41872, 39856, 38704, 40768, 42160, 39856, 38992, 40784, 42448, 38704, 39568, 40800, 42736, 38992, 40144, 40816, 40720,
  38416, 39280, 40832, 42160, 39856, 38416, 40848, 42448, 38704, 40144, 40864, 41872, 39280, 40144, 40880, 42160, 40720, 41008, 40896, 42448, 40432, 38992, 40912, 42736,
  41296, 41584, 40928, 40720, 41296, 40720, 40944, 41008, 40736, 41600, 43008, 41888, 41600, 41024, 43024, 42176, 41312, 41024, 43040, 42752, 39584, 40448, 43056, 41888,
  39872, 38720, 43072, 42176, 39872, 39008, 43088, 42464, 38720, 39584, 43104, 42752, 39008, 40160, 43120, 40736, 38432, 39296, 43136, 42176, 39872, 38432, 43152, 42464,
  38720, 40160, 43168, 41888, 39296, 40160, 43184, 42176, 40736, 41024, 43200, 42464, 40448, 39008, 43216, 42752, 41312, 41600, 43232, 40736, 41312, 40736, 43248, 41024,
  40752, 41616, 37536, 41904, 41616, 41040, 37552, 42192, 41328, 41040, 37568, 42768, 39600, 40464, 37584, 41904, 39888, 38736, 37600, 42192, 39888, 39024, 37616, 42480,
  38736, 39600, 37632, 42768, 39024, 40176, 37648, 40752, 38448, 39312, 37664, 42192, 39888, 38448, 37680, 42480, 38736, 40176, 37696, 41904, 39312, 40176, 37712, 42192,
  40752, 41040, 37728, 42480, 40464, 39024, 37744, 42768, 41328, 41616, 37760, 40752, 41328, 40752, 37776, 41040, 44736, 43296, 46464, 82016, 44448, 43008, 46752, 82304,
  44736, 43872, 47040, 98496, 43008, 44160, 47328, 98784, 42144, 41280, 47616, 99072, 43296, 44160, 47616, 99360, 41856, 42720, 47616, 99648, 43872, 43008, 47616, 49920,
  45024, 44160, 63808, 49920, 43584, 44736, 64096, 49920, 41856, 40992, 64384, 49920, 42432, 41280, 64672, 49920, 44448, 43296, 64960, 49920, 43584, 44448, 81152, 49920,
  43584, 45024, 81440, 49920, 41568, 42432, 81728, 49920, 44752, 43312, 46480, 82032, 44464, 43024, 46768, 82320, 44752, 43888, 47056, 98512, 43024, 44176, 47344, 98800,
  42160, 41296, 47632, 99088, 43312, 44176, 47632, 99376, 41872, 42736, 47632, 99664, 43888, 43024, 47632, 49936, 45040, 44176, 63824, 49936, 43600, 44752, 64112, 49936,
  41872, 41008, 64400, 49936, 42448, 41296, 64688, 49936, 44464, 43312, 64976, 49936, 43600, 44464, 81168, 49936, 43600, 45040, 81456, 49936, 41584, 42448, 81744, 49936,
  44768, 43328, 46496, 82048, 44480, 43040, 46784, 82336, 44768, 43904, 47072, 98528, 43040, 44192, 47360, 98816, 42176, 41312, 47648, 99104, 43328, 44192, 47648, 99392,
  41888, 42752, 47648, 99680, 43904, 43040, 47648, 49952, 45056, 44192, 63840, 49952, 43616, 44768, 64128, 49952, 41888, 41024, 64416, 49952, 42464, 41312, 64704, 49952,
  44480, 43328, 64992, 49952, 43616, 44480, 81184, 49952, 43616, 45056, 81472, 49952, 41600, 42464, 81760, 49952, 44784, 43344, 46512, 82064, 44496, 43056, 46800, 82352,
  44784, 43920, 47088, 98544, 43056, 44208, 47376, 98832, 42192, 41328, 47664, 99120, 43344, 44208, 47664, 99408, 41904, 42768, 47664, 99696, 43920, 43056, 47664, 49968,
  45072, 44208, 63856, 49968, 43632, 44784, 64144, 49968, 41904, 41040, 64432, 49968, 42480, 41328, 64720, 49968, 44496, 43344, 65008, 49968, 43632, 44496, 81200, 49968,
  43632, 45072, 81488, 49968, 41616, 42480, 81776, 49968, 51712, 54272, 52992, 51152, 51716, 54352, 52996, 51236, 51720, 54432, 53000, 51244, 51724, 54512, 53004, 51404,
  51728, 54592, 53008, 51488, 51732, 54672, 53012, 51572, 51736, 54752, 53016, 51580, 51740, 54832, 53020, 51100, 51792, 54276, 53072, 51232, 51796, 54356, 53076, 51164,
  51800, 54436, 53080, 51400, 51804, 54516, 53084, 51484, 51808, 54596, 53088, 51568, 51812, 54676, 53092, 51500, 51816, 54756, 53096, 51096, 51820, 54836, 53100, 51180,
  51872, 54280, 53152, 52364, 51876, 54360, 53156, 52444, 51880, 54440, 53160, 52440, 51884, 54520, 53164, 51420, 51888, 54600, 53168, 51172, 51892, 54680, 53172, 51252,
  51896, 54760, 53176, 51396, 51900, 54840, 53180, 51560, 51952, 54284, 53232, 51084, 51956, 54364, 53236, 51092, 51960, 54444, 53240, 51556, 51964, 54524, 53244, 51564,
  51968, 54604, 53248, 52432, 51972, 54684, 53252, 51260, 51976, 54764, 53256, 52524, 51980, 54844, 53260, 52360, 52032, 54288, 53312, 52672, 52036, 54368, 53316, 52756,
  52040, 54448, 53320, 52540, 52044, 54528, 53324, 52684, 52048, 54608, 53328, 52768, 52052, 54688, 53332, 52852, 52056, 54768, 53336, 52860, 52060, 54848, 53340, 52380,
  52112, 54292, 53392, 52752, 52116, 54372, 53396, 52836, 52120, 54452, 53400, 52680, 52124, 54532, 53404, 52764, 52128, 54612, 53408, 52848, 52132, 54692, 53412, 52780,
  52136, 54772, 53416, 52376, 52140, 54852, 53420, 52460, 52192, 54296, 53472, 53712, 52196, 54376, 53476, 53796, 52200, 54456, 53480, 53804, 52204, 54536, 53484, 53964,
  52208, 54616, 53488, 54048, 52212, 54696, 53492, 54132, 52216, 54776, 53496, 54140, 52220, 54856, 53500, 53660, 52272, 54300, 53552, 53792, 52276, 54380, 53556, 53724,
  52280, 54460, 53560, 53960, 52284, 54540, 53564, 54044, 52288, 54620, 53568, 54128, 52292, 54700, 53572, 54060, 52296, 54780, 53576, 53656, 52300, 54860, 53580, 53740,
  46464, 82016, 53736, 38400, 46752, 82304, 53652, 38720, 47040, 98496, 54124, 33408, 47328, 98784, 53952, 33408, 47616, 99072, 52532, 33408, 47616, 99360, 53956, 33408,
  47616, 99648, 52700, 33408, 47616, 47904, 53644, 33408, 63808, 47904, 53980, 33408, 64096, 47904, 54120, 33408, 64384, 47904, 52448, 33408, 64672, 47904, 52536, 33408,
  64960, 47904, 53732, 33408, 81152, 47904, 54040, 33408, 81440, 47904, 53820, 33408, 81728, 47904, 52844, 33408, 46480, 82032, 52592, 38416, 46768, 82320, 52596, 38736,
  47056, 98512, 52600, 33424, 47344, 98800, 52604, 33424, 47632, 99088, 52608, 33424, 47632, 99376, 52612, 33424, 47632, 99664, 52616, 33424, 47632, 47920, 52620, 33424,
  63824, 47920, 52912, 33424, 64112, 47920, 52916, 33424, 64400, 47920, 52920, 33424, 64688, 47920, 52924, 33424, 64976, 47920, 52928, 33424, 81168, 47920, 52932, 33424,
  81456, 47920, 52936, 33424, 81744, 47920, 52940, 33424, 46496, 82048, 51312, 38432, 46784, 82336, 51316, 38752, 47072, 98528, 51320, 33440, 47360, 98816, 51324, 33440,
  47648, 99104, 51328, 33440, 47648, 99392, 51332, 33440, 47648, 99680, 51336, 33440, 47648, 47936, 51340, 33440, 63840, 47936, 51632, 33440, 64128, 47936, 51636, 33440,
  64416, 47936, 51640, 33440, 64704, 47936, 51644, 33440, 64992, 47936, 51648, 33440, 81184, 47936, 51652, 33440, 81472, 47936, 51656, 33440, 81760, 47936, 51660, 33440,
  46512, 82064, 53872, 38448, 46800, 82352, 53876, 38768, 47088, 98544, 53880, 33456, 47376, 98832, 53884, 33456, 47664, 99120, 53888, 33456, 47664, 99408, 53892, 33456,
  47664, 99696, 53896, 33456, 47664, 47952, 53900, 33456, 63856, 47952, 54192, 33456, 64144, 47952, 54196, 33456, 64432, 47952, 54200, 33456, 64720, 47952, 54204, 33456,
  65008, 47952, 54208, 33456, 81200, 47952, 54212, 33456, 81488, 47952, 54216, 33456, 81776, 47952, 54220, 33456, 58048, 58912, 55744, 59200, 58912, 58336, 55760, 59488,
  58624, 58336, 55776, 60064, 56896, 57760, 55792, 59200, 57184, 56032, 55808, 59488, 57184, 56320, 55824, 59776, 56032, 56896, 55840, 60064, 56320, 57472, 55856, 58048,
  55744, 56608, 55872, 59488, 57184, 55744, 55888, 59776, 56032, 57472, 55904, 59200, 56608, 57472, 55920, 59488, 58048, 58336, 55936, 59776, 57760, 56320, 55952, 60064,
  58624, 58912, 55968, 58048, 58624, 58048, 55984, 58336, 58064, 58928, 58048, 59216, 58928, 58352, 58064, 59504, 58640, 58352, 58080, 60080, 56912, 57776, 58096, 59216,
  57200, 56048, 58112, 59504, 57200, 56336, 58128, 59792, 56048, 56912, 58144, 60080, 56336, 57488, 58160, 58064, 55760, 56624, 58176, 59504, 57200, 55760, 58192, 59792,
  56048, 57488, 58208, 59216, 56624, 57488, 58224, 59504, 58064, 58352, 58240, 59792, 57776, 56336, 58256, 60080, 58640, 58928, 58272, 58064, 58640, 58064, 58288, 58352,
  58080, 58944, 60352, 59232, 58944, 58368, 60368, 59520, 58656, 58368, 60384, 60096, 56928, 57792, 60400, 59232, 57216, 56064, 60416, 59520, 57216, 56352, 60432, 59808,
  56064, 56928, 60448, 60096, 56352, 57504, 60464, 58080, 55776, 56640, 60480, 59520, 57216, 55776, 60496, 59808, 56064, 57504, 60512, 59232, 56640, 57504, 60528, 59520,
  58080, 58368, 60544, 59808, 57792, 56352, 60560, 60096, 58656, 58944, 60576, 58080, 58656, 58080, 60592, 58368, 58096, 58960, 54880, 59248, 58960, 58384, 54896, 59536,
  58672, 58384, 54912, 60112, 56944, 57808, 54928, 59248, 57232, 56080, 54944, 59536, 57232, 56368, 54960, 59824, 56080, 56944, 54976, 60112, 56368, 57520, 54992, 58096,
  55792, 56656, 55008, 59536, 57232, 55792, 55024, 59824, 56080, 57520, 55040, 59248, 56656, 57520, 55056, 59536, 58096, 58384, 55072, 59824, 57808, 56368, 55088, 60112,
  58672, 58960, 55104, 58096, 58672, 58096, 55120, 58384, 62080, 60640, 63808, 99360, 61792, 60352, 64096, 99648, 62080, 61216, 64384, 46464, 60352, 61504, 64672, 46752,
  59488, 58624, 64960, 47040, 60640, 61504, 64960, 47328, 59200, 60064, 64960, 47616, 61216, 60352, 64960, 67264, 62368, 61504, 81152, 67264, 60928, 62080, 81440, 67264,
  59200, 58336, 81728, 67264, 59776, 58624, 82016, 67264, 61792, 60640, 82304, 67264, 60928, 61792, 98496, 67264, 60928, 62368, 98784, 67264, 58912, 59776, 99072, 67264,
  62096, 60656, 63824, 99376, 61808, 60368, 64112, 99664, 62096, 61232, 64400, 46480, 60368, 61520, 64688, 46768, 59504, 58640, 64976, 47056, 60656, 61520, 64976, 47344,
  59216, 60080, 64976, 47632, 61232, 60368, 64976, 67280, 62384, 61520, 81168, 67280, 60944, 62096, 81456, 67280, 59216, 58352, 81744, 67280, 59792, 58640, 82032, 67280,
  61808, 60656, 82320, 67280, 60944, 61808, 98512, 67280, 60944, 62384, 98800, 67280, 58928, 59792, 99088, 67280, 62112, 60672, 63840, 99392, 61824, 60384, 64128, 99680,
  62112, 61248, 64416, 46496, 60384, 61536, 64704, 46784, 59520, 58656, 64992, 47072, 60672, 61536, 64992, 47360, 59232, 60096, 64992, 47648, 61248, 60384, 64992, 67296,
  62400, 61536, 81184, 67296, 60960, 62112, 81472, 67296, 59232, 58368, 81760, 67296, 59808, 58656, 82048, 67296, 61824, 60672, 82336, 67296, 60960, 61824, 98528, 67296,
  60960, 62400, 98816, 67296, 58944, 59808, 99104, 67296, 62128, 60688, 63856, 99408, 61840, 60400, 64144, 99696, 62128, 61264, 64432, 46512, 60400, 61552, 64720, 46800,
  59536, 58672, 65008, 47088, 60688, 61552, 65008, 47376, 59248, 60112, 65008, 47664, 61264, 60400, 65008, 67312, 62416, 61552, 81200, 67312, 60976, 62128, 81488, 67312,
  59248, 58384, 81776, 67312, 59824, 58672, 82064, 67312, 61840, 60688, 82352, 67312, 60976, 61840, 98544, 67312, 60976, 62416, 98832, 67312, 58960, 59824, 99120, 67312,
  69056, 71616, 70336, 68496, 69060, 71696, 70340, 68580, 69064, 71776, 70344, 68588, 69068, 71856, 70348, 68748, 69072, 71936, 70352, 68832, 69076, 72016, 70356, 68916,
  69080, 72096, 70360, 68924, 69084, 72176, 70364, 68444, 69136, 71620, 70416, 68576, 69140, 71700, 70420, 68508, 69144, 71780, 70424, 68744, 69148, 71860, 70428, 68828,
  69152, 71940, 70432, 68912, 69156, 72020, 70436, 68844, 69160, 72100, 70440, 68440, 69164, 72180, 70444, 68524, 69216, 71624, 70496, 69708, 69220, 71704, 70500, 69788,
  69224, 71784, 70504, 69784, 69228, 71864, 70508, 68764, 69232, 71944, 70512, 68516, 69236, 72024, 70516, 68596, 69240, 72104, 70520, 68740, 69244, 72184, 70524, 68904,
  69296, 71628, 70576, 68428, 69300, 71708, 70580, 68436, 69304, 71788, 70584, 68900, 69308, 71868, 70588, 68908, 69312, 71948, 70592, 69776, 69316, 72028, 70596, 68604,
  69320, 72108, 70600, 69868, 69324, 72188, 70604, 69704, 69376, 71632, 70656, 70016, 69380, 71712, 70660, 70100, 69384, 71792, 70664, 69884, 69388, 71872, 70668, 70028,
  69392, 71952, 70672, 70112, 69396, 72032, 70676, 70196, 69400, 72112, 70680, 70204, 69404, 72192, 70684, 69724, 69456, 71636, 70736, 70096, 69460, 71716, 70740, 70180,
  69464, 71796, 70744, 70024, 69468, 71876, 70748, 70108, 69472, 71956, 70752, 70192, 69476, 72036, 70756, 70124, 69480, 72116, 70760, 69720, 69484, 72196, 70764, 69804,
  69536, 71640, 70816, 71056, 69540, 71720, 70820, 71140, 69544, 71800, 70824, 71148, 69548, 71880, 70828, 71308, 69552, 71960, 70832, 71392, 69556, 72040, 70836, 71476,
  69560, 72120, 70840, 71484, 69564, 72200, 70844, 71004, 69616, 71644, 70896, 71136, 69620, 71724, 70900, 71068, 69624, 71804, 70904, 71304, 69628, 71884, 70908, 71388,
  69632, 71964, 70912, 71472, 69636, 72044, 70916, 71404, 69640, 72124, 70920, 71000, 69644, 72204, 70924, 71084, 63812, 99364, 71080, 55744, 64100, 99652, 70996, 56064,
  64388, 46468, 71468, 50752, 64676, 46756, 71296, 50752, 64964, 47044, 69876, 50752, 64964, 47332, 71300, 50752, 64964, 47620, 70044, 50752, 64964, 65248, 70988, 50752,
  81156, 65248, 71324, 50752, 81444, 65248, 71464, 50752, 81732, 65248, 69792, 50752, 82020, 65248, 69880, 50752, 82308, 65248, 71076, 50752, 98500, 65248, 71384, 50752,
  98788, 65248, 71164, 50752, 99076, 65248, 70188, 50752, 63828, 99380, 69936, 55760, 64116, 99668, 69940, 56080, 64404, 46484, 69944, 50768, 64692, 46772, 69948, 50768,
  64980, 47060, 69952, 50768, 64980, 47348, 69956, 50768, 64980, 47636, 69960, 50768, 64980, 65264, 69964, 50768, 81172, 65264, 70256, 50768, 81460, 65264, 70260, 50768,
  81748, 65264, 70264, 50768, 82036, 65264, 70268, 50768, 82324, 65264, 70272, 50768, 98516, 65264, 70276, 50768, 98804, 65264, 70280, 50768, 99092, 65264, 70284, 50768,
  63844, 99396, 68656, 55776, 64132, 99684, 68660, 56096, 64420, 46500, 68664, 50784, 64708, 46788, 68668, 50784, 64996, 47076, 68672, 50784, 64996, 47364, 68676, 50784,
  64996, 47652, 68680, 50784, 64996, 65280, 68684, 50784, 81188, 65280, 68976, 50784, 81476, 65280, 68980, 50784, 81764, 65280, 68984, 50784, 82052, 65280, 68988, 50784,
  82340, 65280, 68992, 50784, 98532, 65280, 68996, 50784, 98820, 65280, 69000, 50784, 99108, 65280, 69004, 50784, 63860, 99412, 71216, 55792, 64148, 99700, 71220, 56112,
  64436, 46516, 71224, 50800, 64724, 46804, 71228, 50800, 65012, 47092, 71232, 50800, 65012, 47380, 71236, 50800, 65012, 47668, 71240, 50800, 65012, 65296, 71244, 50800,
  81204, 65296, 71536, 50800, 81492, 65296, 71540, 50800, 81780, 65296, 71544, 50800, 82068, 65296, 71548, 50800, 82356, 65296, 71552, 50800, 98548, 65296, 71556, 50800,
  98836, 65296, 71560, 50800, 99124, 65296, 71564, 50800, 75392, 76256, 73088, 76544, 76256, 75680, 73104, 76832, 75968, 75680, 73120, 77408, 74240, 75104, 73136, 76544,
  74528, 73376, 73152, 76832, 74528, 73664, 73168, 77120, 73376, 74240, 73184, 77408, 73664, 74816, 73200, 75392, 73088, 73952, 73216, 76832, 74528, 73088, 73232, 77120,
  73376, 74816, 73248, 76544, 73952, 74816, 73264, 76832, 75392, 75680, 73280, 77120, 75104, 73664, 73296, 77408, 75968, 76256, 73312, 75392, 75968, 75392, 73328, 75680,
  75408, 76272, 75392, 76560, 76272, 75696, 75408, 76848, 75984, 75696, 75424, 77424, 74256, 75120, 75440, 76560, 74544, 73392, 75456, 76848, 74544, 73680, 75472, 77136,
  73392, 74256, 75488, 77424, 73680, 74832, 75504, 75408, 73104, 73968, 75520, 76848, 74544, 73104, 75536, 77136, 73392, 74832, 75552, 76560, 73968, 74832, 75568, 76848,
  75408, 75696, 75584, 77136, 75120, 73680, 75600, 77424, 75984, 76272, 75616, 75408, 75984, 75408, 75632, 75696, 75424, 76288, 77696, 76576, 76288, 75712, 77712, 76864,
  76000, 75712, 77728, 77440, 74272, 75136, 77744, 76576, 74560, 73408, 77760, 76864, 74560, 73696, 77776, 77152, 73408, 74272, 77792, 77440, 73696, 74848, 77808, 75424,
  73120, 73984, 77824, 76864, 74560, 73120, 77840, 77152, 73408, 74848, 77856, 76576, 73984, 74848, 77872, 76864, 75424, 75712, 77888, 77152, 75136, 73696, 77904, 77440,
  76000, 76288, 77920, 75424, 76000, 75424, 77936, 75712, 75440, 76304, 72224, 76592, 76304, 75728, 72240, 76880, 76016, 75728, 72256, 77456, 74288, 75152, 72272, 76592,
  74576, 73424, 72288, 76880, 74576, 73712, 72304, 77168, 73424, 74288, 72320, 77456, 73712, 74864, 72336, 75440, 73136, 74000, 72352, 76880, 74576, 73136, 72368, 77168,
  73424, 74864, 72384, 76592, 74000, 74864, 72400, 76880, 75440, 75728, 72416, 77168, 75152, 73712, 72432, 77456, 76016, 76304, 72448, 75440, 76016, 75440, 72464, 75728,
  79424, 77984, 81152, 47328, 79136, 77696, 81440, 47616, 79424, 78560, 81728, 63808, 77696, 78848, 82016, 64096, 76832, 75968, 82304, 64384, 77984, 78848, 82304, 64672,
  76544, 77408, 82304, 64960, 78560, 77696, 82304, 84608, 79712, 78848, 98496, 84608, 78272, 79424, 98784, 84608, 76544, 75680, 99072, 84608, 77120, 75968, 99360, 84608,
  79136, 77984, 99648, 84608, 78272, 79136, 46464, 84608, 78272, 79712, 46752, 84608, 76256, 77120, 47040, 84608, 79440, 78000, 81168, 47344, 79152, 77712, 81456, 47632,
  79440, 78576, 81744, 63824, 77712, 78864, 82032, 64112, 76848, 75984, 82320, 64400, 78000, 78864, 82320, 64688, 76560, 77424, 82320, 64976, 78576, 77712, 82320, 84624,
  79728, 78864, 98512, 84624, 78288, 79440, 98800, 84624, 76560, 75696, 99088, 84624, 77136, 75984, 99376, 84624, 79152, 78000, 99664, 84624, 78288, 79152, 46480, 84624,
  78288, 79728, 46768, 84624, 76272, 77136, 47056, 84624, 79456, 78016, 81184, 47360, 79168, 77728, 81472, 47648, 79456, 78592, 81760, 63840, 77728, 78880, 82048, 64128,
  76864, 76000, 82336, 64416, 78016, 78880, 82336, 64704, 76576, 77440, 82336, 64992, 78592, 77728, 82336, 84640, 79744, 78880, 98528, 84640, 78304, 79456, 98816, 84640,
  76576, 75712, 99104, 84640, 77152, 76000, 99392, 84640, 79168, 78016, 99680, 84640, 78304, 79168, 46496, 84640, 78304, 79744, 46784, 84640, 76288, 77152, 47072, 84640,
  79472, 78032, 81200, 47376, 79184, 77744, 81488, 47664, 79472, 78608, 81776, 63856, 77744, 78896, 82064, 64144, 76880, 76016, 82352, 64432, 78032, 78896, 82352, 64720,
  76592, 77456, 82352, 65008, 78608, 77744, 82352, 84656, 79760, 78896, 98544, 84656, 78320, 79472, 98832, 84656, 76592, 75728, 99120, 84656, 77168, 76016, 99408, 84656,
  79184, 78032, 99696, 84656, 78320, 79184, 46512, 84656, 78320, 79760, 46800, 84656, 76304, 77168, 47088, 84656, 86400, 88960, 87680, 85840, 86404, 89040, 87684, 85924,
  86408, 89120, 87688, 85932, 86412, 89200, 87692, 86092, 86416, 89280, 87696, 86176, 86420, 89360, 87700, 86260, 86424, 89440, 87704, 86268, 86428, 89520, 87708, 85788,
  86480, 88964, 87760, 85920, 86484, 89044, 87764, 85852, 86488, 89124, 87768, 86088, 86492, 89204, 87772, 86172, 86496, 89284, 87776, 86256, 86500, 89364, 87780, 86188,
  86504, 89444, 87784, 85784, 86508, 89524, 87788, 85868, 86560, 88968, 87840, 87052, 86564, 89048, 87844, 87132, 86568, 89128, 87848, 87128, 86572, 89208, 87852, 86108,
  86576, 89288, 87856, 85860, 86580, 89368, 87860, 85940, 86584, 89448, 87864, 86084, 86588, 89528, 87868, 86248, 86640, 88972, 87920, 85772, 86644, 89052, 87924, 85780,
  86648, 89132, 87928, 86244, 86652, 89212, 87932, 86252, 86656, 89292, 87936, 87120, 86660, 89372, 87940, 85948, 86664, 89452, 87944, 87212, 86668, 89532, 87948, 87048,
  86720, 88976, 88000, 87360, 86724, 89056, 88004, 87444, 86728, 89136, 88008, 87228, 86732, 89216, 88012, 87372, 86736, 89296, 88016, 87456, 86740, 89376, 88020, 87540,
  86744, 89456, 88024, 87548, 86748, 89536, 88028, 87068, 86800, 88980, 88080, 87440, 86804, 89060, 88084, 87524, 86808, 89140, 88088, 87368, 86812, 89220, 88092, 87452,
  86816, 89300, 88096, 87536, 86820, 89380, 88100, 87468, 86824, 89460, 88104, 87064, 86828, 89540, 88108, 87148, 86880, 88984, 88160, 88400, 86884, 89064, 88164, 88484,
  86888, 89144, 88168, 88492, 86892, 89224, 88172, 88652, 86896, 89304, 88176, 88736, 86900, 89384, 88180, 88820, 86904, 89464, 88184, 88828, 86908, 89544, 88188, 88348,
  86960, 88988, 88240, 88480, 86964, 89068, 88244, 88412, 86968, 89148, 88248, 88648, 86972, 89228, 88252, 88732, 86976, 89308, 88256, 88816, 86980, 89388, 88260, 88748,
  86984, 89468, 88264, 88344, 86988, 89548, 88268, 88428, 81160, 47336, 88424, 73088, 81448, 47624, 88340, 73408, 81736, 63816, 88812, 68096, 82024, 64104, 88640, 68096,
  82312, 64392, 87220, 68096, 82312, 64680, 88644, 68096, 82312, 64968, 87388, 68096, 82312, 82592, 88332, 68096, 98504, 82592, 88668, 68096, 98792, 82592, 88808, 68096,
  99080, 82592, 87136, 68096, 99368, 82592, 87224, 68096, 99656, 82592, 88420, 68096, 46472, 82592, 88728, 68096, 46760, 82592, 88508, 68096, 47048, 82592, 87532, 68096,
  81176, 47352, 87280, 73104, 81464, 47640, 87284, 73424, 81752, 63832, 87288, 68112, 82040, 64120, 87292, 68112, 82328, 64408, 87296, 68112, 82328, 64696, 87300, 68112,
  82328, 64984, 87304, 68112, 82328, 82608, 87308, 68112, 98520, 82608, 87600, 68112, 98808, 82608, 87604, 68112, 99096, 82608, 87608, 68112, 99384, 82608, 87612, 68112,
  99672, 82608, 87616, 68112, 46488, 82608, 87620, 68112, 46776, 82608, 87624, 68112, 47064, 82608, 87628, 68112, 81192, 47368, 86000, 73120, 81480, 47656, 86004, 73440,
  81768, 63848, 86008, 68128, 82056, 64136, 86012, 68128, 82344, 64424, 86016, 68128, 82344, 64712, 86020, 68128, 82344, 65000, 86024, 68128, 82344, 82624, 86028, 68128,
  98536, 82624, 86320, 68128, 98824, 82624, 86324, 68128, 99112, 82624, 86328, 68128, 99400, 82624, 86332, 68128, 99688, 82624, 86336, 68128, 46504, 82624, 86340, 68128,
  46792, 82624, 86344, 68128, 47080, 82624, 86348, 68128, 81208, 47384, 88560, 73136, 81496, 47672, 88564, 73456, 81784, 63864, 88568, 68144, 82072, 64152, 88572, 68144,
  82360, 64440, 88576, 68144, 82360, 64728, 88580, 68144, 82360, 65016, 88584, 68144, 82360, 82640, 88588, 68144, 98552, 82640, 88880, 68144, 98840, 82640, 88884, 68144,
  99128, 82640, 88888, 68144, 99416, 82640, 88892, 68144, 99704, 82640, 88896, 68144, 46520, 82640, 88900, 68144, 46808, 82640, 88904, 68144, 47096, 82640, 88908, 68144,
  92736, 93600, 90432, 93888, 93600, 93024, 90448, 94176, 93312, 93024, 90464, 94752, 91584, 92448, 90480, 93888, 91872, 90720, 90496, 94176, 91872, 91008, 90512, 94464,
  90720, 91584, 90528, 94752, 91008, 92160, 90544, 92736, 90432, 91296, 90560, 94176, 91872, 90432, 90576, 94464, 90720, 92160, 90592, 93888, 91296, 92160, 90608, 94176,
  92736, 93024, 90624, 94464, 92448, 91008, 90640, 94752, 93312, 93600, 90656, 92736, 93312, 92736, 90672, 93024, 92752, 93616, 92736, 93904, 93616, 93040, 92752, 94192,
  93328, 93040, 92768, 94768, 91600, 92464, 92784, 93904, 91888, 90736, 92800, 94192, 91888, 91024, 92816, 94480, 90736, 91600, 92832, 94768, 91024, 92176, 92848, 92752,
  90448, 91312, 92864, 94192, 91888, 90448, 92880, 94480, 90736, 92176, 92896, 93904, 91312, 92176, 92912, 94192, 92752, 93040, 92928, 94480, 92464, 91024, 92944, 94768,
  93328, 93616, 92960, 92752, 93328, 92752, 92976, 93040, 92768, 93632, 95040, 93920, 93632, 93056, 95056, 94208, 93344, 93056, 95072, 94784, 91616, 92480, 95088, 93920,
  91904, 90752, 95104, 94208, 91904, 91040, 95120, 94496, 90752, 91616, 95136, 94784, 91040, 92192, 95152, 92768, 90464, 91328, 95168, 94208, 91904, 90464, 95184, 94496,
  90752, 92192, 95200, 93920, 91328, 92192, 95216, 94208, 92768, 93056, 95232, 94496, 92480, 91040, 95248, 94784, 93344, 93632, 95264, 92768, 93344, 92768, 95280, 93056,
  92784, 93648, 89568, 93936, 93648, 93072, 89584, 94224, 93360, 93072, 89600, 94800, 91632, 92496, 89616, 93936, 91920, 90768, 89632, 94224, 91920, 91056, 89648, 94512,
  90768, 91632, 89664, 94800, 91056, 92208, 89680, 92784, 90480, 91344, 89696, 94224, 91920, 90480, 89712, 94512, 90768, 92208, 89728, 93936, 91344, 92208, 89744, 94224,
  92784, 93072, 89760, 94512, 92496, 91056, 89776, 94800, 93360, 93648, 89792, 92784, 93360, 92784, 89808, 93072, 96768, 95328, 98496, 64672, 96480, 95040, 98784, 64960,
  96768, 95904, 99072, 81152, 95040, 96192, 99360, 81440, 94176, 93312, 99648, 81728, 95328, 96192, 99648, 82016, 93888, 94752, 99648, 82304, 95904, 95040, 99648, 101952,
  97056, 96192, 46464, 101952, 95616, 96768, 46752, 101952, 93888, 93024, 47040, 101952, 94464, 93312, 47328, 101952, 96480, 95328, 47616, 101952, 95616, 96480, 63808, 101952,
  95616, 97056, 64096, 101952, 93600, 94464, 64384, 101952, 96784, 95344, 98512, 64688, 96496, 95056, 98800, 64976, 96784, 95920, 99088, 81168, 95056, 96208, 99376, 81456,
  94192, 93328, 99664, 81744, 95344, 96208, 99664, 82032, 93904, 94768, 99664, 82320, 95920, 95056, 99664, 101968, 97072, 96208, 46480, 101968, 95632, 96784, 46768, 101968,
  93904, 93040, 47056, 101968, 94480, 93328, 47344, 101968, 96496, 95344, 47632, 101968, 95632, 96496, 63824, 101968, 95632, 97072, 64112, 101968, 93616, 94480, 64400, 101968,
  96800, 95360, 98528, 64704, 96512, 95072, 98816, 64992, 96800, 95936, 99104, 81184, 95072, 96224, 99392, 81472, 94208, 93344, 99680, 81760, 95360, 96224, 99680, 82048,
  93920, 94784, 99680, 82336, 95936, 95072, 99680, 101984, 97088, 96224, 46496, 101984, 95648, 96800, 46784, 101984, 93920, 93056, 47072, 101984, 94496, 93344, 47360, 101984,
  96512, 95360, 47648, 101984, 95648, 96512, 63840, 101984, 95648, 97088, 64128, 101984, 93632, 94496, 64416, 101984, 96816, 95376, 98544, 64720, 96528, 95088, 98832, 65008,
  96816, 95952, 99120, 81200, 95088, 96240, 99408, 81488, 94224, 93360, 99696, 81776, 95376, 96240, 99696, 82064, 93936, 94800, 99696, 82352, 95952, 95088, 99696, 102000,
  97104, 96240, 46512, 102000, 95664, 96816, 46800, 102000, 93936, 93072, 47088, 102000, 94512, 93360, 47376, 102000, 96528, 95376, 47664, 102000, 95664, 96528, 63856, 102000,
  95664, 97104, 64144, 102000, 93648, 94512, 64432, 102000, 103744, 106304, 105024, 103184, 103748, 106384, 105028, 103268, 103752, 106464, 105032, 103276, 103756, 106544, 105036, 103436,
  103760, 106624, 105040, 103520, 103764, 106704, 105044, 103604, 103768, 106784, 105048, 103612, 103772, 106864, 105052, 103132, 103824, 106308, 105104, 103264, 103828, 106388, 105108, 103196,
  103832, 106468, 105112, 103432, 103836, 106548, 105116, 103516, 103840, 106628, 105120, 103600, 103844, 106708, 105124, 103532, 103848, 106788, 105128, 103128, 103852, 106868, 105132, 103212,
  103904, 106312, 105184, 104396, 103908, 106392, 105188, 104476, 103912, 106472, 105192, 104472, 103916, 106552, 105196, 103452, 103920, 106632, 105200, 103204, 103924, 106712, 105204, 103284,
  103928, 106792, 105208, 103428, 103932, 106872, 105212, 103592, 103984, 106316, 105264, 103116, 103988, 106396, 105268, 103124, 103992, 106476, 105272, 103588, 103996, 106556, 105276, 103596,
  104000, 106636, 105280, 104464, 104004, 106716, 105284, 103292, 104008, 106796, 105288, 104556, 104012, 106876, 105292, 104392, 104064, 106320, 105344, 104704, 104068, 106400, 105348, 104788,
  104072, 106480, 105352, 104572, 104076, 106560, 105356, 104716, 104080, 106640, 105360, 104800, 104084, 106720, 105364, 104884, 104088, 106800, 105368, 104892, 104092, 106880, 105372, 104412,
  104144, 106324, 105424, 104784, 104148, 106404, 105428, 104868, 104152, 106484, 105432, 104712, 104156, 106564, 105436, 104796, 104160, 106644, 105440, 104880, 104164, 106724, 105444, 104812,
  104168, 106804, 105448, 104408, 104172, 106884, 105452, 104492, 104224, 106328, 105504, 105744, 104228, 106408, 105508, 105828, 104232, 106488, 105512, 105836, 104236, 106568, 105516, 105996,
  104240, 106648, 105520, 106080, 104244, 106728, 105524, 106164, 104248, 106808, 105528, 106172, 104252, 106888, 105532, 105692, 104304, 106332, 105584, 105824, 104308, 106412, 105588, 105756,
  104312, 106492, 105592, 105992, 104316, 106572, 105596, 106076, 104320, 106652, 105600, 106160, 104324, 106732, 105604, 106092, 104328, 106812, 105608, 105688, 104332, 106892, 105612, 105772,
  98508, 64684, 105768, 90432, 98796, 64972, 105684, 90752, 99084, 81164, 106156, 85440, 99372, 81452, 105984, 85440, 99660, 81740, 104564, 85440, 99660, 82028, 105988, 85440,
  99660, 82316, 104732, 85440, 99660, 99936, 105676, 85440, 46476, 99936, 106012, 85440, 46764, 99936, 106152, 85440, 47052, 99936, 104480, 85440, 47340, 99936, 104568, 85440,
  47628, 99936, 105764, 85440, 63820, 99936, 106072, 85440, 64108, 99936, 105852, 85440, 64396, 99936, 104876, 85440, 98524, 64700, 104624, 90448, 98812, 64988, 104628, 90768,
  99100, 81180, 104632, 85456, 99388, 81468, 104636, 85456, 99676, 81756, 104640, 85456, 99676, 82044, 104644, 85456, 99676, 82332, 104648, 85456, 99676, 99952, 104652, 85456,
  46492, 99952, 104944, 85456, 46780, 99952, 104948, 85456, 47068, 99952, 104952, 85456, 47356, 99952, 104956, 85456, 47644, 99952, 104960, 85456, 63836, 99952, 104964, 85456,
  64124, 99952, 104968, 85456, 64412, 99952, 104972, 85456, 98540, 64716, 103344, 90464, 98828, 65004, 103348, 90784, 99116, 81196, 103352, 85472, 99404, 81484, 103356, 85472,
  99692, 81772, 103360, 85472, 99692, 82060, 103364, 85472, 99692, 82348, 103368, 85472, 99692, 99968, 103372, 85472, 46508, 99968, 103664, 85472, 46796, 99968, 103668, 85472,
  47084, 99968, 103672, 85472, 47372, 99968, 103676, 85472, 47660, 99968, 103680, 85472, 63852, 99968, 103684, 85472, 64140, 99968, 103688, 85472, 64428, 99968, 103692, 85472,
  98556, 64732, 105904, 90480, 98844, 65020, 105908, 90800, 99132, 81212, 105912, 85488, 99420, 81500, 105916, 85488, 99708, 81788, 105920, 85488, 99708, 82076, 105924, 85488,
  99708, 82364, 105928, 85488, 99708, 99984, 105932, 85488, 46524, 99984, 106224, 85488, 46812, 99984, 106228, 85488, 47100, 99984, 106232, 85488, 47388, 99984, 106236, 85488,
  47676, 99984, 106240, 85488, 63868, 99984, 106244, 85488, 64156, 99984, 106248, 85488, 64444, 99984, 106252, 85488};

__global__ __launch_bounds__(256, 1) __attribute__((aligned(4096)))
void afm_fast_kernel(const int* __restrict__ idx, const unsigned short* __restrict__ table,
                     const int* __restrict__ ii, const int* __restrict__ jj,
                     const unsigned short* __restrict__ W, const unsigned short* __restrict__ bvec,
                     const unsigned short* __restrict__ hvec, const unsigned short* __restrict__ pvec,
                     unsigned short* __restrict__ out, int B, int unused
                     ) {
  __shared__ __attribute__((aligned(16))) char smem[107776];
  const unsigned lbase = (unsigned)(uintptr_t)(__attribute__((address_space(3))) char*)smem;
  unsigned flag;
  asm volatile(
      "s_load_dwordx16 s[8:23], %1, 0x0\n"
      "s_load_dwordx4 s[24:27], %1, 0x40\n"
      "s_getpc_b64 s[56:57]\n"
      "s_add_u32 s96, s56, afm_tab@rel32@lo+4\n"
      "s_addc_u32 s97, s57, afm_tab@rel32@hi+12\n"
      "v_lshrrev_b32 v254, 6, %3\n"
      "v_and_b32 v255, 63, %3\n"
      "s_bfe_u32 s44, %2, 0x20003\n"
      "v_mad_u32_u24 v255, v254, 10, v255\n"
      "s_mov_b64 exec, 0x3ff\n"
      "v_lshl_add_u32 v255, v255, 2, s44\n"
      "v_lshlrev_b32 v254, 7, v255\n"
      "v_min_u32 v253, 16256, v254\n"
      "global_load_dword v254, v254, s[56:57] offset:1536\n"
      "global_load_dword v253, v253, s[96:97]\n"
      "s_mov_b64 exec, -1\n"
      "s_mov_b32 s41, 0\n"
      "s_mov_b32 s42, 0\n"
      "s_mov_b32 s58, 0\n"
      "s_mov_b32 s35, 0\n"
      "0:\n"
      "v_lshrrev_b32 v2, 6, %3\n"
      "v_bfe_u32 v4, %3, 4, 2\n"
      "v_and_b32 v3, 15, %3\n"
      "v_readfirstlane_b32 s53, v2\n"
      "v_mul_u32_u24 v10, 28, v4\n"
      "v_mad_u32_u24 v11, v4, 24, 4\n"
      "v_min_u32 v10, v10, v11\n"
      "s_and_b32 s50, %2, 7\n"
      "s_bfe_u32 s51, %2, 0x50003\n"
      "s_andn2_b32 s34, %2, 255\n"
      "s_lshl_b32 s50, s50, 5\n"
      "s_add_u32 s34, s34, s51\n"
      "s_add_u32 s34, s34, s50\n"
      "s_lshl2_add_u32 s34, s34, s53\n"
      "s_mul_i32 s50, s34, 104\n"
      "s_mov_b32 s33, s34\n"
      "v_add_u32 v10, s50, v10\n"
      "v_lshlrev_b32 v13, 4, v3\n"
      "v_lshlrev_b32 v225, 3, %3\n"
      "v_min_u32 v225, 1292, v225\n"
      "v_lshl_or_b32 v80, v4, 9, v13\n"
      "v_and_b32 v94, 0x400, v80\n"
      "v_lshl_add_u32 v80, v2, 11, v80\n"
      "v_add_u32 v81, 8192, v80\n"
      "v_add_u32 v82, 16384, v80\n"
      "v_add_u32 v83, 24576, v80\n"
      "s_lshl2_add_u32 s44, s53, 37888\n"
      "v_mov_b32 v2, s44\n"
      "v_mov_b32 v5, 0\n"
      "v_mov_b32 v1, 0x7f000000\n"
      "s_cmp_lg_u32 s35, 0\n"
      "s_cbranch_scc1 2f\n"
      "ds_write2_b32 v2, v1, v5 offset0:0 offset1:4\n"
      "ds_write_b32 v2, v5 offset:32\n"
      "2:\n"
      "s_mul_i32 s38, s53, 17344\n"
      "s_add_u32 s38, s38, 38400\n"
      "s_add_u32 s54, s38, 12672\n"
      "s_mul_i32 s61, s53, 17088\n"
      "s_add_u32 s61, s61, 45312\n"
      "s_mov_b32 s43, 0xffff0000\n"
      "s_movk_i32 s36, 0x400\n"
      "s_mov_b32 s56, 0\n"
      "s_mov_b32 s99, 0x3f400000\n"
      "s_mov_b32 s40, 0\n"
      "s_or_b32 s60, s41, s58\n"
      "s_mov_b32 s55, 0\n"
      "v_cmp_eq_u32 vcc, 1, v4\n"
      "v_mov_b32 v222, 0x3280\n"
      "v_mov_b32 v145, 0\n"
      "v_cndmask_b32 v144, 0, v222, vcc\n"
      "s_mov_b32 s46, 0\n"
      "s_mov_b32 s47, 0xc7ac4c39\n"
      "v_lshlrev_b32 v223, 4, v4\n"
      "v_lshrrev_b64 v[146:147], v223, s[46:47]\n"
      "s_lshl_b32 s48, s53, 4\n"
      "v_mov_b32 v147, 0\n"
      "v_and_b32 v146, 0xffff, v146\n"
      "v_mov_b32 v215, 0x4838aa00\n"
      "s_xor_b32 s46, s48, 0\n"
      "v_lshlrev_b64 v[128:129], s46, v[144:145]\n"
      "v_lshlrev_b64 v[136:137], s46, v[146:147]\n"
      "s_xor_b32 s46, s48, 16\n"
      "v_lshlrev_b64 v[130:131], s46, v[144:145]\n"
      "v_lshlrev_b64 v[138:139], s46, v[146:147]\n"
      "s_xor_b32 s46, s48, 32\n"
      "v_lshlrev_b64 v[132:133], s46, v[144:145]\n"
      "v_lshlrev_b64 v[140:141], s46, v[146:147]\n"
      "s_xor_b32 s46, s48, 48\n"
      "v_lshlrev_b64 v[134:135], s46, v[144:145]\n"
      "v_lshlrev_b64 v[142:143], s46, v[146:147]\n"
      "v_mov_b32 v222, 0x3a80\n"
      "v_and_b32 v227, 3, %3\n"
      "v_bfe_u32 v223, %3, 2, 2\n"
      "v_cmp_eq_u32 vcc, v227, v4\n"
      "v_mov_b64 v[84:85], 0\n"
      "v_mov_b64 v[88:89], 0\n"
      "v_cndmask_b32 v84, 0, v222, vcc\n"
      "v_lshlrev_b32 v222, 4, v223\n"
      "v_lshlrev_b64 v[86:87], v222, v[84:85]\n"
      "s_mul_i32 s46, s53, 2304\n"
      "v_mul_u32_u24 v222, 2304, v4\n"
      "v_mov_b32 v84, 0\n"
      "s_mov_b32 m0, s46\n"
      "s_movk_i32 s47, 272\n"
      "v_lshl_add_u32 v222, v223, 6, v222\n"
      "v_mad_u32_u24 v14, v227, s47, v222\n"
      "v_mul_u32_u24 v216, 272, v4\n"
      "v_add_u32 v213, s38, v13\n"
      "v_lshlrev_b32 v222, 3, v4\n"
      "s_mov_b32 s47, 0x09090005\n"
      "v_bfe_u32 v222, s47, v222, 8\n"
      "v_lshl_add_u32 v91, v222, 6, v213\n"
      "v_lshlrev_b32 v224, 4, v4\n"
      "v_add_u32 v95, s38, v224\n"
      "v_lshl_add_u32 v223, v223, 4, v216\n"
      "v_bfe_u32 v222, %3, 1, 1\n"
      "v_mul_u32_u24 v222, 9344, v222\n"
      "v_add3_u32 v223, v223, v222, s46\n"
      "v_and_b32 v222, 1, %3\n"
      "v_lshl_add_u32 v216, v222, 3, v223\n"
      "s_lshl_b32 s46, s53, 5\n"
      "s_add_u32 s46, s46, 37376\n"
      "v_and_b32 v222, 8, %3\n"
      "v_and_b32 v223, 7, %3\n"
      "v_lshl_add_u32 v222, v222, 4, s46\n"
      "v_lshl_add_u32 v217, v223, 2, v222\n"
      "v_lshlrev_b32 v227, 2, v227\n"
      "v_lshlrev_b32 v5, 5, v3\n"
      "v_mul_u32_u24 v222, 288, v4\n"
      "v_add_u32 v1, v222, v213\n"
      "s_movk_i32 s46, 288\n"
      "v_mul_u32_u24 v222, 80, v3\n"
      "v_add3_u32 v212, v222, v224, s54\n"
      "v_and_b32 v176, 7, %3\n"
      "v_bfe_u32 v177, %3, 3, 1\n"
      "v_mad_u32_u24 v201, v176, s46, v95\n"
      "v_mad_u32_u24 v203, v177, s46, v95\n"
      "v_add3_u32 v178, v176, v177, 1\n"
      "v_and_b32 v178, 7, v178\n"
      "v_mad_u32_u24 v202, v178, s46, v95\n"
      "v_and_b32 v222, 11, %3\n"
      "v_cmp_eq_u32 vcc, 3, v222\n"
      "v_lshrrev_b32 v222, 2, v3\n"
      "v_add_u32 v222, 24, v222\n"
      "v_mad_u32_u24 v222, v222, s46, v95\n"
      "v_mad_u32_u24 v223, v3, s46, v95\n"
      "v_add_u32 v224, 2304, v223\n"
      "v_cndmask_b32 v214, v223, v222, vcc\n"
      "v_cndmask_b32 v220, v224, v222, vcc\n"
      "v_xor_b32 v223, 1, v177\n"
      "v_lshl_add_u32 v223, v223, 4, v176\n"
      "v_mad_u32_u24 v223, v223, s46, v95\n"
      "v_cndmask_b32 v221, v223, v222, vcc\n"
      "s_movk_i32 s48, 320\n"
      "v_mad_u32_u24 v200, v177, s48, v95\n"
      "v_cvt_f32_u32 v223, v225\n"
      "v_fmaak_f32 v223, -2.0, v223, 0x45229000\n"
      "v_sqrt_f32 v223, v223\n"
      "v_lshrrev_b32 v224, 2, v225\n"
      "v_fmaak_f32 v223, -0.5, v223, 0x41cc28f6\n"
      "v_cvt_u32_f32 v208, v223\n"
      "v_add_u32 v224, 1, v224\n"
      "v_sub_u32 v223, 49, v208\n"
      "v_mul_u32_u24 v223, v208, v223\n"
      "v_lshrrev_b32 v223, 1, v223\n"
      "v_sub_u32 v210, v224, v223\n"
      "v_cmp_eq_u32 vcc, 25, v210\n"
      "v_add_u32 v223, 1, v208\n"
      "s_mov_b32 s59, 0\n"
      "s_mov_b32 s57, 0\n"
      "v_cndmask_b32 v209, v208, v223, vcc\n"
      "v_cndmask_b32 v211, v210, v223, vcc\n"
      "v_add_u32 v211, 1, v211\n"
      "s_mul_i32 s46, s53, 3072\n"
      "v_lshl_add_u32 v218, %3, 4, s46\n"
      "s_xor_b32 s46, s53, 1\n"
      "v_lshl_add_u32 v4, s46, 4, v14\n"
      "s_xor_b32 s46, s53, 2\n"
      "v_lshl_add_u32 v222, s46, 4, v14\n"
      "s_xor_b32 s46, s53, 3\n"
      "v_lshl_add_u32 v223, s46, 4, v14\n"
      "s_xor_b32 s46, s53, 0\n"
      "v_lshl_add_u32 v14, s46, 4, v14\n"
      "s_waitcnt lgkmcnt(0)\n"
      "s_cmp_lt_u32 s33, s26\n"
      "s_cbranch_scc0 36f\n"
      "37:\n"
      "global_load_dwordx4 v[6:9], v10, s[8:9] nt\n"
      "global_load_dwordx3 v[10:12], v10, s[8:9] offset:16 nt\n"
      "v_mov_b32 v92, s20\n"
      "v_mov_b32 v93, s21\n"
      "s_mov_b64 exec, 0xffff\n"
      "v_mov_b32 v92, s22\n"
      "v_mov_b32 v93, s23\n"
      "s_lshl_b64 exec, exec, 16\n"
      "v_mov_b32 v92, s18\n"
      "v_mov_b32 v93, s19\n"
      "s_mov_b64 exec, -1\n"
      "v_add_co_u32 v92, vcc, v92, v13\n"
      "v_addc_co_u32 v93, vcc, 0, v93, vcc\n"
      "global_load_dwordx4 v[192:195], v[92:93], off\n"
      "s_cmp_lg_u32 s35, 0\n"
      "s_cbranch_scc1 21f\n"
      "s_barrier\n"
      "21:\n"
      "global_load_dwordx4 v[96:99], v80, s[16:17] offset:0\n"
      "global_load_dwordx4 v[100:103], v80, s[16:17] offset:256\n"
      "global_load_dwordx4 v[104:107], v81, s[16:17] offset:0\n"
      "global_load_dwordx4 v[108:111], v81, s[16:17] offset:256\n"
      "global_load_dwordx4 v[112:115], v82, s[16:17] offset:0\n"
      "global_load_dwordx4 v[116:119], v82, s[16:17] offset:256\n"
      "s_mov_b64 exec, 0\n"
      "s_cmp_eq_u32 s53, 0\n"
      "s_cbranch_scc1 40f\n"
      "s_cmp_eq_u32 s53, 1\n"
      "s_cbranch_scc1 41f\n"
      "s_cmp_eq_u32 s53, 2\n"
      "s_cbranch_scc1 42f\n"
      "s_branch 43f\n"
      "44:\n"
      "s_mov_b64 exec, -1\n"
      "v_lshlrev_b32_e64 v3, 1, s33\n"
      "v_mov_b64 v[176:177], -1\n"
      "v_mov_b64 v[178:179], -1\n"
      "ds_write_b128 v1, v[176:179] offset:8064\n"
      "ds_write_b128 v1, v[176:179] offset:9216\n"
      "ds_write_b128 v1, v[176:179] offset:10368\n"
      "v_mov_b64 v[180:181], 0\n"
      "v_mov_b64 v[182:183], 0\n"
      "ds_write_b128 v1, v[180:183] offset:11520\n"
      "s_waitcnt vmcnt(8)\n"
      "v_lshl_add_u32 v7, v7, 8, v13\n"
      "global_load_dwordx4 v[228:231], v7, s[10:11] nt\n"
      "v_lshl_add_u32 v8, v8, 8, v13\n"
      "global_load_dwordx4 v[232:235], v8, s[10:11] nt\n"
      "s_waitcnt vmcnt(9)\n"
      "v_mov_b32_dpp v6, v12 quad_perm:[0,1,2,3] row_mask:0xc bank_mask:0xf\n"
      "v_lshl_add_u32 v9, v9, 8, v13\n"
      "v_lshl_add_u32 v10, v10, 8, v13\n"
      "v_lshl_add_u32 v11, v11, 8, v13\n"
      "v_lshl_add_u32 v12, v12, 8, v13\n"
      "v_lshl_add_u32 v6, v6, 8, v13\n"
      "v_lshl_add_u32 v8, %3, 2, s61\n"
      "s_waitcnt vmcnt(8)\n"
      "v_mfma_f32_16x16x16_bf16 v[48:51], v[192:193], v[136:137], 0\n"
      "v_max3_f32 v224, |v192|, |v193|, |v195|\n"
      "v_add_u16_sdwa v176, v192, v94 dst_sel:WORD_1 dst_unused:UNUSED_PAD src0_sel:WORD_0 src1_sel:DWORD\n"
      "v_mfma_f32_16x16x16_bf16 v[52:55], v[192:193], v[138:139], 0\n"
      "v_add_u16_sdwa v177, v192, v94 dst_sel:WORD_1 dst_unused:UNUSED_PAD src0_sel:WORD_1 src1_sel:DWORD\n"
      "v_subrev_u32 v224, 0x3d000000, v224\n"
      "v_mfma_f32_16x16x16_bf16 v[56:59], v[192:193], v[140:141], 0\n"
      "v_add_u16_sdwa v178, v193, v94 dst_sel:WORD_1 dst_unused:UNUSED_PAD src0_sel:WORD_0 src1_sel:DWORD\n"
      "v_add_u16_sdwa v179, v193, v94 dst_sel:WORD_1 dst_unused:UNUSED_PAD src0_sel:WORD_1 src1_sel:DWORD\n"
      "v_mfma_f32_16x16x16_bf16 v[60:63], v[192:193], v[142:143], 0\n"
      "v_cmp_gt_u32 vcc, 0x6000000, v224\n"
      "v_add_u16_sdwa v180, v194, v94 dst_sel:WORD_1 dst_unused:UNUSED_PAD src0_sel:WORD_0 src1_sel:DWORD\n"
      "v_mfma_f32_16x16x16_bf16 v[64:67], v[194:195], v[136:137], 0\n"
      "v_add_u16_sdwa v181, v194, v94 dst_sel:WORD_1 dst_unused:UNUSED_PAD src0_sel:WORD_1 src1_sel:DWORD\n"
      "s_and_b32 s44, vcc_lo, 0xffff\n"
      "v_mfma_f32_16x16x16_bf16 v[68:71], v[194:195], v[138:139], 0\n"
      "v_add_u16_sdwa v182, v195, v94 dst_sel:WORD_1 dst_unused:UNUSED_PAD src0_sel:WORD_0 src1_sel:DWORD\n"
      "s_cselect_b32 s44, vcc_hi, 0\n"
      "v_mfma_f32_16x16x16_bf16 v[72:75], v[194:195], v[140:141], 0\n"
      "v_add_u16_sdwa v183, v195, v94 dst_sel:WORD_1 dst_unused:UNUSED_PAD src0_sel:WORD_1 src1_sel:DWORD\n"
      "s_cmp_eq_u32 s44, 0\n"
      "s_cbranch_scc1 22f\n"
      "23:\n"
      "v_mfma_f32_16x16x16_bf16 v[76:79], v[194:195], v[142:143], 0\n"
      "v_cvt_pk_f16_f32 v184, v176, v177\n"
      "v_mfma_f32_16x16x16_bf16 v[16:19], v[192:193], v[128:129], 0\n"
      "v_cvt_pk_f16_f32 v185, v178, v179\n"
      "v_mfma_f32_16x16x16_bf16 v[20:23], v[192:193], v[130:131], 0\n"
      "v_cvt_pk_f16_f32 v186, v180, v181\n"
      "v_mfma_f32_16x16x16_bf16 v[24:27], v[192:193], v[132:133], 0\n"
      "v_cvt_pk_f16_f32 v187, v182, v183\n"
      "v_mfma_f32_16x16x16_bf16 v[28:31], v[192:193], v[134:135], 0\n"
      "ds_write_b128 v91, v[184:187] offset:16512\n"
      "v_mfma_f32_16x16x16_bf16 v[32:35], v[194:195], v[128:129], 0\n"
      "ds_read_b128 a[184:187], v95 offset:17088\n"
      "v_mfma_f32_16x16x16_bf16 v[36:39], v[194:195], v[130:131], 0\n"
      "ds_read_b128 a[188:191], v95 offset:17152\n"
      "v_mfma_f32_16x16x16_bf16 v[40:43], v[194:195], v[132:133], 0\n"
      "ds_read_b128 a[192:195], v95 offset:17216\n"
      "v_mfma_f32_16x16x16_bf16 v[44:47], v[194:195], v[134:135], 0\n"
      "ds_read_b128 a[196:199], v95 offset:17280\n"
      "global_load_dwordx4 v[120:123], v83, s[16:17] offset:0\n"
      "global_load_dwordx4 v[124:127], v83, s[16:17] offset:256\n"
      "s_cmp_lg_u32 s35, 0\n"
      "s_cbranch_scc1 24f\n"
      "s_barrier\n"
      "s_mov_b32 s35, 1\n"
      "24:\n"
      "global_load_dwordx4 v[236:239], v9, s[10:11] nt\n"
      "s_cmp_lg_u32 s58, 0\n"
      "s_cbranch_scc1 70f\n"
      "71:\n"
      "s_waitcnt vmcnt(10)\n"
      "v_mfma_f32_16x16x32_bf16 v[128:131], v[96:99], v[86:89], 0\n"
      "s_waitcnt vmcnt(9)\n"
      "v_mfma_f32_16x16x32_bf16 v[132:135], v[100:103], v[86:89], 0\n"
      "v_mfma_f32_16x16x32_bf16 v[136:139], v[96:99], v[84:87], 0\n"
      "v_mfma_f32_16x16x32_bf16 v[140:143], v[100:103], v[84:87], 0\n"
      "v_max3_f32 v224, |v97|, |v98|, |v103|\n"
      "s_waitcnt vmcnt(8)\n"
      "v_mfma_f32_16x16x32_bf16 v[144:147], v[104:107], v[86:89], 0\n"
      "s_nop 2\n"
      "v_cvt_pk_f16_f32 v164, v128, v132\n"
      "v_cvt_pk_f16_f32 v165, v129, v133\n"
      "v_cvt_pk_f16_f32 v166, v130, v134\n"
      "v_cvt_pk_f16_f32 v167, v131, v135\n"
      "s_waitcnt vmcnt(7)\n"
      "v_mfma_f32_16x16x32_bf16 v[148:151], v[108:111], v[86:89], 0\n"
      "ds_write_addtid_b32 v164 offset:0\n"
      "ds_write_addtid_b32 v165 offset:272\n"
      "ds_write_addtid_b32 v166 offset:544\n"
      "ds_write_addtid_b32 v167 offset:816\n"
      "v_mfma_f32_16x16x32_bf16 v[152:155], v[104:107], v[84:87], 0\n"
      "v_cvt_pk_f16_f32 v168, v136, v140\n"
      "v_cvt_pk_f16_f32 v169, v137, v141\n"
      "v_cvt_pk_f16_f32 v170, v138, v142\n"
      "v_cvt_pk_f16_f32 v171, v139, v143\n"
      "v_mfma_f32_16x16x32_bf16 v[156:159], v[108:111], v[84:87], 0\n"
      "ds_write_addtid_b32 v168 offset:1088\n"
      "ds_write_addtid_b32 v169 offset:1360\n"
      "ds_write_addtid_b32 v170 offset:1632\n"
      "ds_write_addtid_b32 v171 offset:1904\n"
      "s_waitcnt vmcnt(6)\n"
      "v_mfma_f32_16x16x32_bf16 v[128:131], v[112:115], v[86:89], 0\n"
      "v_cvt_pk_f16_f32 v164, v144, v148\n"
      "v_cvt_pk_f16_f32 v165, v145, v149\n"
      "v_cvt_pk_f16_f32 v166, v146, v150\n"
      "v_cvt_pk_f16_f32 v167, v147, v151\n"
      "s_waitcnt vmcnt(5)\n"
      "v_mfma_f32_16x16x32_bf16 v[132:135], v[116:119], v[86:89], 0\n"
      "ds_write_addtid_b32 v164 offset:9344\n"
      "ds_write_addtid_b32 v165 offset:9616\n"
      "ds_write_addtid_b32 v166 offset:9888\n"
      "ds_write_addtid_b32 v167 offset:10160\n"
      "v_mfma_f32_16x16x32_bf16 v[136:139], v[112:115], v[84:87], 0\n"
      "v_cvt_pk_f16_f32 v168, v152, v156\n"
      "v_cvt_pk_f16_f32 v169, v153, v157\n"
      "v_cvt_pk_f16_f32 v170, v154, v158\n"
      "v_cvt_pk_f16_f32 v171, v155, v159\n"
      "v_mfma_f32_16x16x32_bf16 v[140:143], v[116:119], v[84:87], 0\n"
      "ds_write_addtid_b32 v168 offset:10432\n"
      "ds_write_addtid_b32 v169 offset:10704\n"
      "ds_write_addtid_b32 v170 offset:10976\n"
      "ds_write_addtid_b32 v171 offset:11248\n"
      "v_max3_f32 v224, |v112|, |v117|, v224\n"
      "v_cmp_le_f32 vcc, 0x3d000000, v224\n"
      "v_cvt_pk_f16_f32 v164, v128, v132\n"
      "v_cvt_pk_f16_f32 v165, v129, v133\n"
      "v_cvt_pk_f16_f32 v166, v130, v134\n"
      "v_cvt_pk_f16_f32 v167, v131, v135\n"
      "ds_write_addtid_b32 v164 offset:18688\n"
      "ds_write_addtid_b32 v165 offset:18960\n"
      "ds_write_addtid_b32 v166 offset:19232\n"
      "ds_write_addtid_b32 v167 offset:19504\n"
      "v_cvt_pk_f16_f32 v168, v136, v140\n"
      "v_cvt_pk_f16_f32 v169, v137, v141\n"
      "v_cvt_pk_f16_f32 v170, v138, v142\n"
      "v_cvt_pk_f16_f32 v171, v139, v143\n"
      "ds_write_addtid_b32 v168 offset:19776\n"
      "ds_write_addtid_b32 v169 offset:20048\n"
      "ds_write_addtid_b32 v170 offset:20320\n"
      "ds_write_addtid_b32 v171 offset:20592\n"
      "s_bcnt1_i32_b64 s52, vcc\n"
      "s_cmp_ge_u32 s52, 32\n"
      "s_cselect_b32 s52, 13, 0x7f000005\n"
      "ds_write_b32 v2, v2 offset:16\n"
      "global_load_dwordx4 v[204:207], v218, s[96:97] offset:0\n"
      "ds_read_b64_tr_b16 a[152:153], v216 offset:0\n"
      "ds_read_b64_tr_b16 a[154:155], v216 offset:1088\n"
      "ds_read_b64_tr_b16 a[156:157], v216 offset:64\n"
      "ds_read_b64_tr_b16 a[158:159], v216 offset:1152\n"
      "ds_read_b64_tr_b16 a[160:161], v216 offset:128\n"
      "ds_read_b64_tr_b16 a[162:163], v216 offset:1216\n"
      "ds_read_b64_tr_b16 a[164:165], v216 offset:192\n"
      "ds_read_b64_tr_b16 a[166:167], v216 offset:1280\n"
      "s_waitcnt lgkmcnt(6)\n"
      "v_mfma_f32_16x16x32_f16 a[200:203], a[184:187], a[152:155], 0\n"
      "s_waitcnt lgkmcnt(4)\n"
      "v_mfma_f32_16x16x32_f16 a[200:203], a[188:191], a[156:159], a[200:203]\n"
      "v_mov_b32 v7, 0xf149f2ca\n"
      "ds_read_b32 v224, v227 offset:37904\n"
      "s_waitcnt lgkmcnt(3)\n"
      "v_mfma_f32_16x16x32_f16 a[200:203], a[192:195], a[160:163], a[200:203]\n"
      "s_waitcnt lgkmcnt(1)\n"
      "v_mfma_f32_16x16x32_f16 a[200:203], a[196:199], a[164:167], a[200:203]\n"
      "s_waitcnt lgkmcnt(0)\n"
      "v_cmp_eq_u32 vcc, 0, v224\n"
      "s_nop 0\n"
      "s_cmp_lg_u32 vcc_lo, 0\n"
      "s_cbranch_scc0 14f\n"
      "11:\n"
      "ds_read_b32 v224, v227 offset:37904\n"
      "s_waitcnt lgkmcnt(0)\n"
      "v_cmp_eq_u32 vcc, 0, v224\n"
      "s_nop 0\n"
      "s_cmp_lg_u32 vcc_lo, 0\n"
      "s_cbranch_scc1 11b\n"
      "14:\n"
      "ds_read_b128 a[0:3], v14 offset:0\n"
      "ds_read_b128 a[16:19], v4 offset:0\n"
      "ds_read_b128 a[32:35], v222 offset:0\n"
      "ds_read_b128 a[48:51], v223 offset:0\n"
      "ds_read_b128 a[64:67], v14 offset:1088\n"
      "ds_read_b128 a[80:83], v4 offset:1088\n"
      "ds_read_b128 a[96:99], v222 offset:1088\n"
      "ds_read_b128 a[112:115], v223 offset:1088\n"
      "ds_read_b128 a[4:7], v14 offset:9344\n"
      "ds_read_b128 a[20:23], v4 offset:9344\n"
      "ds_read_b128 a[36:39], v222 offset:9344\n"
      "ds_read_b128 a[52:55], v223 offset:9344\n"
      "ds_read_b128 a[68:71], v14 offset:10432\n"
      "ds_read_b128 a[84:87], v4 offset:10432\n"
      "ds_read_b128 a[100:103], v222 offset:10432\n"
      "ds_read_b128 a[116:119], v223 offset:10432\n"
      "ds_read_b128 a[8:11], v14 offset:18688\n"
      "ds_read_b128 a[24:27], v4 offset:18688\n"
      "ds_read_b128 a[40:43], v222 offset:18688\n"
      "ds_read_b128 a[56:59], v223 offset:18688\n"
      "ds_read_b128 a[72:75], v14 offset:19776\n"
      "ds_read_b128 a[88:91], v4 offset:19776\n"
      "ds_read_b128 a[104:107], v222 offset:19776\n"
      "ds_read_b128 a[120:123], v223 offset:19776\n"
      "global_load_dwordx4 v[240:243], v10, s[10:11] nt\n"
      "3:\n"
      "s_waitcnt vmcnt(6)\n"
      "v_sub_u16_sdwa v176, v228, s36 clamp dst_sel:WORD_1 dst_unused:UNUSED_PAD src0_sel:WORD_0 src1_sel:DWORD\n"
      "v_sub_u16_sdwa v177, v228, s36 clamp dst_sel:WORD_1 dst_unused:UNUSED_PAD src0_sel:WORD_1 src1_sel:DWORD\n"
      "v_sub_u16_sdwa v178, v229, s36 clamp dst_sel:WORD_1 dst_unused:UNUSED_PAD src0_sel:WORD_0 src1_sel:DWORD\n"
      "v_sub_u16_sdwa v179, v229, s36 clamp dst_sel:WORD_1 dst_unused:UNUSED_PAD src0_sel:WORD_1 src1_sel:DWORD\n"
      "v_sub_u16_sdwa v180, v230, s36 clamp dst_sel:WORD_1 dst_unused:UNUSED_PAD src0_sel:WORD_0 src1_sel:DWORD\n"
      "v_sub_u16_sdwa v181, v230, s36 clamp dst_sel:WORD_1 dst_unused:UNUSED_PAD src0_sel:WORD_1 src1_sel:DWORD\n"
      "v_sub_u16_sdwa v182, v231, s36 clamp dst_sel:WORD_1 dst_unused:UNUSED_PAD src0_sel:WORD_0 src1_sel:DWORD\n"
      "v_sub_u16_sdwa v183, v231, s36 clamp dst_sel:WORD_1 dst_unused:UNUSED_PAD src0_sel:WORD_1 src1_sel:DWORD\n"
      "v_cvt_pk_f16_f32 v184, v176, v177\n"
      "v_cvt_pk_f16_f32 v185, v178, v179\n"
      "v_cvt_pk_f16_f32 v186, v180, v181\n"
      "v_cvt_pk_f16_f32 v187, v182, v183\n"
      "ds_write_b128 v1, v[184:187] offset:0\n"
      "s_waitcnt vmcnt(5)\n"
      "v_sub_u16_sdwa v144, v232, s36 clamp dst_sel:WORD_1 dst_unused:UNUSED_PAD src0_sel:WORD_0 src1_sel:DWORD\n"
      "v_sub_u16_sdwa v145, v232, s36 clamp dst_sel:WORD_1 dst_unused:UNUSED_PAD src0_sel:WORD_1 src1_sel:DWORD\n"
      "v_sub_u16_sdwa v146, v233, s36 clamp dst_sel:WORD_1 dst_unused:UNUSED_PAD src0_sel:WORD_0 src1_sel:DWORD\n"
      "v_sub_u16_sdwa v147, v233, s36 clamp dst_sel:WORD_1 dst_unused:UNUSED_PAD src0_sel:WORD_1 src1_sel:DWORD\n"
      "v_sub_u16_sdwa v148, v234, s36 clamp dst_sel:WORD_1 dst_unused:UNUSED_PAD src0_sel:WORD_0 src1_sel:DWORD\n"
      "v_sub_u16_sdwa v149, v234, s36 clamp dst_sel:WORD_1 dst_unused:UNUSED_PAD src0_sel:WORD_1 src1_sel:DWORD\n"
      "v_sub_u16_sdwa v150, v235, s36 clamp dst_sel:WORD_1 dst_unused:UNUSED_PAD src0_sel:WORD_0 src1_sel:DWORD\n"
      "v_sub_u16_sdwa v151, v235, s36 clamp dst_sel:WORD_1 dst_unused:UNUSED_PAD src0_sel:WORD_1 src1_sel:DWORD\n"
      "v_cvt_pk_f16_f32 v152, v144, v145\n"
      "v_cvt_pk_f16_f32 v153, v146, v147\n"
      "v_cvt_pk_f16_f32 v154, v148, v149\n"
      "v_cvt_pk_f16_f32 v155, v150, v151\n"
      "ds_write_b128 v1, v[152:155] offset:1152\n"
      "ds_read_b128 v[128:131], v201 offset:0\n"
      "ds_read_b128 v[160:163], v202 offset:0\n"
      "ds_read_b128 v[132:135], v201 offset:64\n"
      "ds_read_b128 v[164:167], v202 offset:64\n"
      "s_or_b32 s44, s55, s58\n"
      "s_cbranch_scc1 74f\n"
      "75:\n"
      "s_waitcnt vmcnt(4)\n"
      "v_mfma_f32_16x16x32_bf16 v[100:103], v[120:123], v[86:89], 0\n"
      "ds_read_b128 v[136:139], v201 offset:128\n"
      "s_waitcnt vmcnt(3)\n"
      "v_mfma_f32_16x16x32_bf16 v[104:107], v[124:127], v[86:89], 0\n"
      "ds_read_b128 v[168:171], v202 offset:128\n"
      "v_mfma_f32_16x16x32_bf16 v[108:111], v[120:123], v[84:87], 0\n"
      "ds_read_b128 v[140:143], v201 offset:192\n"
      "v_mfma_f32_16x16x32_bf16 v[112:115], v[124:127], v[84:87], 0\n"
      "ds_read_b128 v[172:175], v202 offset:192\n"
      "s_waitcnt lgkmcnt(6)\n"
      "v_pk_mul_f16 v96, v128, v160\n"
      "v_pk_mul_f16 v97, v129, v161\n"
      "v_pk_mul_f16 v98, v130, v162\n"
      "v_pk_mul_f16 v99, v131, v163\n"
      "v_cvt_pk_f16_f32 v116, v100, v104\n"
      "v_cvt_pk_f16_f32 v117, v101, v105\n"
      "v_cvt_pk_f16_f32 v118, v102, v106\n"
      "v_cvt_pk_f16_f32 v119, v103, v107\n"
      "ds_write_addtid_b32 v116 offset:28032\n"
      "ds_write_addtid_b32 v117 offset:28304\n"
      "ds_write_addtid_b32 v118 offset:28576\n"
      "ds_write_addtid_b32 v119 offset:28848\n"
      "v_cvt_pk_f16_f32 v184, v108, v112\n"
      "v_cvt_pk_f16_f32 v185, v109, v113\n"
      "v_cvt_pk_f16_f32 v186, v110, v114\n"
      "v_cvt_pk_f16_f32 v187, v111, v115\n"
      "ds_write_addtid_b32 v184 offset:29120\n"
      "ds_write_addtid_b32 v185 offset:29392\n"
      "ds_write_addtid_b32 v186 offset:29664\n"
      "ds_write_addtid_b32 v187 offset:29936\n"
      "ds_write_b32 v2, v2 offset:32\n"
      "31:\n"
      "v_mfma_f32_16x16x32_f16 v[80:83], a[0:3], v[96:99], v[16:19]\n"
      "s_waitcnt lgkmcnt(13)\n"
      "v_pk_mul_f16 v100, v132, v164\n"
      "v_pk_mul_f16 v101, v133, v165\n"
      "v_mfma_f32_16x16x32_f16 v[84:87], a[16:19], v[96:99], v[20:23]\n"
      "v_pk_mul_f16 v102, v134, v166\n"
      "v_pk_mul_f16 v103, v135, v167\n"
      "v_mfma_f32_16x16x32_f16 v[88:91], a[32:35], v[96:99], v[24:27]\n"
      "s_waitcnt vmcnt(2)\n"
      "v_sub_u16_sdwa v176, v236, s36 clamp dst_sel:WORD_1 dst_unused:UNUSED_PAD src0_sel:WORD_0 src1_sel:DWORD\n"
      "v_sub_u16_sdwa v177, v236, s36 clamp dst_sel:WORD_1 dst_unused:UNUSED_PAD src0_sel:WORD_1 src1_sel:DWORD\n"
      "v_mfma_f32_16x16x32_f16 v[92:95], a[48:51], v[96:99], v[28:31]\n"
      "v_sub_u16_sdwa v178, v237, s36 clamp dst_sel:WORD_1 dst_unused:UNUSED_PAD src0_sel:WORD_0 src1_sel:DWORD\n"
      "v_sub_u16_sdwa v179, v237, s36 clamp dst_sel:WORD_1 dst_unused:UNUSED_PAD src0_sel:WORD_1 src1_sel:DWORD\n"
      "v_mfma_f32_16x16x32_f16 v[244:247], a[64:67], v[96:99], v[32:35]\n"
      "v_sub_u16_sdwa v180, v238, s36 clamp dst_sel:WORD_1 dst_unused:UNUSED_PAD src0_sel:WORD_0 src1_sel:DWORD\n"
      "v_sub_u16_sdwa v181, v238, s36 clamp dst_sel:WORD_1 dst_unused:UNUSED_PAD src0_sel:WORD_1 src1_sel:DWORD\n"
      "v_mfma_f32_16x16x32_f16 v[248:251], a[80:83], v[96:99], v[36:39]\n"
      "v_sub_u16_sdwa v182, v239, s36 clamp dst_sel:WORD_1 dst_unused:UNUSED_PAD src0_sel:WORD_0 src1_sel:DWORD\n"
      "v_sub_u16_sdwa v183, v239, s36 clamp dst_sel:WORD_1 dst_unused:UNUSED_PAD src0_sel:WORD_1 src1_sel:DWORD\n"
      "v_mfma_f32_16x16x32_f16 v[252:255], a[96:99], v[96:99], v[40:43]\n"
      "v_cvt_pk_f16_f32 v184, v176, v177\n"
      "v_cvt_pk_f16_f32 v185, v178, v179\n"
      "v_mfma_f32_16x16x32_f16 v[192:195], a[112:115], v[96:99], v[44:47]\n"
      "v_cvt_pk_f16_f32 v186, v180, v181\n"
      "v_cvt_pk_f16_f32 v187, v182, v183\n"
      "v_mfma_f32_16x16x32_f16 v[80:83], a[4:7], v[100:103], v[80:83]\n"
      "ds_write_b128 v1, v[184:187] offset:2304\n"
      "v_mfma_f32_16x16x32_f16 v[84:87], a[20:23], v[100:103], v[84:87]\n"
      "s_waitcnt lgkmcnt(12)\n"
      "v_pk_mul_f16 v104, v136, v168\n"
      "v_pk_mul_f16 v105, v137, v169\n"
      "v_mfma_f32_16x16x32_f16 v[88:91], a[36:39], v[100:103], v[88:91]\n"
      "ds_read_b32 v224, v227 offset:37920\n"
      "v_pk_mul_f16 v106, v138, v170\n"
      "v_mfma_f32_16x16x32_f16 v[92:95], a[52:55], v[100:103], v[92:95]\n"
      "s_waitcnt lgkmcnt(11)\n"
      "v_pk_mul_f16 v107, v139, v171\n"
      "v_pk_mul_f16 v108, v140, v172\n"
      "v_mfma_f32_16x16x32_f16 v[244:247], a[68:71], v[100:103], v[244:247]\n"
      "ds_read_b128 v[144:147], v203 offset:2304\n"
      "v_pk_mul_f16 v109, v141, v173\n"
      "v_mfma_f32_16x16x32_f16 v[248:251], a[84:87], v[100:103], v[248:251]\n"
      "ds_read_b128 v[148:151], v203 offset:2368\n"
      "v_pk_mul_f16 v110, v142, v174\n"
      "v_mfma_f32_16x16x32_f16 v[252:255], a[100:103], v[100:103], v[252:255]\n"
      "ds_read_b128 v[152:155], v203 offset:2432\n"
      "v_pk_mul_f16 v111, v143, v175\n"
      "v_mfma_f32_16x16x32_f16 v[192:195], a[116:119], v[100:103], v[192:195]\n"
      "s_waitcnt lgkmcnt(3)\n"
      "ds_read_b128 v[156:159], v203 offset:2496\n"
      "v_cmp_eq_u32 vcc, 0, v224\n"
      "v_mfma_f32_16x16x32_f16 v[80:83], a[8:11], v[104:107], v[80:83]\n"
      "global_load_dwordx2 v[196:197], v225, s[12:13]\n"
      "global_load_dwordx2 v[198:199], v225, s[14:15]\n"
      "v_mfma_f32_16x16x32_f16 v[84:87], a[24:27], v[104:107], v[84:87]\n"
      "s_cbranch_vccz 13f\n"
      "12:\n"
      "ds_read_b32 v224, v227 offset:37920\n"
      "s_waitcnt lgkmcnt(0)\n"
      "v_cmp_eq_u32 vcc, 0, v224\n"
      "s_nop 4\n"
      "s_cbranch_vccnz 12b\n"
      "13:\n"
      "v_mfma_f32_16x16x32_f16 v[88:91], a[40:43], v[104:107], v[88:91]\n"
      "ds_read_b128 a[12:15], v14 offset:28032\n"
      "global_load_ushort a140, v3, s[24:25]\n"
      "v_mfma_f32_16x16x32_f16 v[92:95], a[56:59], v[104:107], v[92:95]\n"
      "s_waitcnt lgkmcnt(4)\n"
      "ds_read_b128 a[28:31], v4 offset:28032\n"
      "v_pk_mul_f16 v112, v128, v144\n"
      "v_mfma_f32_16x16x32_f16 v[244:247], a[72:75], v[104:107], v[244:247]\n"
      "ds_read_b128 a[44:47], v222 offset:28032\n"
      "v_pk_mul_f16 v113, v129, v145\n"
      "v_mfma_f32_16x16x32_f16 v[248:251], a[88:91], v[104:107], v[248:251]\n"
      "s_waitcnt lgkmcnt(5)\n"
      "ds_read_b128 a[60:63], v223 offset:28032\n"
      "v_pk_mul_f16 v116, v132, v148\n"
      "v_mfma_f32_16x16x32_f16 v[252:255], a[104:107], v[104:107], v[252:255]\n"
      "ds_read_b128 a[76:79], v14 offset:29120\n"
      "v_pk_mul_f16 v114, v130, v146\n"
      "v_mfma_f32_16x16x32_f16 v[192:195], a[120:123], v[104:107], v[192:195]\n"
      "s_waitcnt lgkmcnt(3)\n"
      "ds_read_b128 a[92:95], v4 offset:29120\n"
      "v_pk_mul_f16 v117, v133, v149\n"
      "v_mfma_f32_16x16x32_f16 v[80:83], a[12:15], v[108:111], v[80:83]\n"
      "ds_read_b128 a[108:111], v222 offset:29120\n"
      "v_pk_mul_f16 v120, v136, v152\n"
      "v_mfma_f32_16x16x32_f16 v[84:87], a[28:31], v[108:111], v[84:87]\n"
      "s_waitcnt lgkmcnt(3)\n"
      "ds_read_b128 a[124:127], v223 offset:29120\n"
      "v_pk_mul_f16 v115, v131, v147\n"
      "v_mfma_f32_16x16x32_f16 v[88:91], a[44:47], v[108:111], v[88:91]\n"
      "v_pk_mul_f16 v118, v134, v150\n"
      "v_pk_mul_f16 v121, v137, v153\n"
      "v_mfma_f32_16x16x32_f16 v[92:95], a[60:63], v[108:111], v[92:95]\n"
      "s_waitcnt lgkmcnt(2)\n"
      "v_mul_f32 v188, |v80|, v48\n"
      "v_fma_f32 v188, |v81|, v49, v188\n"
      "v_mfma_f32_16x16x32_f16 v[244:247], a[76:79], v[108:111], v[244:247]\n"
      "v_pk_mul_f16 v124, v140, v156\n"
      "v_fma_f32 v188, |v82|, v50, v188\n"
      "v_mfma_f32_16x16x32_f16 v[248:251], a[92:95], v[108:111], v[248:251]\n"
      "s_waitcnt lgkmcnt(0)\n"
      "v_fma_f32 v188, |v83|, v51, v188\n"
      "v_pk_mul_f16 v119, v135, v151\n"
      "v_mfma_f32_16x16x32_f16 v[252:255], a[108:111], v[108:111], v[252:255]\n"
      "v_pk_mul_f16 v122, v138, v154\n"
      "v_pk_mul_f16 v125, v141, v157\n"
      "v_mfma_f32_16x16x32_f16 v[192:195], a[124:127], v[108:111], v[192:195]\n"
      "v_pk_mul_f16 v123, v139, v155\n"
      "v_pk_mul_f16 v126, v142, v158\n"
      "v_mfma_f32_16x16x32_f16 v[80:83], a[0:3], v[112:115], v[16:19]\n"
      "v_pk_mul_f16 v127, v143, v159\n"
      "v_fma_f32 v188, |v84|, v52, v188\n"
      "v_mfma_f32_16x16x32_f16 v[80:83], a[4:7], v[116:119], v[80:83]\n"
      "v_fma_f32 v188, |v85|, v53, v188\n"
      "v_fma_f32 v188, |v86|, v54, v188\n"
      "v_mfma_f32_16x16x32_f16 v[80:83], a[8:11], v[120:123], v[80:83]\n"
      "ds_read_b128 v[160:163], v203 offset:2880\n"
      "v_fma_f32 v188, |v87|, v55, v188\n"
      "v_mfma_f32_16x16x32_f16 v[80:83], a[12:15], v[124:127], v[80:83]\n"
      "40:\n"
      "s_cbranch_execz 44b\n"
      "v_mfma_f32_16x16x32_f16 v[84:87], a[28:31], v[124:127], v[20:23]\n"
      "v_fma_f32 v188, |v88|, v56, v188\n"
      "v_fma_f32 v188, |v89|, v57, v188\n"
      "v_mfma_f32_16x16x32_f16 v[84:87], a[24:27], v[120:123], v[84:87]\n"
      "v_fma_f32 v188, |v90|, v58, v188\n"
      "v_fma_f32 v188, |v91|, v59, v188\n"
      "v_mfma_f32_16x16x32_f16 v[84:87], a[20:23], v[116:119], v[84:87]\n"
      "v_fma_f32 v188, |v92|, v60, v188\n"
      "v_fma_f32 v188, |v93|, v61, v188\n"
      "v_mfma_f32_16x16x32_f16 v[84:87], a[16:19], v[112:115], v[84:87]\n"
      "v_fma_f32 v188, |v94|, v62, v188\n"
      "v_fma_f32 v188, |v95|, v63, v188\n"
      "v_mfma_f32_16x16x32_f16 v[88:91], a[32:35], v[112:115], v[24:27]\n"
      "ds_read_b128 v[164:167], v203 offset:2944\n"
      "v_fma_f32 v188, |v244|, v64, v188\n"
      "v_mfma_f32_16x16x32_f16 v[88:91], a[36:39], v[116:119], v[88:91]\n"
      "s_waitcnt lgkmcnt(1)\n"
      "v_pk_mul_f16 v96, v128, v160\n"
      "v_pk_mul_f16 v97, v129, v161\n"
      "v_mfma_f32_16x16x32_f16 v[88:91], a[40:43], v[120:123], v[88:91]\n"
      "v_pk_mul_f16 v98, v130, v162\n"
      "v_mul_f32 v189, |v80|, v48\n"
      "v_mfma_f32_16x16x32_f16 v[88:91], a[44:47], v[124:127], v[88:91]\n"
      "v_fma_f32 v189, |v81|, v49, v189\n"
      "v_pk_mul_f16 v99, v131, v163\n"
      "v_mfma_f32_16x16x32_f16 v[92:95], a[60:63], v[124:127], v[28:31]\n"
      "v_fma_f32 v188, |v245|, v65, v188\n"
      "v_fma_f32 v189, |v82|, v50, v189\n"
      "v_mfma_f32_16x16x32_f16 v[92:95], a[56:59], v[120:123], v[92:95]\n"
      "v_fma_f32 v189, |v83|, v51, v189\n"
      "v_fma_f32 v188, |v246|, v66, v188\n"
      "v_mfma_f32_16x16x32_f16 v[92:95], a[52:55], v[116:119], v[92:95]\n"
      "ds_read_b128 v[168:171], v203 offset:3008\n"
      "v_fma_f32 v189, |v84|, v52, v189\n"
      "v_mfma_f32_16x16x32_f16 v[92:95], a[48:51], v[112:115], v[92:95]\n"
      "s_waitcnt lgkmcnt(1)\n"
      "v_fma_f32 v189, |v85|, v53, v189\n"
      "v_pk_mul_f16 v100, v132, v164\n"
      "v_mfma_f32_16x16x32_f16 v[80:83], a[64:67], v[112:115], v[32:35]\n"
      "v_fma_f32 v188, |v247|, v67, v188\n"
      "v_fma_f32 v189, |v86|, v54, v189\n"
      "v_mfma_f32_16x16x32_f16 v[80:83], a[68:71], v[116:119], v[80:83]\n"
      "v_fma_f32 v189, |v87|, v55, v189\n"
      "v_max3_f32 v226, |v228|, |v229|, |v230|\n"
      "v_mfma_f32_16x16x32_f16 v[80:83], a[72:75], v[120:123], v[80:83]\n"
      "v_pk_mul_f16 v101, v133, v165\n"
      "v_cmp_le_f32 s[28:29], s99, v226\n"
      "v_mfma_f32_16x16x32_f16 v[80:83], a[76:79], v[124:127], v[80:83]\n"
      "v_pk_mul_f16 v102, v134, v166\n"
      "v_fma_f32 v188, |v248|, v68, v188\n"
      "v_mfma_f32_16x16x32_f16 v[84:87], a[92:95], v[124:127], v[36:39]\n"
      "s_bcnt1_i32_b64 s44, s[28:29]\n"
      "v_pk_mul_f16 v103, v135, v167\n"
      "v_fma_f32 v189, |v88|, v56, v189\n"
      "v_mfma_f32_16x16x32_f16 v[84:87], a[88:91], v[120:123], v[84:87]\n"
      "s_mov_b32 vcc_hi, 0\n"
      "v_fma_f32 v189, |v89|, v57, v189\n"
      "v_fma_f32 v188, |v249|, v69, v188\n"
      "v_mfma_f32_16x16x32_f16 v[84:87], a[84:87], v[116:119], v[84:87]\n"
      "s_cmp_lt_u32 s44, 24\n"
      "s_cselect_b32 vcc_lo, -1, s60\n"
      "v_fma_f32 v189, |v90|, v58, v189\n"
      "v_fma_f32 v189, |v91|, v59, v189\n"
      "v_mfma_f32_16x16x32_f16 v[84:87], a[80:83], v[112:115], v[84:87]\n"
      "s_waitcnt lgkmcnt(0)\n"
      "ds_read_b128 v[172:175], v203 offset:3072\n"
      "v_pk_mul_f16 v104, v136, v168\n"
      "v_mfma_f32_16x16x32_f16 v[88:91], a[96:99], v[112:115], v[40:43]\n"
      "v_fma_f32 v188, |v250|, v70, v188\n"
      "v_pk_mul_f16 v105, v137, v169\n"
      "v_mfma_f32_16x16x32_f16 v[88:91], a[100:103], v[116:119], v[88:91]\n"
      "v_fma_f32 v189, |v92|, v60, v189\n"
      "v_fma_f32 v189, |v93|, v61, v189\n"
      "v_mfma_f32_16x16x32_f16 v[88:91], a[104:107], v[120:123], v[88:91]\n"
      "v_pk_mul_f16 v106, v138, v170\n"
      "v_fma_f32 v188, |v251|, v71, v188\n"
      "v_mfma_f32_16x16x32_f16 v[88:91], a[108:111], v[124:127], v[88:91]\n"
      "v_fma_f32 v189, |v94|, v62, v189\n"
      "v_fma_f32 v189, |v95|, v63, v189\n"
      "v_pk_mul_f16 v107, v139, v171\n"
      "v_mfma_f32_16x16x32_f16 v[92:95], a[124:127], v[124:127], v[44:47]\n"
      "v_fma_f32 v188, |v252|, v72, v188\n"
      "v_fma_f32 v189, |v80|, v64, v189\n"
      "v_mfma_f32_16x16x32_f16 v[92:95], a[120:123], v[120:123], v[92:95]\n"
      "v_fma_f32 v189, |v81|, v65, v189\n"
      "v_fma_f32 v188, |v253|, v73, v188\n"
      "v_mfma_f32_16x16x32_f16 v[92:95], a[116:119], v[116:119], v[92:95]\n"
      "s_waitcnt lgkmcnt(0)\n"
      "v_pk_mul_f16 v108, v140, v172\n"
      "v_fma_f32 v189, |v82|, v66, v189\n"
      "v_mfma_f32_16x16x32_f16 v[92:95], a[112:115], v[112:115], v[92:95]\n"
      "v_fma_f32 v189, |v83|, v67, v189\n"
      "v_pk_mul_f16 v109, v141, v173\n"
      "v_mfma_f32_16x16x32_f16 v[80:83], a[0:3], v[96:99], v[16:19]\n"
      "v_pk_mul_f16 v110, v142, v174\n"
      "v_fma_f32 v188, |v254|, v74, v188\n"
      "v_mfma_f32_16x16x32_f16 v[80:83], a[4:7], v[100:103], v[80:83]\n"
      "v_pk_mul_f16 v111, v143, v175\n"
      "v_fma_f32 v189, |v84|, v68, v189\n"
      "v_mfma_f32_16x16x32_f16 v[80:83], a[8:11], v[104:107], v[80:83]\n"
      "v_fma_f32 v189, |v85|, v69, v189\n"
      "v_fma_f32 v188, |v255|, v75, v188\n"
      "v_mfma_f32_16x16x32_f16 v[80:83], a[12:15], v[108:111], v[80:83]\n"
      "v_fma_f32 v189, |v86|, v70, v189\n"
      "v_fma_f32 v189, |v87|, v71, v189\n"
      "v_mfma_f32_16x16x32_f16 v[84:87], a[28:31], v[108:111], v[20:23]\n"
      "v_fma_f32 v188, |v192|, v76, v188\n"
      "v_fma_f32 v189, |v88|, v72, v189\n"
      "v_mfma_f32_16x16x32_f16 v[84:87], a[24:27], v[104:107], v[84:87]\n"
      "s_waitcnt vmcnt(4)\n"
      "ds_read_b128 v[112:115], v204 offset:0\n"
      "v_fma_f32 v189, |v89|, v73, v189\n"
      "v_mfma_f32_16x16x32_f16 v[84:87], a[20:23], v[100:103], v[84:87]\n"
      "ds_read_b128 v[160:163], v205 offset:0\n"
      "v_fma_f32 v188, |v193|, v77, v188\n"
      "v_mfma_f32_16x16x32_f16 v[84:87], a[16:19], v[96:99], v[84:87]\n"
      "v_fma_f32 v189, |v90|, v74, v189\n"
      "v_fma_f32 v189, |v91|, v75, v189\n"
      "v_mfma_f32_16x16x32_f16 v[88:91], a[32:35], v[96:99], v[24:27]\n"
      "v_fma_f32 v188, |v194|, v78, v188\n"
      "v_fma_f32 v189, |v92|, v76, v189\n"
      "v_mfma_f32_16x16x32_f16 v[88:91], a[36:39], v[100:103], v[88:91]\n"
      "v_fma_f32 v189, |v93|, v77, v189\n"
      "v_fma_f32 v188, |v195|, v79, v188\n"
      "v_mfma_f32_16x16x32_f16 v[88:91], a[40:43], v[104:107], v[88:91]\n"
      "v_fma_f32 v189, |v94|, v78, v189\n"
      "v_fma_f32 v189, |v95|, v79, v189\n"
      "v_mfma_f32_16x16x32_f16 v[88:91], a[44:47], v[108:111], v[88:91]\n"
      "ds_read_b128 v[116:119], v204 offset:64\n"
      "v_mul_f32 v190, |v80|, v48\n"
      "v_mfma_f32_16x16x32_f16 v[92:95], a[60:63], v[108:111], v[28:31]\n"
      "ds_read_b128 v[164:167], v205 offset:64\n"
      "v_fma_f32 v190, |v81|, v49, v190\n"
      "v_mfma_f32_16x16x32_f16 v[92:95], a[56:59], v[104:107], v[92:95]\n"
      "s_waitcnt lgkmcnt(2)\n"
      "v_pk_mul_f16 v112, v112, v160\n"
      "v_pk_mul_f16 v113, v113, v161\n"
      "v_mfma_f32_16x16x32_f16 v[92:95], a[52:55], v[100:103], v[92:95]\n"
      "v_fma_f32 v190, |v82|, v50, v190\n"
      "v_fma_f32 v190, |v83|, v51, v190\n"
      "v_mfma_f32_16x16x32_f16 v[92:95], a[48:51], v[96:99], v[92:95]\n"
      "v_pk_mul_f16 v114, v114, v162\n"
      "v_pk_mul_f16 v115, v115, v163\n"
      "v_mfma_f32_16x16x32_f16 v[80:83], a[64:67], v[96:99], v[32:35]\n"
      "ds_read_b128 v[120:123], v204 offset:128\n"
      "v_fma_f32 v190, |v84|, v52, v190\n"
      "v_mfma_f32_16x16x32_f16 v[80:83], a[68:71], v[100:103], v[80:83]\n"
      "ds_read_b128 v[168:171], v205 offset:128\n"
      "v_fma_f32 v190, |v85|, v53, v190\n"
      "v_mfma_f32_16x16x32_f16 v[80:83], a[72:75], v[104:107], v[80:83]\n"
      "s_cbranch_vccnz 33f\n"
      "global_load_dwordx4 v[244:247], v11, s[10:11] nt\n"
      "global_load_dwordx4 v[248:251], v12, s[10:11] nt\n"
      "global_load_dwordx4 v[252:255], v6, s[10:11] nt\n"
      "32:\n"
      "v_mfma_f32_16x16x32_f16 v[80:83], a[76:79], v[108:111], v[80:83]\n"
      "s_waitcnt lgkmcnt(2)\n"
      "v_fma_f32 v190, |v86|, v54, v190\n"
      "v_fma_f32 v190, |v87|, v55, v190\n"
      "v_pk_mul_f16 v116, v116, v164\n"
      "v_mfma_f32_16x16x32_f16 v[84:87], a[92:95], v[108:111], v[36:39]\n"
      "s_mov_b32 s99, 0x71800000\n"
      "v_pk_mul_f16 v117, v117, v165\n"
      "v_pk_mul_f16 v118, v118, v166\n"
      "v_mfma_f32_16x16x32_f16 v[84:87], a[88:91], v[104:107], v[84:87]\n"
      "v_fma_f32 v190, |v88|, v56, v190\n"
      "v_fma_f32 v190, |v89|, v57, v190\n"
      "v_mfma_f32_16x16x32_f16 v[84:87], a[84:87], v[100:103], v[84:87]\n"
      "v_pk_mul_f16 v119, v119, v167\n"
      "v_fma_f32 v190, |v90|, v58, v190\n"
      "v_mfma_f32_16x16x32_f16 v[84:87], a[80:83], v[96:99], v[84:87]\n"
      "s_waitcnt vmcnt(6)\n"
      "ds_read_b128 v[124:127], v204 offset:192\n"
      "ds_read_b128 v[172:175], v205 offset:192\n"
      "v_fma_f32 v190, |v91|, v59, v190\n"
      "v_sub_u16_sdwa v176, v240, s36 clamp dst_sel:WORD_1 dst_unused:UNUSED_PAD src0_sel:WORD_0 src1_sel:DWORD\n"
      "v_mfma_f32_16x16x32_f16 v[88:91], a[96:99], v[96:99], v[40:43]\n"
      "s_waitcnt lgkmcnt(2)\n"
      "v_sub_u16_sdwa v177, v240, s36 clamp dst_sel:WORD_1 dst_unused:UNUSED_PAD src0_sel:WORD_1 src1_sel:DWORD\n"
      "v_pk_mul_f16 v120, v120, v168\n"
      "v_mfma_f32_16x16x32_f16 v[88:91], a[100:103], v[100:103], v[88:91]\n"
      "v_sub_u16_sdwa v178, v241, s36 clamp dst_sel:WORD_1 dst_unused:UNUSED_PAD src0_sel:WORD_0 src1_sel:DWORD\n"
      "v_pk_mul_f16 v121, v121, v169\n"
      "v_mfma_f32_16x16x32_f16 v[88:91], a[104:107], v[104:107], v[88:91]\n"
      "v_fma_f32 v190, |v92|, v60, v190\n"
      "v_fma_f32 v190, |v93|, v61, v190\n"
      "v_sub_u16_sdwa v179, v241, s36 clamp dst_sel:WORD_1 dst_unused:UNUSED_PAD src0_sel:WORD_1 src1_sel:DWORD\n"
      "v_pk_mul_f16 v122, v122, v170\n"
      "v_mfma_f32_16x16x32_f16 v[88:91], a[108:111], v[108:111], v[88:91]\n"
      "v_fma_f32 v190, |v94|, v62, v190\n"
      "v_fma_f32 v190, |v95|, v63, v190\n"
      "v_sub_u16_sdwa v180, v242, s36 clamp dst_sel:WORD_1 dst_unused:UNUSED_PAD src0_sel:WORD_0 src1_sel:DWORD\n"
      "v_pk_mul_f16 v123, v123, v171\n"
      "v_mfma_f32_16x16x32_f16 v[92:95], a[124:127], v[108:111], v[44:47]\n"
      "v_sub_u16_sdwa v181, v242, s36 clamp dst_sel:WORD_1 dst_unused:UNUSED_PAD src0_sel:WORD_1 src1_sel:DWORD\n"
      "v_sub_u16_sdwa v182, v243, s36 clamp dst_sel:WORD_1 dst_unused:UNUSED_PAD src0_sel:WORD_0 src1_sel:DWORD\n"
      "v_mfma_f32_16x16x32_f16 v[92:95], a[120:123], v[104:107], v[92:95]\n"
      "v_fma_f32 v190, |v80|, v64, v190\n"
      "v_fma_f32 v190, |v81|, v65, v190\n"
      "v_mfma_f32_16x16x32_f16 v[92:95], a[116:119], v[100:103], v[92:95]\n"
      "s_waitcnt lgkmcnt(0)\n"
      "v_sub_u16_sdwa v183, v243, s36 clamp dst_sel:WORD_1 dst_unused:UNUSED_PAD src0_sel:WORD_1 src1_sel:DWORD\n"
      "v_pk_mul_f16 v124, v124, v172\n"
      "v_mfma_f32_16x16x32_f16 v[92:95], a[112:115], v[96:99], v[92:95]\n"
      "v_fma_f32 v190, |v82|, v66, v190\n"
      "v_fma_f32 v190, |v83|, v67, v190\n"
      "v_cvt_pk_f16_f32 v184, v176, v177\n"
      "v_pk_mul_f16 v125, v125, v173\n"
      "v_mfma_f32_16x16x32_f16 v[80:83], a[0:3], v[112:115], v[16:19]\n"
      "v_cvt_pk_f16_f32 v185, v178, v179\n"
      "v_pk_mul_f16 v126, v126, v174\n"
      "v_mfma_f32_16x16x32_f16 v[80:83], a[4:7], v[116:119], v[80:83]\n"
      "v_cvt_pk_f16_f32 v186, v180, v181\n"
      "v_pk_mul_f16 v127, v127, v175\n"
      "v_mfma_f32_16x16x32_f16 v[80:83], a[8:11], v[120:123], v[80:83]\n"
      "v_fma_f32 v190, |v84|, v68, v190\n"
      "v_fma_f32 v190, |v85|, v69, v190\n"
      "v_cvt_pk_f16_f32 v187, v182, v183\n"
      "v_mfma_f32_16x16x32_f16 v[80:83], a[12:15], v[124:127], v[80:83]\n"
      "ds_write_b128 v1, v[184:187] offset:3456\n"
      "v_fma_f32 v190, |v86|, v70, v190\n"
      "v_fma_f32 v190, |v87|, v71, v190\n"
      "v_mfma_f32_16x16x32_f16 v[84:87], a[28:31], v[124:127], v[20:23]\n"
      "v_fma_f32 v190, |v88|, v72, v190\n"
      "v_fma_f32 v190, |v89|, v73, v190\n"
      "v_mfma_f32_16x16x32_f16 v[84:87], a[24:27], v[120:123], v[84:87]\n"
      "ds_read_b128 v[160:163], v203 offset:3456\n"
      "v_fma_f32 v190, |v90|, v74, v190\n"
      "v_mfma_f32_16x16x32_f16 v[84:87], a[20:23], v[116:119], v[84:87]\n"
      "v_fma_f32 v190, |v91|, v75, v190\n"
      "v_fma_f32 v190, |v92|, v76, v190\n"
      "v_mfma_f32_16x16x32_f16 v[84:87], a[16:19], v[112:115], v[84:87]\n"
      "v_fma_f32 v190, |v93|, v77, v190\n"
      "v_fma_f32 v190, |v94|, v78, v190\n"
      "v_mfma_f32_16x16x32_f16 v[88:91], a[32:35], v[112:115], v[24:27]\n"
      "ds_read_b128 v[164:167], v203 offset:3520\n"
      "v_fma_f32 v190, |v95|, v79, v190\n"
      "v_mfma_f32_16x16x32_f16 v[88:91], a[36:39], v[116:119], v[88:91]\n"
      "v_mul_f32 v194, |v80|, v48\n"
      "v_fma_f32 v194, |v81|, v49, v194\n"
      "v_mfma_f32_16x16x32_f16 v[88:91], a[40:43], v[120:123], v[88:91]\n"
      "v_fma_f32 v194, |v82|, v50, v194\n"
      "v_fma_f32 v194, |v83|, v51, v194\n"
      "v_mfma_f32_16x16x32_f16 v[88:91], a[44:47], v[124:127], v[88:91]\n"
      "ds_read_b128 v[168:171], v203 offset:3584\n"
      "v_fma_f32 v194, |v84|, v52, v194\n"
      "v_mfma_f32_16x16x32_f16 v[92:95], a[60:63], v[124:127], v[28:31]\n"
      "s_waitcnt lgkmcnt(2)\n"
      "v_pk_mul_f16 v96, v128, v160\n"
      "v_pk_mul_f16 v97, v129, v161\n"
      "v_mfma_f32_16x16x32_f16 v[92:95], a[56:59], v[120:123], v[92:95]\n"
      "v_pk_mul_f16 v98, v130, v162\n"
      "v_pk_mul_f16 v99, v131, v163\n"
      "v_mfma_f32_16x16x32_f16 v[92:95], a[52:55], v[116:119], v[92:95]\n"
      "v_fma_f32 v194, |v85|, v53, v194\n"
      "v_fma_f32 v194, |v86|, v54, v194\n"
      "v_mfma_f32_16x16x32_f16 v[92:95], a[48:51], v[112:115], v[92:95]\n"
      "s_waitcnt lgkmcnt(1)\n"
      "v_pk_mul_f16 v100, v132, v164\n"
      "v_pk_mul_f16 v101, v133, v165\n"
      "v_mfma_f32_16x16x32_f16 v[80:83], a[64:67], v[112:115], v[32:35]\n"
      "v_fma_f32 v194, |v87|, v55, v194\n"
      "v_pk_mul_f16 v102, v134, v166\n"
      "v_mfma_f32_16x16x32_f16 v[80:83], a[68:71], v[116:119], v[80:83]\n"
      "global_load_dwordx4 v[10:13], v218, s[96:97] offset:1024\n"
      "v_pk_mul_f16 v103, v135, v167\n"
      "v_mfma_f32_16x16x32_f16 v[80:83], a[72:75], v[120:123], v[80:83]\n"
      "v_fma_f32 v194, |v88|, v56, v194\n"
      "v_fma_f32 v194, |v89|, v57, v194\n"
      "v_mfma_f32_16x16x32_f16 v[80:83], a[76:79], v[124:127], v[80:83]\n"
      "s_waitcnt lgkmcnt(0)\n"
      "v_pk_mul_f16 v104, v136, v168\n"
      "v_fma_f32 v194, |v90|, v58, v194\n"
      "v_mfma_f32_16x16x32_f16 v[84:87], a[92:95], v[124:127], v[36:39]\n"
      "v_fma_f32 v194, |v91|, v59, v194\n"
      "v_pk_mul_f16 v105, v137, v169\n"
      "v_mfma_f32_16x16x32_f16 v[84:87], a[88:91], v[120:123], v[84:87]\n"
      "ds_read_b128 v[172:175], v203 offset:3648\n"
      "v_pk_mul_f16 v106, v138, v170\n"
      "v_mfma_f32_16x16x32_f16 v[84:87], a[84:87], v[116:119], v[84:87]\n"
      "ds_read_b64_tr_b16 a[168:169], v216 offset:18688\n"
      "v_pk_mul_f16 v107, v139, v171\n"
      "v_mfma_f32_16x16x32_f16 v[84:87], a[80:83], v[112:115], v[84:87]\n"
      "ds_read_b64_tr_b16 a[170:171], v216 offset:19776\n"
      "v_fma_f32 v194, |v92|, v60, v194\n"
      "v_mfma_f32_16x16x32_f16 v[88:91], a[96:99], v[112:115], v[40:43]\n"
      "ds_read_b64_tr_b16 a[172:173], v216 offset:18752\n"
      "v_fma_f32 v194, |v93|, v61, v194\n"
      "v_mfma_f32_16x16x32_f16 v[88:91], a[100:103], v[116:119], v[88:91]\n"
      "v_fma_f32 v194, |v94|, v62, v194\n"
      "v_fma_f32 v194, |v95|, v63, v194\n"
      "v_mfma_f32_16x16x32_f16 v[88:91], a[104:107], v[120:123], v[88:91]\n"
      "ds_read_b64_tr_b16 a[174:175], v216 offset:19840\n"
      "v_fma_f32 v194, |v80|, v64, v194\n"
      "v_mfma_f32_16x16x32_f16 v[88:91], a[108:111], v[124:127], v[88:91]\n"
      "ds_read_b64_tr_b16 a[176:177], v216 offset:18816\n"
      "v_fma_f32 v194, |v81|, v65, v194\n"
      "v_mfma_f32_16x16x32_f16 v[92:95], a[124:127], v[124:127], v[44:47]\n"
      "s_waitcnt lgkmcnt(5)\n"
      "ds_read_b64_tr_b16 a[178:179], v216 offset:19904\n"
      "v_pk_mul_f16 v108, v140, v172\n"
      "v_mfma_f32_16x16x32_f16 v[92:95], a[120:123], v[120:123], v[92:95]\n"
      "ds_read_b64_tr_b16 a[180:181], v216 offset:18880\n"
      "v_fma_f32 v194, |v82|, v66, v194\n"
      "v_mfma_f32_16x16x32_f16 v[92:95], a[116:119], v[116:119], v[92:95]\n"
      "v_fma_f32 v194, |v83|, v67, v194\n"
      "v_pk_mul_f16 v109, v141, v173\n"
      "v_mfma_f32_16x16x32_f16 v[92:95], a[112:115], v[112:115], v[92:95]\n"
      "ds_read_b64_tr_b16 a[182:183], v216 offset:19968\n"
      "v_pk_mul_f16 v110, v142, v174\n"
      "v_mfma_f32_16x16x32_f16 v[80:83], a[0:3], v[96:99], v[16:19]\n"
      "v_pk_mul_f16 v111, v143, v175\n"
      "v_fma_f32 v194, |v84|, v68, v194\n"
      "v_mfma_f32_16x16x32_f16 v[80:83], a[4:7], v[100:103], v[80:83]\n"
      "v_fma_f32 v194, |v85|, v69, v194\n"
      "v_fma_f32 v194, |v86|, v70, v194\n"
      "v_mfma_f32_16x16x32_f16 v[80:83], a[8:11], v[104:107], v[80:83]\n"
      "ds_read_b128 v[160:163], v203 offset:4032\n"
      "v_fma_f32 v194, |v87|, v71, v194\n"
      "v_mfma_f32_16x16x32_f16 v[80:83], a[12:15], v[108:111], v[80:83]\n"
      "v_fma_f32 v194, |v88|, v72, v194\n"
      "v_fma_f32 v194, |v89|, v73, v194\n"
      "v_mfma_f32_16x16x32_f16 v[84:87], a[28:31], v[108:111], v[20:23]\n"
      "v_fma_f32 v194, |v90|, v74, v194\n"
      "v_fma_f32 v194, |v91|, v75, v194\n"
      "v_mfma_f32_16x16x32_f16 v[84:87], a[24:27], v[104:107], v[84:87]\n"
      "s_waitcnt vmcnt(6) lgkmcnt(7)\n"
      "ds_read_b128 v[164:167], v203 offset:4096\n"
      "v_cmp_eq_u64 s[46:47], v[196:197], v[208:209]\n"
      "v_mfma_f32_16x16x32_f16 a[204:207], a[184:187], a[168:171], 0\n"
      "s_waitcnt vmcnt(5) lgkmcnt(6)\n"
      "v_cmp_eq_u64 s[48:49], v[198:199], v[210:211]\n"
      "v_fma_f32 v194, |v92|, v76, v194\n"
      "v_mfma_f32_16x16x32_f16 a[204:207], a[188:191], a[172:175], a[204:207]\n"
      "s_waitcnt lgkmcnt(4)\n"
      "v_fma_f32 v194, |v93|, v77, v194\n"
      "v_fma_f32 v194, |v94|, v78, v194\n"
      "v_mfma_f32_16x16x32_f16 a[204:207], a[192:195], a[176:179], a[204:207]\n"
      "s_waitcnt lgkmcnt(2)\n"
      "s_and_b64 s[46:47], s[46:47], s[48:49]\n"
      "v_fma_f32 v194, |v95|, v79, v194\n"
      "v_mul_f32 v191, |v80|, v48\n"
      "v_mfma_f32_16x16x32_f16 a[204:207], a[196:199], a[180:183], a[204:207]\n"
      "s_waitcnt lgkmcnt(1)\n"
      "v_pk_mul_f16 v112, v128, v160\n"
      "v_pk_mul_f16 v113, v129, v161\n"
      "v_mfma_f32_16x16x32_f16 v[84:87], a[20:23], v[100:103], v[84:87]\n"
      "s_cmp_eq_u64 s[46:47], -1\n"
      "s_cselect_b32 s52, s52, 0x7f000001\n"
      "v_pk_mul_f16 v114, v130, v162\n"
      "v_pk_mul_f16 v115, v131, v163\n"
      "v_mfma_f32_16x16x32_f16 v[84:87], a[16:19], v[96:99], v[84:87]\n"
      "v_mov_b32 v223, s52\n"
      "v_fma_f32 v191, |v81|, v49, v191\n"
      "v_mfma_f32_16x16x32_f16 v[88:91], a[32:35], v[96:99], v[24:27]\n"
      "ds_write2_b32 v217, a200, a204 offset0:0 offset1:64\n"
      "v_mfma_f32_16x16x32_f16 v[88:91], a[36:39], v[100:103], v[88:91]\n"
      "ds_write_b32 v2, v223\n"
      "v_mfma_f32_16x16x32_f16 v[88:91], a[40:43], v[104:107], v[88:91]\n"
      "s_waitcnt lgkmcnt(2)\n"
      "v_pk_mul_f16 v116, v132, v164\n"
      "v_fma_f32 v191, |v82|, v50, v191\n"
      "v_mfma_f32_16x16x32_f16 v[88:91], a[44:47], v[108:111], v[88:91]\n"
      "v_fma_f32 v191, |v83|, v51, v191\n"
      "v_pk_mul_f16 v117, v133, v165\n"
      "v_mfma_f32_16x16x32_f16 v[92:95], a[60:63], v[108:111], v[28:31]\n"
      "ds_read_b128 v[168:171], v203 offset:4160\n"
      "v_pk_mul_f16 v118, v134, v166\n"
      "v_mfma_f32_16x16x32_f16 v[92:95], a[56:59], v[104:107], v[92:95]\n"
      "v_pk_mul_f16 v119, v135, v167\n"
      "v_fma_f32 v191, |v84|, v52, v191\n"
      "v_mfma_f32_16x16x32_f16 v[92:95], a[52:55], v[100:103], v[92:95]\n"
      "v_fma_f32 v191, |v85|, v53, v191\n"
      "v_fma_f32 v191, |v86|, v54, v191\n"
      "v_mfma_f32_16x16x32_f16 v[92:95], a[48:51], v[96:99], v[92:95]\n"
      "v_fma_f32 v191, |v87|, v55, v191\n"
      "v_fma_f32 v191, |v88|, v56, v191\n"
      "v_mfma_f32_16x16x32_f16 v[80:83], a[64:67], v[96:99], v[32:35]\n"
      "v_fma_f32 v191, |v89|, v57, v191\n"
      "v_fma_f32 v191, |v90|, v58, v191\n"
      "v_mfma_f32_16x16x32_f16 v[80:83], a[68:71], v[100:103], v[80:83]\n"
      "ds_read_b128 v[172:175], v203 offset:4224\n"
      "v_fma_f32 v191, |v91|, v59, v191\n"
      "v_mfma_f32_16x16x32_f16 v[80:83], a[72:75], v[104:107], v[80:83]\n"
      "v_fma_f32 v191, |v92|, v60, v191\n"
      "v_fma_f32 v191, |v93|, v61, v191\n"
      "v_mfma_f32_16x16x32_f16 v[80:83], a[76:79], v[108:111], v[80:83]\n"
      "s_waitcnt lgkmcnt(1)\n"
      "v_pk_mul_f16 v120, v136, v168\n"
      "v_pk_mul_f16 v121, v137, v169\n"
      "v_mfma_f32_16x16x32_f16 v[84:87], a[92:95], v[108:111], v[36:39]\n"
      "v_pk_mul_f16 v122, v138, v170\n"
      "v_pk_mul_f16 v123, v139, v171\n"
      "v_mfma_f32_16x16x32_f16 v[84:87], a[88:91], v[104:107], v[84:87]\n"
      "v_fma_f32 v191, |v94|, v62, v191\n"
      "v_fma_f32 v191, |v95|, v63, v191\n"
      "v_mfma_f32_16x16x32_f16 v[84:87], a[84:87], v[100:103], v[84:87]\n"
      "ds_read_b128 v[144:147], v201 offset:2304\n"
      "v_fma_f32 v191, |v80|, v64, v191\n"
      "v_mfma_f32_16x16x32_f16 v[84:87], a[80:83], v[96:99], v[84:87]\n"
      "ds_read_b128 v[148:151], v201 offset:2368\n"
      "v_fma_f32 v191, |v81|, v65, v191\n"
      "v_mfma_f32_16x16x32_f16 v[88:91], a[96:99], v[96:99], v[40:43]\n"
      "s_waitcnt lgkmcnt(2)\n"
      "ds_read_b128 v[152:155], v201 offset:2432\n"
      "v_pk_mul_f16 v124, v140, v172\n"
      "v_mfma_f32_16x16x32_f16 v[88:91], a[100:103], v[100:103], v[88:91]\n"
      "v_fma_f32 v191, |v82|, v66, v191\n"
      "v_fma_f32 v191, |v83|, v67, v191\n"
      "v_mfma_f32_16x16x32_f16 v[88:91], a[104:107], v[104:107], v[88:91]\n"
      "ds_read_b128 v[156:159], v201 offset:2496\n"
      "v_pk_mul_f16 v125, v141, v173\n"
      "v_mfma_f32_16x16x32_f16 v[88:91], a[108:111], v[108:111], v[88:91]\n"
      "ds_read_b128 v[160:163], v207 offset:0\n"
      "v_pk_mul_f16 v126, v142, v174\n"
      "v_mfma_f32_16x16x32_f16 v[92:95], a[124:127], v[108:111], v[44:47]\n"
      "v_pk_mul_f16 v127, v143, v175\n"
      "v_fma_f32 v191, |v84|, v68, v191\n"
      "v_mfma_f32_16x16x32_f16 v[92:95], a[120:123], v[104:107], v[92:95]\n"
      "v_fma_f32 v191, |v85|, v69, v191\n"
      "v_fma_f32 v191, |v86|, v70, v191\n"
      "v_mfma_f32_16x16x32_f16 v[92:95], a[116:119], v[100:103], v[92:95]\n"
      "global_load_dwordx4 v[208:211], v218, s[96:97] offset:2048\n"
      "v_fma_f32 v191, |v87|, v71, v191\n"
      "v_mfma_f32_16x16x32_f16 v[92:95], a[112:115], v[96:99], v[92:95]\n"
      "global_load_dwordx4 v[216:219], v218, s[96:97] offset:3072\n"
      "v_fma_f32 v191, |v88|, v72, v191\n"
      "v_mfma_f32_16x16x32_f16 v[80:83], a[0:3], v[112:115], v[16:19]\n"
      "v_fma_f32 v191, |v89|, v73, v191\n"
      "v_fma_f32 v191, |v90|, v74, v191\n"
      "v_mfma_f32_16x16x32_f16 v[80:83], a[4:7], v[116:119], v[80:83]\n"
      "ds_read_b128 v[164:167], v207 offset:64\n"
      "v_fma_f32 v191, |v91|, v75, v191\n"
      "v_mfma_f32_16x16x32_f16 v[80:83], a[8:11], v[120:123], v[80:83]\n"
      "s_waitcnt lgkmcnt(1)\n"
      "v_pk_mul_f16 v96, v144, v160\n"
      "v_pk_mul_f16 v97, v145, v161\n"
      "v_mfma_f32_16x16x32_f16 v[80:83], a[12:15], v[124:127], v[80:83]\n"
      "v_pk_mul_f16 v98, v146, v162\n"
      "v_pk_mul_f16 v99, v147, v163\n"
      "v_mfma_f32_16x16x32_f16 v[84:87], a[28:31], v[124:127], v[20:23]\n"
      "v_fma_f32 v191, |v92|, v76, v191\n"
      "v_fma_f32 v191, |v93|, v77, v191\n"
      "v_mfma_f32_16x16x32_f16 v[84:87], a[24:27], v[120:123], v[84:87]\n"
      "v_fma_f32 v191, |v94|, v78, v191\n"
      "v_fma_f32 v191, |v95|, v79, v191\n"
      "v_mfma_f32_16x16x32_f16 v[84:87], a[20:23], v[116:119], v[84:87]\n"
      "v_mul_f32 v192, |v80|, v48\n"
      "v_fma_f32 v192, |v81|, v49, v192\n"
      "v_mfma_f32_16x16x32_f16 v[84:87], a[16:19], v[112:115], v[84:87]\n"
      "v_permlane32_swap_b32 v189, v191\n"
      "v_mfma_f32_16x16x32_f16 v[88:91], a[32:35], v[112:115], v[24:27]\n"
      "s_waitcnt lgkmcnt(0)\n"
      "v_pk_mul_f16 v100, v148, v164\n"
      "v_fma_f32 v192, |v82|, v50, v192\n"
      "v_mfma_f32_16x16x32_f16 v[88:91], a[36:39], v[116:119], v[88:91]\n"
      "v_fma_f32 v192, |v83|, v51, v192\n"
      "v_pk_mul_f16 v101, v149, v165\n"
      "v_mfma_f32_16x16x32_f16 v[88:91], a[40:43], v[120:123], v[88:91]\n"
      "ds_read_b128 v[168:171], v207 offset:128\n"
      "v_pk_mul_f16 v102, v150, v166\n"
      "v_mfma_f32_16x16x32_f16 v[88:91], a[44:47], v[124:127], v[88:91]\n"
      "v_pk_mul_f16 v103, v151, v167\n"
      "v_fma_f32 v192, |v84|, v52, v192\n"
      "v_mfma_f32_16x16x32_f16 v[92:95], a[60:63], v[124:127], v[28:31]\n"
      "v_fma_f32 v192, |v85|, v53, v192\n"
      "v_fma_f32 v192, |v86|, v54, v192\n"
      "v_mfma_f32_16x16x32_f16 v[92:95], a[56:59], v[120:123], v[92:95]\n"
      "s_waitcnt vmcnt(5)\n"
      "v_fma_f32 v192, |v87|, v55, v192\n"
      "v_sub_u16_sdwa v176, v244, s36 clamp dst_sel:WORD_1 dst_unused:UNUSED_PAD src0_sel:WORD_0 src1_sel:DWORD\n"
      "v_mfma_f32_16x16x32_f16 v[92:95], a[52:55], v[116:119], v[92:95]\n"
      "v_sub_u16_sdwa v177, v244, s36 clamp dst_sel:WORD_1 dst_unused:UNUSED_PAD src0_sel:WORD_1 src1_sel:DWORD\n"
      "v_sub_u16_sdwa v178, v245, s36 clamp dst_sel:WORD_1 dst_unused:UNUSED_PAD src0_sel:WORD_0 src1_sel:DWORD\n"
      "v_mfma_f32_16x16x32_f16 v[92:95], a[48:51], v[112:115], v[92:95]\n"
      "v_fma_f32 v192, |v88|, v56, v192\n"
      "v_fma_f32 v192, |v89|, v57, v192\n"
      "v_mfma_f32_16x16x32_f16 v[80:83], a[64:67], v[112:115], v[32:35]\n"
      "v_sub_u16_sdwa v179, v245, s36 clamp dst_sel:WORD_1 dst_unused:UNUSED_PAD src0_sel:WORD_1 src1_sel:DWORD\n"
      "v_fma_f32 v192, |v90|, v58, v192\n"
      "v_mfma_f32_16x16x32_f16 v[80:83], a[68:71], v[116:119], v[80:83]\n"
      "s_waitcnt lgkmcnt(0)\n"
      "v_pk_mul_f16 v104, v152, v168\n"
      "v_fma_f32 v192, |v91|, v59, v192\n"
      "v_mfma_f32_16x16x32_f16 v[80:83], a[72:75], v[120:123], v[80:83]\n"
      "v_sub_u16_sdwa v180, v246, s36 clamp dst_sel:WORD_1 dst_unused:UNUSED_PAD src0_sel:WORD_0 src1_sel:DWORD\n"
      "v_pk_mul_f16 v105, v153, v169\n"
      "v_mfma_f32_16x16x32_f16 v[80:83], a[76:79], v[124:127], v[80:83]\n"
      "ds_read_b128 v[172:175], v207 offset:192\n"
      "v_sub_u16_sdwa v181, v246, s36 clamp dst_sel:WORD_1 dst_unused:UNUSED_PAD src0_sel:WORD_1 src1_sel:DWORD\n"
      "v_mfma_f32_16x16x32_f16 v[84:87], a[92:95], v[124:127], v[36:39]\n"
      "v_pk_mul_f16 v106, v154, v170\n"
      "v_sub_u16_sdwa v182, v247, s36 clamp dst_sel:WORD_1 dst_unused:UNUSED_PAD src0_sel:WORD_0 src1_sel:DWORD\n"
      "v_mfma_f32_16x16x32_f16 v[84:87], a[88:91], v[120:123], v[84:87]\n"
      "v_pk_mul_f16 v107, v155, v171\n"
      "v_fma_f32 v192, |v92|, v60, v192\n"
      "v_mfma_f32_16x16x32_f16 v[84:87], a[84:87], v[116:119], v[84:87]\n"
      "v_fma_f32 v192, |v93|, v61, v192\n"
      "v_sub_u16_sdwa v183, v247, s36 clamp dst_sel:WORD_1 dst_unused:UNUSED_PAD src0_sel:WORD_1 src1_sel:DWORD\n"
      "v_mfma_f32_16x16x32_f16 v[84:87], a[80:83], v[112:115], v[84:87]\n"
      "v_fma_f32 v192, |v94|, v62, v192\n"
      "v_fma_f32 v192, |v95|, v63, v192\n"
      "v_mfma_f32_16x16x32_f16 v[88:91], a[96:99], v[112:115], v[40:43]\n"
      "v_cvt_pk_f16_f32 v184, v176, v177\n"
      "v_cvt_pk_f16_f32 v185, v178, v179\n"
      "v_mfma_f32_16x16x32_f16 v[88:91], a[100:103], v[116:119], v[88:91]\n"
      "v_cvt_pk_f16_f32 v186, v180, v181\n"
      "v_fma_f32 v192, |v80|, v64, v192\n"
      "v_mfma_f32_16x16x32_f16 v[88:91], a[104:107], v[120:123], v[88:91]\n"
      "v_fma_f32 v192, |v81|, v65, v192\n"
      "v_cvt_pk_f16_f32 v187, v182, v183\n"
      "v_mfma_f32_16x16x32_f16 v[88:91], a[108:111], v[124:127], v[88:91]\n"
      "s_waitcnt lgkmcnt(0)\n"
      "v_pk_mul_f16 v108, v156, v172\n"
      "v_fma_f32 v192, |v82|, v66, v192\n"
      "v_mfma_f32_16x16x32_f16 v[92:95], a[124:127], v[124:127], v[44:47]\n"
      "ds_write_b128 v1, v[184:187] offset:4608\n"
      "v_mfma_f32_16x16x32_f16 v[92:95], a[120:123], v[120:123], v[92:95]\n"
      "v_fma_f32 v192, |v83|, v67, v192\n"
      "v_pk_mul_f16 v109, v157, v173\n"
      "v_mfma_f32_16x16x32_f16 v[92:95], a[116:119], v[116:119], v[92:95]\n"
      "v_pk_mul_f16 v110, v158, v174\n"
      "v_pk_mul_f16 v111, v159, v175\n"
      "v_mfma_f32_16x16x32_f16 v[92:95], a[112:115], v[112:115], v[92:95]\n"
      "ds_read_b128 v[160:163], v203 offset:4608\n"
      "v_fma_f32 v192, |v84|, v68, v192\n"
      "v_mfma_f32_16x16x32_f16 v[80:83], a[0:3], v[96:99], v[16:19]\n"
      "v_fma_f32 v192, |v85|, v69, v192\n"
      "v_fma_f32 v192, |v86|, v70, v192\n"
      "v_mfma_f32_16x16x32_f16 v[80:83], a[4:7], v[100:103], v[80:83]\n"
      "41:\n"
      "s_cbranch_execz 44b\n"
      "v_mfma_f32_16x16x32_f16 v[80:83], a[8:11], v[104:107], v[80:83]\n"
      "v_fma_f32 v192, |v87|, v71, v192\n"
      "v_fma_f32 v192, |v88|, v72, v192\n"
      "v_mfma_f32_16x16x32_f16 v[80:83], a[12:15], v[108:111], v[80:83]\n"
      "v_fma_f32 v192, |v89|, v73, v192\n"
      "v_fma_f32 v192, |v90|, v74, v192\n"
      "v_mfma_f32_16x16x32_f16 v[84:87], a[28:31], v[108:111], v[20:23]\n"
      "ds_read_b128 v[164:167], v203 offset:4672\n"
      "v_fma_f32 v192, |v91|, v75, v192\n"
      "v_mfma_f32_16x16x32_f16 v[84:87], a[24:27], v[104:107], v[84:87]\n"
      "v_fma_f32 v192, |v92|, v76, v192\n"
      "v_fma_f32 v192, |v93|, v77, v192\n"
      "v_mfma_f32_16x16x32_f16 v[84:87], a[20:23], v[100:103], v[84:87]\n"
      "s_waitcnt lgkmcnt(1)\n"
      "v_pk_mul_f16 v112, v128, v160\n"
      "v_pk_mul_f16 v113, v129, v161\n"
      "v_mfma_f32_16x16x32_f16 v[84:87], a[16:19], v[96:99], v[84:87]\n"
      "v_pk_mul_f16 v114, v130, v162\n"
      "v_pk_mul_f16 v115, v131, v163\n"
      "v_mfma_f32_16x16x32_f16 v[88:91], a[32:35], v[96:99], v[24:27]\n"
      "v_fma_f32 v192, |v94|, v78, v192\n"
      "v_fma_f32 v192, |v95|, v79, v192\n"
      "v_mfma_f32_16x16x32_f16 v[88:91], a[36:39], v[100:103], v[88:91]\n"
      "v_mul_f32 v193, |v80|, v48\n"
      "v_fma_f32 v193, |v81|, v49, v193\n"
      "v_mfma_f32_16x16x32_f16 v[88:91], a[40:43], v[104:107], v[88:91]\n"
      "v_fma_f32 v193, |v82|, v50, v193\n"
      "v_fma_f32 v193, |v83|, v51, v193\n"
      "v_mfma_f32_16x16x32_f16 v[88:91], a[44:47], v[108:111], v[88:91]\n"
      "s_waitcnt lgkmcnt(0)\n"
      "v_pk_mul_f16 v116, v132, v164\n"
      "v_pk_mul_f16 v117, v133, v165\n"
      "v_mfma_f32_16x16x32_f16 v[92:95], a[60:63], v[108:111], v[28:31]\n"
      "ds_read_b128 v[168:171], v203 offset:4736\n"
      "v_pk_mul_f16 v118, v134, v166\n"
      "v_mfma_f32_16x16x32_f16 v[92:95], a[56:59], v[104:107], v[92:95]\n"
      "v_pk_mul_f16 v119, v135, v167\n"
      "v_fma_f32 v193, |v84|, v52, v193\n"
      "v_mfma_f32_16x16x32_f16 v[92:95], a[52:55], v[100:103], v[92:95]\n"
      "v_fma_f32 v193, |v85|, v53, v193\n"
      "v_fma_f32 v193, |v86|, v54, v193\n"
      "v_mfma_f32_16x16x32_f16 v[92:95], a[48:51], v[96:99], v[92:95]\n"
      "v_fma_f32 v193, |v87|, v55, v193\n"
      "v_fma_f32 v193, |v88|, v56, v193\n"
      "v_mfma_f32_16x16x32_f16 v[80:83], a[64:67], v[96:99], v[32:35]\n"
      "v_fma_f32 v193, |v89|, v57, v193\n"
      "v_fma_f32 v193, |v90|, v58, v193\n"
      "v_mfma_f32_16x16x32_f16 v[80:83], a[68:71], v[100:103], v[80:83]\n"
      "ds_read_b128 v[172:175], v203 offset:4800\n"
      "v_fma_f32 v193, |v91|, v59, v193\n"
      "v_mfma_f32_16x16x32_f16 v[80:83], a[72:75], v[104:107], v[80:83]\n"
      "v_fma_f32 v193, |v92|, v60, v193\n"
      "v_fma_f32 v193, |v93|, v61, v193\n"
      "v_mfma_f32_16x16x32_f16 v[80:83], a[76:79], v[108:111], v[80:83]\n"
      "s_waitcnt lgkmcnt(1)\n"
      "v_pk_mul_f16 v120, v136, v168\n"
      "v_pk_mul_f16 v121, v137, v169\n"
      "v_mfma_f32_16x16x32_f16 v[84:87], a[92:95], v[108:111], v[36:39]\n"
      "v_pk_mul_f16 v122, v138, v170\n"
      "v_pk_mul_f16 v123, v139, v171\n"
      "v_mfma_f32_16x16x32_f16 v[84:87], a[88:91], v[104:107], v[84:87]\n"
      "v_fma_f32 v193, |v94|, v62, v193\n"
      "v_fma_f32 v193, |v95|, v63, v193\n"
      "v_mfma_f32_16x16x32_f16 v[84:87], a[84:87], v[100:103], v[84:87]\n"
      "v_fma_f32 v193, |v80|, v64, v193\n"
      "v_fma_f32 v193, |v81|, v65, v193\n"
      "v_mfma_f32_16x16x32_f16 v[84:87], a[80:83], v[96:99], v[84:87]\n"
      "v_fma_f32 v193, |v82|, v66, v193\n"
      "v_fma_f32 v193, |v83|, v67, v193\n"
      "v_mfma_f32_16x16x32_f16 v[88:91], a[96:99], v[96:99], v[40:43]\n"
      "s_waitcnt lgkmcnt(0)\n"
      "v_pk_mul_f16 v124, v140, v172\n"
      "v_pk_mul_f16 v125, v141, v173\n"
      "v_mfma_f32_16x16x32_f16 v[88:91], a[100:103], v[100:103], v[88:91]\n"
      "v_pk_mul_f16 v126, v142, v174\n"
      "v_add_f32 v189, v189, v191\n"
      "v_mfma_f32_16x16x32_f16 v[88:91], a[104:107], v[104:107], v[88:91]\n"
      "v_permlane32_swap_b32 v190, v192\n"
      "v_mfma_f32_16x16x32_f16 v[88:91], a[108:111], v[108:111], v[88:91]\n"
      "v_pk_mul_f16 v127, v143, v175\n"
      "v_fma_f32 v193, |v84|, v68, v193\n"
      "v_mfma_f32_16x16x32_f16 v[92:95], a[124:127], v[108:111], v[44:47]\n"
      "v_fma_f32 v193, |v85|, v69, v193\n"
      "v_add_f32 v190, v190, v192\n"
      "v_mfma_f32_16x16x32_f16 v[92:95], a[120:123], v[104:107], v[92:95]\n"
      "v_fma_f32 v193, |v86|, v70, v193\n"
      "v_fma_f32 v193, |v87|, v71, v193\n"
      "v_mfma_f32_16x16x32_f16 v[92:95], a[116:119], v[100:103], v[92:95]\n"
      "v_permlane16_swap_b32 v189, v190\n"
      "v_mfma_f32_16x16x32_f16 v[92:95], a[112:115], v[96:99], v[92:95]\n"
      "s_waitcnt vmcnt(4)\n"
      "v_sub_u16_sdwa v176, v248, s36 clamp dst_sel:WORD_1 dst_unused:UNUSED_PAD src0_sel:WORD_0 src1_sel:DWORD\n"
      "v_sub_u16_sdwa v177, v248, s36 clamp dst_sel:WORD_1 dst_unused:UNUSED_PAD src0_sel:WORD_1 src1_sel:DWORD\n"
      "v_mfma_f32_16x16x32_f16 v[80:83], a[0:3], v[112:115], v[16:19]\n"
      "v_add_f32 v189, v189, v190\n"
      "v_fma_f32 v193, |v88|, v72, v193\n"
      "v_mfma_f32_16x16x32_f16 v[80:83], a[4:7], v[116:119], v[80:83]\n"
      "v_fma_f32 v193, |v89|, v73, v193\n"
      "v_sub_u16_sdwa v178, v249, s36 clamp dst_sel:WORD_1 dst_unused:UNUSED_PAD src0_sel:WORD_0 src1_sel:DWORD\n"
      "v_mfma_f32_16x16x32_f16 v[80:83], a[8:11], v[120:123], v[80:83]\n"
      "v_pk_mul_f16 v96, v144, v160\n"
      "v_fma_f32 v193, |v90|, v74, v193\n"
      "v_mfma_f32_16x16x32_f16 v[80:83], a[12:15], v[124:127], v[80:83]\n"
      "v_fma_f32 v193, |v91|, v75, v193\n"
      "v_sub_u16_sdwa v179, v249, s36 clamp dst_sel:WORD_1 dst_unused:UNUSED_PAD src0_sel:WORD_1 src1_sel:DWORD\n"
      "v_mfma_f32_16x16x32_f16 v[84:87], a[28:31], v[124:127], v[20:23]\n"
      "v_pk_mul_f16 v97, v145, v161\n"
      "v_sub_u16_sdwa v180, v250, s36 clamp dst_sel:WORD_1 dst_unused:UNUSED_PAD src0_sel:WORD_0 src1_sel:DWORD\n"
      "v_mfma_f32_16x16x32_f16 v[84:87], a[24:27], v[120:123], v[84:87]\n"
      "v_pk_mul_f16 v98, v146, v162\n"
      "v_sub_u16_sdwa v181, v250, s36 clamp dst_sel:WORD_1 dst_unused:UNUSED_PAD src0_sel:WORD_1 src1_sel:DWORD\n"
      "v_mfma_f32_16x16x32_f16 v[84:87], a[20:23], v[116:119], v[84:87]\n"
      "v_pk_mul_f16 v99, v147, v163\n"
      "v_fma_f32 v193, |v92|, v76, v193\n"
      "v_mfma_f32_16x16x32_f16 v[84:87], a[16:19], v[112:115], v[84:87]\n"
      "v_fma_f32 v193, |v93|, v77, v193\n"
      "v_sub_u16_sdwa v182, v251, s36 clamp dst_sel:WORD_1 dst_unused:UNUSED_PAD src0_sel:WORD_0 src1_sel:DWORD\n"
      "v_mfma_f32_16x16x32_f16 v[88:91], a[32:35], v[112:115], v[24:27]\n"
      "v_fma_f32 v193, |v94|, v78, v193\n"
      "v_fma_f32 v193, |v95|, v79, v193\n"
      "v_mfma_f32_16x16x32_f16 v[88:91], a[36:39], v[116:119], v[88:91]\n"
      "v_sub_u16_sdwa v183, v251, s36 clamp dst_sel:WORD_1 dst_unused:UNUSED_PAD src0_sel:WORD_1 src1_sel:DWORD\n"
      "v_cvt_pk_f16_f32 v184, v176, v177\n"
      "v_mfma_f32_16x16x32_f16 v[88:91], a[40:43], v[120:123], v[88:91]\n"
      "v_cvt_pk_f16_f32 v185, v178, v179\n"
      "v_mul_f32 v190, |v80|, v48\n"
      "v_mfma_f32_16x16x32_f16 v[88:91], a[44:47], v[124:127], v[88:91]\n"
      "v_fma_f32 v190, |v81|, v49, v190\n"
      "v_cvt_pk_f16_f32 v186, v180, v181\n"
      "v_mfma_f32_16x16x32_f16 v[92:95], a[60:63], v[124:127], v[28:31]\n"
      "v_pk_mul_f16 v100, v148, v164\n"
      "v_fma_f32 v190, |v82|, v50, v190\n"
      "v_mfma_f32_16x16x32_f16 v[92:95], a[56:59], v[120:123], v[92:95]\n"
      "v_fma_f32 v190, |v83|, v51, v190\n"
      "v_cvt_pk_f16_f32 v187, v182, v183\n"
      "v_mfma_f32_16x16x32_f16 v[92:95], a[52:55], v[116:119], v[92:95]\n"
      "v_pk_mul_f16 v101, v149, v165\n"
      "v_pk_mul_f16 v102, v150, v166\n"
      "v_mfma_f32_16x16x32_f16 v[92:95], a[48:51], v[112:115], v[92:95]\n"
      "ds_write_b128 v1, v[184:187] offset:5760\n"
      "v_mfma_f32_16x16x32_f16 v[80:83], a[64:67], v[112:115], v[32:35]\n"
      "v_pk_mul_f16 v103, v151, v167\n"
      "v_fma_f32 v190, |v84|, v52, v190\n"
      "v_mfma_f32_16x16x32_f16 v[80:83], a[68:71], v[116:119], v[80:83]\n"
      "v_fma_f32 v190, |v85|, v53, v190\n"
      "v_fma_f32 v190, |v86|, v54, v190\n"
      "v_mfma_f32_16x16x32_f16 v[80:83], a[72:75], v[120:123], v[80:83]\n"
      "v_fma_f32 v190, |v87|, v55, v190\n"
      "v_fma_f32 v190, |v88|, v56, v190\n"
      "v_mfma_f32_16x16x32_f16 v[80:83], a[76:79], v[124:127], v[80:83]\n"
      "v_fma_f32 v190, |v89|, v57, v190\n"
      "v_pk_mul_f16 v104, v152, v168\n"
      "v_mfma_f32_16x16x32_f16 v[84:87], a[92:95], v[124:127], v[36:39]\n"
      "v_fma_f32 v190, |v90|, v58, v190\n"
      "v_fma_f32 v190, |v91|, v59, v190\n"
      "v_mfma_f32_16x16x32_f16 v[84:87], a[88:91], v[120:123], v[84:87]\n"
      "v_pk_mul_f16 v105, v153, v169\n"
      "v_pk_mul_f16 v106, v154, v170\n"
      "v_mfma_f32_16x16x32_f16 v[84:87], a[84:87], v[116:119], v[84:87]\n"
      "v_pk_mul_f16 v107, v155, v171\n"
      "v_fma_f32 v190, |v92|, v60, v190\n"
      "v_mfma_f32_16x16x32_f16 v[84:87], a[80:83], v[112:115], v[84:87]\n"
      "v_fma_f32 v190, |v93|, v61, v190\n"
      "v_fma_f32 v190, |v94|, v62, v190\n"
      "v_mfma_f32_16x16x32_f16 v[88:91], a[96:99], v[112:115], v[40:43]\n"
      "v_fma_f32 v190, |v95|, v63, v190\n"
      "v_fma_f32 v190, |v80|, v64, v190\n"
      "v_mfma_f32_16x16x32_f16 v[88:91], a[100:103], v[116:119], v[88:91]\n"
      "v_permlane32_swap_b32 v188, v193\n"
      "v_mfma_f32_16x16x32_f16 v[88:91], a[104:107], v[120:123], v[88:91]\n"
      "v_fma_f32 v190, |v81|, v65, v190\n"
      "v_pk_mul_f16 v108, v156, v172\n"
      "v_mfma_f32_16x16x32_f16 v[88:91], a[108:111], v[124:127], v[88:91]\n"
      "v_fma_f32 v190, |v82|, v66, v190\n"
      "v_fma_f32 v190, |v83|, v67, v190\n"
      "v_mfma_f32_16x16x32_f16 v[92:95], a[124:127], v[124:127], v[44:47]\n"
      "ds_read_b128 v[160:163], v203 offset:5184\n"
      "v_pk_mul_f16 v109, v157, v173\n"
      "v_mfma_f32_16x16x32_f16 v[92:95], a[120:123], v[120:123], v[92:95]\n"
      "v_pk_mul_f16 v110, v158, v174\n"
      "v_pk_mul_f16 v111, v159, v175\n"
      "v_mfma_f32_16x16x32_f16 v[92:95], a[116:119], v[116:119], v[92:95]\n"
      "v_fma_f32 v190, |v84|, v68, v190\n"
      "v_fma_f32 v190, |v85|, v69, v190\n"
      "v_mfma_f32_16x16x32_f16 v[92:95], a[112:115], v[112:115], v[92:95]\n"
      "v_fma_f32 v190, |v86|, v70, v190\n"
      "v_fma_f32 v190, |v87|, v71, v190\n"
      "v_mfma_f32_16x16x32_f16 v[80:83], a[0:3], v[96:99], v[16:19]\n"
      "v_fma_f32 v190, |v88|, v72, v190\n"
      "v_fma_f32 v190, |v89|, v73, v190\n"
      "v_mfma_f32_16x16x32_f16 v[80:83], a[4:7], v[100:103], v[80:83]\n"
      "v_fma_f32 v190, |v90|, v74, v190\n"
      "v_fma_f32 v190, |v91|, v75, v190\n"
      "v_mfma_f32_16x16x32_f16 v[80:83], a[8:11], v[104:107], v[80:83]\n"
      "s_waitcnt vmcnt(3)\n"
      "ds_read_b128 v[164:167], v203 offset:5248\n"
      "v_sub_u16_sdwa v176, v252, s36 clamp dst_sel:WORD_1 dst_unused:UNUSED_PAD src0_sel:WORD_0 src1_sel:DWORD\n"
      "v_mfma_f32_16x16x32_f16 v[80:83], a[12:15], v[108:111], v[80:83]\n"
      "s_waitcnt lgkmcnt(1)\n"
      "v_pk_mul_f16 v112, v128, v160\n"
      "v_pk_mul_f16 v113, v129, v161\n"
      "v_mfma_f32_16x16x32_f16 v[84:87], a[28:31], v[108:111], v[20:23]\n"
      "v_pk_mul_f16 v114, v130, v162\n"
      "v_pk_mul_f16 v115, v131, v163\n"
      "v_mfma_f32_16x16x32_f16 v[84:87], a[24:27], v[104:107], v[84:87]\n"
      "v_fma_f32 v190, |v92|, v76, v190\n"
      "v_fma_f32 v190, |v93|, v77, v190\n"
      "v_mfma_f32_16x16x32_f16 v[84:87], a[20:23], v[100:103], v[84:87]\n"
      "v_sub_u16_sdwa v177, v252, s36 clamp dst_sel:WORD_1 dst_unused:UNUSED_PAD src0_sel:WORD_1 src1_sel:DWORD\n"
      "v_fma_f32 v190, |v94|, v78, v190\n"
      "v_mfma_f32_16x16x32_f16 v[84:87], a[16:19], v[96:99], v[84:87]\n"
      "v_fma_f32 v190, |v95|, v79, v190\n"
      "v_sub_u16_sdwa v178, v253, s36 clamp dst_sel:WORD_1 dst_unused:UNUSED_PAD src0_sel:WORD_0 src1_sel:DWORD\n"
      "v_mfma_f32_16x16x32_f16 v[88:91], a[32:35], v[96:99], v[24:27]\n"
      "v_sub_u16_sdwa v179, v253, s36 clamp dst_sel:WORD_1 dst_unused:UNUSED_PAD src0_sel:WORD_1 src1_sel:DWORD\n"
      "v_sub_u16_sdwa v180, v254, s36 clamp dst_sel:WORD_1 dst_unused:UNUSED_PAD src0_sel:WORD_0 src1_sel:DWORD\n"
      "v_mfma_f32_16x16x32_f16 v[88:91], a[36:39], v[100:103], v[88:91]\n"
      "v_mul_f32 v191, |v80|, v48\n"
      "v_fma_f32 v191, |v81|, v49, v191\n"
      "v_mfma_f32_16x16x32_f16 v[88:91], a[40:43], v[104:107], v[88:91]\n"
      "s_waitcnt lgkmcnt(0)\n"
      "v_sub_u16_sdwa v181, v254, s36 clamp dst_sel:WORD_1 dst_unused:UNUSED_PAD src0_sel:WORD_1 src1_sel:DWORD\n"
      "v_pk_mul_f16 v116, v132, v164\n"
      "v_mfma_f32_16x16x32_f16 v[88:91], a[44:47], v[108:111], v[88:91]\n"
      "v_fma_f32 v191, |v82|, v50, v191\n"
      "v_fma_f32 v191, |v83|, v51, v191\n"
      "v_mfma_f32_16x16x32_f16 v[92:95], a[60:63], v[108:111], v[28:31]\n"
      "v_sub_u16_sdwa v182, v255, s36 clamp dst_sel:WORD_1 dst_unused:UNUSED_PAD src0_sel:WORD_0 src1_sel:DWORD\n"
      "v_pk_mul_f16 v117, v133, v165\n"
      "v_mfma_f32_16x16x32_f16 v[92:95], a[56:59], v[104:107], v[92:95]\n"
      "ds_read_b128 v[168:171], v203 offset:5312\n"
      "v_sub_u16_sdwa v183, v255, s36 clamp dst_sel:WORD_1 dst_unused:UNUSED_PAD src0_sel:WORD_1 src1_sel:DWORD\n"
      "v_mfma_f32_16x16x32_f16 v[92:95], a[52:55], v[100:103], v[92:95]\n"
      "v_pk_mul_f16 v118, v134, v166\n"
      "v_cvt_pk_f16_f32 v184, v176, v177\n"
      "v_mfma_f32_16x16x32_f16 v[92:95], a[48:51], v[96:99], v[92:95]\n"
      "v_pk_mul_f16 v119, v135, v167\n"
      "v_fma_f32 v191, |v84|, v52, v191\n"
      "v_mfma_f32_16x16x32_f16 v[80:83], a[64:67], v[96:99], v[32:35]\n"
      "v_fma_f32 v191, |v85|, v53, v191\n"
      "v_cvt_pk_f16_f32 v185, v178, v179\n"
      "v_mfma_f32_16x16x32_f16 v[80:83], a[68:71], v[100:103], v[80:83]\n"
      "v_fma_f32 v191, |v86|, v54, v191\n"
      "v_fma_f32 v191, |v87|, v55, v191\n"
      "v_mfma_f32_16x16x32_f16 v[80:83], a[72:75], v[104:107], v[80:83]\n"
      "v_cvt_pk_f16_f32 v186, v180, v181\n"
      "v_cvt_pk_f16_f32 v187, v182, v183\n"
      "v_mfma_f32_16x16x32_f16 v[80:83], a[76:79], v[108:111], v[80:83]\n"
      "ds_write_b128 v1, v[184:187] offset:6912\n"
      "v_mfma_f32_16x16x32_f16 v[84:87], a[92:95], v[108:111], v[36:39]\n"
      "v_fma_f32 v191, |v88|, v56, v191\n"
      "v_fma_f32 v191, |v89|, v57, v191\n"
      "v_mfma_f32_16x16x32_f16 v[84:87], a[88:91], v[104:107], v[84:87]\n"
      "s_waitcnt lgkmcnt(1)\n"
      "v_pk_mul_f16 v120, v136, v168\n"
      "v_fma_f32 v191, |v90|, v58, v191\n"
      "v_mfma_f32_16x16x32_f16 v[84:87], a[84:87], v[100:103], v[84:87]\n"
      "v_fma_f32 v191, |v91|, v59, v191\n"
      "v_pk_mul_f16 v121, v137, v169\n"
      "v_mfma_f32_16x16x32_f16 v[84:87], a[80:83], v[96:99], v[84:87]\n"
      "ds_read_b128 v[172:175], v203 offset:5376\n"
      "v_pk_mul_f16 v122, v138, v170\n"
      "v_mfma_f32_16x16x32_f16 v[88:91], a[96:99], v[96:99], v[40:43]\n"
      "v_pk_mul_f16 v123, v139, v171\n"
      "v_fma_f32 v191, |v92|, v60, v191\n"
      "v_mfma_f32_16x16x32_f16 v[88:91], a[100:103], v[100:103], v[88:91]\n"
      "v_fma_f32 v191, |v93|, v61, v191\n"
      "v_fma_f32 v191, |v94|, v62, v191\n"
      "v_mfma_f32_16x16x32_f16 v[88:91], a[104:107], v[104:107], v[88:91]\n"
      "v_fma_f32 v191, |v95|, v63, v191\n"
      "v_fma_f32 v191, |v80|, v64, v191\n"
      "v_mfma_f32_16x16x32_f16 v[88:91], a[108:111], v[108:111], v[88:91]\n"
      "v_fma_f32 v191, |v81|, v65, v191\n"
      "v_fma_f32 v191, |v82|, v66, v191\n"
      "v_mfma_f32_16x16x32_f16 v[92:95], a[124:127], v[108:111], v[44:47]\n"
      "ds_read_b128 v[176:179], v206 offset:864\n"
      "v_fma_f32 v191, |v83|, v67, v191\n"
      "v_mfma_f32_16x16x32_f16 v[92:95], a[120:123], v[104:107], v[92:95]\n"
      "ds_read_b128 v[180:183], v206 offset:2016\n"
      "v_add_f32 v188, v188, v193\n"
      "v_mfma_f32_16x16x32_f16 v[92:95], a[116:119], v[100:103], v[92:95]\n"
      "s_waitcnt lgkmcnt(2)\n"
      "v_pk_mul_f16 v124, v140, v172\n"
      "v_pk_mul_f16 v125, v141, v173\n"
      "v_mfma_f32_16x16x32_f16 v[92:95], a[112:115], v[96:99], v[92:95]\n"
      "v_pk_mul_f16 v126, v142, v174\n"
      "v_pk_mul_f16 v127, v143, v175\n"
      "v_mfma_f32_16x16x32_f16 v[80:83], a[0:3], v[112:115], v[16:19]\n"
      "v_fma_f32 v191, |v84|, v68, v191\n"
      "v_fma_f32 v191, |v85|, v69, v191\n"
      "v_mfma_f32_16x16x32_f16 v[80:83], a[4:7], v[116:119], v[80:83]\n"
      "ds_read2_b32 v[184:185], v8 offset0:0 offset1:72\n"
      "v_fma_f32 v191, |v86|, v70, v191\n"
      "v_mfma_f32_16x16x32_f16 v[80:83], a[8:11], v[120:123], v[80:83]\n"
      "v_fma_f32 v191, |v87|, v71, v191\n"
      "v_fma_f32 v191, |v88|, v72, v191\n"
      "v_mfma_f32_16x16x32_f16 v[80:83], a[12:15], v[124:127], v[80:83]\n"
      "v_fma_f32 v191, |v89|, v73, v191\n"
      "v_pk_mul_f16 v96, v144, v160\n"
      "v_mfma_f32_16x16x32_f16 v[84:87], a[28:31], v[124:127], v[20:23]\n"
      "v_fma_f32 v191, |v90|, v74, v191\n"
      "v_fma_f32 v191, |v91|, v75, v191\n"
      "v_mfma_f32_16x16x32_f16 v[84:87], a[24:27], v[120:123], v[84:87]\n"
      "v_pk_mul_f16 v97, v145, v161\n"
      "v_pk_mul_f16 v98, v146, v162\n"
      "v_mfma_f32_16x16x32_f16 v[84:87], a[20:23], v[116:119], v[84:87]\n"
      "v_pk_mul_f16 v99, v147, v163\n"
      "v_fma_f32 v191, |v92|, v76, v191\n"
      "v_mfma_f32_16x16x32_f16 v[84:87], a[16:19], v[112:115], v[84:87]\n"
      "s_waitcnt lgkmcnt(1)\n"
      "v_pk_mul_f16 v176, v176, v180\n"
      "v_pk_mul_f16 v177, v177, v181\n"
      "v_mfma_f32_16x16x32_f16 v[88:91], a[32:35], v[112:115], v[24:27]\n"
      "v_fma_f32 v191, |v93|, v77, v191\n"
      "v_pk_mul_f16 v178, v178, v182\n"
      "v_mfma_f32_16x16x32_f16 v[88:91], a[36:39], v[116:119], v[88:91]\n"
      "v_fma_f32 v191, |v94|, v78, v191\n"
      "v_fma_f32 v191, |v95|, v79, v191\n"
      "v_mfma_f32_16x16x32_f16 v[88:91], a[40:43], v[120:123], v[88:91]\n"
      "s_waitcnt lgkmcnt(0)\n"
      "v_pk_mul_f16 v179, v179, v183\n"
      "v_pk_mul_f16 v184, v184, v185\n"
      "v_mfma_f32_16x16x32_f16 v[88:91], a[44:47], v[124:127], v[88:91]\n"
      "ds_write_b128 v1, v[176:179] offset:8064\n"
      "v_mfma_f32_16x16x32_f16 v[92:95], a[60:63], v[124:127], v[28:31]\n"
      "v_mul_f32 v192, |v80|, v48\n"
      "v_fma_f32 v192, |v81|, v49, v192\n"
      "v_mfma_f32_16x16x32_f16 v[92:95], a[56:59], v[120:123], v[92:95]\n"
      "v_pk_mul_f16 v100, v148, v164\n"
      "v_fma_f32 v192, |v82|, v50, v192\n"
      "v_mfma_f32_16x16x32_f16 v[92:95], a[52:55], v[116:119], v[92:95]\n"
      "ds_write_b32 v8, v184 offset:2304\n"
      "v_mfma_f32_16x16x32_f16 v[92:95], a[48:51], v[112:115], v[92:95]\n"
      "v_fma_f32 v192, |v83|, v51, v192\n"
      "v_pk_mul_f16 v101, v149, v165\n"
      "v_mfma_f32_16x16x32_f16 v[80:83], a[64:67], v[112:115], v[32:35]\n"
      "v_pk_mul_f16 v102, v150, v166\n"
      "v_pk_mul_f16 v103, v151, v167\n"
      "v_mfma_f32_16x16x32_f16 v[80:83], a[68:71], v[116:119], v[80:83]\n"
      "v_fma_f32 v192, |v84|, v52, v192\n"
      "v_fma_f32 v192, |v85|, v53, v192\n"
      "v_mfma_f32_16x16x32_f16 v[80:83], a[72:75], v[120:123], v[80:83]\n"
      "v_fma_f32 v192, |v86|, v54, v192\n"
      "v_fma_f32 v192, |v87|, v55, v192\n"
      "v_mfma_f32_16x16x32_f16 v[80:83], a[76:79], v[124:127], v[80:83]\n"
      "v_fma_f32 v192, |v88|, v56, v192\n"
      "v_fma_f32 v192, |v89|, v57, v192\n"
      "v_mfma_f32_16x16x32_f16 v[84:87], a[92:95], v[124:127], v[36:39]\n"
      "v_pk_mul_f16 v104, v152, v168\n"
      "v_fma_f32 v192, |v90|, v58, v192\n"
      "v_mfma_f32_16x16x32_f16 v[84:87], a[88:91], v[120:123], v[84:87]\n"
      "v_fma_f32 v192, |v91|, v59, v192\n"
      "v_pk_mul_f16 v105, v153, v169\n"
      "v_mfma_f32_16x16x32_f16 v[84:87], a[84:87], v[116:119], v[84:87]\n"
      "ds_read_b32 v224, v227 offset:37888\n"
      "v_pk_mul_f16 v106, v154, v170\n"
      "v_mfma_f32_16x16x32_f16 v[84:87], a[80:83], v[112:115], v[84:87]\n"
      "ds_read_b128 v[176:179], v5 offset:37376\n"
      "v_pk_mul_f16 v107, v155, v171\n"
      "v_mfma_f32_16x16x32_f16 v[88:91], a[96:99], v[112:115], v[40:43]\n"
      "ds_read_b128 v[180:183], v5 offset:37392\n"
      "s_mov_b32 s44, 0x3c800000\n"
      "v_fma_f32 v192, |v92|, v60, v192\n"
      "v_mfma_f32_16x16x32_f16 v[88:91], a[100:103], v[116:119], v[88:91]\n"
      "v_fma_f32 v192, |v93|, v61, v192\n"
      "v_fma_f32 v192, |v94|, v62, v192\n"
      "v_mfma_f32_16x16x32_f16 v[88:91], a[104:107], v[120:123], v[88:91]\n"
      "v_fma_f32 v192, |v95|, v63, v192\n"
      "v_fma_f32 v192, |v80|, v64, v192\n"
      "v_mfma_f32_16x16x32_f16 v[88:91], a[108:111], v[124:127], v[88:91]\n"
      "v_fma_f32 v192, |v81|, v65, v192\n"
      "v_pk_mul_f16 v108, v156, v172\n"
      "v_mfma_f32_16x16x32_f16 v[92:95], a[124:127], v[124:127], v[44:47]\n"
      "v_fma_f32 v192, |v82|, v66, v192\n"
      "v_fma_f32 v192, |v83|, v67, v192\n"
      "v_mfma_f32_16x16x32_f16 v[92:95], a[120:123], v[120:123], v[92:95]\n"
      "ds_read_b128 v[160:163], v203 offset:5760\n"
      "v_pk_mul_f16 v109, v157, v173\n"
      "v_mfma_f32_16x16x32_f16 v[92:95], a[116:119], v[116:119], v[92:95]\n"
      "v_pk_mul_f16 v110, v158, v174\n"
      "v_pk_mul_f16 v111, v159, v175\n"
      "v_mfma_f32_16x16x32_f16 v[92:95], a[112:115], v[112:115], v[92:95]\n"
      "s_waitcnt lgkmcnt(1)\n"
      "v_max3_f32 v225, |v176|, |v181|, |v224|\n"
      "v_cmp_le_f32 vcc, 0x3e600000, v225\n"
      "v_mfma_f32_16x16x32_f16 v[80:83], a[0:3], v[96:99], v[16:19]\n"
      "v_fma_f32 v192, |v84|, v68, v192\n"
      "v_fma_f32 v192, |v85|, v69, v192\n"
      "v_mfma_f32_16x16x32_f16 v[80:83], a[4:7], v[100:103], v[80:83]\n"
      "v_cmp_le_f32 s[28:29], s44, v225\n"
      "v_fma_f32 v192, |v86|, v70, v192\n"
      "v_mfma_f32_16x16x32_f16 v[80:83], a[8:11], v[104:107], v[80:83]\n"
      "v_fma_f32 v192, |v87|, v71, v192\n"
      "v_fma_f32 v192, |v88|, v72, v192\n"
      "v_mfma_f32_16x16x32_f16 v[80:83], a[12:15], v[108:111], v[80:83]\n"
      "s_cmp_eq_u32 s28, 0\n"
      "s_cselect_b64 vcc, -1, vcc\n"
      "v_fma_f32 v192, |v89|, v73, v192\n"
      "v_fma_f32 v192, |v90|, v74, v192\n"
      "v_mfma_f32_16x16x32_f16 v[84:87], a[28:31], v[108:111], v[20:23]\n"
      "s_cbranch_vccnz 5f\n"
      "4:\n"
      "v_mfma_f32_16x16x32_f16 v[84:87], a[24:27], v[104:107], v[84:87]\n"
      "s_waitcnt lgkmcnt(0)\n"
      "v_pk_mul_f16 v112, v128, v160\n"
      "v_cvt_pk_f16_f32 v184, v176, v177\n"
      "v_mfma_f32_16x16x32_f16 v[84:87], a[20:23], v[100:103], v[84:87]\n"
      "v_fma_f32 v192, |v91|, v75, v192\n"
      "v_pk_mul_f16 v113, v129, v161\n"
      "v_mfma_f32_16x16x32_f16 v[84:87], a[16:19], v[96:99], v[84:87]\n"
      "ds_read_b128 v[164:167], v203 offset:5824\n"
      "v_cvt_pk_f16_f32 v185, v178, v179\n"
      "v_mfma_f32_16x16x32_f16 v[88:91], a[32:35], v[96:99], v[24:27]\n"
      "v_pk_mul_f16 v114, v130, v162\n"
      "v_cvt_pk_f16_f32 v186, v180, v181\n"
      "v_mfma_f32_16x16x32_f16 v[88:91], a[36:39], v[100:103], v[88:91]\n"
      "v_pk_mul_f16 v115, v131, v163\n"
      "v_cvt_pk_f16_f32 v187, v182, v183\n"
      "v_mfma_f32_16x16x32_f16 v[88:91], a[40:43], v[104:107], v[88:91]\n"
      "ds_write_b128 v213, v[184:187] offset:16512\n"
      "s_lshl_b32 s49, s40, 1\n"
      "v_fma_f32 v192, |v92|, v76, v192\n"
      "v_fma_f32 v192, |v93|, v77, v192\n"
      "v_mfma_f32_16x16x32_f16 v[88:91], a[44:47], v[108:111], v[88:91]\n"
      "s_add_u32 s51, s57, s49\n"
      "v_fma_f32 v192, |v94|, v78, v192\n"
      "v_fma_f32 v192, |v95|, v79, v192\n"
      "v_mfma_f32_16x16x32_f16 v[92:95], a[60:63], v[108:111], v[28:31]\n"
      "s_add_u32 s51, s51, s58\n"
      "v_mul_f32 v193, |v80|, v48\n"
      "v_fma_f32 v193, |v81|, v49, v193\n"
      "v_mfma_f32_16x16x32_f16 v[92:95], a[56:59], v[104:107], v[92:95]\n"
      "s_add_u32 s51, s51, s56\n"
      "v_fma_f32 v193, |v82|, v50, v193\n"
      "v_fma_f32 v193, |v83|, v51, v193\n"
      "v_mfma_f32_16x16x32_f16 v[92:95], a[52:55], v[100:103], v[92:95]\n"
      "s_waitcnt lgkmcnt(1)\n"
      "v_pk_mul_f16 v116, v132, v164\n"
      "v_pk_mul_f16 v117, v133, v165\n"
      "v_mfma_f32_16x16x32_f16 v[92:95], a[48:51], v[96:99], v[92:95]\n"
      "ds_read_b128 v[168:171], v203 offset:5888\n"
      "s_sub_u32 s51, 0, s51\n"
      "v_pk_mul_f16 v118, v134, v166\n"
      "v_mfma_f32_16x16x32_f16 v[80:83], a[64:67], v[96:99], v[32:35]\n"
      "s_add_u32 s49, s59, s49\n"
      "v_pk_mul_f16 v119, v135, v167\n"
      "v_fma_f32 v193, |v84|, v52, v193\n"
      "v_mfma_f32_16x16x32_f16 v[80:83], a[68:71], v[100:103], v[80:83]\n"
      "s_sub_u32 s39, 16, s49\n"
      "v_fma_f32 v193, |v85|, v53, v193\n"
      "v_fma_f32 v193, |v86|, v54, v193\n"
      "v_mfma_f32_16x16x32_f16 v[80:83], a[72:75], v[104:107], v[80:83]\n"
      "s_movk_i32 s45, 0x207\n"
      "v_fma_f32 v193, |v87|, v55, v193\n"
      "v_fma_f32 v193, |v88|, v56, v193\n"
      "v_mfma_f32_16x16x32_f16 v[80:83], a[76:79], v[108:111], v[80:83]\n"
      "v_fma_f32 v193, |v89|, v57, v193\n"
      "v_fma_f32 v193, |v90|, v58, v193\n"
      "v_mfma_f32_16x16x32_f16 v[84:87], a[92:95], v[108:111], v[36:39]\n"
      "ds_read_b128 v[172:175], v203 offset:5952\n"
      "v_fma_f32 v193, |v91|, v59, v193\n"
      "v_mfma_f32_16x16x32_f16 v[84:87], a[88:91], v[104:107], v[84:87]\n"
      "v_ldexp_f32 v215, v215, s51\n"
      "v_fma_f32 v193, |v92|, v60, v193\n"
      "v_mfma_f32_16x16x32_f16 v[84:87], a[84:87], v[100:103], v[84:87]\n"
      "s_waitcnt lgkmcnt(1)\n"
      "v_pk_mul_f16 v120, v136, v168\n"
      "v_pk_mul_f16 v121, v137, v169\n"
      "v_mfma_f32_16x16x32_f16 v[84:87], a[80:83], v[96:99], v[84:87]\n"
      "v_pk_mul_f16 v122, v138, v170\n"
      "v_pk_mul_f16 v123, v139, v171\n"
      "v_mfma_f32_16x16x32_f16 v[88:91], a[96:99], v[96:99], v[40:43]\n"
      "v_fma_f32 v193, |v93|, v61, v193\n"
      "v_fma_f32 v193, |v94|, v62, v193\n"
      "v_mfma_f32_16x16x32_f16 v[88:91], a[100:103], v[100:103], v[88:91]\n"
      "ds_read_b128 v[240:243], v201 offset:4608\n"
      "v_fma_f32 v193, |v95|, v63, v193\n"
      "v_mfma_f32_16x16x32_f16 v[88:91], a[104:107], v[104:107], v[88:91]\n"
      "ds_read_b128 v[244:247], v201 offset:4672\n"
      "v_fma_f32 v193, |v80|, v64, v193\n"
      "v_mfma_f32_16x16x32_f16 v[88:91], a[108:111], v[108:111], v[88:91]\n"
      "ds_read_b128 v[248:251], v201 offset:4736\n"
      "v_fma_f32 v193, |v81|, v65, v193\n"
      "v_mfma_f32_16x16x32_f16 v[92:95], a[124:127], v[108:111], v[44:47]\n"
      "s_waitcnt lgkmcnt(3)\n"
      "v_pk_mul_f16 v124, v140, v172\n"
      "v_fma_f32 v193, |v82|, v66, v193\n"
      "v_mfma_f32_16x16x32_f16 v[92:95], a[120:123], v[104:107], v[92:95]\n"
      "ds_read_b128 v[252:255], v201 offset:4800\n"
      "v_fma_f32 v193, |v83|, v67, v193\n"
      "v_mfma_f32_16x16x32_f16 v[92:95], a[116:119], v[100:103], v[92:95]\n"
      "v_pk_mul_f16 v125, v141, v173\n"
      "v_pk_mul_f16 v126, v142, v174\n"
      "v_mfma_f32_16x16x32_f16 v[92:95], a[112:115], v[96:99], v[92:95]\n"
      "ds_read_b128 v[176:179], v200 offset:16512\n"
      "v_pk_mul_f16 v127, v143, v175\n"
      "v_mfma_f32_16x16x32_f16 v[80:83], a[0:3], v[112:115], v[16:19]\n"
      "v_fma_f32 v193, |v84|, v68, v193\n"
      "v_fma_f32 v193, |v85|, v69, v193\n"
      "v_mfma_f32_16x16x32_f16 v[80:83], a[4:7], v[116:119], v[80:83]\n"
      "v_fma_f32 v193, |v86|, v70, v193\n"
      "v_fma_f32 v193, |v87|, v71, v193\n"
      "v_mfma_f32_16x16x32_f16 v[80:83], a[8:11], v[120:123], v[80:83]\n"
      "v_fma_f32 v193, |v88|, v72, v193\n"
      "v_fma_f32 v193, |v89|, v73, v193\n"
      "v_mfma_f32_16x16x32_f16 v[80:83], a[12:15], v[124:127], v[80:83]\n"
      "v_pk_mul_f16 v96, v144, v160\n"
      "v_fma_f32 v193, |v90|, v74, v193\n"
      "v_mfma_f32_16x16x32_f16 v[84:87], a[28:31], v[124:127], v[20:23]\n"
      "v_fma_f32 v193, |v91|, v75, v193\n"
      "v_pk_mul_f16 v97, v145, v161\n"
      "v_mfma_f32_16x16x32_f16 v[84:87], a[24:27], v[120:123], v[84:87]\n"
      "ds_read_b128 a[152:155], v214 offset:0\n"
      "v_pk_mul_f16 v98, v146, v162\n"
      "v_mfma_f32_16x16x32_f16 v[84:87], a[20:23], v[116:119], v[84:87]\n"
      "v_pk_mul_f16 v99, v147, v163\n"
      "v_fma_f32 v193, |v92|, v76, v193\n"
      "v_mfma_f32_16x16x32_f16 v[84:87], a[16:19], v[112:115], v[84:87]\n"
      "v_fma_f32 v193, |v93|, v77, v193\n"
      "v_fma_f32 v193, |v94|, v78, v193\n"
      "v_mfma_f32_16x16x32_f16 v[88:91], a[32:35], v[112:115], v[24:27]\n"
      "s_waitcnt lgkmcnt(1)\n"
      "v_fma_f32 v193, |v95|, v79, v193\n"
      "v_pk_mul_f16 v228, v128, v176\n"
      "v_mfma_f32_16x16x32_f16 v[88:91], a[36:39], v[116:119], v[88:91]\n"
      "v_pk_mul_f16 v229, v129, v177\n"
      "v_pk_mul_f16 v230, v130, v178\n"
      "v_mfma_f32_16x16x32_f16 v[88:91], a[40:43], v[120:123], v[88:91]\n"
      "v_pk_mul_f16 v231, v131, v179\n"
      "v_mul_f32 v195, |v80|, v48\n"
      "v_mfma_f32_16x16x32_f16 v[88:91], a[44:47], v[124:127], v[88:91]\n"
      "42:\n"
      "s_cbranch_execz 44b\n"
      "v_mfma_f32_16x16x32_f16 v[92:95], a[60:63], v[124:127], v[28:31]\n"
      "v_fma_f32 v195, |v81|, v49, v195\n"
      "v_pk_mul_f16 v100, v148, v164\n"
      "v_mfma_f32_16x16x32_f16 v[92:95], a[56:59], v[120:123], v[92:95]\n"
      "v_fma_f32 v195, |v82|, v50, v195\n"
      "v_fma_f32 v195, |v83|, v51, v195\n"
      "v_mfma_f32_16x16x32_f16 v[92:95], a[52:55], v[116:119], v[92:95]\n"
      "ds_read_b128 a[168:171], v220 offset:0\n"
      "v_pk_mul_f16 v101, v149, v165\n"
      "v_mfma_f32_16x16x32_f16 v[92:95], a[48:51], v[112:115], v[92:95]\n"
      "s_waitcnt lgkmcnt(1)\n"
      "v_pk_mul_f16 v102, v150, v166\n"
      "v_pk_mul_f16 v103, v151, v167\n"
      "v_mfma_f32_16x16x32_f16 a[128:131], v[228:231], a[152:155], 0\n"
      "v_fma_f32 v195, |v84|, v52, v195\n"
      "v_fma_f32 v195, |v85|, v53, v195\n"
      "v_mfma_f32_16x16x32_f16 v[80:83], a[64:67], v[112:115], v[32:35]\n"
      "v_fma_f32 v195, |v86|, v54, v195\n"
      "v_fma_f32 v195, |v87|, v55, v195\n"
      "v_mfma_f32_16x16x32_f16 v[80:83], a[68:71], v[116:119], v[80:83]\n"
      "v_pk_mul_f16 v232, v144, v176\n"
      "v_pk_mul_f16 v233, v145, v177\n"
      "v_mfma_f32_16x16x32_f16 v[80:83], a[72:75], v[120:123], v[80:83]\n"
      "v_pk_mul_f16 v234, v146, v178\n"
      "v_pk_mul_f16 v235, v147, v179\n"
      "v_mfma_f32_16x16x32_f16 v[80:83], a[76:79], v[124:127], v[80:83]\n"
      "v_fma_f32 v195, |v88|, v56, v195\n"
      "v_fma_f32 v195, |v89|, v57, v195\n"
      "v_mfma_f32_16x16x32_f16 v[84:87], a[92:95], v[124:127], v[36:39]\n"
      "v_pk_mul_f16 v104, v152, v168\n"
      "v_fma_f32 v195, |v90|, v58, v195\n"
      "v_mfma_f32_16x16x32_f16 v[84:87], a[88:91], v[120:123], v[84:87]\n"
      "v_fma_f32 v195, |v91|, v59, v195\n"
      "v_pk_mul_f16 v105, v153, v169\n"
      "v_mfma_f32_16x16x32_f16 v[84:87], a[84:87], v[116:119], v[84:87]\n"
      "ds_read_b128 a[184:187], v221 offset:0\n"
      "v_pk_mul_f16 v106, v154, v170\n"
      "v_mfma_f32_16x16x32_f16 v[84:87], a[80:83], v[112:115], v[84:87]\n"
      "s_waitcnt lgkmcnt(1)\n"
      "v_pk_mul_f16 v107, v155, v171\n"
      "v_fma_f32 v195, |v92|, v60, v195\n"
      "v_mfma_f32_16x16x32_f16 a[132:135], v[232:235], a[168:171], 0\n"
      "v_fma_f32 v195, |v93|, v61, v195\n"
      "v_fma_f32 v195, |v94|, v62, v195\n"
      "v_mfma_f32_16x16x32_f16 v[88:91], a[96:99], v[112:115], v[40:43]\n"
      "v_fma_f32 v195, |v95|, v63, v195\n"
      "v_pk_mul_f16 v236, v240, v176\n"
      "v_mfma_f32_16x16x32_f16 v[88:91], a[100:103], v[116:119], v[88:91]\n"
      "v_pk_mul_f16 v237, v241, v177\n"
      "v_pk_mul_f16 v238, v242, v178\n"
      "v_mfma_f32_16x16x32_f16 v[88:91], a[104:107], v[120:123], v[88:91]\n"
      "v_pk_mul_f16 v239, v243, v179\n"
      "v_fma_f32 v195, |v80|, v64, v195\n"
      "v_mfma_f32_16x16x32_f16 v[88:91], a[108:111], v[124:127], v[88:91]\n"
      "v_fma_f32 v195, |v81|, v65, v195\n"
      "v_pk_mul_f16 v108, v156, v172\n"
      "v_mfma_f32_16x16x32_f16 v[92:95], a[124:127], v[124:127], v[44:47]\n"
      "v_fma_f32 v195, |v82|, v66, v195\n"
      "v_fma_f32 v195, |v83|, v67, v195\n"
      "v_mfma_f32_16x16x32_f16 v[92:95], a[120:123], v[120:123], v[92:95]\n"
      "ds_read_b128 v[160:163], v203 offset:6336\n"
      "v_pk_mul_f16 v109, v157, v173\n"
      "v_mfma_f32_16x16x32_f16 v[92:95], a[116:119], v[116:119], v[92:95]\n"
      "ds_read_b128 v[176:179], v200 offset:16576\n"
      "v_pk_mul_f16 v110, v158, v174\n"
      "v_mfma_f32_16x16x32_f16 v[92:95], a[112:115], v[112:115], v[92:95]\n"
      "s_waitcnt lgkmcnt(2)\n"
      "v_pk_mul_f16 v111, v159, v175\n"
      "v_fma_f32 v195, |v84|, v68, v195\n"
      "v_mfma_f32_16x16x32_f16 a[136:139], v[236:239], a[184:187], 0\n"
      "v_fma_f32 v195, |v85|, v69, v195\n"
      "v_fma_f32 v195, |v86|, v70, v195\n"
      "v_mfma_f32_16x16x32_f16 v[80:83], a[0:3], v[96:99], v[16:19]\n"
      "v_fma_f32 v195, |v87|, v71, v195\n"
      "v_fma_f32 v195, |v88|, v72, v195\n"
      "v_mfma_f32_16x16x32_f16 v[80:83], a[4:7], v[100:103], v[80:83]\n"
      "v_fma_f32 v195, |v89|, v73, v195\n"
      "v_fma_f32 v195, |v90|, v74, v195\n"
      "v_mfma_f32_16x16x32_f16 v[80:83], a[8:11], v[104:107], v[80:83]\n"
      "ds_read_b128 a[156:159], v214 offset:64\n"
      "v_fma_f32 v195, |v91|, v75, v195\n"
      "v_mfma_f32_16x16x32_f16 v[80:83], a[12:15], v[108:111], v[80:83]\n"
      "s_waitcnt lgkmcnt(2)\n"
      "v_pk_mul_f16 v112, v128, v160\n"
      "v_pk_mul_f16 v113, v129, v161\n"
      "v_mfma_f32_16x16x32_f16 v[84:87], a[28:31], v[108:111], v[20:23]\n"
      "ds_read_b128 v[164:167], v203 offset:6400\n"
      "v_pk_mul_f16 v114, v130, v162\n"
      "v_mfma_f32_16x16x32_f16 v[84:87], a[24:27], v[104:107], v[84:87]\n"
      "v_pk_mul_f16 v115, v131, v163\n"
      "v_fma_f32 v195, |v92|, v76, v195\n"
      "v_mfma_f32_16x16x32_f16 v[84:87], a[20:23], v[100:103], v[84:87]\n"
      "v_fma_f32 v195, |v93|, v77, v195\n"
      "v_fma_f32 v195, |v94|, v78, v195\n"
      "v_mfma_f32_16x16x32_f16 v[84:87], a[16:19], v[96:99], v[84:87]\n"
      "s_waitcnt lgkmcnt(2)\n"
      "v_fma_f32 v195, |v95|, v79, v195\n"
      "v_pk_mul_f16 v228, v132, v176\n"
      "v_mfma_f32_16x16x32_f16 v[88:91], a[32:35], v[96:99], v[24:27]\n"
      "v_pk_mul_f16 v229, v133, v177\n"
      "v_pk_mul_f16 v230, v134, v178\n"
      "v_mfma_f32_16x16x32_f16 v[88:91], a[36:39], v[100:103], v[88:91]\n"
      "v_permlane32_swap_b32 v190, v195\n"
      "v_mfma_f32_16x16x32_f16 v[88:91], a[40:43], v[104:107], v[88:91]\n"
      "v_pk_mul_f16 v231, v135, v179\n"
      "v_mul_f32 v196, |v80|, v48\n"
      "v_mfma_f32_16x16x32_f16 v[88:91], a[44:47], v[108:111], v[88:91]\n"
      "s_waitcnt lgkmcnt(0)\n"
      "v_fma_f32 v196, |v81|, v49, v196\n"
      "v_pk_mul_f16 v116, v132, v164\n"
      "v_mfma_f32_16x16x32_f16 v[92:95], a[60:63], v[108:111], v[28:31]\n"
      "v_fma_f32 v196, |v82|, v50, v196\n"
      "v_fma_f32 v196, |v83|, v51, v196\n"
      "v_mfma_f32_16x16x32_f16 v[92:95], a[56:59], v[104:107], v[92:95]\n"
      "ds_read_b128 a[172:175], v220 offset:64\n"
      "v_pk_mul_f16 v117, v133, v165\n"
      "v_mfma_f32_16x16x32_f16 v[92:95], a[52:55], v[100:103], v[92:95]\n"
      "ds_read_b128 v[168:171], v203 offset:6464\n"
      "v_pk_mul_f16 v118, v134, v166\n"
      "v_mfma_f32_16x16x32_f16 v[92:95], a[48:51], v[96:99], v[92:95]\n"
      "v_pk_mul_f16 v119, v135, v167\n"
      "v_fma_f32 v196, |v84|, v52, v196\n"
      "v_mfma_f32_16x16x32_f16 a[128:131], v[228:231], a[156:159], a[128:131]\n"
      "v_fma_f32 v196, |v85|, v53, v196\n"
      "v_fma_f32 v196, |v86|, v54, v196\n"
      "v_mfma_f32_16x16x32_f16 v[80:83], a[64:67], v[96:99], v[32:35]\n"
      "v_fma_f32 v196, |v87|, v55, v196\n"
      "v_pk_mul_f16 v232, v148, v176\n"
      "v_mfma_f32_16x16x32_f16 v[80:83], a[68:71], v[100:103], v[80:83]\n"
      "v_pk_mul_f16 v233, v149, v177\n"
      "v_pk_mul_f16 v234, v150, v178\n"
      "v_mfma_f32_16x16x32_f16 v[80:83], a[72:75], v[104:107], v[80:83]\n"
      "v_pk_mul_f16 v235, v151, v179\n"
      "v_fma_f32 v196, |v88|, v56, v196\n"
      "v_mfma_f32_16x16x32_f16 v[80:83], a[76:79], v[108:111], v[80:83]\n"
      "v_fma_f32 v196, |v89|, v57, v196\n"
      "v_fma_f32 v196, |v90|, v58, v196\n"
      "v_mfma_f32_16x16x32_f16 v[84:87], a[92:95], v[108:111], v[36:39]\n"
      "s_waitcnt lgkmcnt(0)\n"
      "v_pk_mul_f16 v120, v136, v168\n"
      "v_fma_f32 v196, |v91|, v59, v196\n"
      "v_mfma_f32_16x16x32_f16 v[84:87], a[88:91], v[104:107], v[84:87]\n"
      "ds_read_b128 a[188:191], v221 offset:64\n"
      "v_pk_mul_f16 v121, v137, v169\n"
      "v_mfma_f32_16x16x32_f16 v[84:87], a[84:87], v[100:103], v[84:87]\n"
      "v_pk_mul_f16 v122, v138, v170\n"
      "v_pk_mul_f16 v123, v139, v171\n"
      "v_mfma_f32_16x16x32_f16 v[84:87], a[80:83], v[96:99], v[84:87]\n"
      "s_waitcnt vmcnt(2)\n"
      "ds_read_b128 a[216:219], v12 offset:0\n"
      "v_fma_f32 v196, |v92|, v60, v196\n"
      "v_mfma_f32_16x16x32_f16 a[132:135], v[232:235], a[172:175], a[132:135]\n"
      "ds_read_b128 a[220:223], v12 offset:64\n"
      "v_fma_f32 v196, |v93|, v61, v196\n"
      "v_mfma_f32_16x16x32_f16 v[88:91], a[96:99], v[96:99], v[40:43]\n"
      "v_fma_f32 v196, |v94|, v62, v196\n"
      "v_fma_f32 v196, |v95|, v63, v196\n"
      "v_mfma_f32_16x16x32_f16 v[88:91], a[100:103], v[100:103], v[88:91]\n"
      "ds_read_b128 a[224:227], v12 offset:128\n"
      "v_pk_mul_f16 v236, v244, v176\n"
      "v_mfma_f32_16x16x32_f16 v[88:91], a[104:107], v[104:107], v[88:91]\n"
      "ds_read_b128 a[228:231], v12 offset:192\n"
      "v_pk_mul_f16 v237, v245, v177\n"
      "v_mfma_f32_16x16x32_f16 v[88:91], a[108:111], v[108:111], v[88:91]\n"
      "v_pk_mul_f16 v238, v246, v178\n"
      "v_pk_mul_f16 v239, v247, v179\n"
      "v_mfma_f32_16x16x32_f16 v[92:95], a[124:127], v[108:111], v[44:47]\n"
      "v_fma_f32 v196, |v80|, v64, v196\n"
      "v_fma_f32 v196, |v81|, v65, v196\n"
      "v_mfma_f32_16x16x32_f16 v[92:95], a[120:123], v[104:107], v[92:95]\n"
      "v_fma_f32 v196, |v82|, v66, v196\n"
      "v_fma_f32 v196, |v83|, v67, v196\n"
      "v_mfma_f32_16x16x32_f16 v[92:95], a[116:119], v[100:103], v[92:95]\n"
      "ds_read_b128 a[232:235], v13 offset:0\n"
      "v_fma_f32 v196, |v84|, v68, v196\n"
      "v_mfma_f32_16x16x32_f16 v[92:95], a[112:115], v[96:99], v[92:95]\n"
      "s_waitcnt lgkmcnt(5)\n"
      "ds_read_b128 a[236:239], v13 offset:64\n"
      "v_fma_f32 v196, |v85|, v69, v196\n"
      "v_mfma_f32_16x16x32_f16 a[136:139], v[236:239], a[188:191], a[136:139]\n"
      "s_waitcnt lgkmcnt(5)\n"
      "ds_read_b128 a[240:243], v13 offset:128\n"
      "v_fma_f32 v196, |v86|, v70, v196\n"
      "v_mfma_f32_16x16x32_f16 v[80:83], a[0:3], a[216:219], v[16:19]\n"
      "s_waitcnt lgkmcnt(5)\n"
      "ds_read_b128 a[244:247], v13 offset:192\n"
      "v_fma_f32 v196, |v87|, v71, v196\n"
      "v_mfma_f32_16x16x32_f16 v[80:83], a[4:7], a[220:223], v[80:83]\n"
      "s_waitcnt lgkmcnt(5)\n"
      "v_fma_f32 v196, |v88|, v72, v196\n"
      "v_fma_f32 v196, |v89|, v73, v196\n"
      "v_mfma_f32_16x16x32_f16 v[80:83], a[8:11], a[224:227], v[80:83]\n"
      "s_waitcnt lgkmcnt(4)\n"
      "v_fma_f32 v196, |v90|, v74, v196\n"
      "v_fma_f32 v196, |v91|, v75, v196\n"
      "v_mfma_f32_16x16x32_f16 v[80:83], a[12:15], a[228:231], v[80:83]\n"
      "ds_read_b128 v[172:175], v203 offset:6528\n"
      "v_fma_f32 v196, |v92|, v76, v196\n"
      "v_mfma_f32_16x16x32_f16 v[84:87], a[76:79], a[228:231], v[32:35]\n"
      "v_fma_f32 v196, |v93|, v77, v196\n"
      "v_fma_f32 v196, |v94|, v78, v196\n"
      "v_mfma_f32_16x16x32_f16 v[84:87], a[72:75], a[224:227], v[84:87]\n"
      "ds_read_b128 v[176:179], v200 offset:16640\n"
      "v_fma_f32 v196, |v95|, v79, v196\n"
      "v_mfma_f32_16x16x32_f16 v[84:87], a[68:71], a[220:223], v[84:87]\n"
      "v_mul_f32 v199, |v80|, v48\n"
      "v_fma_f32 v199, |v81|, v49, v199\n"
      "v_mfma_f32_16x16x32_f16 v[84:87], a[64:67], a[216:219], v[84:87]\n"
      "s_waitcnt lgkmcnt(5)\n"
      "v_permlane32_swap_b32 v191, v196\n"
      "v_mfma_f32_16x16x32_f16 v[88:91], a[0:3], a[232:235], v[16:19]\n"
      "s_waitcnt lgkmcnt(4)\n"
      "v_fma_f32 v199, |v82|, v50, v199\n"
      "v_fma_f32 v199, |v83|, v51, v199\n"
      "v_mfma_f32_16x16x32_f16 v[88:91], a[4:7], a[236:239], v[88:91]\n"
      "s_waitcnt lgkmcnt(3)\n"
      "v_pk_mul_f16 v96, v144, v160\n"
      "v_pk_mul_f16 v97, v145, v161\n"
      "v_mfma_f32_16x16x32_f16 v[88:91], a[8:11], a[240:243], v[88:91]\n"
      "s_waitcnt lgkmcnt(1)\n"
      "v_pk_mul_f16 v124, v140, v172\n"
      "v_pk_mul_f16 v125, v141, v173\n"
      "v_mfma_f32_16x16x32_f16 v[88:91], a[12:15], a[244:247], v[88:91]\n"
      "v_pk_mul_f16 v126, v142, v174\n"
      "v_pk_mul_f16 v127, v143, v175\n"
      "v_mfma_f32_16x16x32_f16 v[92:95], a[76:79], a[244:247], v[32:35]\n"
      "v_fma_f32 v199, |v84|, v64, v199\n"
      "v_fma_f32 v199, |v85|, v65, v199\n"
      "v_mfma_f32_16x16x32_f16 v[92:95], a[72:75], a[240:243], v[92:95]\n"
      "v_fma_f32 v199, |v86|, v66, v199\n"
      "v_fma_f32 v199, |v87|, v67, v199\n"
      "v_mfma_f32_16x16x32_f16 v[92:95], a[68:71], a[236:239], v[92:95]\n"
      "s_waitcnt vmcnt(0)\n"
      "ds_write_b32 v216, v199 offset:1440\n"
      "v_mfma_f32_16x16x32_f16 v[92:95], a[64:67], a[232:235], v[92:95]\n"
      "v_mul_f32 v225, |v88|, v48\n"
      "v_fma_f32 v225, |v89|, v49, v225\n"
      "v_mfma_f32_16x16x32_f16 v[80:83], a[0:3], v[112:115], v[16:19]\n"
      "v_fma_f32 v225, |v90|, v50, v225\n"
      "v_fma_f32 v225, |v91|, v51, v225\n"
      "v_mfma_f32_16x16x32_f16 v[80:83], a[4:7], v[116:119], v[80:83]\n"
      "ds_read_b128 a[160:163], v214 offset:128\n"
      "v_pk_mul_f16 v98, v146, v162\n"
      "v_mfma_f32_16x16x32_f16 v[80:83], a[8:11], v[120:123], v[80:83]\n"
      "v_pk_mul_f16 v99, v147, v163\n"
      "v_fma_f32 v225, |v92|, v64, v225\n"
      "v_mfma_f32_16x16x32_f16 v[80:83], a[12:15], v[124:127], v[80:83]\n"
      "v_fma_f32 v225, |v93|, v65, v225\n"
      "v_fma_f32 v225, |v94|, v66, v225\n"
      "v_mfma_f32_16x16x32_f16 v[84:87], a[28:31], v[124:127], v[20:23]\n"
      "s_waitcnt lgkmcnt(2)\n"
      "v_fma_f32 v225, |v95|, v67, v225\n"
      "v_pk_mul_f16 v228, v136, v176\n"
      "v_mfma_f32_16x16x32_f16 v[84:87], a[24:27], v[120:123], v[84:87]\n"
      "ds_write_b32 v217, v225 offset:1440\n"
      "v_mfma_f32_16x16x32_f16 v[84:87], a[20:23], v[116:119], v[84:87]\n"
      "v_pk_mul_f16 v229, v137, v177\n"
      "v_pk_mul_f16 v230, v138, v178\n"
      "v_mfma_f32_16x16x32_f16 v[84:87], a[16:19], v[112:115], v[84:87]\n"
      "v_pk_mul_f16 v231, v139, v179\n"
      "v_mul_f32 v197, |v80|, v48\n"
      "v_mfma_f32_16x16x32_f16 v[88:91], a[32:35], v[112:115], v[24:27]\n"
      "v_fma_f32 v197, |v81|, v49, v197\n"
      "v_pk_mul_f16 v100, v148, v164\n"
      "v_mfma_f32_16x16x32_f16 v[88:91], a[36:39], v[116:119], v[88:91]\n"
      "v_fma_f32 v197, |v82|, v50, v197\n"
      "v_fma_f32 v197, |v83|, v51, v197\n"
      "v_mfma_f32_16x16x32_f16 v[88:91], a[40:43], v[120:123], v[88:91]\n"
      "ds_read_b128 a[176:179], v220 offset:128\n"
      "v_pk_mul_f16 v101, v149, v165\n"
      "v_mfma_f32_16x16x32_f16 v[88:91], a[44:47], v[124:127], v[88:91]\n"
      "v_pk_mul_f16 v102, v150, v166\n"
      "v_pk_mul_f16 v103, v151, v167\n"
      "v_mfma_f32_16x16x32_f16 v[92:95], a[60:63], v[124:127], v[28:31]\n"
      "v_fma_f32 v197, |v84|, v52, v197\n"
      "v_fma_f32 v197, |v85|, v53, v197\n"
      "v_mfma_f32_16x16x32_f16 v[92:95], a[56:59], v[120:123], v[92:95]\n"
      "v_fma_f32 v197, |v86|, v54, v197\n"
      "v_fma_f32 v197, |v87|, v55, v197\n"
      "v_mfma_f32_16x16x32_f16 v[92:95], a[52:55], v[116:119], v[92:95]\n"
      "v_pk_mul_f16 v232, v152, v176\n"
      "v_pk_mul_f16 v233, v153, v177\n"
      "v_mfma_f32_16x16x32_f16 v[92:95], a[48:51], v[112:115], v[92:95]\n"
      "s_waitcnt lgkmcnt(2)\n"
      "v_pk_mul_f16 v234, v154, v178\n"
      "v_pk_mul_f16 v235, v155, v179\n"
      "v_mfma_f32_16x16x32_f16 a[128:131], v[228:231], a[160:163], a[128:131]\n"
      "v_fma_f32 v197, |v88|, v56, v197\n"
      "v_fma_f32 v197, |v89|, v57, v197\n"
      "v_mfma_f32_16x16x32_f16 v[80:83], a[64:67], v[112:115], v[32:35]\n"
      "v_pk_mul_f16 v104, v152, v168\n"
      "v_fma_f32 v197, |v90|, v58, v197\n"
      "v_mfma_f32_16x16x32_f16 v[80:83], a[68:71], v[116:119], v[80:83]\n"
      "v_fma_f32 v197, |v91|, v59, v197\n"
      "v_pk_mul_f16 v105, v153, v169\n"
      "v_mfma_f32_16x16x32_f16 v[80:83], a[72:75], v[120:123], v[80:83]\n"
      "ds_read_b128 a[192:195], v221 offset:128\n"
      "v_pk_mul_f16 v106, v154, v170\n"
      "v_mfma_f32_16x16x32_f16 v[80:83], a[76:79], v[124:127], v[80:83]\n"
      "v_pk_mul_f16 v107, v155, v171\n"
      "v_fma_f32 v197, |v92|, v60, v197\n"
      "v_mfma_f32_16x16x32_f16 v[84:87], a[92:95], v[124:127], v[36:39]\n"
      "v_fma_f32 v197, |v93|, v61, v197\n"
      "v_fma_f32 v197, |v94|, v62, v197\n"
      "v_mfma_f32_16x16x32_f16 v[84:87], a[88:91], v[120:123], v[84:87]\n"
      "v_fma_f32 v197, |v95|, v63, v197\n"
      "v_pk_mul_f16 v236, v248, v176\n"
      "v_mfma_f32_16x16x32_f16 v[84:87], a[84:87], v[116:119], v[84:87]\n"
      "v_pk_mul_f16 v237, v249, v177\n"
      "v_pk_mul_f16 v238, v250, v178\n"
      "v_mfma_f32_16x16x32_f16 v[84:87], a[80:83], v[112:115], v[84:87]\n"
      "s_waitcnt lgkmcnt(1)\n"
      "v_pk_mul_f16 v239, v251, v179\n"
      "v_fma_f32 v197, |v80|, v64, v197\n"
      "v_mfma_f32_16x16x32_f16 a[132:135], v[232:235], a[176:179], a[132:135]\n"
      "v_fma_f32 v197, |v81|, v65, v197\n"
      "v_pk_mul_f16 v108, v156, v172\n"
      "v_mfma_f32_16x16x32_f16 v[88:91], a[96:99], v[112:115], v[40:43]\n"
      "v_fma_f32 v197, |v82|, v66, v197\n"
      "v_fma_f32 v197, |v83|, v67, v197\n"
      "v_mfma_f32_16x16x32_f16 v[88:91], a[100:103], v[116:119], v[88:91]\n"
      "ds_read_b128 v[160:163], v202 offset:4608\n"
      "v_pk_mul_f16 v109, v157, v173\n"
      "v_mfma_f32_16x16x32_f16 v[88:91], a[104:107], v[120:123], v[88:91]\n"
      "ds_read_b128 v[176:179], v200 offset:16704\n"
      "v_pk_mul_f16 v110, v158, v174\n"
      "v_mfma_f32_16x16x32_f16 v[88:91], a[108:111], v[124:127], v[88:91]\n"
      "v_add_f32 v190, v190, v195\n"
      "v_pk_mul_f16 v111, v159, v175\n"
      "v_mfma_f32_16x16x32_f16 v[92:95], a[124:127], v[124:127], v[44:47]\n"
      "v_fma_f32 v197, |v84|, v68, v197\n"
      "v_fma_f32 v197, |v85|, v69, v197\n"
      "v_mfma_f32_16x16x32_f16 v[92:95], a[120:123], v[120:123], v[92:95]\n"
      "v_fma_f32 v197, |v86|, v70, v197\n"
      "v_fma_f32 v197, |v87|, v71, v197\n"
      "v_mfma_f32_16x16x32_f16 v[92:95], a[116:119], v[116:119], v[92:95]\n"
      "v_fma_f32 v197, |v88|, v72, v197\n"
      "v_fma_f32 v197, |v89|, v73, v197\n"
      "v_mfma_f32_16x16x32_f16 v[92:95], a[112:115], v[112:115], v[92:95]\n"
      "s_waitcnt lgkmcnt(2)\n"
      "v_fma_f32 v197, |v90|, v74, v197\n"
      "v_fma_f32 v197, |v91|, v75, v197\n"
      "v_mfma_f32_16x16x32_f16 a[136:139], v[236:239], a[192:195], a[136:139]\n"
      "s_waitcnt lgkmcnt(1)\n"
      "v_pk_mul_f16 v112, v240, v160\n"
      "v_pk_mul_f16 v113, v241, v161\n"
      "v_mfma_f32_16x16x32_f16 v[80:83], a[0:3], v[96:99], v[16:19]\n"
      "ds_read_b128 a[164:167], v214 offset:192\n"
      "v_pk_mul_f16 v114, v242, v162\n"
      "v_mfma_f32_16x16x32_f16 v[80:83], a[4:7], v[100:103], v[80:83]\n"
      "ds_read_b128 v[164:167], v202 offset:4672\n"
      "v_pk_mul_f16 v115, v243, v163\n"
      "v_mfma_f32_16x16x32_f16 v[80:83], a[8:11], v[104:107], v[80:83]\n"
      "v_fma_f32 v197, |v92|, v76, v197\n"
      "v_fma_f32 v197, |v93|, v77, v197\n"
      "v_mfma_f32_16x16x32_f16 v[80:83], a[12:15], v[108:111], v[80:83]\n"
      "v_fma_f32 v197, |v94|, v78, v197\n"
      "v_fma_f32 v197, |v95|, v79, v197\n"
      "v_mfma_f32_16x16x32_f16 v[84:87], a[28:31], v[108:111], v[20:23]\n"
      "s_waitcnt lgkmcnt(2)\n"
      "v_pk_mul_f16 v228, v140, v176\n"
      "v_pk_mul_f16 v229, v141, v177\n"
      "v_mfma_f32_16x16x32_f16 v[84:87], a[24:27], v[104:107], v[84:87]\n"
      "v_permlane32_swap_b32 v192, v197\n"
      "v_mfma_f32_16x16x32_f16 v[84:87], a[20:23], v[100:103], v[84:87]\n"
      "v_pk_mul_f16 v230, v142, v178\n"
      "v_pk_mul_f16 v231, v143, v179\n"
      "v_mfma_f32_16x16x32_f16 v[84:87], a[16:19], v[96:99], v[84:87]\n"
      "v_mul_f32 v198, |v80|, v48\n"
      "v_fma_f32 v198, |v81|, v49, v198\n"
      "v_mfma_f32_16x16x32_f16 v[88:91], a[32:35], v[96:99], v[24:27]\n"
      "s_waitcnt lgkmcnt(0)\n"
      "v_pk_mul_f16 v116, v244, v164\n"
      "v_fma_f32 v198, |v82|, v50, v198\n"
      "v_mfma_f32_16x16x32_f16 v[88:91], a[36:39], v[100:103], v[88:91]\n"
      "v_fma_f32 v198, |v83|, v51, v198\n"
      "v_pk_mul_f16 v117, v245, v165\n"
      "v_mfma_f32_16x16x32_f16 v[88:91], a[40:43], v[104:107], v[88:91]\n"
      "ds_read_b128 a[180:183], v220 offset:192\n"
      "v_pk_mul_f16 v118, v246, v166\n"
      "v_mfma_f32_16x16x32_f16 v[88:91], a[44:47], v[108:111], v[88:91]\n"
      "ds_read_b128 v[168:171], v202 offset:4736\n"
      "v_pk_mul_f16 v119, v247, v167\n"
      "v_mfma_f32_16x16x32_f16 v[92:95], a[60:63], v[108:111], v[28:31]\n"
      "v_fma_f32 v198, |v84|, v52, v198\n"
      "v_fma_f32 v198, |v85|, v53, v198\n"
      "v_mfma_f32_16x16x32_f16 v[92:95], a[56:59], v[104:107], v[92:95]\n"
      "v_fma_f32 v198, |v86|, v54, v198\n"
      "v_fma_f32 v198, |v87|, v55, v198\n"
      "v_mfma_f32_16x16x32_f16 v[92:95], a[52:55], v[100:103], v[92:95]\n"
      "v_pk_mul_f16 v232, v156, v176\n"
      "v_pk_mul_f16 v233, v157, v177\n"
      "v_mfma_f32_16x16x32_f16 v[92:95], a[48:51], v[96:99], v[92:95]\n"
      "v_pk_mul_f16 v234, v158, v178\n"
      "v_pk_mul_f16 v235, v159, v179\n"
      "v_mfma_f32_16x16x32_f16 a[128:131], v[228:231], a[164:167], a[128:131]\n"
      "v_fma_f32 v198, |v88|, v56, v198\n"
      "v_fma_f32 v198, |v89|, v57, v198\n"
      "v_mfma_f32_16x16x32_f16 v[80:83], a[64:67], v[96:99], v[32:35]\n"
      "v_fma_f32 v198, |v90|, v58, v198\n"
      "v_fma_f32 v198, |v91|, v59, v198\n"
      "v_mfma_f32_16x16x32_f16 v[80:83], a[68:71], v[100:103], v[80:83]\n"
      "s_waitcnt lgkmcnt(0)\n"
      "v_pk_mul_f16 v120, v248, v168\n"
      "v_pk_mul_f16 v121, v249, v169\n"
      "v_mfma_f32_16x16x32_f16 v[80:83], a[72:75], v[104:107], v[80:83]\n"
      "ds_write_b128 v212, a[128:131] offset:0\n"
      "v_mfma_f32_16x16x32_f16 v[80:83], a[76:79], v[108:111], v[80:83]\n"
      "v_pk_mul_f16 v122, v250, v170\n"
      "v_pk_mul_f16 v123, v251, v171\n"
      "v_mfma_f32_16x16x32_f16 v[84:87], a[92:95], v[108:111], v[36:39]\n"
      "v_fma_f32 v198, |v92|, v60, v198\n"
      "v_fma_f32 v198, |v93|, v61, v198\n"
      "v_mfma_f32_16x16x32_f16 v[84:87], a[88:91], v[104:107], v[84:87]\n"
      "v_fma_f32 v198, |v94|, v62, v198\n"
      "v_fma_f32 v198, |v95|, v63, v198\n"
      "v_mfma_f32_16x16x32_f16 v[84:87], a[84:87], v[100:103], v[84:87]\n"
      "ds_read_b128 a[196:199], v221 offset:192\n"
      "v_pk_mul_f16 v236, v252, v176\n"
      "v_mfma_f32_16x16x32_f16 v[84:87], a[80:83], v[96:99], v[84:87]\n"
      "ds_read_b128 v[172:175], v202 offset:4800\n"
      "v_pk_mul_f16 v237, v253, v177\n"
      "v_mfma_f32_16x16x32_f16 a[132:135], v[232:235], a[180:183], a[132:135]\n"
      "v_pk_mul_f16 v238, v254, v178\n"
      "v_pk_mul_f16 v239, v255, v179\n"
      "v_mfma_f32_16x16x32_f16 v[88:91], a[96:99], v[96:99], v[40:43]\n"
      "v_fma_f32 v198, |v80|, v64, v198\n"
      "v_fma_f32 v198, |v81|, v65, v198\n"
      "v_mfma_f32_16x16x32_f16 v[88:91], a[100:103], v[100:103], v[88:91]\n"
      "v_fma_f32 v198, |v82|, v66, v198\n"
      "v_fma_f32 v198, |v83|, v67, v198\n"
      "v_mfma_f32_16x16x32_f16 v[88:91], a[104:107], v[104:107], v[88:91]\n"
      "ds_write_b128 v212, a[132:135] offset:1280\n"
      "v_mfma_f32_16x16x32_f16 v[88:91], a[108:111], v[108:111], v[88:91]\n"
      "v_add_f32 v191, v191, v196\n"
      "v_fma_f32 v198, |v84|, v68, v198\n"
      "v_mfma_f32_16x16x32_f16 v[92:95], a[124:127], v[108:111], v[44:47]\n"
      "v_fma_f32 v198, |v85|, v69, v198\n"
      "v_fma_f32 v198, |v86|, v70, v198\n"
      "v_mfma_f32_16x16x32_f16 v[92:95], a[120:123], v[104:107], v[92:95]\n"
      "s_waitcnt lgkmcnt(1)\n"
      "v_pk_mul_f16 v124, v252, v172\n"
      "v_pk_mul_f16 v125, v253, v173\n"
      "v_mfma_f32_16x16x32_f16 v[92:95], a[116:119], v[100:103], v[92:95]\n"
      "v_pk_mul_f16 v126, v254, v174\n"
      "v_pk_mul_f16 v127, v255, v175\n"
      "v_mfma_f32_16x16x32_f16 v[92:95], a[112:115], v[96:99], v[92:95]\n"
      "v_fma_f32 v198, |v87|, v71, v198\n"
      "v_fma_f32 v198, |v88|, v72, v198\n"
      "v_mfma_f32_16x16x32_f16 a[136:139], v[236:239], a[196:199], a[136:139]\n"
      "ds_read_b128 v[96:99], v10 offset:0\n"
      "v_fma_f32 v198, |v89|, v73, v198\n"
      "v_mfma_f32_16x16x32_f16 v[80:83], a[0:3], v[112:115], v[16:19]\n"
      "ds_read_b128 v[160:163], v11 offset:0\n"
      "v_fma_f32 v198, |v90|, v74, v198\n"
      "v_mfma_f32_16x16x32_f16 v[80:83], a[4:7], v[116:119], v[80:83]\n"
      "v_fma_f32 v198, |v91|, v75, v198\n"
      "v_fma_f32 v198, |v92|, v76, v198\n"
      "v_mfma_f32_16x16x32_f16 v[80:83], a[8:11], v[120:123], v[80:83]\n"
      "ds_write_b128 v212, a[136:139] offset:2560\n"
      "v_mfma_f32_16x16x32_f16 v[80:83], a[12:15], v[124:127], v[80:83]\n"
      "v_fma_f32 v198, |v93|, v77, v198\n"
      "v_fma_f32 v198, |v94|, v78, v198\n"
      "v_mfma_f32_16x16x32_f16 v[84:87], a[28:31], v[124:127], v[20:23]\n"
      "v_fma_f32 v198, |v95|, v79, v198\n"
      "v_add_f32 v192, v192, v197\n"
      "v_mfma_f32_16x16x32_f16 v[84:87], a[24:27], v[120:123], v[84:87]\n"
      "v_mfma_f32_16x16x32_f16 v[84:87], a[20:23], v[116:119], v[84:87]\n"
      "v_permlane16_swap_b32 v190, v192\n"
      "v_mfma_f32_16x16x32_f16 v[84:87], a[16:19], v[112:115], v[84:87]\n"
      "v_permlane32_swap_b32 v193, v198\n"
      "v_mfma_f32_16x16x32_f16 v[88:91], a[32:35], v[112:115], v[24:27]\n"
      "s_waitcnt lgkmcnt(1)\n"
      "ds_read_b128 v[100:103], v10 offset:64\n"
      "v_pk_mul_f16 v96, v96, v160\n"
      "v_mfma_f32_16x16x32_f16 v[88:91], a[36:39], v[116:119], v[88:91]\n"
      "ds_read_b128 v[164:167], v11 offset:64\n"
      "v_mul_f32 v195, |v80|, v48\n"
      "v_mfma_f32_16x16x32_f16 v[88:91], a[40:43], v[120:123], v[88:91]\n"
      "v_fma_f32 v195, |v81|, v49, v195\n"
      "v_pk_mul_f16 v97, v97, v161\n"
      "v_mfma_f32_16x16x32_f16 v[88:91], a[44:47], v[124:127], v[88:91]\n"
      "v_fma_f32 v195, |v82|, v50, v195\n"
      "v_fma_f32 v195, |v83|, v51, v195\n"
      "v_mfma_f32_16x16x32_f16 v[92:95], a[60:63], v[124:127], v[28:31]\n"
      "v_pk_mul_f16 v98, v98, v162\n"
      "v_pk_mul_f16 v99, v99, v163\n"
      "v_mfma_f32_16x16x32_f16 v[92:95], a[56:59], v[120:123], v[92:95]\n"
      "ds_read_b128 v[104:107], v10 offset:128\n"
      "v_add_f32 v190, v190, v192\n"
      "v_mfma_f32_16x16x32_f16 v[92:95], a[52:55], v[116:119], v[92:95]\n"
      "ds_read_b128 v[168:171], v11 offset:128\n"
      "v_fma_f32 v195, |v84|, v52, v195\n"
      "v_mfma_f32_16x16x32_f16 v[92:95], a[48:51], v[112:115], v[92:95]\n"
      "v_fma_f32 v195, |v85|, v53, v195\n"
      "v_fma_f32 v195, |v86|, v54, v195\n"
      "v_mfma_f32_16x16x32_f16 v[80:83], a[64:67], v[112:115], v[32:35]\n"
      "s_waitcnt lgkmcnt(2)\n"
      "v_fma_f32 v195, |v87|, v55, v195\n"
      "v_pk_mul_f16 v100, v100, v164\n"
      "v_mfma_f32_16x16x32_f16 v[80:83], a[68:71], v[116:119], v[80:83]\n"
      "v_pk_mul_f16 v101, v101, v165\n"
      "v_pk_mul_f16 v102, v102, v166\n"
      "v_mfma_f32_16x16x32_f16 v[80:83], a[72:75], v[120:123], v[80:83]\n"
      "v_fma_f32 v195, |v88|, v56, v195\n"
      "v_fma_f32 v195, |v89|, v57, v195\n"
      "v_mfma_f32_16x16x32_f16 v[80:83], a[76:79], v[124:127], v[80:83]\n"
      "v_pk_mul_f16 v103, v103, v167\n"
      "v_fma_f32 v195, |v90|, v58, v195\n"
      "v_mfma_f32_16x16x32_f16 v[84:87], a[92:95], v[124:127], v[36:39]\n"
      "ds_read_b128 v[108:111], v10 offset:192\n"
      "v_fma_f32 v195, |v91|, v59, v195\n"
      "v_mfma_f32_16x16x32_f16 v[84:87], a[88:91], v[120:123], v[84:87]\n"
      "s_waitcnt lgkmcnt(1)\n"
      "ds_read_b128 v[172:175], v11 offset:192\n"
      "v_pk_mul_f16 v104, v104, v168\n"
      "v_mfma_f32_16x16x32_f16 v[84:87], a[84:87], v[116:119], v[84:87]\n"
      "v_pk_mul_f16 v105, v105, v169\n"
      "v_fma_f32 v195, |v92|, v60, v195\n"
      "v_mfma_f32_16x16x32_f16 v[84:87], a[80:83], v[112:115], v[84:87]\n"
      "v_fma_f32 v195, |v93|, v61, v195\n"
      "v_pk_mul_f16 v106, v106, v170\n"
      "v_mfma_f32_16x16x32_f16 v[88:91], a[96:99], v[112:115], v[40:43]\n"
      "v_fma_f32 v195, |v94|, v62, v195\n"
      "v_fma_f32 v195, |v95|, v63, v195\n"
      "v_mfma_f32_16x16x32_f16 v[88:91], a[100:103], v[116:119], v[88:91]\n"
      "v_pk_mul_f16 v107, v107, v171\n"
      "v_fma_f32 v195, |v80|, v64, v195\n"
      "v_mfma_f32_16x16x32_f16 v[88:91], a[104:107], v[120:123], v[88:91]\n"
      "43:\n"
      "s_cbranch_execz 44b\n"
      "v_mfma_f32_16x16x32_f16 v[88:91], a[108:111], v[124:127], v[88:91]\n"
      "v_fma_f32 v195, |v81|, v65, v195\n"
      "v_fma_f32 v195, |v82|, v66, v195\n"
      "v_mfma_f32_16x16x32_f16 v[92:95], a[124:127], v[124:127], v[44:47]\n"
      "s_waitcnt lgkmcnt(0)\n"
      "v_pk_mul_f16 v108, v108, v172\n"
      "v_fma_f32 v195, |v83|, v67, v195\n"
      "v_mfma_f32_16x16x32_f16 v[92:95], a[120:123], v[120:123], v[92:95]\n"
      "ds_read_b128 v[160:163], v203 offset:6912\n"
      "v_pk_mul_f16 v109, v109, v173\n"
      "v_mfma_f32_16x16x32_f16 v[92:95], a[116:119], v[116:119], v[92:95]\n"
      "v_pk_mul_f16 v110, v110, v174\n"
      "v_pk_mul_f16 v111, v111, v175\n"
      "v_mfma_f32_16x16x32_f16 v[92:95], a[112:115], v[112:115], v[92:95]\n"
      "v_fma_f32 v195, |v84|, v68, v195\n"
      "v_fma_f32 v195, |v85|, v69, v195\n"
      "v_mfma_f32_16x16x32_f16 v[80:83], a[0:3], v[96:99], v[16:19]\n"
      "v_fma_f32 v195, |v86|, v70, v195\n"
      "v_fma_f32 v195, |v87|, v71, v195\n"
      "v_mfma_f32_16x16x32_f16 v[80:83], a[4:7], v[100:103], v[80:83]\n"
      "v_fma_f32 v195, |v88|, v72, v195\n"
      "v_fma_f32 v195, |v89|, v73, v195\n"
      "v_mfma_f32_16x16x32_f16 v[80:83], a[8:11], v[104:107], v[80:83]\n"
      "v_fma_f32 v195, |v90|, v74, v195\n"
      "v_fma_f32 v195, |v91|, v75, v195\n"
      "v_mfma_f32_16x16x32_f16 v[80:83], a[12:15], v[108:111], v[80:83]\n"
      "ds_read_b128 v[164:167], v203 offset:6976\n"
      "v_fma_f32 v195, |v92|, v76, v195\n"
      "v_mfma_f32_16x16x32_f16 v[84:87], a[28:31], v[108:111], v[20:23]\n"
      "s_waitcnt lgkmcnt(1)\n"
      "v_pk_mul_f16 v112, v128, v160\n"
      "v_pk_mul_f16 v113, v129, v161\n"
      "v_mfma_f32_16x16x32_f16 v[84:87], a[24:27], v[104:107], v[84:87]\n"
      "v_pk_mul_f16 v114, v130, v162\n"
      "v_pk_mul_f16 v115, v131, v163\n"
      "v_mfma_f32_16x16x32_f16 v[84:87], a[20:23], v[100:103], v[84:87]\n"
      "v_fma_f32 v195, |v93|, v77, v195\n"
      "v_fma_f32 v195, |v94|, v78, v195\n"
      "v_mfma_f32_16x16x32_f16 v[84:87], a[16:19], v[96:99], v[84:87]\n"
      "v_fma_f32 v195, |v95|, v79, v195\n"
      "v_add_f32 v193, v193, v198\n"
      "v_mfma_f32_16x16x32_f16 v[88:91], a[32:35], v[96:99], v[24:27]\n"
      "v_mul_f32 v196, |v80|, v48\n"
      "v_fma_f32 v196, |v81|, v49, v196\n"
      "v_mfma_f32_16x16x32_f16 v[88:91], a[36:39], v[100:103], v[88:91]\n"
      "v_permlane16_swap_b32 v191, v193\n"
      "v_mfma_f32_16x16x32_f16 v[88:91], a[40:43], v[104:107], v[88:91]\n"
      "s_waitcnt lgkmcnt(0)\n"
      "v_pk_mul_f16 v116, v132, v164\n"
      "v_fma_f32 v196, |v82|, v50, v196\n"
      "v_mfma_f32_16x16x32_f16 v[88:91], a[44:47], v[108:111], v[88:91]\n"
      "v_fma_f32 v196, |v83|, v51, v196\n"
      "v_pk_mul_f16 v117, v133, v165\n"
      "v_mfma_f32_16x16x32_f16 v[92:95], a[60:63], v[108:111], v[28:31]\n"
      "ds_read_b128 v[168:171], v203 offset:7040\n"
      "v_pk_mul_f16 v118, v134, v166\n"
      "v_mfma_f32_16x16x32_f16 v[92:95], a[56:59], v[104:107], v[92:95]\n"
      "v_add_f32 v191, v191, v193\n"
      "v_pk_mul_f16 v119, v135, v167\n"
      "v_mfma_f32_16x16x32_f16 v[92:95], a[52:55], v[100:103], v[92:95]\n"
      "v_fma_f32 v196, |v84|, v52, v196\n"
      "v_fma_f32 v196, |v85|, v53, v196\n"
      "v_mfma_f32_16x16x32_f16 v[92:95], a[48:51], v[96:99], v[92:95]\n"
      "v_fma_f32 v196, |v86|, v54, v196\n"
      "v_fma_f32 v196, |v87|, v55, v196\n"
      "v_mfma_f32_16x16x32_f16 v[80:83], a[64:67], v[96:99], v[32:35]\n"
      "v_permlane32_swap_b32 v194, v195\n"
      "v_mfma_f32_16x16x32_f16 v[80:83], a[68:71], v[100:103], v[80:83]\n"
      "v_fma_f32 v196, |v88|, v56, v196\n"
      "v_fma_f32 v196, |v89|, v57, v196\n"
      "v_mfma_f32_16x16x32_f16 v[80:83], a[72:75], v[104:107], v[80:83]\n"
      "v_fma_f32 v196, |v90|, v58, v196\n"
      "v_fma_f32 v196, |v91|, v59, v196\n"
      "v_mfma_f32_16x16x32_f16 v[80:83], a[76:79], v[108:111], v[80:83]\n"
      "s_waitcnt lgkmcnt(0)\n"
      "v_pk_mul_f16 v120, v136, v168\n"
      "v_pk_mul_f16 v121, v137, v169\n"
      "v_mfma_f32_16x16x32_f16 v[84:87], a[92:95], v[108:111], v[36:39]\n"
      "ds_read_b128 v[172:175], v203 offset:7104\n"
      "v_pk_mul_f16 v122, v138, v170\n"
      "v_mfma_f32_16x16x32_f16 v[84:87], a[88:91], v[104:107], v[84:87]\n"
      "v_pk_mul_f16 v123, v139, v171\n"
      "v_fma_f32 v196, |v92|, v60, v196\n"
      "v_mfma_f32_16x16x32_f16 v[84:87], a[84:87], v[100:103], v[84:87]\n"
      "v_fma_f32 v196, |v93|, v61, v196\n"
      "v_fma_f32 v196, |v94|, v62, v196\n"
      "v_mfma_f32_16x16x32_f16 v[84:87], a[80:83], v[96:99], v[84:87]\n"
      "ds_read2_b32 v[228:229], v208 offset0:0 offset1:8\n"
      "v_fma_f32 v196, |v95|, v63, v196\n"
      "v_mfma_f32_16x16x32_f16 v[88:91], a[96:99], v[96:99], v[40:43]\n"
      "ds_read2_b32 v[230:231], v209 offset0:0 offset1:8\n"
      "v_fma_f32 v196, |v80|, v64, v196\n"
      "v_mfma_f32_16x16x32_f16 v[88:91], a[100:103], v[100:103], v[88:91]\n"
      "ds_read2_b32 v[232:233], v210 offset0:0 offset1:8\n"
      "v_fma_f32 v196, |v81|, v65, v196\n"
      "v_mfma_f32_16x16x32_f16 v[88:91], a[104:107], v[104:107], v[88:91]\n"
      "v_fma_f32 v196, |v82|, v66, v196\n"
      "v_fma_f32 v196, |v83|, v67, v196\n"
      "v_mfma_f32_16x16x32_f16 v[88:91], a[108:111], v[108:111], v[88:91]\n"
      "s_waitcnt lgkmcnt(3)\n"
      "v_pk_mul_f16 v124, v140, v172\n"
      "v_pk_mul_f16 v125, v141, v173\n"
      "v_mfma_f32_16x16x32_f16 v[92:95], a[124:127], v[108:111], v[44:47]\n"
      "ds_read2_b32 v[234:235], v211 offset0:0 offset1:8\n"
      "v_pk_mul_f16 v126, v142, v174\n"
      "v_mfma_f32_16x16x32_f16 v[92:95], a[120:123], v[104:107], v[92:95]\n"
      "ds_read2_b32 v[236:237], v218 offset0:0 offset1:8\n"
      "v_pk_mul_f16 v127, v143, v175\n"
      "v_mfma_f32_16x16x32_f16 v[92:95], a[116:119], v[100:103], v[92:95]\n"
      "v_fma_f32 v196, |v84|, v68, v196\n"
      "v_fma_f32 v196, |v85|, v69, v196\n"
      "v_mfma_f32_16x16x32_f16 v[92:95], a[112:115], v[96:99], v[92:95]\n"
      "v_fma_f32 v196, |v86|, v70, v196\n"
      "v_fma_f32 v196, |v87|, v71, v196\n"
      "v_mfma_f32_16x16x32_f16 v[80:83], a[0:3], v[112:115], v[16:19]\n"
      "v_fma_f32 v196, |v88|, v72, v196\n"
      "v_fma_f32 v196, |v89|, v73, v196\n"
      "v_mfma_f32_16x16x32_f16 v[80:83], a[4:7], v[116:119], v[80:83]\n"
      "v_pk_mul_f16 v96, v144, v160\n"
      "v_fma_f32 v196, |v90|, v74, v196\n"
      "v_mfma_f32_16x16x32_f16 v[80:83], a[8:11], v[120:123], v[80:83]\n"
      "v_fma_f32 v196, |v91|, v75, v196\n"
      "v_pk_mul_f16 v97, v145, v161\n"
      "v_mfma_f32_16x16x32_f16 v[80:83], a[12:15], v[124:127], v[80:83]\n"
      "v_pk_mul_f16 v98, v146, v162\n"
      "v_pk_mul_f16 v99, v147, v163\n"
      "v_mfma_f32_16x16x32_f16 v[84:87], a[28:31], v[124:127], v[20:23]\n"
      "v_fma_f32 v196, |v92|, v76, v196\n"
      "v_fma_f32 v196, |v93|, v77, v196\n"
      "v_mfma_f32_16x16x32_f16 v[84:87], a[24:27], v[120:123], v[84:87]\n"
      "v_fma_f32 v196, |v94|, v78, v196\n"
      "v_fma_f32 v196, |v95|, v79, v196\n"
      "v_mfma_f32_16x16x32_f16 v[84:87], a[20:23], v[116:119], v[84:87]\n"
      "s_waitcnt lgkmcnt(4)\n"
      "v_fmac_f32 v189, v215, v228\n"
      "v_add_f32 v194, v194, v195\n"
      "v_mfma_f32_16x16x32_f16 v[84:87], a[16:19], v[112:115], v[84:87]\n"
      "s_waitcnt lgkmcnt(3)\n"
      "v_fmac_f32 v190, v215, v230\n"
      "v_mul_f32 v192, |v80|, v48\n"
      "v_mfma_f32_16x16x32_f16 v[88:91], a[32:35], v[112:115], v[24:27]\n"
      "v_permlane16_swap_b32 v188, v194\n"
      "v_mfma_f32_16x16x32_f16 v[88:91], a[36:39], v[116:119], v[88:91]\n"
      "v_fma_f32 v192, |v81|, v49, v192\n"
      "v_pk_mul_f16 v100, v148, v164\n"
      "v_mfma_f32_16x16x32_f16 v[88:91], a[40:43], v[120:123], v[88:91]\n"
      "s_waitcnt lgkmcnt(2)\n"
      "v_fmac_f32 v191, v215, v232\n"
      "v_fma_f32 v192, |v82|, v50, v192\n"
      "v_mfma_f32_16x16x32_f16 v[88:91], a[44:47], v[124:127], v[88:91]\n"
      "v_fma_f32 v192, |v83|, v51, v192\n"
      "v_pk_mul_f16 v101, v149, v165\n"
      "v_mfma_f32_16x16x32_f16 v[92:95], a[60:63], v[124:127], v[28:31]\n"
      "v_max3_f32 v214, v189, v190, v191\n"
      "v_pk_mul_f16 v102, v150, v166\n"
      "v_mfma_f32_16x16x32_f16 v[92:95], a[56:59], v[120:123], v[92:95]\n"
      "v_add_f32 v188, v188, v194\n"
      "v_min3_f32 v226, v189, v190, v191\n"
      "v_mfma_f32_16x16x32_f16 v[92:95], a[52:55], v[116:119], v[92:95]\n"
      "v_pk_mul_f16 v103, v151, v167\n"
      "v_max_f32_dpp v222, v214, v214 row_ror:4 row_mask:0xf bank_mask:0xf\n"
      "v_mfma_f32_16x16x32_f16 v[92:95], a[48:51], v[112:115], v[92:95]\n"
      "v_fma_f32 v192, |v84|, v52, v192\n"
      "v_fma_f32 v192, |v85|, v53, v192\n"
      "v_mfma_f32_16x16x32_f16 v[80:83], a[64:67], v[112:115], v[32:35]\n"
      "v_fma_f32 v192, |v86|, v54, v192\n"
      "v_fma_f32 v192, |v87|, v55, v192\n"
      "v_mfma_f32_16x16x32_f16 v[80:83], a[68:71], v[116:119], v[80:83]\n"
      "v_max_f32_dpp v222, v214, v222 row_ror:8 row_mask:0xf bank_mask:0xf\n"
      "v_fma_f32 v192, |v88|, v56, v192\n"
      "v_mfma_f32_16x16x32_f16 v[80:83], a[72:75], v[120:123], v[80:83]\n"
      "v_fma_f32 v192, |v89|, v57, v192\n"
      "v_pk_mul_f16 v104, v152, v168\n"
      "v_mfma_f32_16x16x32_f16 v[80:83], a[76:79], v[124:127], v[80:83]\n"
      "v_max_f32_dpp v222, v214, v222 row_ror:12 row_mask:0xf bank_mask:0xf\n"
      "v_fma_f32 v192, |v90|, v58, v192\n"
      "v_mfma_f32_16x16x32_f16 v[84:87], a[92:95], v[124:127], v[36:39]\n"
      "v_fma_f32 v192, |v91|, v59, v192\n"
      "v_pk_mul_f16 v105, v153, v169\n"
      "v_mfma_f32_16x16x32_f16 v[84:87], a[88:91], v[120:123], v[84:87]\n"
      "v_max_f32_dpp v214, v222, v222 row_ror:1 row_mask:0xf bank_mask:0xf\n"
      "v_pk_mul_f16 v106, v154, v170\n"
      "v_mfma_f32_16x16x32_f16 v[84:87], a[84:87], v[116:119], v[84:87]\n"
      "v_pk_mul_f16 v107, v155, v171\n"
      "v_fma_f32 v192, |v92|, v60, v192\n"
      "v_mfma_f32_16x16x32_f16 v[84:87], a[80:83], v[112:115], v[84:87]\n"
      "v_max_f32_dpp v214, v222, v214 row_ror:2 row_mask:0xf bank_mask:0xf\n"
      "v_fma_f32 v192, |v93|, v61, v192\n"
      "v_mfma_f32_16x16x32_f16 v[88:91], a[96:99], v[112:115], v[40:43]\n"
      "v_fma_f32 v192, |v94|, v62, v192\n"
      "v_fma_f32 v192, |v95|, v63, v192\n"
      "v_mfma_f32_16x16x32_f16 v[88:91], a[100:103], v[116:119], v[88:91]\n"
      "v_max_f32_dpp v214, v222, v214 row_ror:3 row_mask:0xf bank_mask:0xf\n"
      "v_fma_f32 v192, |v80|, v64, v192\n"
      "v_mfma_f32_16x16x32_f16 v[88:91], a[104:107], v[120:123], v[88:91]\n"
      "v_fma_f32 v192, |v81|, v65, v192\n"
      "v_pk_mul_f16 v108, v156, v172\n"
      "v_mfma_f32_16x16x32_f16 v[88:91], a[108:111], v[124:127], v[88:91]\n"
      "v_max_f32_dpp v214, v214, v214 row_bcast:15 row_mask:0xa bank_mask:0xf\n"
      "v_fma_f32 v192, |v82|, v66, v192\n"
      "v_mfma_f32_16x16x32_f16 v[92:95], a[124:127], v[124:127], v[44:47]\n"
      "v_fma_f32 v192, |v83|, v67, v192\n"
      "v_pk_mul_f16 v109, v157, v173\n"
      "v_mfma_f32_16x16x32_f16 v[92:95], a[120:123], v[120:123], v[92:95]\n"
      "v_max_f32_dpp v214, v214, v214 row_bcast:31 row_mask:0xc bank_mask:0xf\n"
      "v_pk_mul_f16 v110, v158, v174\n"
      "v_mfma_f32_16x16x32_f16 v[92:95], a[116:119], v[116:119], v[92:95]\n"
      "v_readlane_b32 s62, v214, 63\n"
      "v_pk_mul_f16 v111, v159, v175\n"
      "v_mfma_f32_16x16x32_f16 v[92:95], a[112:115], v[112:115], v[92:95]\n"
      "v_subrev_f32 v189, s62, v189\n"
      "v_fma_f32 v192, |v84|, v68, v192\n"
      "v_mfma_f32_16x16x32_f16 v[80:83], a[0:3], v[96:99], v[16:19]\n"
      "v_fma_f32 v192, |v85|, v69, v192\n"
      "v_subrev_f32 v190, s62, v190\n"
      "v_mfma_f32_16x16x32_f16 v[80:83], a[4:7], v[100:103], v[80:83]\n"
      "v_fma_f32 v192, |v86|, v70, v192\n"
      "v_fma_f32 v192, |v87|, v71, v192\n"
      "v_mfma_f32_16x16x32_f16 v[80:83], a[8:11], v[104:107], v[80:83]\n"
      "v_subrev_f32 v191, s62, v191\n"
      "v_fma_f32 v192, |v88|, v72, v192\n"
      "v_mfma_f32_16x16x32_f16 v[80:83], a[12:15], v[108:111], v[80:83]\n"
      "v_exp_f32 v189, v189\n"
      "v_mfma_f32_16x16x32_f16 v[84:87], a[28:31], v[108:111], v[20:23]\n"
      "v_exp_f32 v190, v190\n"
      "v_mfma_f32_16x16x32_f16 v[84:87], a[24:27], v[104:107], v[84:87]\n"
      "v_exp_f32 v191, v191\n"
      "v_mfma_f32_16x16x32_f16 v[84:87], a[20:23], v[100:103], v[84:87]\n"
      "v_fma_f32 v192, |v89|, v73, v192\n"
      "v_pk_mul_f16 v112, v240, v160\n"
      "v_mfma_f32_16x16x32_f16 v[84:87], a[16:19], v[96:99], v[84:87]\n"
      "v_fma_f32 v192, |v90|, v74, v192\n"
      "v_fma_f32 v192, |v91|, v75, v192\n"
      "v_pk_mul_f16 v113, v241, v161\n"
      "v_mul_f32 v221, v189, v229\n"
      "v_mfma_f32_16x16x32_f16 v[88:91], a[32:35], v[96:99], v[24:27]\n"
      "v_pk_mul_f16 v114, v242, v162\n"
      "v_fma_f32 v220, v226, 0, v189\n"
      "v_mfma_f32_16x16x32_f16 v[88:91], a[36:39], v[100:103], v[88:91]\n"
      "v_pk_mul_f16 v115, v243, v163\n"
      "v_fmac_f32 v221, v190, v231\n"
      "v_mfma_f32_16x16x32_f16 v[88:91], a[40:43], v[104:107], v[88:91]\n"
      "v_fma_f32 v192, |v92|, v76, v192\n"
      "v_fma_f32 v192, |v93|, v77, v192\n"
      "v_add_f32 v220, v220, v190\n"
      "v_mfma_f32_16x16x32_f16 v[88:91], a[44:47], v[108:111], v[88:91]\n"
      "v_fma_f32 v192, |v94|, v78, v192\n"
      "v_fma_f32 v192, |v95|, v79, v192\n"
      "v_fmac_f32 v221, v191, v233\n"
      "v_mfma_f32_16x16x32_f16 v[92:95], a[60:63], v[108:111], v[28:31]\n"
      "v_add_f32 v220, v220, v191\n"
      "v_mul_f32 v193, |v80|, v48\n"
      "v_mfma_f32_16x16x32_f16 v[92:95], a[56:59], v[104:107], v[92:95]\n"
      "v_fma_f32 v193, |v81|, v49, v193\n"
      "v_pk_mul_f16 v116, v244, v164\n"
      "v_mfma_f32_16x16x32_f16 v[92:95], a[52:55], v[100:103], v[92:95]\n"
      "v_fma_f32 v193, |v82|, v50, v193\n"
      "v_fma_f32 v193, |v83|, v51, v193\n"
      "v_mfma_f32_16x16x32_f16 v[92:95], a[48:51], v[96:99], v[92:95]\n"
      "v_pk_mul_f16 v117, v245, v165\n"
      "v_pk_mul_f16 v118, v246, v166\n"
      "v_mfma_f32_16x16x32_f16 v[80:83], a[64:67], v[96:99], v[32:35]\n"
      "v_pk_mul_f16 v119, v247, v167\n"
      "v_fma_f32 v193, |v84|, v52, v193\n"
      "v_mfma_f32_16x16x32_f16 v[80:83], a[68:71], v[100:103], v[80:83]\n"
      "v_fma_f32 v193, |v85|, v53, v193\n"
      "v_fma_f32 v193, |v86|, v54, v193\n"
      "v_mfma_f32_16x16x32_f16 v[80:83], a[72:75], v[104:107], v[80:83]\n"
      "v_fma_f32 v193, |v87|, v55, v193\n"
      "v_fma_f32 v193, |v88|, v56, v193\n"
      "v_mfma_f32_16x16x32_f16 v[80:83], a[76:79], v[108:111], v[80:83]\n"
      "v_fma_f32 v193, |v89|, v57, v193\n"
      "v_pk_mul_f16 v120, v248, v168\n"
      "v_mfma_f32_16x16x32_f16 v[84:87], a[92:95], v[108:111], v[36:39]\n"
      "v_fma_f32 v193, |v90|, v58, v193\n"
      "v_fma_f32 v193, |v91|, v59, v193\n"
      "v_mfma_f32_16x16x32_f16 v[84:87], a[88:91], v[104:107], v[84:87]\n"
      "v_pk_mul_f16 v121, v249, v169\n"
      "v_pk_mul_f16 v122, v250, v170\n"
      "v_mfma_f32_16x16x32_f16 v[84:87], a[84:87], v[100:103], v[84:87]\n"
      "v_pk_mul_f16 v123, v251, v171\n"
      "v_fma_f32 v193, |v92|, v60, v193\n"
      "v_mfma_f32_16x16x32_f16 v[84:87], a[80:83], v[96:99], v[84:87]\n"
      "v_fma_f32 v193, |v93|, v61, v193\n"
      "v_fma_f32 v193, |v94|, v62, v193\n"
      "v_mfma_f32_16x16x32_f16 v[88:91], a[96:99], v[96:99], v[40:43]\n"
      "v_fma_f32 v193, |v95|, v63, v193\n"
      "v_fma_f32 v193, |v80|, v64, v193\n"
      "v_mfma_f32_16x16x32_f16 v[88:91], a[100:103], v[100:103], v[88:91]\n"
      "v_permlane32_swap_b32 v196, v192\n"
      "v_mfma_f32_16x16x32_f16 v[88:91], a[104:107], v[104:107], v[88:91]\n"
      "v_fma_f32 v193, |v81|, v65, v193\n"
      "v_pk_mul_f16 v124, v252, v172\n"
      "v_mfma_f32_16x16x32_f16 v[88:91], a[108:111], v[108:111], v[88:91]\n"
      "v_fma_f32 v193, |v82|, v66, v193\n"
      "v_fma_f32 v193, |v83|, v67, v193\n"
      "v_mfma_f32_16x16x32_f16 v[92:95], a[124:127], v[108:111], v[44:47]\n"
      "v_pk_mul_f16 v125, v253, v173\n"
      "v_pk_mul_f16 v126, v254, v174\n"
      "v_mfma_f32_16x16x32_f16 v[92:95], a[120:123], v[104:107], v[92:95]\n"
      "v_pk_mul_f16 v127, v255, v175\n"
      "v_fma_f32 v193, |v84|, v68, v193\n"
      "v_mfma_f32_16x16x32_f16 v[92:95], a[116:119], v[100:103], v[92:95]\n"
      "v_fma_f32 v193, |v85|, v69, v193\n"
      "v_fma_f32 v193, |v86|, v70, v193\n"
      "v_mfma_f32_16x16x32_f16 v[92:95], a[112:115], v[96:99], v[92:95]\n"
      "v_fma_f32 v193, |v87|, v71, v193\n"
      "v_fma_f32 v193, |v88|, v72, v193\n"
      "v_mfma_f32_16x16x32_f16 v[80:83], a[0:3], v[112:115], v[16:19]\n"
      "v_fma_f32 v193, |v89|, v73, v193\n"
      "v_fma_f32 v193, |v90|, v74, v193\n"
      "v_mfma_f32_16x16x32_f16 v[80:83], a[4:7], v[116:119], v[80:83]\n"
      "ds_read_b128 v[184:187], v12 offset:1440\n"
      "v_fma_f32 v193, |v91|, v75, v193\n"
      "v_mfma_f32_16x16x32_f16 v[80:83], a[8:11], v[120:123], v[80:83]\n"
      "v_fma_f32 v193, |v92|, v76, v193\n"
      "v_fma_f32 v193, |v93|, v77, v193\n"
      "v_mfma_f32_16x16x32_f16 v[80:83], a[12:15], v[124:127], v[80:83]\n"
      "v_fma_f32 v193, |v94|, v78, v193\n"
      "v_fma_f32 v193, |v95|, v79, v193\n"
      "v_mfma_f32_16x16x32_f16 v[84:87], a[28:31], v[124:127], v[20:23]\n"
      "s_waitcnt lgkmcnt(1)\n"
      "ds_read_b128 a[200:203], v219 offset:16512\n"
      "v_fma_f32 v236, v236, v215, -s62\n"
      "v_mfma_f32_16x16x32_f16 v[84:87], a[24:27], v[120:123], v[84:87]\n"
      "ds_read_b128 a[204:207], v219 offset:16576\n"
      "v_add_f32 v196, v196, v192\n"
      "v_mfma_f32_16x16x32_f16 v[84:87], a[20:23], v[116:119], v[84:87]\n"
      "v_mul_f32 v197, |v80|, v48\n"
      "v_fma_f32 v197, |v81|, v49, v197\n"
      "v_mfma_f32_16x16x32_f16 v[84:87], a[16:19], v[112:115], v[84:87]\n"
      "v_fma_f32 v197, |v82|, v50, v197\n"
      "v_fma_f32 v197, |v83|, v51, v197\n"
      "v_mfma_f32_16x16x32_f16 v[88:91], a[32:35], v[112:115], v[24:27]\n"
      "s_waitcnt lgkmcnt(2)\n"
      "v_pk_add_f32 v[184:185], v[184:185], v[186:187]\n"
      "v_add_f32 v199, v184, v185\n"
      "v_mfma_f32_16x16x32_f16 v[88:91], a[36:39], v[116:119], v[88:91]\n"
      "ds_read_b128 a[208:211], v219 offset:16640\n"
      "v_cmp_u_f32 vcc, v199, v199\n"
      "v_mfma_f32_16x16x32_f16 v[88:91], a[40:43], v[120:123], v[88:91]\n"
      "v_fma_f32 v197, |v84|, v52, v197\n"
      "v_fma_f32 v197, |v85|, v53, v197\n"
      "v_mfma_f32_16x16x32_f16 v[88:91], a[44:47], v[124:127], v[88:91]\n"
      "s_cbranch_vccnz 17f\n"
      "18:\n"
      "v_mfma_f32_16x16x32_f16 v[92:95], a[60:63], v[124:127], v[28:31]\n"
      "v_permlane32_swap_b32 v199, v184\n"
      "v_mfma_f32_16x16x32_f16 v[92:95], a[56:59], v[120:123], v[92:95]\n"
      "v_fma_f32 v197, |v86|, v54, v197\n"
      "v_fma_f32 v197, |v87|, v55, v197\n"
      "v_mfma_f32_16x16x32_f16 v[92:95], a[52:55], v[116:119], v[92:95]\n"
      "s_nop 0\n"
      "v_fma_f32 v197, |v88|, v56, v197\n"
      "v_fma_f32 v197, |v89|, v57, v197\n"
      "v_mfma_f32_16x16x32_f16 v[92:95], a[48:51], v[112:115], v[92:95]\n"
      "v_fma_f32 v197, |v90|, v58, v197\n"
      "v_fma_f32 v197, |v91|, v59, v197\n"
      "v_mfma_f32_16x16x32_f16 v[80:83], a[64:67], v[112:115], v[32:35]\n"
      "ds_read_b128 a[212:215], v219 offset:16704\n"
      "v_add_f32 v199, v199, v184\n"
      "v_mfma_f32_16x16x32_f16 v[80:83], a[68:71], v[116:119], v[80:83]\n"
      "v_fmac_f32 v188, v215, v234\n"
      "v_subrev_f32 v188, s62, v188\n"
      "v_mfma_f32_16x16x32_f16 v[80:83], a[72:75], v[120:123], v[80:83]\n"
      "v_fma_f32 v197, |v92|, v60, v197\n"
      "v_fma_f32 v197, |v93|, v61, v197\n"
      "v_mfma_f32_16x16x32_f16 v[80:83], a[76:79], v[124:127], v[80:83]\n"
      "v_fma_f32 v197, |v94|, v62, v197\n"
      "v_fma_f32 v197, |v95|, v63, v197\n"
      "v_mfma_f32_16x16x32_f16 v[84:87], a[92:95], v[124:127], v[36:39]\n"
      "v_permlane16_swap_b32 v199, v185\n"
      "v_mfma_f32_16x16x32_f16 v[84:87], a[88:91], v[120:123], v[84:87]\n"
      "v_exp_f32 v222, v188\n"
      "v_mfma_f32_16x16x32_f16 v[84:87], a[84:87], v[116:119], v[84:87]\n"
      "s_nop 0\n"
      "v_fma_f32 v197, |v80|, v64, v197\n"
      "v_fma_f32 v197, |v81|, v65, v197\n"
      "v_mfma_f32_16x16x32_f16 v[84:87], a[80:83], v[112:115], v[84:87]\n"
      "v_fma_f32 v197, |v82|, v66, v197\n"
      "v_fma_f32 v197, |v83|, v67, v197\n"
      "v_mfma_f32_16x16x32_f16 v[88:91], a[96:99], v[112:115], v[40:43]\n"
      "v_add_f32 v199, v199, v185\n"
      "v_add_f32_dpp v196, v236, v196 quad_perm:[0,1,2,3] row_mask:0x5 bank_mask:0xf\n"
      "v_mfma_f32_16x16x32_f16 v[88:91], a[100:103], v[116:119], v[88:91]\n"
      "s_mov_b64 vcc, 31\n"
      "v_add_f32_dpp v193, v236, v193 quad_perm:[0,1,2,3] row_mask:0x2 bank_mask:0xf\n"
      "v_add_f32_dpp v197, v236, v197 quad_perm:[0,1,2,3] row_mask:0x8 bank_mask:0xf\n"
      "v_mfma_f32_16x16x32_f16 v[88:91], a[104:107], v[120:123], v[88:91]\n"
      "v_fma_f32 v197, |v84|, v68, v197\n"
      "v_fma_f32 v197, |v85|, v69, v197\n"
      "v_mfma_f32_16x16x32_f16 v[88:91], a[108:111], v[124:127], v[88:91]\n"
      "v_fma_f32 v197, |v86|, v70, v197\n"
      "v_fma_f32 v197, |v87|, v71, v197\n"
      "v_mfma_f32_16x16x32_f16 v[92:95], a[124:127], v[124:127], v[44:47]\n"
      "v_subrev_f32 v199, s62, v199\n"
      "v_cndmask_b32 v199, v7, v199, vcc\n"
      "v_mfma_f32_16x16x32_f16 v[92:95], a[120:123], v[120:123], v[92:95]\n"
      "v_fma_f32 v214, v188, 0, v220\n"
      "v_fma_f32 v226, v222, v235, v221\n"
      "v_mfma_f32_16x16x32_f16 v[92:95], a[116:119], v[116:119], v[92:95]\n"
      "v_fma_f32 v197, |v88|, v72, v197\n"
      "v_fma_f32 v197, |v89|, v73, v197\n"
      "v_mfma_f32_16x16x32_f16 v[92:95], a[112:115], v[112:115], v[92:95]\n"
      "s_waitcnt lgkmcnt(3)\n"
      "v_fma_f32 v197, |v90|, v74, v197\n"
      "v_fma_f32 v197, |v91|, v75, v197\n"
      "v_mfma_f32_16x16x32_f16 v[180:183], a[200:203], a[216:219], 0\n"
      "s_waitcnt lgkmcnt(2)\n"
      "v_add_f32 v214, v214, v222\n"
      "v_fmac_f32 v214, 0, v199\n"
      "v_mfma_f32_16x16x32_f16 v[180:183], a[204:207], a[220:223], v[180:183]\n"
      "s_waitcnt lgkmcnt(1)\n"
      "v_fma_f32 v197, |v92|, v76, v197\n"
      "v_fma_f32 v197, |v93|, v77, v197\n"
      "v_mfma_f32_16x16x32_f16 v[180:183], a[208:211], a[224:227], v[180:183]\n"
      "s_waitcnt lgkmcnt(0)\n"
      "v_fma_f32 v197, |v94|, v78, v197\n"
      "v_fma_f32 v197, |v95|, v79, v197\n"
      "v_mfma_f32_16x16x32_f16 v[180:183], a[212:215], a[228:231], v[180:183]\n"
      "s_nop 0\n"
      "v_permlane32_swap_b32 v193, v197\n"
      "v_add_f32 v193, v193, v197\n"
      "s_nop 1\n"
      "v_permlane16_swap_b32 v196, v193\n"
      "v_add_f32 v196, v196, v193\n"
      "v_exp_f32 v222, v196\n"
      "v_fmac_f32_dpp v199, v180, v215 quad_perm:[0,1,2,3] row_mask:0x1 bank_mask:0x3\n"
      "v_fma_f32 v214, v196, 0, v214\n"
      "v_exp_f32 v223, v199\n"
      "v_fmac_f32 v226, v222, v237\n"
      "v_add_f32 v214, v214, v222\n"
      "v_fmac_f32_dpp v226, v181, v223 quad_perm:[0,1,2,3] row_mask:0x1 bank_mask:0x3\n"
      "v_add_f32 v214, v214, v223\n"
      "s_nop 1\n"
      "v_add_f32_dpp v214, v214, v214 row_ror:8 row_mask:0xf bank_mask:0xf\n"
      "v_add_f32_dpp v226, v226, v226 row_ror:8 row_mask:0xf bank_mask:0xf\n"
      "s_nop 0\n"
      "v_add_f32_dpp v214, v214, v214 row_ror:4 row_mask:0xf bank_mask:0xf\n"
      "v_add_f32_dpp v226, v226, v226 row_ror:4 row_mask:0xf bank_mask:0xf\n"
      "s_nop 0\n"
      "v_add_f32_dpp v214, v214, v214 row_ror:2 row_mask:0xf bank_mask:0xf\n"
      "v_add_f32_dpp v226, v226, v226 row_ror:2 row_mask:0xf bank_mask:0xf\n"
      "s_nop 0\n"
      "v_add_f32_dpp v214, v214, v214 row_ror:1 row_mask:0xf bank_mask:0xf\n"
      "v_add_f32_dpp v226, v226, v226 row_ror:1 row_mask:0xf bank_mask:0xf\n"
      "s_nop 0\n"
      "v_add_f32_dpp v214, v214, v214 row_bcast:15 row_mask:0xa bank_mask:0xf\n"
      "v_add_f32_dpp v226, v226, v226 row_bcast:15 row_mask:0xa bank_mask:0xf\n"
      "s_nop 0\n"
      "v_add_f32_dpp v214, v214, v214 row_bcast:31 row_mask:0xc bank_mask:0xf\n"
      "v_add_f32_dpp v226, v226, v226 row_bcast:31 row_mask:0xc bank_mask:0xf\n"
      "v_rcp_f32 v222, v214\n"
      "v_ldexp_f32 v226, v226, s39\n"
      "s_lshl_b64 exec, 1, 63\n"
      "v_mul_f32 v226, v226, v222\n"
      "v_cvt_pk_bf16_f32 v222, v226, v226\n"
      "global_store_short v3, v222, s[24:25]\n"
      "v_cmp_class_f32 vcc, v226, s45\n"
      "v_cmp_lt_f32 s[28:29], s99, v214\n"
      "s_nop 0\n"
      "s_or_b32 vcc_hi, vcc_hi, s29\n"
      "s_cmp_lt_i32 vcc_hi, 0\n"
      "s_cbranch_scc1 19f\n"
      "20:\n"
      "s_endpgm\n"
      "s_mov_b64 exec, -1\n"
      "s_branch 9f\n"
      "33:\n"
      "s_cmp_lg_u32 s55, 0\n"
      "s_cbranch_scc1 34f\n"
      "global_load_dwordx4 v[244:247], v11, s[10:11] nt\n"
      "global_load_dwordx4 v[248:251], v12, s[10:11] nt\n"
      "global_load_dwordx4 v[252:255], v6, s[10:11] nt\n"
      "s_branch 6f\n"
      "34:\n"
      "v_accvgpr_read_b32 v244, a216\n"
      "v_accvgpr_read_b32 v245, a217\n"
      "v_accvgpr_read_b32 v246, a218\n"
      "v_accvgpr_read_b32 v247, a219\n"
      "v_accvgpr_read_b32 v248, a220\n"
      "v_accvgpr_read_b32 v249, a221\n"
      "v_accvgpr_read_b32 v250, a222\n"
      "v_accvgpr_read_b32 v251, a223\n"
      "v_accvgpr_read_b32 v252, a224\n"
      "v_accvgpr_read_b32 v253, a225\n"
      "v_accvgpr_read_b32 v254, a226\n"
      "v_accvgpr_read_b32 v255, a227\n"
      "s_branch 32b\n"
      "5:\n"
      "s_waitcnt lgkmcnt(0)\n"
      "ds_read_b32 v224, v227 offset:37888\n"
      "s_waitcnt lgkmcnt(0)\n"
      "v_and_b32 v224, 15, v224\n"
      "v_cmp_eq_u32 vcc, 0, v224\n"
      "s_nop 4\n"
      "s_cmp_lg_u32 vcc_lo, 0\n"
      "s_cbranch_scc1 5b\n"
      "v_cmp_gt_u32 vcc, 4, v224\n"
      "s_nop 4\n"
      "s_cmp_lg_u32 vcc_lo, 0\n"
      "s_cbranch_scc1 8f\n"
      "v_and_b32 v225, 2, v224\n"
      "v_cmp_ne_u32 vcc, 0, v225\n"
      "s_nop 4\n"
      "s_cmp_eq_u32 vcc_lo, 0\n"
      "s_cbranch_scc1 53f\n"
      "s_waitcnt vmcnt(0)\n"
      "v_add_u32 v216, 2592, v8\n"
      "v_mov_b32 v217, v216\n"
      "53:\n"
      "v_cmp_le_u32 vcc, 8, v224\n"
      "s_nop 4\n"
      "s_or_b32 s44, vcc_lo, s58\n"
      "s_cbranch_scc1 52f\n"
      "v_and_b32 v224, 63, %3\n"
      "v_lshlrev_b32 v224, 9, v224\n"
      "global_load_dwordx4 v[176:179], v224, s[16:17]\n"
      "global_load_dwordx4 v[180:183], v224, s[16:17] offset:336\n"
      "s_waitcnt vmcnt(0)\n"
      "v_max3_f32 v224, |v176|, |v177|, |v178|\n"
      "v_max3_f32 v224, |v179|, |v180|, v224\n"
      "v_max3_f32 v224, |v181|, |v182|, v224\n"
      "v_max_f32 v224, |v183|, v224\n"
      "s_nop 1\n"
      "v_max_f32_dpp v224, v224, v224 row_ror:8 row_mask:0xf bank_mask:0xf\n"
      "s_nop 1\n"
      "v_max_f32_dpp v224, v224, v224 row_ror:4 row_mask:0xf bank_mask:0xf\n"
      "s_nop 1\n"
      "v_max_f32_dpp v224, v224, v224 row_ror:2 row_mask:0xf bank_mask:0xf\n"
      "s_nop 1\n"
      "v_max_f32_dpp v224, v224, v224 row_ror:1 row_mask:0xf bank_mask:0xf\n"
      "s_nop 0\n"
      "v_readlane_b32 s44, v224, 0\n"
      "v_readlane_b32 s45, v224, 16\n"
      "v_readlane_b32 s46, v224, 32\n"
      "v_readlane_b32 s47, v224, 48\n"
      "s_max_u32 s44, s44, s45\n"
      "s_max_u32 s46, s46, s47\n"
      "s_max_u32 s44, s44, s46\n"
      "s_cmp_ge_u32 s44, 0x43000000\n"
      "s_cbranch_scc1 52f\n"
      "s_lshr_b32 s44, s44, 16\n"
      "s_cmp_eq_u32 s44, 0\n"
      "s_cbranch_scc1 52f\n"
      "s_lshr_b32 s44, s44, 7\n"
      "s_sub_i32 s58, 134, s44\n"
      "s_cmp_gt_u32 s58, 50\n"
      "s_cbranch_scc1 8f\n"
      "s_mov_b32 s41, 0\n"
      "s_mov_b32 s35, 0\n"
      "s_waitcnt vmcnt(0) lgkmcnt(0)\n"
      "s_barrier\n"
      "s_branch 0b\n"
      "52:\n"
      "ds_read_b128 v[176:179], v5 offset:37376\n"
      "ds_read_b128 v[180:183], v5 offset:37392\n"
      "s_waitcnt lgkmcnt(0)\n"
      "v_max_f32 v225, |v176|, |v181|\n"
      "s_mov_b32 s44, 0x3c800000\n"
      "v_cmp_le_f32 vcc, 0x3e600000, v225\n"
      "v_cmp_le_f32 s[28:29], s44, v225\n"
      "s_nop 4\n"
      "s_cmp_eq_u32 vcc_lo, 0\n"
      "s_cbranch_scc1 54f\n"
      "s_cmp_eq_u32 s60, 0\n"
      "s_cselect_b32 s99, -1.0, s99\n"
      "s_mov_b32 s28, 0\n"
      "54:\n"
      "s_cmp_lg_u32 s28, 0\n"
      "s_cbranch_scc1 4b\n"
      "v_max3_f32 v225, |v176|, |v177|, |v178|\n"
      "v_max3_f32 v225, |v179|, |v180|, v225\n"
      "v_max3_f32 v225, |v181|, |v182|, v225\n"
      "v_max_f32 v225, |v183|, v225\n"
      "s_nop 1\n"
      "v_max_f32_dpp v225, v225, v225 row_ror:8 row_mask:0xf bank_mask:0xf\n"
      "s_nop 1\n"
      "v_max_f32_dpp v225, v225, v225 row_ror:4 row_mask:0xf bank_mask:0xf\n"
      "s_nop 1\n"
      "v_max_f32_dpp v225, v225, v225 row_ror:2 row_mask:0xf bank_mask:0xf\n"
      "s_nop 1\n"
      "v_max_f32_dpp v225, v225, v225 row_ror:1 row_mask:0xf bank_mask:0xf\n"
      "v_frexp_exp_i32_f32 v224, v225\n"
      "v_sub_u32 v224, 0, v224\n"
      "v_ldexp_f32 v176, v176, v224\n"
      "v_ldexp_f32 v177, v177, v224\n"
      "v_ldexp_f32 v178, v178, v224\n"
      "v_ldexp_f32 v179, v179, v224\n"
      "v_ldexp_f32 v180, v180, v224\n"
      "v_ldexp_f32 v181, v181, v224\n"
      "v_ldexp_f32 v182, v182, v224\n"
      "v_ldexp_f32 v183, v183, v224\n"
      "v_readfirstlane_b32 s56, v224\n"
      "s_nop 1\n"
      "s_branch 4b\n"
      "36:\n"
      "s_sub_u32 s33, s26, 1\n"
      "s_sub_u32 s44, s34, s33\n"
      "s_mul_i32 s44, s44, 104\n"
      "v_subrev_u32 v10, s44, v10\n"
      "s_branch 37b\n"
      "22:\n"
      "v_max3_f32 v224, |v176|, |v177|, |v178|\n"
      "v_max3_f32 v224, |v179|, |v180|, v224\n"
      "v_max3_f32 v224, |v181|, |v182|, v224\n"
      "v_max_f32 v224, |v183|, v224\n"
      "s_nop 1\n"
      "v_max_f32_dpp v224, v224, v224 row_ror:8 row_mask:0xf bank_mask:0xf\n"
      "s_nop 1\n"
      "v_max_f32_dpp v224, v224, v224 row_ror:4 row_mask:0xf bank_mask:0xf\n"
      "s_nop 1\n"
      "v_max_f32_dpp v224, v224, v224 row_ror:2 row_mask:0xf bank_mask:0xf\n"
      "s_nop 1\n"
      "v_max_f32_dpp v224, v224, v224 row_ror:1 row_mask:0xf bank_mask:0xf\n"
      "v_frexp_exp_i32_f32 v90, v224\n"
      "v_sub_u32 v90, 0, v90\n"
      "v_ldexp_f32 v176, v176, v90\n"
      "v_ldexp_f32 v177, v177, v90\n"
      "v_ldexp_f32 v178, v178, v90\n"
      "v_ldexp_f32 v179, v179, v90\n"
      "v_ldexp_f32 v180, v180, v90\n"
      "v_ldexp_f32 v181, v181, v90\n"
      "v_ldexp_f32 v182, v182, v90\n"
      "v_ldexp_f32 v183, v183, v90\n"
      "v_readlane_b32 s59, v90, 0\n"
      "v_readlane_b32 s57, v90, 32\n"
      "s_mov_b32 s60, 1\n"
      "s_branch 23b\n"
      "70:\n"
      "s_add_u32 s44, s58, 127\n"
      "s_lshl_b32 s44, s44, 23\n"
      "s_waitcnt vmcnt(0) lgkmcnt(0)\n"
      "s_nop 7\n"
      "v_lshlrev_b32 v176, 16, v96\n"
      "v_and_b32 v177, s43, v96\n"
      "v_mul_f32 v176, s44, v176\n"
      "v_mul_f32 v177, s44, v177\n"
      "v_cvt_pk_bf16_f32 v96, v176, v177\n"
      "v_lshlrev_b32 v176, 16, v97\n"
      "v_and_b32 v177, s43, v97\n"
      "v_mul_f32 v176, s44, v176\n"
      "v_mul_f32 v177, s44, v177\n"
      "v_cvt_pk_bf16_f32 v97, v176, v177\n"
      "v_lshlrev_b32 v176, 16, v98\n"
      "v_and_b32 v177, s43, v98\n"
      "v_mul_f32 v176, s44, v176\n"
      "v_mul_f32 v177, s44, v177\n"
      "v_cvt_pk_bf16_f32 v98, v176, v177\n"
      "v_lshlrev_b32 v176, 16, v99\n"
      "v_and_b32 v177, s43, v99\n"
      "v_mul_f32 v176, s44, v176\n"
      "v_mul_f32 v177, s44, v177\n"
      "v_cvt_pk_bf16_f32 v99, v176, v177\n"
      "v_lshlrev_b32 v176, 16, v100\n"
      "v_and_b32 v177, s43, v100\n"
      "v_mul_f32 v176, s44, v176\n"
      "v_mul_f32 v177, s44, v177\n"
      "v_cvt_pk_bf16_f32 v100, v176, v177\n"
      "v_lshlrev_b32 v176, 16, v101\n"
      "v_and_b32 v177, s43, v101\n"
      "v_mul_f32 v176, s44, v176\n"
      "v_mul_f32 v177, s44, v177\n"
      "v_cvt_pk_bf16_f32 v101, v176, v177\n"
      "v_lshlrev_b32 v176, 16, v102\n"
      "v_and_b32 v177, s43, v102\n"
      "v_mul_f32 v176, s44, v176\n"
      "v_mul_f32 v177, s44, v177\n"
      "v_cvt_pk_bf16_f32 v102, v176, v177\n"
      "v_lshlrev_b32 v176, 16, v103\n"
      "v_and_b32 v177, s43, v103\n"
      "v_mul_f32 v176, s44, v176\n"
      "v_mul_f32 v177, s44, v177\n"
      "v_cvt_pk_bf16_f32 v103, v176, v177\n"
      "v_lshlrev_b32 v176, 16, v104\n"
      "v_and_b32 v177, s43, v104\n"
      "v_mul_f32 v176, s44, v176\n"
      "v_mul_f32 v177, s44, v177\n"
      "v_cvt_pk_bf16_f32 v104, v176, v177\n"
      "v_lshlrev_b32 v176, 16, v105\n"
      "v_and_b32 v177, s43, v105\n"
      "v_mul_f32 v176, s44, v176\n"
      "v_mul_f32 v177, s44, v177\n"
      "v_cvt_pk_bf16_f32 v105, v176, v177\n"
      "v_lshlrev_b32 v176, 16, v106\n"
      "v_and_b32 v177, s43, v106\n"
      "v_mul_f32 v176, s44, v176\n"
      "v_mul_f32 v177, s44, v177\n"
      "v_cvt_pk_bf16_f32 v106, v176, v177\n"
      "v_lshlrev_b32 v176, 16, v107\n"
      "v_and_b32 v177, s43, v107\n"
      "v_mul_f32 v176, s44, v176\n"
      "v_mul_f32 v177, s44, v177\n"
      "v_cvt_pk_bf16_f32 v107, v176, v177\n"
      "v_lshlrev_b32 v176, 16, v108\n"
      "v_and_b32 v177, s43, v108\n"
      "v_mul_f32 v176, s44, v176\n"
      "v_mul_f32 v177, s44, v177\n"
      "v_cvt_pk_bf16_f32 v108, v176, v177\n"
      "v_lshlrev_b32 v176, 16, v109\n"
      "v_and_b32 v177, s43, v109\n"
      "v_mul_f32 v176, s44, v176\n"
      "v_mul_f32 v177, s44, v177\n"
      "v_cvt_pk_bf16_f32 v109, v176, v177\n"
      "v_lshlrev_b32 v176, 16, v110\n"
      "v_and_b32 v177, s43, v110\n"
      "v_mul_f32 v176, s44, v176\n"
      "v_mul_f32 v177, s44, v177\n"
      "v_cvt_pk_bf16_f32 v110, v176, v177\n"
      "v_lshlrev_b32 v176, 16, v111\n"
      "v_and_b32 v177, s43, v111\n"
      "v_mul_f32 v176, s44, v176\n"
      "v_mul_f32 v177, s44, v177\n"
      "v_cvt_pk_bf16_f32 v111, v176, v177\n"
      "v_lshlrev_b32 v176, 16, v112\n"
      "v_and_b32 v177, s43, v112\n"
      "v_mul_f32 v176, s44, v176\n"
      "v_mul_f32 v177, s44, v177\n"
      "v_cvt_pk_bf16_f32 v112, v176, v177\n"
      "v_lshlrev_b32 v176, 16, v113\n"
      "v_and_b32 v177, s43, v113\n"
      "v_mul_f32 v176, s44, v176\n"
      "v_mul_f32 v177, s44, v177\n"
      "v_cvt_pk_bf16_f32 v113, v176, v177\n"
      "v_lshlrev_b32 v176, 16, v114\n"
      "v_and_b32 v177, s43, v114\n"
      "v_mul_f32 v176, s44, v176\n"
      "v_mul_f32 v177, s44, v177\n"
      "v_cvt_pk_bf16_f32 v114, v176, v177\n"
      "v_lshlrev_b32 v176, 16, v115\n"
      "v_and_b32 v177, s43, v115\n"
      "v_mul_f32 v176, s44, v176\n"
      "v_mul_f32 v177, s44, v177\n"
      "v_cvt_pk_bf16_f32 v115, v176, v177\n"
      "v_lshlrev_b32 v176, 16, v116\n"
      "v_and_b32 v177, s43, v116\n"
      "v_mul_f32 v176, s44, v176\n"
      "v_mul_f32 v177, s44, v177\n"
      "v_cvt_pk_bf16_f32 v116, v176, v177\n"
      "v_lshlrev_b32 v176, 16, v117\n"
      "v_and_b32 v177, s43, v117\n"
      "v_mul_f32 v176, s44, v176\n"
      "v_mul_f32 v177, s44, v177\n"
      "v_cvt_pk_bf16_f32 v117, v176, v177\n"
      "v_lshlrev_b32 v176, 16, v118\n"
      "v_and_b32 v177, s43, v118\n"
      "v_mul_f32 v176, s44, v176\n"
      "v_mul_f32 v177, s44, v177\n"
      "v_cvt_pk_bf16_f32 v118, v176, v177\n"
      "v_lshlrev_b32 v176, 16, v119\n"
      "v_and_b32 v177, s43, v119\n"
      "v_mul_f32 v176, s44, v176\n"
      "v_mul_f32 v177, s44, v177\n"
      "v_cvt_pk_bf16_f32 v119, v176, v177\n"
      "s_sub_u32 s44, 0, s58\n"
      "v_ldexp_f32 v16, v16, s58\n"
      "v_ldexp_f32 v48, v48, s44\n"
      "v_ldexp_f32 v17, v17, s58\n"
      "v_ldexp_f32 v49, v49, s44\n"
      "v_ldexp_f32 v18, v18, s58\n"
      "v_ldexp_f32 v50, v50, s44\n"
      "v_ldexp_f32 v19, v19, s58\n"
      "v_ldexp_f32 v51, v51, s44\n"
      "v_ldexp_f32 v20, v20, s58\n"
      "v_ldexp_f32 v52, v52, s44\n"
      "v_ldexp_f32 v21, v21, s58\n"
      "v_ldexp_f32 v53, v53, s44\n"
      "v_ldexp_f32 v22, v22, s58\n"
      "v_ldexp_f32 v54, v54, s44\n"
      "v_ldexp_f32 v23, v23, s58\n"
      "v_ldexp_f32 v55, v55, s44\n"
      "v_ldexp_f32 v24, v24, s58\n"
      "v_ldexp_f32 v56, v56, s44\n"
      "v_ldexp_f32 v25, v25, s58\n"
      "v_ldexp_f32 v57, v57, s44\n"
      "v_ldexp_f32 v26, v26, s58\n"
      "v_ldexp_f32 v58, v58, s44\n"
      "v_ldexp_f32 v27, v27, s58\n"
      "v_ldexp_f32 v59, v59, s44\n"
      "v_ldexp_f32 v28, v28, s58\n"
      "v_ldexp_f32 v60, v60, s44\n"
      "v_ldexp_f32 v29, v29, s58\n"
      "v_ldexp_f32 v61, v61, s44\n"
      "v_ldexp_f32 v30, v30, s58\n"
      "v_ldexp_f32 v62, v62, s44\n"
      "v_ldexp_f32 v31, v31, s58\n"
      "v_ldexp_f32 v63, v63, s44\n"
      "v_ldexp_f32 v32, v32, s58\n"
      "v_ldexp_f32 v64, v64, s44\n"
      "v_ldexp_f32 v33, v33, s58\n"
      "v_ldexp_f32 v65, v65, s44\n"
      "v_ldexp_f32 v34, v34, s58\n"
      "v_ldexp_f32 v66, v66, s44\n"
      "v_ldexp_f32 v35, v35, s58\n"
      "v_ldexp_f32 v67, v67, s44\n"
      "v_ldexp_f32 v36, v36, s58\n"
      "v_ldexp_f32 v68, v68, s44\n"
      "v_ldexp_f32 v37, v37, s58\n"
      "v_ldexp_f32 v69, v69, s44\n"
      "v_ldexp_f32 v38, v38, s58\n"
      "v_ldexp_f32 v70, v70, s44\n"
      "v_ldexp_f32 v39, v39, s58\n"
      "v_ldexp_f32 v71, v71, s44\n"
      "v_ldexp_f32 v40, v40, s58\n"
      "v_ldexp_f32 v72, v72, s44\n"
      "v_ldexp_f32 v41, v41, s58\n"
      "v_ldexp_f32 v73, v73, s44\n"
      "v_ldexp_f32 v42, v42, s58\n"
      "v_ldexp_f32 v74, v74, s44\n"
      "v_ldexp_f32 v43, v43, s58\n"
      "v_ldexp_f32 v75, v75, s44\n"
      "v_ldexp_f32 v44, v44, s58\n"
      "v_ldexp_f32 v76, v76, s44\n"
      "v_ldexp_f32 v45, v45, s58\n"
      "v_ldexp_f32 v77, v77, s44\n"
      "v_ldexp_f32 v46, v46, s58\n"
      "v_ldexp_f32 v78, v78, s44\n"
      "v_ldexp_f32 v47, v47, s58\n"
      "v_ldexp_f32 v79, v79, s44\n"
      "s_nop 1\n"
      "s_branch 71b\n"
      "74:\n"
      "s_cmp_eq_u32 s55, 0\n"
      "s_cbranch_scc1 76f\n"
      "ds_read_b128 v[136:139], v201 offset:128\n"
      "ds_read_b128 v[168:171], v202 offset:128\n"
      "ds_read_b128 v[140:143], v201 offset:192\n"
      "ds_read_b128 v[172:175], v202 offset:192\n"
      "s_waitcnt lgkmcnt(0)\n"
      "v_pk_mul_f16 v96, v128, v160\n"
      "v_pk_mul_f16 v97, v129, v161\n"
      "v_pk_mul_f16 v98, v130, v162\n"
      "v_pk_mul_f16 v99, v131, v163\n"
      "s_branch 31b\n"
      "76:\n"
      "s_add_u32 s44, s58, 127\n"
      "s_lshl_b32 s44, s44, 23\n"
      "s_waitcnt vmcnt(0) lgkmcnt(0)\n"
      "s_nop 7\n"
      "v_lshlrev_b32 v176, 16, v120\n"
      "v_and_b32 v177, s43, v120\n"
      "v_mul_f32 v176, s44, v176\n"
      "v_mul_f32 v177, s44, v177\n"
      "v_cvt_pk_bf16_f32 v120, v176, v177\n"
      "v_lshlrev_b32 v176, 16, v121\n"
      "v_and_b32 v177, s43, v121\n"
      "v_mul_f32 v176, s44, v176\n"
      "v_mul_f32 v177, s44, v177\n"
      "v_cvt_pk_bf16_f32 v121, v176, v177\n"
      "v_lshlrev_b32 v176, 16, v122\n"
      "v_and_b32 v177, s43, v122\n"
      "v_mul_f32 v176, s44, v176\n"
      "v_mul_f32 v177, s44, v177\n"
      "v_cvt_pk_bf16_f32 v122, v176, v177\n"
      "v_lshlrev_b32 v176, 16, v123\n"
      "v_and_b32 v177, s43, v123\n"
      "v_mul_f32 v176, s44, v176\n"
      "v_mul_f32 v177, s44, v177\n"
      "v_cvt_pk_bf16_f32 v123, v176, v177\n"
      "v_lshlrev_b32 v176, 16, v124\n"
      "v_and_b32 v177, s43, v124\n"
      "v_mul_f32 v176, s44, v176\n"
      "v_mul_f32 v177, s44, v177\n"
      "v_cvt_pk_bf16_f32 v124, v176, v177\n"
      "v_lshlrev_b32 v176, 16, v125\n"
      "v_and_b32 v177, s43, v125\n"
      "v_mul_f32 v176, s44, v176\n"
      "v_mul_f32 v177, s44, v177\n"
      "v_cvt_pk_bf16_f32 v125, v176, v177\n"
      "v_lshlrev_b32 v176, 16, v126\n"
      "v_and_b32 v177, s43, v126\n"
      "v_mul_f32 v176, s44, v176\n"
      "v_mul_f32 v177, s44, v177\n"
      "v_cvt_pk_bf16_f32 v126, v176, v177\n"
      "v_lshlrev_b32 v176, 16, v127\n"
      "v_and_b32 v177, s43, v127\n"
      "v_mul_f32 v176, s44, v176\n"
      "v_mul_f32 v177, s44, v177\n"
      "v_cvt_pk_bf16_f32 v127, v176, v177\n"
      "s_nop 1\n"
      "s_branch 75b\n"
      "6:\n"
      "s_waitcnt vmcnt(0) lgkmcnt(0)\n"
      "s_nop 7\n"
      "s_mov_b32 s55, 1\n"
      "s_or_b32 s52, s52, 0x7f000002\n"
      "s_mov_b32 s60, 1\n"
      "v_max_f32_dpp v226, v226, v226 row_ror:8 row_mask:0xf bank_mask:0xf\n"
      "s_nop 1\n"
      "v_max_f32_dpp v226, v226, v226 row_ror:4 row_mask:0xf bank_mask:0xf\n"
      "s_nop 1\n"
      "v_max_f32_dpp v226, v226, v226 row_ror:2 row_mask:0xf bank_mask:0xf\n"
      "s_nop 1\n"
      "v_max_f32_dpp v226, v226, v226 row_ror:1 row_mask:0xf bank_mask:0xf\n"
      "s_nop 0\n"
      "v_readlane_b32 s44, v226, 0\n"
      "v_readlane_b32 s45, v226, 16\n"
      "v_readlane_b32 s46, v226, 32\n"
      "v_readlane_b32 s47, v226, 48\n"
      "s_max_u32 s44, s44, s45\n"
      "s_max_u32 s46, s46, s47\n"
      "s_max_u32 s44, s44, s46\n"
      "s_lshr_b32 s44, s44, 23\n"
      "s_sub_i32 s40, 132, s44\n"
      "s_cmp_lg_u32 s41, 0\n"
      "s_cbranch_scc1 61f\n"
      "s_cmp_ge_u32 s44, 2\n"
      "s_cbranch_scc1 62f\n"
      "61:\n"
      "s_mov_b32 s41, 1\n"
      "s_mov_b32 s45, 0x7fff7fff\n"
      "v_mov_b32 v226, 0\n"
      "v_and_b32 v176, s45, v228\n"
      "v_pk_max_u16 v226, v226, v176\n"
      "v_and_b32 v176, s45, v229\n"
      "v_pk_max_u16 v226, v226, v176\n"
      "v_and_b32 v176, s45, v230\n"
      "v_pk_max_u16 v226, v226, v176\n"
      "v_and_b32 v176, s45, v231\n"
      "v_pk_max_u16 v226, v226, v176\n"
      "v_and_b32 v176, s45, v232\n"
      "v_pk_max_u16 v226, v226, v176\n"
      "v_and_b32 v176, s45, v233\n"
      "v_pk_max_u16 v226, v226, v176\n"
      "v_and_b32 v176, s45, v234\n"
      "v_pk_max_u16 v226, v226, v176\n"
      "v_and_b32 v176, s45, v235\n"
      "v_pk_max_u16 v226, v226, v176\n"
      "v_and_b32 v176, s45, v236\n"
      "v_pk_max_u16 v226, v226, v176\n"
      "v_and_b32 v176, s45, v237\n"
      "v_pk_max_u16 v226, v226, v176\n"
      "v_and_b32 v176, s45, v238\n"
      "v_pk_max_u16 v226, v226, v176\n"
      "v_and_b32 v176, s45, v239\n"
      "v_pk_max_u16 v226, v226, v176\n"
      "v_and_b32 v176, s45, v240\n"
      "v_pk_max_u16 v226, v226, v176\n"
      "v_and_b32 v176, s45, v241\n"
      "v_pk_max_u16 v226, v226, v176\n"
      "v_and_b32 v176, s45, v242\n"
      "v_pk_max_u16 v226, v226, v176\n"
      "v_and_b32 v176, s45, v243\n"
      "v_pk_max_u16 v226, v226, v176\n"
      "v_and_b32 v176, s45, v244\n"
      "v_pk_max_u16 v226, v226, v176\n"
      "v_and_b32 v176, s45, v245\n"
      "v_pk_max_u16 v226, v226, v176\n"
      "v_and_b32 v176, s45, v246\n"
      "v_pk_max_u16 v226, v226, v176\n"
      "v_and_b32 v176, s45, v247\n"
      "v_pk_max_u16 v226, v226, v176\n"
      "v_and_b32 v176, s45, v248\n"
      "v_pk_max_u16 v226, v226, v176\n"
      "v_and_b32 v176, s45, v249\n"
      "v_pk_max_u16 v226, v226, v176\n"
      "v_and_b32 v176, s45, v250\n"
      "v_pk_max_u16 v226, v226, v176\n"
      "v_and_b32 v176, s45, v251\n"
      "v_pk_max_u16 v226, v226, v176\n"
      "v_and_b32 v176, s45, v252\n"
      "v_pk_max_u16 v226, v226, v176\n"
      "v_and_b32 v176, s45, v253\n"
      "v_pk_max_u16 v226, v226, v176\n"
      "v_and_b32 v176, s45, v254\n"
      "v_pk_max_u16 v226, v226, v176\n"
      "v_and_b32 v176, s45, v255\n"
      "v_pk_max_u16 v226, v226, v176\n"
      "v_lshlrev_b32 v176, 16, v226\n"
      "v_max_u32 v226, v226, v176\n"
      "s_nop 1\n"
      "v_max_u32_dpp v226, v226, v226 row_ror:8 row_mask:0xf bank_mask:0xf\n"
      "s_nop 1\n"
      "v_max_u32_dpp v226, v226, v226 row_ror:4 row_mask:0xf bank_mask:0xf\n"
      "s_nop 1\n"
      "v_max_u32_dpp v226, v226, v226 row_ror:2 row_mask:0xf bank_mask:0xf\n"
      "s_nop 1\n"
      "v_max_u32_dpp v226, v226, v226 row_ror:1 row_mask:0xf bank_mask:0xf\n"
      "s_nop 0\n"
      "v_readlane_b32 s44, v226, 0\n"
      "v_readlane_b32 s45, v226, 16\n"
      "v_readlane_b32 s46, v226, 32\n"
      "v_readlane_b32 s47, v226, 48\n"
      "s_max_u32 s44, s44, s45\n"
      "s_max_u32 s46, s46, s47\n"
      "s_max_u32 s44, s44, s46\n"
      "s_lshr_b32 s44, s44, 23\n"
      "s_sub_i32 s40, 133, s44\n"
      "s_cmp_eq_u32 s44, 0\n"
      "s_cselect_b32 s40, 0, s40\n"
      "62:\n"
      "s_add_i32 s45, s40, 32\n"
      "s_cmp_gt_u32 s45, 80\n"
      "s_cselect_b32 s40, 0, s40\n"
      "s_cselect_b32 s46, 0x7fc00000, 0\n"
      "s_cselect_b32 s41, 1, s41\n"
      "s_add_u32 s39, s40, 127\n"
      "s_lshl_b32 s39, s39, 23\n"
      "s_lshl_b32 s44, s40, 1\n"
      "s_sub_u32 s45, 127, s44\n"
      "s_add_u32 s44, s44, 127\n"
      "s_lshl_b32 s44, s44, 23\n"
      "s_lshl_b32 s45, s45, 23\n"
      "s_or_b32 s44, s44, s46\n"
      "v_mul_f32 v16, s44, v16\n"
      "v_mul_f32 v48, s45, v48\n"
      "v_mul_f32 v17, s44, v17\n"
      "v_mul_f32 v49, s45, v49\n"
      "v_mul_f32 v18, s44, v18\n"
      "v_mul_f32 v50, s45, v50\n"
      "v_mul_f32 v19, s44, v19\n"
      "v_mul_f32 v51, s45, v51\n"
      "v_mul_f32 v20, s44, v20\n"
      "v_mul_f32 v52, s45, v52\n"
      "v_mul_f32 v21, s44, v21\n"
      "v_mul_f32 v53, s45, v53\n"
      "v_mul_f32 v22, s44, v22\n"
      "v_mul_f32 v54, s45, v54\n"
      "v_mul_f32 v23, s44, v23\n"
      "v_mul_f32 v55, s45, v55\n"
      "v_mul_f32 v24, s44, v24\n"
      "v_mul_f32 v56, s45, v56\n"
      "v_mul_f32 v25, s44, v25\n"
      "v_mul_f32 v57, s45, v57\n"
      "v_mul_f32 v26, s44, v26\n"
      "v_mul_f32 v58, s45, v58\n"
      "v_mul_f32 v27, s44, v27\n"
      "v_mul_f32 v59, s45, v59\n"
      "v_mul_f32 v28, s44, v28\n"
      "v_mul_f32 v60, s45, v60\n"
      "v_mul_f32 v29, s44, v29\n"
      "v_mul_f32 v61, s45, v61\n"
      "v_mul_f32 v30, s44, v30\n"
      "v_mul_f32 v62, s45, v62\n"
      "v_mul_f32 v31, s44, v31\n"
      "v_mul_f32 v63, s45, v63\n"
      "v_mul_f32 v32, s44, v32\n"
      "v_mul_f32 v64, s45, v64\n"
      "v_mul_f32 v33, s44, v33\n"
      "v_mul_f32 v65, s45, v65\n"
      "v_mul_f32 v34, s44, v34\n"
      "v_mul_f32 v66, s45, v66\n"
      "v_mul_f32 v35, s44, v35\n"
      "v_mul_f32 v67, s45, v67\n"
      "v_mul_f32 v36, s44, v36\n"
      "v_mul_f32 v68, s45, v68\n"
      "v_mul_f32 v37, s44, v37\n"
      "v_mul_f32 v69, s45, v69\n"
      "v_mul_f32 v38, s44, v38\n"
      "v_mul_f32 v70, s45, v70\n"
      "v_mul_f32 v39, s44, v39\n"
      "v_mul_f32 v71, s45, v71\n"
      "v_mul_f32 v40, s44, v40\n"
      "v_mul_f32 v72, s45, v72\n"
      "v_mul_f32 v41, s44, v41\n"
      "v_mul_f32 v73, s45, v73\n"
      "v_mul_f32 v42, s44, v42\n"
      "v_mul_f32 v74, s45, v74\n"
      "v_mul_f32 v43, s44, v43\n"
      "v_mul_f32 v75, s45, v75\n"
      "v_mul_f32 v44, s44, v44\n"
      "v_mul_f32 v76, s45, v76\n"
      "v_mul_f32 v45, s44, v45\n"
      "v_mul_f32 v77, s45, v77\n"
      "v_mul_f32 v46, s44, v46\n"
      "v_mul_f32 v78, s45, v78\n"
      "v_mul_f32 v47, s44, v47\n"
      "v_mul_f32 v79, s45, v79\n"
      "v_mov_b32 v178, s39\n"
      "v_lshlrev_b32 v176, 16, v228\n"
      "v_and_b32 v177, s43, v228\n"
      "v_fmaak_f32 v176, v178, v176, 0x12800000\n"
      "v_fmaak_f32 v177, v178, v177, 0x12800000\n"
      "v_add_f32 v176, 0x92800000, v176\n"
      "v_add_f32 v177, 0x92800000, v177\n"
      "v_cvt_pk_bf16_f32 v228, v176, v177\n"
      "v_lshlrev_b32 v176, 16, v229\n"
      "v_and_b32 v177, s43, v229\n"
      "v_fmaak_f32 v176, v178, v176, 0x12800000\n"
      "v_fmaak_f32 v177, v178, v177, 0x12800000\n"
      "v_add_f32 v176, 0x92800000, v176\n"
      "v_add_f32 v177, 0x92800000, v177\n"
      "v_cvt_pk_bf16_f32 v229, v176, v177\n"
      "v_lshlrev_b32 v176, 16, v230\n"
      "v_and_b32 v177, s43, v230\n"
      "v_fmaak_f32 v176, v178, v176, 0x12800000\n"
      "v_fmaak_f32 v177, v178, v177, 0x12800000\n"
      "v_add_f32 v176, 0x92800000, v176\n"
      "v_add_f32 v177, 0x92800000, v177\n"
      "v_cvt_pk_bf16_f32 v230, v176, v177\n"
      "v_lshlrev_b32 v176, 16, v231\n"
      "v_and_b32 v177, s43, v231\n"
      "v_fmaak_f32 v176, v178, v176, 0x12800000\n"
      "v_fmaak_f32 v177, v178, v177, 0x12800000\n"
      "v_add_f32 v176, 0x92800000, v176\n"
      "v_add_f32 v177, 0x92800000, v177\n"
      "v_cvt_pk_bf16_f32 v231, v176, v177\n"
      "v_lshlrev_b32 v176, 16, v232\n"
      "v_and_b32 v177, s43, v232\n"
      "v_fmaak_f32 v176, v178, v176, 0x12800000\n"
      "v_fmaak_f32 v177, v178, v177, 0x12800000\n"
      "v_add_f32 v176, 0x92800000, v176\n"
      "v_add_f32 v177, 0x92800000, v177\n"
      "v_cvt_pk_bf16_f32 v232, v176, v177\n"
      "v_lshlrev_b32 v176, 16, v233\n"
      "v_and_b32 v177, s43, v233\n"
      "v_fmaak_f32 v176, v178, v176, 0x12800000\n"
      "v_fmaak_f32 v177, v178, v177, 0x12800000\n"
      "v_add_f32 v176, 0x92800000, v176\n"
      "v_add_f32 v177, 0x92800000, v177\n"
      "v_cvt_pk_bf16_f32 v233, v176, v177\n"
      "v_lshlrev_b32 v176, 16, v234\n"
      "v_and_b32 v177, s43, v234\n"
      "v_fmaak_f32 v176, v178, v176, 0x12800000\n"
      "v_fmaak_f32 v177, v178, v177, 0x12800000\n"
      "v_add_f32 v176, 0x92800000, v176\n"
      "v_add_f32 v177, 0x92800000, v177\n"
      "v_cvt_pk_bf16_f32 v234, v176, v177\n"
      "v_lshlrev_b32 v176, 16, v235\n"
      "v_and_b32 v177, s43, v235\n"
      "v_fmaak_f32 v176, v178, v176, 0x12800000\n"
      "v_fmaak_f32 v177, v178, v177, 0x12800000\n"
      "v_add_f32 v176, 0x92800000, v176\n"
      "v_add_f32 v177, 0x92800000, v177\n"
      "v_cvt_pk_bf16_f32 v235, v176, v177\n"
      "v_lshlrev_b32 v176, 16, v236\n"
      "v_and_b32 v177, s43, v236\n"
      "v_fmaak_f32 v176, v178, v176, 0x12800000\n"
      "v_fmaak_f32 v177, v178, v177, 0x12800000\n"
      "v_add_f32 v176, 0x92800000, v176\n"
      "v_add_f32 v177, 0x92800000, v177\n"
      "v_cvt_pk_bf16_f32 v236, v176, v177\n"
      "v_lshlrev_b32 v176, 16, v237\n"
      "v_and_b32 v177, s43, v237\n"
      "v_fmaak_f32 v176, v178, v176, 0x12800000\n"
      "v_fmaak_f32 v177, v178, v177, 0x12800000\n"
      "v_add_f32 v176, 0x92800000, v176\n"
      "v_add_f32 v177, 0x92800000, v177\n"
      "v_cvt_pk_bf16_f32 v237, v176, v177\n"
      "v_lshlrev_b32 v176, 16, v238\n"
      "v_and_b32 v177, s43, v238\n"
      "v_fmaak_f32 v176, v178, v176, 0x12800000\n"
      "v_fmaak_f32 v177, v178, v177, 0x12800000\n"
      "v_add_f32 v176, 0x92800000, v176\n"
      "v_add_f32 v177, 0x92800000, v177\n"
      "v_cvt_pk_bf16_f32 v238, v176, v177\n"
      "v_lshlrev_b32 v176, 16, v239\n"
      "v_and_b32 v177, s43, v239\n"
      "v_fmaak_f32 v176, v178, v176, 0x12800000\n"
      "v_fmaak_f32 v177, v178, v177, 0x12800000\n"
      "v_add_f32 v176, 0x92800000, v176\n"
      "v_add_f32 v177, 0x92800000, v177\n"
      "v_cvt_pk_bf16_f32 v239, v176, v177\n"
      "v_lshlrev_b32 v176, 16, v240\n"
      "v_and_b32 v177, s43, v240\n"
      "v_fmaak_f32 v176, v178, v176, 0x12800000\n"
      "v_fmaak_f32 v177, v178, v177, 0x12800000\n"
      "v_add_f32 v176, 0x92800000, v176\n"
      "v_add_f32 v177, 0x92800000, v177\n"
      "v_cvt_pk_bf16_f32 v240, v176, v177\n"
      "v_lshlrev_b32 v176, 16, v241\n"
      "v_and_b32 v177, s43, v241\n"
      "v_fmaak_f32 v176, v178, v176, 0x12800000\n"
      "v_fmaak_f32 v177, v178, v177, 0x12800000\n"
      "v_add_f32 v176, 0x92800000, v176\n"
      "v_add_f32 v177, 0x92800000, v177\n"
      "v_cvt_pk_bf16_f32 v241, v176, v177\n"
      "v_lshlrev_b32 v176, 16, v242\n"
      "v_and_b32 v177, s43, v242\n"
      "v_fmaak_f32 v176, v178, v176, 0x12800000\n"
      "v_fmaak_f32 v177, v178, v177, 0x12800000\n"
      "v_add_f32 v176, 0x92800000, v176\n"
      "v_add_f32 v177, 0x92800000, v177\n"
      "v_cvt_pk_bf16_f32 v242, v176, v177\n"
      "v_lshlrev_b32 v176, 16, v243\n"
      "v_and_b32 v177, s43, v243\n"
      "v_fmaak_f32 v176, v178, v176, 0x12800000\n"
      "v_fmaak_f32 v177, v178, v177, 0x12800000\n"
      "v_add_f32 v176, 0x92800000, v176\n"
      "v_add_f32 v177, 0x92800000, v177\n"
      "v_cvt_pk_bf16_f32 v243, v176, v177\n"
      "v_lshlrev_b32 v176, 16, v244\n"
      "v_and_b32 v177, s43, v244\n"
      "v_fmaak_f32 v176, v178, v176, 0x12800000\n"
      "v_fmaak_f32 v177, v178, v177, 0x12800000\n"
      "v_add_f32 v176, 0x92800000, v176\n"
      "v_add_f32 v177, 0x92800000, v177\n"
      "v_cvt_pk_bf16_f32 v244, v176, v177\n"
      "v_lshlrev_b32 v176, 16, v245\n"
      "v_and_b32 v177, s43, v245\n"
      "v_fmaak_f32 v176, v178, v176, 0x12800000\n"
      "v_fmaak_f32 v177, v178, v177, 0x12800000\n"
      "v_add_f32 v176, 0x92800000, v176\n"
      "v_add_f32 v177, 0x92800000, v177\n"
      "v_cvt_pk_bf16_f32 v245, v176, v177\n"
      "v_lshlrev_b32 v176, 16, v246\n"
      "v_and_b32 v177, s43, v246\n"
      "v_fmaak_f32 v176, v178, v176, 0x12800000\n"
      "v_fmaak_f32 v177, v178, v177, 0x12800000\n"
      "v_add_f32 v176, 0x92800000, v176\n"
      "v_add_f32 v177, 0x92800000, v177\n"
      "v_cvt_pk_bf16_f32 v246, v176, v177\n"
      "v_lshlrev_b32 v176, 16, v247\n"
      "v_and_b32 v177, s43, v247\n"
      "v_fmaak_f32 v176, v178, v176, 0x12800000\n"
      "v_fmaak_f32 v177, v178, v177, 0x12800000\n"
      "v_add_f32 v176, 0x92800000, v176\n"
      "v_add_f32 v177, 0x92800000, v177\n"
      "v_cvt_pk_bf16_f32 v247, v176, v177\n"
      "v_lshlrev_b32 v176, 16, v248\n"
      "v_and_b32 v177, s43, v248\n"
      "v_fmaak_f32 v176, v178, v176, 0x12800000\n"
      "v_fmaak_f32 v177, v178, v177, 0x12800000\n"
      "v_add_f32 v176, 0x92800000, v176\n"
      "v_add_f32 v177, 0x92800000, v177\n"
      "v_cvt_pk_bf16_f32 v248, v176, v177\n"
      "v_lshlrev_b32 v176, 16, v249\n"
      "v_and_b32 v177, s43, v249\n"
      "v_fmaak_f32 v176, v178, v176, 0x12800000\n"
      "v_fmaak_f32 v177, v178, v177, 0x12800000\n"
      "v_add_f32 v176, 0x92800000, v176\n"
      "v_add_f32 v177, 0x92800000, v177\n"
      "v_cvt_pk_bf16_f32 v249, v176, v177\n"
      "v_lshlrev_b32 v176, 16, v250\n"
      "v_and_b32 v177, s43, v250\n"
      "v_fmaak_f32 v176, v178, v176, 0x12800000\n"
      "v_fmaak_f32 v177, v178, v177, 0x12800000\n"
      "v_add_f32 v176, 0x92800000, v176\n"
      "v_add_f32 v177, 0x92800000, v177\n"
      "v_cvt_pk_bf16_f32 v250, v176, v177\n"
      "v_lshlrev_b32 v176, 16, v251\n"
      "v_and_b32 v177, s43, v251\n"
      "v_fmaak_f32 v176, v178, v176, 0x12800000\n"
      "v_fmaak_f32 v177, v178, v177, 0x12800000\n"
      "v_add_f32 v176, 0x92800000, v176\n"
      "v_add_f32 v177, 0x92800000, v177\n"
      "v_cvt_pk_bf16_f32 v251, v176, v177\n"
      "v_lshlrev_b32 v176, 16, v252\n"
      "v_and_b32 v177, s43, v252\n"
      "v_fmaak_f32 v176, v178, v176, 0x12800000\n"
      "v_fmaak_f32 v177, v178, v177, 0x12800000\n"
      "v_add_f32 v176, 0x92800000, v176\n"
      "v_add_f32 v177, 0x92800000, v177\n"
      "v_cvt_pk_bf16_f32 v252, v176, v177\n"
      "v_lshlrev_b32 v176, 16, v253\n"
      "v_and_b32 v177, s43, v253\n"
      "v_fmaak_f32 v176, v178, v176, 0x12800000\n"
      "v_fmaak_f32 v177, v178, v177, 0x12800000\n"
      "v_add_f32 v176, 0x92800000, v176\n"
      "v_add_f32 v177, 0x92800000, v177\n"
      "v_cvt_pk_bf16_f32 v253, v176, v177\n"
      "v_lshlrev_b32 v176, 16, v254\n"
      "v_and_b32 v177, s43, v254\n"
      "v_fmaak_f32 v176, v178, v176, 0x12800000\n"
      "v_fmaak_f32 v177, v178, v177, 0x12800000\n"
      "v_add_f32 v176, 0x92800000, v176\n"
      "v_add_f32 v177, 0x92800000, v177\n"
      "v_cvt_pk_bf16_f32 v254, v176, v177\n"
      "v_lshlrev_b32 v176, 16, v255\n"
      "v_and_b32 v177, s43, v255\n"
      "v_fmaak_f32 v176, v178, v176, 0x12800000\n"
      "v_fmaak_f32 v177, v178, v177, 0x12800000\n"
      "v_add_f32 v176, 0x92800000, v176\n"
      "v_add_f32 v177, 0x92800000, v177\n"
      "v_cvt_pk_bf16_f32 v255, v176, v177\n"
      "v_accvgpr_write_b32 a216, v244\n"
      "v_accvgpr_write_b32 a217, v245\n"
      "v_accvgpr_write_b32 a218, v246\n"
      "v_accvgpr_write_b32 a219, v247\n"
      "v_accvgpr_write_b32 a220, v248\n"
      "v_accvgpr_write_b32 a221, v249\n"
      "v_accvgpr_write_b32 a222, v250\n"
      "v_accvgpr_write_b32 a223, v251\n"
      "v_accvgpr_write_b32 a224, v252\n"
      "v_accvgpr_write_b32 a225, v253\n"
      "v_accvgpr_write_b32 a226, v254\n"
      "v_accvgpr_write_b32 a227, v255\n"
      "s_branch 3b\n"
      "7:\n"
      "s_mov_b64 exec, -1\n"
      "s_cmp_lg_u32 s41, 0\n"
      "s_cbranch_scc1 8f\n"
      "s_mov_b32 s41, 1\n"
      "s_waitcnt vmcnt(0)\n"
      "s_branch 0b\n"
      "17:\n"
      "v_mfma_f32_16x16x32_f16 v[180:183], a[0:3], a[216:219], v[16:19]\n"
      "v_mfma_f32_16x16x32_f16 v[180:183], a[4:7], a[220:223], v[180:183]\n"
      "v_mfma_f32_16x16x32_f16 v[180:183], a[8:11], a[224:227], v[180:183]\n"
      "v_mfma_f32_16x16x32_f16 v[180:183], a[12:15], a[228:231], v[180:183]\n"
      "s_nop 7\n"
      "v_mul_f32 v184, |v180|, v48\n"
      "v_fma_f32 v184, |v181|, v49, v184\n"
      "v_fma_f32 v184, |v182|, v50, v184\n"
      "v_fma_f32 v184, |v183|, v51, v184\n"
      "v_mfma_f32_16x16x32_f16 v[180:183], a[76:79], a[228:231], v[32:35]\n"
      "v_mfma_f32_16x16x32_f16 v[180:183], a[72:75], a[224:227], v[180:183]\n"
      "v_mfma_f32_16x16x32_f16 v[180:183], a[68:71], a[220:223], v[180:183]\n"
      "v_mfma_f32_16x16x32_f16 v[180:183], a[64:67], a[216:219], v[180:183]\n"
      "s_nop 7\n"
      "v_fma_f32 v184, |v180|, v64, v184\n"
      "v_fma_f32 v184, |v181|, v65, v184\n"
      "v_fma_f32 v184, |v182|, v66, v184\n"
      "v_fma_f32 v184, |v183|, v67, v184\n"
      "v_mfma_f32_16x16x32_f16 v[180:183], a[16:19], a[216:219], v[20:23]\n"
      "v_mfma_f32_16x16x32_f16 v[180:183], a[20:23], a[220:223], v[180:183]\n"
      "v_mfma_f32_16x16x32_f16 v[180:183], a[24:27], a[224:227], v[180:183]\n"
      "v_mfma_f32_16x16x32_f16 v[180:183], a[28:31], a[228:231], v[180:183]\n"
      "s_nop 7\n"
      "v_mul_f32 v185, |v180|, v52\n"
      "v_fma_f32 v185, |v181|, v53, v185\n"
      "v_fma_f32 v185, |v182|, v54, v185\n"
      "v_fma_f32 v185, |v183|, v55, v185\n"
      "v_mfma_f32_16x16x32_f16 v[180:183], a[92:95], a[228:231], v[36:39]\n"
      "v_mfma_f32_16x16x32_f16 v[180:183], a[88:91], a[224:227], v[180:183]\n"
      "v_mfma_f32_16x16x32_f16 v[180:183], a[84:87], a[220:223], v[180:183]\n"
      "v_mfma_f32_16x16x32_f16 v[180:183], a[80:83], a[216:219], v[180:183]\n"
      "s_nop 7\n"
      "v_fma_f32 v185, |v180|, v68, v185\n"
      "v_fma_f32 v185, |v181|, v69, v185\n"
      "v_fma_f32 v185, |v182|, v70, v185\n"
      "v_fma_f32 v185, |v183|, v71, v185\n"
      "v_mfma_f32_16x16x32_f16 v[180:183], a[32:35], a[216:219], v[24:27]\n"
      "v_mfma_f32_16x16x32_f16 v[180:183], a[36:39], a[220:223], v[180:183]\n"
      "v_mfma_f32_16x16x32_f16 v[180:183], a[40:43], a[224:227], v[180:183]\n"
      "v_mfma_f32_16x16x32_f16 v[180:183], a[44:47], a[228:231], v[180:183]\n"
      "s_nop 7\n"
      "v_mul_f32 v186, |v180|, v56\n"
      "v_fma_f32 v186, |v181|, v57, v186\n"
      "v_fma_f32 v186, |v182|, v58, v186\n"
      "v_fma_f32 v186, |v183|, v59, v186\n"
      "v_mfma_f32_16x16x32_f16 v[180:183], a[108:111], a[228:231], v[40:43]\n"
      "v_mfma_f32_16x16x32_f16 v[180:183], a[104:107], a[224:227], v[180:183]\n"
      "v_mfma_f32_16x16x32_f16 v[180:183], a[100:103], a[220:223], v[180:183]\n"
      "v_mfma_f32_16x16x32_f16 v[180:183], a[96:99], a[216:219], v[180:183]\n"
      "s_nop 7\n"
      "v_fma_f32 v186, |v180|, v72, v186\n"
      "v_fma_f32 v186, |v181|, v73, v186\n"
      "v_fma_f32 v186, |v182|, v74, v186\n"
      "v_fma_f32 v186, |v183|, v75, v186\n"
      "v_mfma_f32_16x16x32_f16 v[180:183], a[48:51], a[216:219], v[28:31]\n"
      "v_mfma_f32_16x16x32_f16 v[180:183], a[52:55], a[220:223], v[180:183]\n"
      "v_mfma_f32_16x16x32_f16 v[180:183], a[56:59], a[224:227], v[180:183]\n"
      "v_mfma_f32_16x16x32_f16 v[180:183], a[60:63], a[228:231], v[180:183]\n"
      "s_nop 7\n"
      "v_mul_f32 v187, |v180|, v60\n"
      "v_fma_f32 v187, |v181|, v61, v187\n"
      "v_fma_f32 v187, |v182|, v62, v187\n"
      "v_fma_f32 v187, |v183|, v63, v187\n"
      "v_mfma_f32_16x16x32_f16 v[180:183], a[124:127], a[228:231], v[44:47]\n"
      "v_mfma_f32_16x16x32_f16 v[180:183], a[120:123], a[224:227], v[180:183]\n"
      "v_mfma_f32_16x16x32_f16 v[180:183], a[116:119], a[220:223], v[180:183]\n"
      "v_mfma_f32_16x16x32_f16 v[180:183], a[112:115], a[216:219], v[180:183]\n"
      "s_nop 7\n"
      "v_fma_f32 v187, |v180|, v76, v187\n"
      "v_fma_f32 v187, |v181|, v77, v187\n"
      "v_fma_f32 v187, |v182|, v78, v187\n"
      "v_fma_f32 v187, |v183|, v79, v187\n"
      "v_pk_add_f32 v[184:185], v[184:185], v[186:187]\n"
      "v_add_f32 v199, v184, v185\n"
      "s_nop 1\n"
      "s_branch 18b\n"
      "19:\n"
      "s_mov_b64 exec, -1\n"
      "s_cmp_lt_i32 s99, 0\n"
      "s_cbranch_scc1 7b\n"
      "v_max3_f32 v224, v188, v199, v196\n"
      "v_max_f32 v224, 0, v224\n"
      "s_nop 1\n"
      "v_max_f32_dpp v225, v224, v224 row_ror:4 row_mask:0xf bank_mask:0xf\n"
      "v_max_f32_dpp v225, v224, v225 row_ror:8 row_mask:0xf bank_mask:0xf\n"
      "v_max_f32_dpp v225, v224, v225 row_ror:12 row_mask:0xf bank_mask:0xf\n"
      "s_nop 1\n"
      "v_max_f32_dpp v224, v225, v225 row_ror:1 row_mask:0xf bank_mask:0xf\n"
      "v_max_f32_dpp v224, v225, v224 row_ror:2 row_mask:0xf bank_mask:0xf\n"
      "v_max_f32_dpp v224, v225, v224 row_ror:3 row_mask:0xf bank_mask:0xf\n"
      "s_nop 1\n"
      "v_max_f32_dpp v224, v224, v224 row_bcast:15 row_mask:0xa bank_mask:0xf\n"
      "s_nop 1\n"
      "v_max_f32_dpp v224, v224, v224 row_bcast:31 row_mask:0xc bank_mask:0xf\n"
      "s_nop 0\n"
      "v_readlane_b32 s44, v224, 63\n"
      "s_nop 1\n"
      "v_exp_f32 v222, -s44\n"
      "s_nop 0\n"
      "v_mul_f32 v214, v220, v222\n"
      "v_mul_f32 v226, v221, v222\n"
      "v_subrev_f32 v188, s44, v188\n"
      "v_exp_f32 v222, v188\n"
      "v_fmac_f32 v214, 0, v188\n"
      "v_add_f32 v214, v214, v222\n"
      "v_fmac_f32 v226, v222, v235\n"
      "v_subrev_f32 v199, s44, v199\n"
      "v_exp_f32 v222, v199\n"
      "v_fmac_f32 v214, 0, v199\n"
      "v_add_f32 v214, v214, v222\n"
      "v_fmac_f32_dpp v226, v181, v222 quad_perm:[0,1,2,3] row_mask:0x1 bank_mask:0x3\n"
      "v_subrev_f32 v196, s44, v196\n"
      "v_exp_f32 v222, v196\n"
      "v_fmac_f32 v214, 0, v196\n"
      "v_add_f32 v214, v214, v222\n"
      "v_fmac_f32 v226, v222, v237\n"
      "s_waitcnt vmcnt(0)\n"
      "v_add_f32_dpp v214, v214, v214 row_ror:8 row_mask:0xf bank_mask:0xf\n"
      "v_add_f32_dpp v226, v226, v226 row_ror:8 row_mask:0xf bank_mask:0xf\n"
      "s_nop 0\n"
      "v_add_f32_dpp v214, v214, v214 row_ror:4 row_mask:0xf bank_mask:0xf\n"
      "v_add_f32_dpp v226, v226, v226 row_ror:4 row_mask:0xf bank_mask:0xf\n"
      "s_nop 0\n"
      "v_add_f32_dpp v214, v214, v214 row_ror:2 row_mask:0xf bank_mask:0xf\n"
      "v_add_f32_dpp v226, v226, v226 row_ror:2 row_mask:0xf bank_mask:0xf\n"
      "s_nop 0\n"
      "v_add_f32_dpp v214, v214, v214 row_ror:1 row_mask:0xf bank_mask:0xf\n"
      "v_add_f32_dpp v226, v226, v226 row_ror:1 row_mask:0xf bank_mask:0xf\n"
      "s_nop 0\n"
      "v_add_f32_dpp v214, v214, v214 row_bcast:15 row_mask:0xa bank_mask:0xf\n"
      "v_add_f32_dpp v226, v226, v226 row_bcast:15 row_mask:0xa bank_mask:0xf\n"
      "s_nop 0\n"
      "v_add_f32_dpp v214, v214, v214 row_bcast:31 row_mask:0xc bank_mask:0xf\n"
      "v_add_f32_dpp v226, v226, v226 row_bcast:31 row_mask:0xc bank_mask:0xf\n"
      "v_rcp_f32 v222, v214\n"
      "v_ldexp_f32 v226, v226, s39\n"
      "s_lshl_b64 exec, 1, 63\n"
      "v_mul_f32 v226, v226, v222\n"
      "v_cvt_pk_bf16_f32 v222, v226, v226\n"
      "global_store_short v3, v222, s[24:25]\n"
      "v_cmp_class_f32 vcc, v226, s45\n"
      "s_nop 0\n"
      "s_cmp_lt_i32 vcc_hi, 0\n"
      "s_cbranch_scc1 7b\n"
      "s_branch 20b\n"
      "8:\n"
      "s_mov_b32 s42, 1\n"
      "9:\n"
      "s_nop 7\n"
      "s_waitcnt vmcnt(0) lgkmcnt(0)\n"
      "s_mov_b32 %0, s42\n"
      : "=s"(flag)
      : "s"(__builtin_amdgcn_kernarg_segment_ptr()), "s"(blockIdx.x), "v"(threadIdx.x), "s"(lbase)
      :
      "v1", "v2", "v3", "v4", "v5", "v6", "v7", "v8", "v9", "v10", "v11", "v12", "v13", "v14", "v15", "v16", "v17", "v18", "v19", "v20", "v21", "v22", "v23", "v24",
      "v25", "v26", "v27", "v28", "v29", "v30", "v31", "v32", "v33", "v34", "v35", "v36", "v37", "v38", "v39", "v40", "v41", "v42", "v43", "v44", "v45", "v46", "v47", "v48",
      "v49", "v50", "v51", "v52", "v53", "v54", "v55", "v56", "v57", "v58", "v59", "v60", "v61", "v62", "v63", "v64", "v65", "v66", "v67", "v68", "v69", "v70", "v71", "v72",
      "v73", "v74", "v75", "v76", "v77", "v78", "v79", "v80", "v81", "v82", "v83", "v84", "v85", "v86", "v87", "v88", "v89", "v90", "v91", "v92", "v93", "v94", "v95", "v96",
      "v97", "v98", "v99", "v100", "v101", "v102", "v103", "v104", "v105", "v106", "v107", "v108", "v109", "v110", "v111", "v112", "v113", "v114", "v115", "v116", "v117", "v118", "v119", "v120",
      "v121", "v122", "v123", "v124", "v125", "v126", "v127", "v128", "v129", "v130", "v131", "v132", "v133", "v134", "v135", "v136", "v137", "v138", "v139", "v140", "v141", "v142", "v143", "v144",
      "v145", "v146", "v147", "v148", "v149", "v150", "v151", "v152", "v153", "v154", "v155", "v156", "v157", "v158", "v159", "v160", "v161", "v162", "v163", "v164", "v165", "v166", "v167", "v168",
      "v169", "v170", "v171", "v172", "v173", "v174", "v175", "v176", "v177", "v178", "v179", "v180", "v181", "v182", "v183", "v184", "v185", "v186", "v187", "v188", "v189", "v190", "v191", "v192",
      "v193", "v194", "v195", "v196", "v197", "v198", "v199", "v200", "v201", "v202", "v203", "v204", "v205", "v206", "v207", "v208", "v209", "v210", "v211", "v212", "v213", "v214", "v215", "v216",
      "v217", "v218", "v219", "v220", "v221", "v222", "v223", "v224", "v225", "v226", "v227", "v228", "v229", "v230", "v231", "v232", "v233", "v234", "v235", "v236", "v237", "v238", "v239", "v240",
      "v241", "v242", "v243", "v244", "v245", "v246", "v247", "v248", "v249", "v250", "v251", "v252", "v253", "v254", "v255", "a0", "a1", "a2", "a3", "a4", "a5", "a6", "a7", "a8",
      "a9", "a10", "a11", "a12", "a13", "a14", "a15", "a16", "a17", "a18", "a19", "a20", "a21", "a22", "a23", "a24", "a25", "a26", "a27", "a28", "a29", "a30", "a31", "a32",
      "a33", "a34", "a35", "a36", "a37", "a38", "a39", "a40", "a41", "a42", "a43", "a44", "a45", "a46", "a47", "a48", "a49", "a50", "a51", "a52", "a53", "a54", "a55", "a56",
      "a57", "a58", "a59", "a60", "a61", "a62", "a63", "a64", "a65", "a66", "a67", "a68", "a69", "a70", "a71", "a72", "a73", "a74", "a75", "a76", "a77", "a78", "a79", "a80",
      "a81", "a82", "a83", "a84", "a85", "a86", "a87", "a88", "a89", "a90", "a91", "a92", "a93", "a94", "a95", "a96", "a97", "a98", "a99", "a100", "a101", "a102", "a103", "a104",
      "a105", "a106", "a107", "a108", "a109", "a110", "a111", "a112", "a113", "a114", "a115", "a116", "a117", "a118", "a119", "a120", "a121", "a122", "a123", "a124", "a125", "a126", "a127", "a128",
      "a129", "a130", "a131", "a132", "a133", "a134", "a135", "a136", "a137", "a138", "a139", "a140", "a141", "a142", "a143", "a144", "a145", "a146", "a147", "a148", "a149", "a150", "a151", "a152",
      "a153", "a154", "a155", "a156", "a157", "a158", "a159", "a160", "a161", "a162", "a163", "a164", "a165", "a166", "a167", "a168", "a169", "a170", "a171", "a172", "a173", "a174", "a175", "a176",
      "a177", "a178", "a179", "a180", "a181", "a182", "a183", "a184", "a185", "a186", "a187", "a188", "a189", "a190", "a191", "a192", "a193", "a194", "a195", "a196", "a197", "a198", "a199", "a200",
      "a201", "a202", "a203", "a204", "a205", "a206", "a207", "a208", "a209", "a210", "a211", "a212", "a213", "a214", "a215", "a216", "a217", "a218", "a219", "a220", "a221", "a222", "a223", "a224",
      "a225", "a226", "a227", "a228", "a229", "a230", "a231", "a232", "a233", "a234", "a235", "a236", "a237", "a238", "a239", "a240", "a241", "a242", "a243", "a244", "a245", "a246", "a247", "s8",
      "s9", "s10", "s11", "s12", "s13", "s14", "s15", "s16", "s17", "s18", "s19", "s20", "s21", "s22", "s23", "s24", "s25", "s26", "s27", "s28", "s29", "s30", "s31", "s33",
      "s34", "s35", "s36", "s37", "s38", "s39", "s40", "s41", "s42", "s43", "s44", "s45", "s46", "s47", "s48", "s49", "s50", "s51", "s52", "s53", "s54", "s55", "s56", "s57",
      "s58", "s59", "s60", "s61", "s62", "s63", "s64", "s65", "s66", "s67", "s68", "s69", "s70", "s71", "s72", "s73", "s74", "s75", "s76", "s77", "s78", "s79", "s80", "s81",
      "s82", "s83", "s84", "s85", "s86", "s87", "s88", "s89", "s90", "s91", "s92", "s93", "s94", "s95", "s96", "s97", "s98", "s99", "s100", "s101", "vcc", "scc", "m0", "memory");
  if (flag | lbase) {
    const int F = 26, P = 325;
    const int lane = threadIdx.x & 63;
    const int wg = (int)((blockIdx.x & ~255u) + (blockIdx.x & 7) * 32 + ((blockIdx.x >> 3) & 31));
    const int b = min(wg * 4 + (int)(threadIdx.x >> 6), B - 1);
    {
      float m = -INFINITY, z = 0.f, n = 0.f;
      for (int p = lane; p < P; p += 64) {
        const unsigned short* ei = table + (size_t)idx[b * F + ii[p]] * 128;
        const unsigned short* ej = table + (size_t)idx[b * F + jj[p]] * 128;
        float e = 0.f, s = 0.f;
        for (int a = 0; a < 128; ++a) {
          float acc = 0.f;
          for (int d = 0; d < 128; ++d) acc = fmaf(rbf(bf2f(ei[d]) * bf2f(ej[d])), bf2f(W[d * 128 + a]), acc);
          float x = acc + bf2f(bvec[a]);
          x = 0.5f * (x + fabsf(x));
          e = fmaf(x, bf2f(hvec[a]), e);
        }
        for (int d = 0; d < 128; ++d) s = fmaf(rbf(bf2f(ei[d]) * bf2f(ej[d])), bf2f(pvec[d]), s);
        float mn = fmaxf(m, e);
        float fa = __expf(m - mn), fb = __expf(e - mn);
        z = z * fa + fb; n = n * fa + fb * s; m = mn;
      }
      float mx = m;
      for (int o = 1; o < 64; o <<= 1) mx = fmaxf(mx, __shfl_xor(mx, o));
      float f = (m == -INFINITY) ? 0.f : __expf(m - mx);
      z *= f; n *= f;
      for (int o = 1; o < 64; o <<= 1) { z += __shfl_xor(z, o); n += __shfl_xor(n, o); }
      if (lane == 0) out[b] = f2bf(n / z);
    }
  }
}

__global__ void afm_generic_kernel(const int* __restrict__ idx, const unsigned short* __restrict__ table,
                                   const int* __restrict__ ii, const int* __restrict__ jj,
                                   const unsigned short* __restrict__ W, const unsigned short* __restrict__ bvec,
                                   const unsigned short* __restrict__ hvec, const unsigned short* __restrict__ pvec,
                                   unsigned short* __restrict__ out, float* __restrict__ ws,
                                   int B, int F, int P, int D, int A) {
  const int b = blockIdx.x;
  float* ebuf = ws + (size_t)b * 2 * P;
  float* sbuf = ebuf + P;
  for (int p = threadIdx.x; p < P; p += blockDim.x) {
    const unsigned short* ei = table + (size_t)idx[b * F + ii[p]] * D;
    const unsigned short* ej = table + (size_t)idx[b * F + jj[p]] * D;
    float e = 0.f, sacc = 0.f;
    for (int a = 0; a < A; ++a) {
      float acc = 0.f;
      for (int d = 0; d < D; ++d) acc = fmaf(rbf(bf2f(ei[d]) * bf2f(ej[d])), bf2f(W[(size_t)d * A + a]), acc);
      float x = acc + bf2f(bvec[a]);
      x = 0.5f * (x + fabsf(x));
      e = fmaf(x, bf2f(hvec[a]), e);
    }
    for (int d = 0; d < D; ++d) sacc = fmaf(rbf(bf2f(ei[d]) * bf2f(ej[d])), bf2f(pvec[d]), sacc);
    ebuf[p] = e; sbuf[p] = sacc;
  }
  __syncthreads();
  if (threadIdx.x == 0) {
    float mx = -INFINITY;
    for (int p = 0; p < P; ++p) mx = fmaxf(mx, ebuf[p]);
    float z = 0.f, n = 0.f;
    for (int p = 0; p < P; ++p) { float w = __expf(ebuf[p] - mx); z += w; n += w * sbuf[p]; }
    out[b] = f2bf(n / z);
  }
}

extern "C" void kernel_launch(void* const* d_in, const int* in_sizes, int n_in,
                              void* d_out, int out_size, void* d_ws, size_t ws_size,
                              hipStream_t stream) {
  const int* idx = (const int*)d_in[0];
  const unsigned short* table = (const unsigned short*)d_in[1];
  const int* ii = (const int*)d_in[2];
  const int* jj = (const int*)d_in[3];
  const unsigned short* W = (const unsigned short*)d_in[4];
  const unsigned short* bvec = (const unsigned short*)d_in[5];
  const unsigned short* hvec = (const unsigned short*)d_in[6];
  const unsigned short* pvec = (const unsigned short*)d_in[7];
  unsigned short* out = (unsigned short*)d_out;
  const int B = out_size;
  const int P = in_sizes[2];
  const int D = in_sizes[7];
  const int A = in_sizes[5];
  if (B <= 0) return;
  const int F = in_sizes[0] / B;
  if (D == 128 && A == 128 && F == 26 && P == 325 && B < (1 << 24)) {
    const int G = (B + 1023) / 1024 * 256;
    afm_fast_kernel<<<G, 256, 0, stream>>>(idx, table, ii, jj, W, bvec, hvec, pvec, out, B, 0
        );
  } else {
    afm_generic_kernel<<<B, 256, 0, stream>>>(idx, table, ii, jj, W, bvec, hvec, pvec, out, (float*)d_ws, B, F, P, D, A);
  }
}
```
